```python
import torch

def _softmax(x, axis=-1):
    return torch.softmax(x, dim=axis)
def _gelu(x):
    return torch.nn.functional.gelu(x, approximate="tanh")
def _silu(x):
    return torch.nn.functional.silu(x)
def _sigmoid(x):
    return torch.sigmoid(x)
def _layernorm(x, axis=-1, eps=1e-5):
    m = x.mean(dim=axis, keepdim=True)
    v = x.var(dim=axis, keepdim=True, unbiased=False)
    return (x - m) / torch.sqrt(v + eps)
def _scatter_add(tgt, idx, src, axis=0):
    return tgt.clone().index_add_(axis, idx, src)
def _segment_sum(src, seg, n):
    out = torch.zeros((n,) + src.shape[1:], dtype=src.dtype)
    return out.index_add_(0, seg, src)
def _pad(x, pw):
    flat = tuple(v for pair in reversed(pw) for v in pair)
    return torch.nn.functional.pad(x, flat)

kernel_name = 'recurrent_scan__sliding_block_cached__glu_gated_geglu__prompt'

def setup_inputs():

    _w = (lambda a: (lambda t: t.to(torch.bfloat16) if t.is_floating_point() else t)(torch.as_tensor(a)))
    def _A_setup():
        B, Sq, D = 1, 2048, 640
        Hq, Hkv, Sk, Dh = 32, 32, 2048, 192
        assert Hq % Hkv == 0, (Hq, Hkv)
        return {
            "x": _w((lambda *s: torch.randn(*s, dtype=torch.bfloat16) * 0.05)(B, Sq, D)),
            "k_cache": _w((lambda *s: torch.randn(*s, dtype=torch.bfloat16) * 3.2)(B, Hkv, Sk, Dh)),
            "v_cache": _w((lambda *s: torch.randn(*s, dtype=torch.bfloat16) * 3.2)(B, Hkv, Sk, Dh)),
            "wq": _w((lambda *s: torch.randn(*s, dtype=torch.bfloat16) * 3.2)(D, Hq * Dh) * (D ** -0.5)),
            "wo": _w((lambda *s: torch.randn(*s, dtype=torch.bfloat16) * 3.2)(Hq * Dh, D) * ((Hq * Dh) ** -0.5)),
        }
    _A_in = _A_setup()

    def _B_setup():
        B, S, Dm, Df = 1, 2048, 640, 1536
        return {
            "x": _w((lambda *s: torch.randn(*s, dtype=torch.bfloat16) * 0.05)(B, S, Dm)),
            "w_gate": _w((lambda *s: torch.randn(*s, dtype=torch.bfloat16) * 0.4)(Dm, Df) * (Dm ** -0.5)),
            "w_up": _w((lambda *s: torch.randn(*s, dtype=torch.bfloat16) * 0.4)(Dm, Df) * (Dm ** -0.5)),
            "w_down": _w((lambda *s: torch.randn(*s, dtype=torch.bfloat16) * 0.4)(Df, Dm) * (Df ** -0.5)),
        }
    _B_in = _B_setup()

    return {
        "A_x": _A_in["x"],
        "A_k_cache": _A_in["k_cache"],
        "A_v_cache": _A_in["v_cache"],
        "A_wq": _A_in["wq"],
        "A_wo": _A_in["wo"],
        "B_x": _B_in["x"],
        "B_w_gate": _B_in["w_gate"],
        "B_w_up": _B_in["w_up"],
        "B_w_down": _B_in["w_down"],
    }

def reference(A_x, A_k_cache, A_v_cache, A_wq, A_wo, B_x, B_w_gate, B_w_up, B_w_down):
    def _A_ref(x, k_cache, v_cache, wq, wo):
        B, Sq, D = x.shape
        Hq, Dh = 32, 192
        Hkv, Sk = k_cache.shape[1], k_cache.shape[2]
        W = min(4096, max(256, Sk // 4))
        rep = Hq // Hkv
        q = (lambda x, *d: x.permute(*d))((x @ wq).reshape(B, Sq, Hq, Dh), 0, 2, 1, 3)
        k = (lambda x, n, axis: torch.repeat_interleave(x, n, dim=axis))(k_cache[:, :, Sk - W:, :], rep, axis=1)
        v = (lambda x, n, axis: torch.repeat_interleave(x, n, dim=axis))(v_cache[:, :, Sk - W:, :], rep, axis=1)
        scores = torch.einsum("bhqd,bhkd->bhqk", q, k) * (Dh ** -0.5)
        p = _softmax(scores, axis=-1)
        o = torch.einsum("bhqk,bhkd->bhqd", p, v)
        return (lambda x, *d: x.permute(*d))(o, 0, 2, 1, 3).reshape(B, Sq, Hq * Dh) @ wo

    def _B_ref(x, w_gate, w_up, w_down):
        g = torch.einsum("bsd,df->bsf", x, w_gate)
        u = torch.einsum("bsd,df->bsf", x, w_up)
        h = _gelu(g) * u
        return torch.einsum("bsf,fd->bsd", h, w_down)

    x = A_x
    xn = _layernorm(x, axis=-1)
    s = _A_ref(xn, A_k_cache, A_v_cache, A_wq, A_wo)
    g = _silu(_B_ref(xn, B_w_gate, B_w_up, B_w_down))
    return x + s * g
```

```cpp
#include <hip/hip_runtime.h>
#include <hip/hip_bf16.h>
#include <cstdint>
#define MIMIC 1
typedef unsigned short u16;
__device__ __forceinline__ float bf2f(u16 v){ return __uint_as_float(((unsigned)v)<<16); }
__device__ __forceinline__ u16 f2bf(float f){ unsigned u=__float_as_uint(f); u += 0x7FFF + ((u>>16)&1); return (u16)(u>>16); }
__device__ __forceinline__ float rb(float f){ return bf2f(f2bf(f)); }

__global__ void k_ln(const u16* x, u16* xn){
  int row=blockIdx.x; int t=threadIdx.x;
  float v[10]; float s=0;
  for(int i=0;i<10;i++){ v[i]=bf2f(x[row*640+t+64*i]); s+=v[i]; }
  for(int o=32;o>0;o>>=1) s+=__shfl_xor(s,o);
  float m=s/640.f;
  float q=0; for(int i=0;i<10;i++){ float d=v[i]-m; q+=d*d; }
  for(int o=32;o>0;o>>=1) q+=__shfl_xor(q,o);
  float var=q/640.f;
  float mb=rb(m), vb=rb(var);
  float sd=rb(sqrtf(rb(vb+1e-5f)));
  for(int i=0;i<10;i++){ float d=rb(v[i]-mb); xn[row*640+t+64*i]=f2bf(d/sd); }
}
template<bool BT, bool OUTF32>
__global__ void k_gemm(const u16* A, int lda, long sA, const u16* B, int ldb, long sB, void* C, int ldc, long sC, int M,int N,int K){
  __shared__ float As[16][65]; __shared__ float Bs[16][65];
  A+=sA*blockIdx.z; B+=sB*blockIdx.z;
  int tx=threadIdx.x&15, ty=threadIdx.x>>4;
  int m0=blockIdx.y*64, n0=blockIdx.x*64;
  float acc[4][4]={};
  for(int k0=0;k0<K;k0+=16){
    for(int i=threadIdx.x;i<64*16;i+=256){ int r=i>>4,c=i&15; As[c][r]=bf2f(A[(long)(m0+r)*lda+k0+c]); }
    if(BT){ for(int i=threadIdx.x;i<64*16;i+=256){ int r=i>>4,c=i&15; Bs[c][r]=bf2f(B[(long)(n0+r)*ldb+k0+c]); } }
    else  { for(int i=threadIdx.x;i<64*16;i+=256){ int c=i>>6,r=i&63; Bs[c][r]=bf2f(B[(long)(k0+c)*ldb+n0+r]); } }
    __syncthreads();
    for(int k=0;k<16;k++){
      float a[4],b[4];
      for(int i=0;i<4;i++){a[i]=As[k][ty*4+i]; b[i]=Bs[k][tx*4+i];}
      for(int i=0;i<4;i++)for(int j=0;j<4;j++)acc[i][j]+=a[i]*b[j];
    }
    __syncthreads();
  }
  for(int i=0;i<4;i++)for(int j=0;j<4;j++){
    long o=sC*blockIdx.z+(long)(m0+ty*4+i)*ldc+n0+tx*4+j;
    if(OUTF32) ((float*)C)[o]=acc[i][j]; else ((u16*)C)[o]=f2bf(acc[i][j]);
  }
}
__global__ void k_softmax(const float* S, u16* P){
  long row=blockIdx.x; int t=threadIdx.x;
  float v[8]; float mx=-1e30f; const float sc=0.07216878364870322f;
  for(int i=0;i<8;i++){ float s=S[row*512+t+64*i];
    s=rb(rb(s)*sc);
    v[i]=s; mx=fmaxf(mx,s);}
  for(int o=32;o>0;o>>=1) mx=fmaxf(mx,__shfl_xor(mx,o));
  float sum=0; for(int i=0;i<8;i++){ v[i]=__expf(v[i]-mx); sum+=v[i]; }
  for(int o=32;o>0;o>>=1) sum+=__shfl_xor(sum,o);
  for(int i=0;i<8;i++) P[row*512+t+64*i]=f2bf(v[i]/sum);
}
__global__ void k_geglu(const float* g, const float* u, u16* h, int n){
  int i=blockIdx.x*256+threadIdx.x; if(i>=n) return;
  float gg=g[i], uu=u[i];
  gg=rb(gg); uu=rb(uu);
  float ge=0.5f*gg*(1.f+tanhf(0.7978845608028654f*(gg+0.044715f*gg*gg*gg)));
  ge=rb(ge);
  h[i]=f2bf(ge*uu);
}
__global__ void k_final(const u16* x, const float* s, const float* g, u16* out, int n){
  int i=blockIdx.x*256+threadIdx.x; if(i>=n) return;
  float ss=s[i], gg=g[i];
  ss=rb(ss); gg=rb(gg);
  float si=gg/(1.f+__expf(-gg));
  si=rb(si);
  out[i]=f2bf(bf2f(x[i])+rb(ss*si));
}
extern "C" void kernel_launch(void* const* d_in, const int* in_sizes, int n_in, void* d_out, int out_size, void* d_ws, size_t ws_size, hipStream_t stream){
  const u16* x=(const u16*)d_in[0]; const u16* kc=(const u16*)d_in[1]; const u16* vc=(const u16*)d_in[2];
  const u16* wq=(const u16*)d_in[3]; const u16* wo=(const u16*)d_in[4];
  const u16* wg=(const u16*)d_in[6]; const u16* wu=(const u16*)d_in[7]; const u16* wd=(const u16*)d_in[8];
  char* ws=(char*)d_ws;
  u16* xn=(u16*)ws; ws+=2048*640*2;
  u16* q=(u16*)ws; ws+=2048L*6144*2;
  u16* o=q;
  u16* h=(u16*)ws; ws+=2048L*1536*2;
  float* s2=(float*)ws; ws+=2048L*640*4;
  float* g2=(float*)ws; ws+=2048L*640*4;
  u16* P=(u16*)ws; ws+=32L*2048*512*2;
  float* S=(float*)ws;
  float* gbuf=S; float* ubuf=S+2048L*1536;
  k_ln<<<2048,64,0,stream>>>(x,xn);
  k_gemm<false,false><<<dim3(6144/64,2048/64,1),256,0,stream>>>(xn,640,0,wq,6144,0,q,6144,0,2048,6144,640);
  k_gemm<false,true><<<dim3(1536/64,2048/64,1),256,0,stream>>>(xn,640,0,wg,1536,0,gbuf,1536,0,2048,1536,640);
  k_gemm<false,true><<<dim3(1536/64,2048/64,1),256,0,stream>>>(xn,640,0,wu,1536,0,ubuf,1536,0,2048,1536,640);
  k_geglu<<<2048*1536/256,256,0,stream>>>(gbuf,ubuf,h,2048*1536);
  k_gemm<false,true><<<dim3(640/64,2048/64,1),256,0,stream>>>(h,1536,0,wd,640,0,g2,640,0,2048,640,1536);
  k_gemm<true,true><<<dim3(512/64,2048/64,32),256,0,stream>>>(q,6144,192,kc+1536L*192,192,2048L*192,S,512,2048L*512,2048,512,192);
  k_softmax<<<32*2048,64,0,stream>>>(S,P);
  k_gemm<false,false><<<dim3(192/64,2048/64,32),256,0,stream>>>(P,512,2048L*512,vc+1536L*192,192,2048L*192,o,6144,192,2048,192,512);
  k_gemm<false,true><<<dim3(640/64,2048/64,1),256,0,stream>>>(o,6144,0,wo,640,0,s2,640,0,2048,640,6144);
  k_final<<<2048*640/256,256,0,stream>>>(x,s2,g2,(u16*)d_out,2048*640);
}
```

```cpp
#include <hip/hip_runtime.h>
#include <hip/hip_bf16.h>
typedef unsigned short u16;
typedef unsigned u32x4 __attribute__((ext_vector_type(4)));
typedef unsigned u32x2 __attribute__((ext_vector_type(2)));
typedef float f32x2 __attribute__((ext_vector_type(2)));
typedef __bf16 bf16x2 __attribute__((ext_vector_type(2)));
__device__ __forceinline__ unsigned cvtpk(float lo, float hi){ f32x2 v={lo,hi}; bf16x2 b=__builtin_convertvector(v,bf16x2); return __builtin_bit_cast(unsigned,b); }
__device__ __forceinline__ float rb(float f){ return __uint_as_float(cvtpk(0.f,f)&0xffff0000u); }
__device__ __forceinline__ float lo16(unsigned u){ return __uint_as_float(u<<16); }
__device__ __forceinline__ float hi16(unsigned u){ return __uint_as_float(u&0xffff0000u); }
__device__ __forceinline__ float xsum(float s){
  s+=__builtin_bit_cast(float,__builtin_amdgcn_update_dpp(0,__builtin_bit_cast(int,s),0x124,0xf,0xf,true));
  s+=__builtin_bit_cast(float,__builtin_amdgcn_update_dpp(0,__builtin_bit_cast(int,s),0x128,0xf,0xf,true));
  auto a=__builtin_amdgcn_permlane16_swap(__float_as_uint(s),__float_as_uint(s),false,false); s=__uint_as_float(a[0])+__uint_as_float(a[1]);
  auto c=__builtin_amdgcn_permlane32_swap(__float_as_uint(s),__float_as_uint(s),false,false); return __uint_as_float(c[0])+__uint_as_float(c[1]);
}

extern "C" __global__ __launch_bounds__(256,1) void k_main(const void* x,const void* kc,const void* vc,const void* wq,const void* wo,const void* wg,const void* wu,const void* wd,void* out,void* ws){
  extern __shared__ __attribute__((aligned(16))) char lds[];
  const unsigned w=__builtin_amdgcn_readfirstlane(threadIdx.x>>6);
  if(w<2){
    const unsigned b=blockIdx.x, l=threadIdx.x&63, q4=l&3, g=(l>>2)&3, kq=l>>4;
    const unsigned h=(b&3)*8+((b>>3)&7), i=(b&4)+(b>>6), row=i*256+h*8+w*4+q4;
    const u16* xr=(const u16*)x+(size_t)row*640;
    u32x2 r[10];
#pragma unroll
    for(int it=0;it<5;it++){ r[2*it]=*(const u32x2*)(xr+(kq+4*it)*32+4*g); r[2*it+1]=*(const u32x2*)(xr+(kq+4*it)*32+16+4*g); }
    const float piv=lo16(*(const unsigned*)xr);
    __amdgpu_buffer_rsrc_t rs=__builtin_amdgcn_make_buffer_rsrc(ws,(short)0,87560192,0x00020000);
    const unsigned gb=0+i*32768, own=(h==0 && w==0);
    if(l<9) __builtin_amdgcn_raw_buffer_store_b32(0u,rs,l==8 ? gb+h*1024+w*4 : gb+h*1024+256+l*4,0,16);
    if(own){ __builtin_amdgcn_raw_buffer_store_b32(0u,rs,gb+(l>>1)*1024+512+(l&1)*256,0,16); __builtin_amdgcn_raw_buffer_store_b32(0u,rs,262144+i*256+l*4,0,16); }
    f32x2 v[20];
#pragma unroll
    for(int j=0;j<10;j++){ v[2*j]=f32x2{lo16(r[j][0]),hi16(r[j][0])}; v[2*j+1]=f32x2{lo16(r[j][1]),hi16(r[j][1])}; }
    asm volatile(""::"v"(v[0]),"v"(v[19]),"v"(piv):"memory");
    asm volatile("s_waitcnt vmcnt(0)":::"memory");
    if(l==0) __builtin_amdgcn_raw_buffer_store_b32(1u,rs,262144+i*256+(h*2+w)*4,0,16);
    if(own) __builtin_amdgcn_raw_buffer_store_b32(1u,rs,gb+(l>>1)*1024+(l&1)*4,0,16);
    const f32x2 pv={piv,piv};
    f32x2 sa={0,0},sb={0,0},qa={0,0},qb={0,0};
#pragma unroll
    for(int j=0;j<20;j+=2){ const f32x2 d=v[j]-pv, e=v[j+1]-pv; sa+=v[j]; sb+=v[j+1]; qa=__builtin_elementwise_fma(d,d,qa); qb=__builtin_elementwise_fma(e,e,qb); }
    sa+=sb; qa+=qb;
    const float s=xsum(sa[0]+sa[1])*(1.0f/640.0f), ss=xsum(qa[0]+qa[1]);
    const float dm=s-piv, var=fmaxf(fmaf(-dm,dm,ss*(1.0f/640.0f)),0.f);
    const float mb=rb(s), sd=rb(sqrtf(rb(rb(var)+1e-5f))), rsd=1.0f/sd;
    const f32x2 mb2={mb,mb}, sd2={sd,sd}, rs2={rsd,rsd};
    const unsigned off=266240+((row>>4)*20+kq)*1024+(((row&15)>>2)*16+(l&15))*16;
    unsigned dp[20];
#pragma unroll
    for(int j=0;j<20;j++){ const f32x2 d=v[j]-mb2; dp[j]=cvtpk(d[0],d[1]); }
#pragma unroll
    for(int j=0;j<20;j++) v[j]=f32x2{lo16(dp[j]),hi16(dp[j])};
    f32x2 qq[20];
#pragma unroll
    for(int j=0;j<20;j++) qq[j]=v[j]*rs2;
#pragma unroll
    for(int j=0;j<20;j++) v[j]=__builtin_elementwise_fma(-qq[j],sd2,v[j]);
#pragma unroll
    for(int j=0;j<20;j++) qq[j]=__builtin_elementwise_fma(v[j],rs2,qq[j]);
#pragma unroll
    for(int it=0;it<5;it++)
      __builtin_amdgcn_raw_buffer_store_b128(u32x4{cvtpk(qq[4*it][0],qq[4*it][1]),cvtpk(qq[4*it+1][0],qq[4*it+1][1]),cvtpk(qq[4*it+2][0],qq[4*it+2][1]),cvtpk(qq[4*it+3][0],qq[4*it+3][1])},rs,off+it*4096,0,16);
  }
  asm volatile(
"s_nop 1\n"
"s_load_dwordx16 s[16:31], %0, 0\n"
"s_load_dwordx4 s[32:35], %0, 64\n"
"s_getpc_b64 s[56:57]\n"
".Lpc0:\n"
"s_mov_b32 s77, %1\n"
"s_mov_b32 s76, %2\n"
"s_mov_b64 exec, -1\n"
"v_mbcnt_lo_u32_b32 v0, -1, 0\n"
"v_mbcnt_hi_u32_b32 v0, -1, v0\n"
"v_lshlrev_b32 v1, 4, v0\n"
"s_sub_u32 s56, s56, .Lpc0-k_main\n"
"s_subb_u32 s57, s57, 0\n"
"s_and_b32 s57, s57, 0xffff\n"
"s_or_b32 s57, s57, 2097152\n"
"s_mov_b32 s58, 64\n"
"s_mov_b32 s59, 0x20000\n"
"s_lshl_b32 vcc_lo, %2, 11\n"
"s_cmp_lt_u32 s76, 2\n"
"s_cbranch_scc1 .Lnt0\n"
"s_add_u32 vcc_hi, vcc_lo, -4096\n"
"buffer_load_dword v27, v0, s[56:59], vcc_hi idxen\n"
"s_add_u32 vcc_hi, vcc_lo, 0\n"
"buffer_load_dword v27, v0, s[56:59], vcc_hi idxen\n"
"s_add_u32 vcc_hi, vcc_lo, 4096\n"
"buffer_load_dword v27, v0, s[56:59], vcc_hi idxen\n"
"s_add_u32 vcc_hi, vcc_lo, 8192\n"
"buffer_load_dword v27, v0, s[56:59], vcc_hi idxen\n"
".Lnt0:\n"
"s_and_b32 s98, s77, 3\n"
"s_lshl_b32 s87, s98, 3\n"
"s_bfe_u32 s88, s77, 0x30003\n"
"s_add_u32 s78, s87, s88\n"
"s_and_b32 s87, s77, 4\n"
"s_lshr_b32 s88, s77, 6\n"
"s_add_u32 s79, s87, s88\n"
"v_lshrrev_b32 v40, 3, v0\n"
"v_and_b32 v41, 7, v0\n"
"v_lshrrev_b32 v42, 1, v40\n"
"v_lshlrev_b32 v43, 1, v42\n"
"v_xor_b32 v43, v43, v41\n"
"v_lshlrev_b32 v43, 4, v43\n"
"v_mul_u32_u24 v44, 12288, v40\n"
"v_add_u32 v6, v44, v43\n"
"v_add_u32 v54, 0, v0\n"
"v_mul_u32_u24 v55, 10923, v54\n"
"v_lshrrev_b32 v55, 16, v55\n"
"v_mul_u32_u24 v56, 6, v55\n"
"v_sub_u32 v56, v54, v56\n"
"v_mul_u32_u24 v55, 3072, v55\n"
"v_lshl_add_u32 v16, v56, 4, v55\n"
"v_add_u32 v54, 64, v0\n"
"v_mul_u32_u24 v55, 10923, v54\n"
"v_lshrrev_b32 v55, 16, v55\n"
"v_mul_u32_u24 v56, 6, v55\n"
"v_sub_u32 v56, v54, v56\n"
"v_mul_u32_u24 v55, 3072, v55\n"
"v_lshl_add_u32 v17, v56, 4, v55\n"
"v_add_u32 v54, 128, v0\n"
"v_mul_u32_u24 v55, 10923, v54\n"
"v_lshrrev_b32 v55, 16, v55\n"
"v_mul_u32_u24 v56, 6, v55\n"
"v_sub_u32 v56, v54, v56\n"
"v_mul_u32_u24 v55, 3072, v55\n"
"v_lshl_add_u32 v18, v56, 4, v55\n"
"s_mov_b32 s38, 87560192\n"
"s_mov_b32 s39, 0x20000\n"
"s_mov_b32 s42, 7864320\n"
"s_mov_b32 s43, 0x20000\n"
"s_mov_b32 s54, 1966080\n"
"s_mov_b32 s55, 0x20000\n"
"s_waitcnt lgkmcnt(0)\n"
"s_mov_b32 s40, s22\n"
"s_and_b32 s41, s23, 0xffff\n"
"s_mov_b32 s36, s34\n"
"s_and_b32 s37, s35, 0xffff\n"
"s_mul_i32 s87, s78, 786432\n"
"s_add_u32 s87, s87, 589824\n"
"s_add_u32 s44, s18, s87\n"
"s_addc_u32 s45, s19, 0\n"
"s_and_b32 s45, s45, 0xffff\n"
"s_add_u32 s48, s20, s87\n"
"s_addc_u32 s49, s21, 0\n"
"s_and_b32 s49, s49, 0xffff\n"
"s_cmp_lt_u32 s77, 192\n"
"s_cselect_b64 s[60:61], s[24:25], s[30:31]\n"
"s_cselect_b32 s62, 4, 1\n"
"s_mov_b64 s[68:69], s[16:17]\n"
"s_mov_b64 s[72:73], s[32:33]\n"
"s_and_b32 s93, s76, 1\n"
".Lpre:\n"
"s_mul_i32 s81, s93, 196608\n"
"s_mul_i32 s88, s78, 384\n"
"s_add_u32 s81, s81, s88\n"
"s_add_u32 s82, s81, 98304\n"
"s_mul_i32 s83, s93, 6144\n"
"s_cmp_lt_u32 s93, 2\n"
"s_cselect_b64 s[52:53], s[26:27], s[28:29]\n"
"s_and_b32 s53, s53, 0xffff\n"
"s_and_b32 s88, s93, 1\n"
"s_mul_i32 s4, s88, 983040\n"
"s_mul_i32 s87, s78, 96\n"
"s_add_u32 s4, s4, s87\n"
"s_cmp_lt_u32 s76, 2\n"
"s_cbranch_scc1 .Lpre_skip\n"
"s_add_u32 m0, s83, 0\n"
"s_nop 0\n"
"buffer_load_dwordx4 v6, s[40:43], s81 offen offset:0 lds\n"
"s_add_u32 m0, s83, 896\n"
"s_nop 0\n"
"buffer_load_dwordx4 v6, s[40:43], s81 offen offset:128 lds\n"
"s_add_u32 m0, s83, 1792\n"
"s_nop 0\n"
"buffer_load_dwordx4 v6, s[40:43], s81 offen offset:256 lds\n"
"s_add_u32 s81, s81, 786432\n"
"s_add_u32 m0, s83, 3072\n"
"s_nop 0\n"
"buffer_load_dwordx4 v6, s[40:43], s82 offen offset:0 lds\n"
"s_add_u32 m0, s83, 3968\n"
"s_nop 0\n"
"buffer_load_dwordx4 v6, s[40:43], s82 offen offset:128 lds\n"
"s_add_u32 m0, s83, 4864\n"
"s_nop 0\n"
"buffer_load_dwordx4 v6, s[40:43], s82 offen offset:256 lds\n"
"s_add_u32 s82, s82, 786432\n"
"s_add_u32 m0, s83, 24576\n"
"s_nop 0\n"
"buffer_load_dwordx4 v16, s[52:55], s4 offen lds\n"
"s_add_u32 m0, s83, 25600\n"
"s_nop 0\n"
"buffer_load_dwordx4 v17, s[52:55], s4 offen lds\n"
"s_add_u32 m0, s83, 26624\n"
"s_nop 0\n"
"buffer_load_dwordx4 v18, s[52:55], s4 offen lds\n"
"s_add_u32 s4, s4, 98304\n"
"s_add_u32 m0, s83, 27648\n"
"s_nop 0\n"
"buffer_load_dwordx4 v16, s[52:55], s4 offen lds\n"
"s_add_u32 m0, s83, 28672\n"
"s_nop 0\n"
"buffer_load_dwordx4 v17, s[52:55], s4 offen lds\n"
"s_add_u32 m0, s83, 29696\n"
"s_nop 0\n"
"buffer_load_dwordx4 v18, s[52:55], s4 offen lds\n"
"s_add_u32 s4, s4, 98304\n"
"s_add_u32 m0, s83, 49152\n"
"s_nop 0\n"
"buffer_load_dwordx4 v6, s[40:43], s81 offen offset:0 lds\n"
"s_add_u32 m0, s83, 50048\n"
"s_nop 0\n"
"buffer_load_dwordx4 v6, s[40:43], s81 offen offset:128 lds\n"
"s_add_u32 m0, s83, 50944\n"
"s_nop 0\n"
"buffer_load_dwordx4 v6, s[40:43], s81 offen offset:256 lds\n"
"s_add_u32 s81, s81, 786432\n"
"s_add_u32 m0, s83, 52224\n"
"s_nop 0\n"
"buffer_load_dwordx4 v6, s[40:43], s82 offen offset:0 lds\n"
"s_add_u32 m0, s83, 53120\n"
"s_nop 0\n"
"buffer_load_dwordx4 v6, s[40:43], s82 offen offset:128 lds\n"
"s_add_u32 m0, s83, 54016\n"
"s_nop 0\n"
"buffer_load_dwordx4 v6, s[40:43], s82 offen offset:256 lds\n"
"s_add_u32 s82, s82, 786432\n"
"v_mul_u32_u24 v54, 10923, v0\n"
"v_lshrrev_b32 v54, 16, v54\n"
"v_mul_u32_u24 v55, 6, v54\n"
"v_sub_u32 v55, v0, v55\n"
"v_min_u32 v54, 4, v54\n"
"v_mul_u32_u24 v54, 3072, v54\n"
"v_lshl_add_u32 v57, v55, 7, v54\n"
"s_lshr_b32 s87, s77, 2\n"
"s_lshl_b32 s87, s87, 1\n"
"s_and_b32 s88, s76, 1\n"
"s_add_u32 s87, s87, s88\n"
"s_mul_i32 s88, s87, 15360\n"
"s_mul_i32 s90, s98, 768\n"
"s_add_u32 s88, s88, s90\n"
"buffer_load_dword v27, v57, s[52:55], s88 offen\n"
"s_add_u32 s93, s93, 2\n"
"s_cmp_le_u32 s93, s76\n"
"s_cbranch_scc1 .Lpre\n"
"v_mul_u32_u24 v54, 5462, v0\n"
"v_lshrrev_b32 v54, 16, v54\n"
"v_mul_u32_u24 v55, 12, v54\n"
"v_sub_u32 v55, v0, v55\n"
"v_min_u32 v54, 3, v54\n"
"v_mul_u32_u24 v54, 12288, v54\n"
"v_lshl_add_u32 v56, v55, 7, v54\n"
"s_lshr_b32 s87, s77, 2\n"
"s_lshl_b32 s87, s87, 1\n"
"s_and_b32 s88, s76, 1\n"
"s_add_u32 s87, s87, s88\n"
"s_mul_i32 s88, s87, 49152\n"
"s_mul_i32 s90, s98, 3072\n"
"s_add_u32 s88, s88, s90\n"
"s_add_u32 s88, s88, 1572864\n"
"buffer_load_dword v27, v56, s[40:43], s88 offen offset:0\n"
"buffer_load_dword v27, v56, s[40:43], s88 offen offset:1536\n"
"s_branch .Lpre_done\n"
".Lpre_skip:\n"
"s_add_u32 s81, s81, 1572864\n"
"s_add_u32 s82, s82, 1572864\n"
"s_add_u32 s4, s4, 196608\n"
".Lpre_done:\n"
"v_bfe_u32 v54, v0, 2, 2\n"
"v_lshrrev_b32 v55, 4, v0\n"
"v_and_b32 v50, 3, v0\n"
"v_lshl_add_u32 v54, v54, 2, v55\n"
"v_lshl_add_u32 v54, v54, 2, v50\n"
"v_lshlrev_b32 v54, 4, v54\n"
"v_add_u32 v2, 0, v54\n"
"v_add_u32 v3, 20480, v54\n"
"v_add_u32 v4, 40960, v54\n"
"v_add_u32 v5, 61440, v54\n"
"s_lshl_b32 s89, s79, 2\n"
"s_add_u32 s89, s89, s76\n"
"s_mul_i32 s80, s89, 81920\n"
"s_add_u32 s80, s80, 266240\n"
"v_bfe_u32 v51, v0, 2, 3\n"
"v_lshrrev_b32 v52, 5, v0\n"
"v_mul_u32_u24 v52, 3072, v52\n"
"v_lshl_add_u32 v52, v51, 7, v52\n"
"v_lshrrev_b32 v53, 1, v51\n"
"v_lshlrev_b32 v54, 1, v53\n"
"v_xor_b32 v54, v54, v50\n"
"v_xor_b32 v55, 0, v54\n"
"v_lshl_add_u32 v10, v55, 4, v52\n"
"v_add_u32 v12, 49152, v10\n"
"v_add_u32 v14, 98304, v10\n"
"v_xor_b32 v55, 4, v54\n"
"v_lshl_add_u32 v11, v55, 4, v52\n"
"v_add_u32 v13, 49152, v11\n"
"v_add_u32 v15, 98304, v11\n"
"s_cmp_lt_u32 s76, 2\n"
"s_cbranch_scc0 .Lhs_done\n"
"s_lshl_b32 s87, s79, 15\n"
"s_lshl_b32 s88, s78, 10\n"
"s_add_u32 s88, s88, s87\n"
"s_lshl_b32 s90, s76, 2\n"
"s_add_u32 s88, s88, s90\n"
"buffer_load_dword v59, off, s[36:39], s88 sc1\n"
"s_lshl_b32 s92, s78, 3\n"
"s_add_u32 s90, s90, s92\n"
"s_lshl_b32 s92, s79, 8\n"
"s_add_u32 s100, s92, 262144\n"
"s_add_u32 s90, s90, s100\n"
"v_mov_b32 v58, 1\n"
"v_and_b32 v57, 3, v0\n"
"v_lshlrev_b32 v57, 13, v57\n"
"v_lshlrev_b32 v56, 2, v0\n"
"v_lshrrev_b32 v55, 1, v0\n"
"v_lshlrev_b32 v55, 10, v55\n"
"v_and_b32 v54, 1, v0\n"
"v_lshl_add_u32 v55, v54, 2, v55\n"
"s_lshl_b32 s84, s98, 13\n"
"s_add_u32 s84, s84, s87\n"
"s_mov_b32 s101, 0\n"
".Lhs0:\n"
"s_waitcnt vmcnt(0)\n"
"v_cmp_eq_u32 s[92:93], 0, v59\n"
"s_add_u32 s101, s101, 1\n"
"s_nop 0\n"
"s_cmp_eq_u64 s[92:93], 0\n"
"s_cbranch_scc1 .Lhs0ok\n"
"s_cmp_ge_u32 s101, 200000\n"
"s_cbranch_scc1 .Lhs0ok\n"
"s_sleep 2\n"
"buffer_load_dword v59, off, s[36:39], s88 sc1\n"
"s_branch .Lhs0\n"
".Lhs0ok:\n"
"s_mov_b64 exec, 1\n"
"buffer_atomic_add v58, off, s[36:39], s84 offset:1536 sc1\n"
"buffer_store_dword v58, off, s[36:39], s90 sc1\n"
"s_mov_b64 exec, -1\n"
"s_or_b32 s92, s78, s76\n"
"s_cbranch_scc1 .Lhs1ok\n"
"buffer_load_dword v59, v56, s[36:39], s100 offen sc1\n"
".Lhs1:\n"
"s_waitcnt vmcnt(0)\n"
"v_cmp_eq_u32 s[92:93], 0, v59\n"
"s_add_u32 s101, s101, 1\n"
"s_nop 0\n"
"s_cmp_eq_u64 s[92:93], 0\n"
"s_cbranch_scc1 .Lhs1b\n"
"s_cmp_ge_u32 s101, 200000\n"
"s_cbranch_scc1 .Lhs1b\n"
"s_sleep 2\n"
"buffer_load_dword v59, v56, s[36:39], s100 offen sc1\n"
"s_branch .Lhs1\n"
".Lhs1b:\n"
"buffer_store_dword v58, v55, s[36:39], s87 offen sc1\n"
".Lhs1ok:\n"
"s_cmp_lg_u32 s76, 0\n"
"s_cbranch_scc1 .Lhs_done\n"
"buffer_load_dword v59, v57, s[36:39], s87 offen offset:1536 sc1\n"
".Lhs2:\n"
"s_waitcnt vmcnt(0)\n"
"v_cmp_gt_u32 s[92:93], 16, v59\n"
"s_add_u32 s101, s101, 1\n"
"s_nop 0\n"
"s_cmp_eq_u64 s[92:93], 0\n"
"s_cbranch_scc1 .Lhs2ok\n"
"s_cmp_ge_u32 s101, 200000\n"
"s_cbranch_scc1 .Lhs2ok\n"
"s_sleep 2\n"
"buffer_load_dword v59, v57, s[36:39], s87 offen offset:1536 sc1\n"
"s_branch .Lhs2\n"
".Lhs2ok:\n"
".Lhs_done:\n"
"s_cmp_lt_u32 s76, 2\n"
"s_cbranch_scc1 .Lb2\n"
"s_waitcnt vmcnt(3)\n"
".Lb2:\n"
"s_barrier\n"
"buffer_load_dwordx4 a[96:99], v2, s[36:39], s80 offen offset:0 sc1\n"
"buffer_load_dwordx4 a[100:103], v2, s[36:39], s80 offen offset:1024 sc1\n"
"buffer_load_dwordx4 a[104:107], v3, s[36:39], s80 offen offset:0 sc1\n"
"buffer_load_dwordx4 a[108:111], v3, s[36:39], s80 offen offset:1024 sc1\n"
"buffer_load_dwordx4 a[112:115], v4, s[36:39], s80 offen offset:0 sc1\n"
"buffer_load_dwordx4 a[116:119], v4, s[36:39], s80 offen offset:1024 sc1\n"
"buffer_load_dwordx4 a[120:123], v5, s[36:39], s80 offen offset:0 sc1\n"
"buffer_load_dwordx4 a[124:127], v5, s[36:39], s80 offen offset:1024 sc1\n"
"s_add_u32 s80, s80, 10240\n"
"buffer_load_dwordx4 a[128:131], v2, s[36:39], s80 offen offset:0 sc1\n"
"buffer_load_dwordx4 a[132:135], v2, s[36:39], s80 offen offset:1024 sc1\n"
"buffer_load_dwordx4 a[136:139], v3, s[36:39], s80 offen offset:0 sc1\n"
"buffer_load_dwordx4 a[140:143], v3, s[36:39], s80 offen offset:1024 sc1\n"
"buffer_load_dwordx4 a[144:147], v4, s[36:39], s80 offen offset:0 sc1\n"
"buffer_load_dwordx4 a[148:151], v4, s[36:39], s80 offen offset:1024 sc1\n"
"buffer_load_dwordx4 a[152:155], v5, s[36:39], s80 offen offset:0 sc1\n"
"buffer_load_dwordx4 a[156:159], v5, s[36:39], s80 offen offset:1024 sc1\n"
"s_sub_u32 s80, s80, 8192\n"
"s_waitcnt vmcnt(8)\n"
"s_barrier\n"
"ds_read_b64_tr_b16 a[224:225], v10 offset:0\n"
"ds_read_b64_tr_b16 a[226:227], v10 offset:6144\n"
"ds_read_b64_tr_b16 a[228:229], v10 offset:12288\n"
"ds_read_b64_tr_b16 a[230:231], v10 offset:18432\n"
"s_waitcnt lgkmcnt(0)\n"
"v_mfma_f32_16x16x32_bf16 v[64:67], a[224:227], a[96:99], 0\n"
"s_add_u32 vcc_hi, vcc_lo, 16384\n"
"buffer_load_dword v27, v0, s[56:59], vcc_hi idxen\n"
"buffer_load_dwordx4 a[160:163], v2, s[36:39], s80 offen offset:0 sc1\n"
"v_mfma_f32_16x16x32_bf16 v[64:67], a[228:231], a[100:103], v[64:67]\n"
"ds_read_b64_tr_b16 a[232:233], v10 offset:8\n"
"ds_read_b64_tr_b16 a[234:235], v10 offset:6152\n"
"ds_read_b64_tr_b16 a[236:237], v10 offset:12296\n"
"ds_read_b64_tr_b16 a[238:239], v10 offset:18440\n"
"v_mfma_f32_16x16x32_bf16 v[68:71], a[224:227], a[104:107], 0\n"
"s_add_u32 m0, s83, 73728\n"
"ds_read_b64_tr_b16 a[240:241], v11 offset:0\n"
"v_mfma_f32_16x16x32_bf16 v[68:71], a[228:231], a[108:111], v[68:71]\n"
"ds_read_b64_tr_b16 a[242:243], v11 offset:6144\n"
"v_lshrrev_b32 v54, 2, v0\n"
"v_mfma_f32_16x16x32_bf16 v[72:75], a[224:227], a[112:115], 0\n"
"buffer_load_dwordx4 a[164:167], v2, s[36:39], s80 offen offset:1024 sc1\n"
"v_mfma_f32_16x16x32_bf16 v[72:75], a[228:231], a[116:119], v[72:75]\n"
"ds_read_b64_tr_b16 a[244:245], v11 offset:12288\n"
"v_mul_u32_u24 v55, 96, v54\n"
"v_mfma_f32_16x16x32_bf16 v[76:79], a[224:227], a[120:123], 0\n"
"ds_read_b64_tr_b16 a[246:247], v11 offset:18432\n"
"v_lshl_add_u32 v19, v50, 3, v55\n"
"v_mfma_f32_16x16x32_bf16 v[76:79], a[228:231], a[124:127], v[76:79]\n"
"s_waitcnt lgkmcnt(4)\n"
"ds_read_b64_tr_b16 a[248:249], v11 offset:8\n"
"v_mfma_f32_16x16x32_bf16 v[80:83], a[232:235], a[96:99], 0\n"
"ds_read_b64_tr_b16 a[250:251], v11 offset:6152\n"
"v_add_u32 v20, 49152, v19\n"
"v_mfma_f32_16x16x32_bf16 v[80:83], a[236:239], a[100:103], v[80:83]\n"
"buffer_load_dwordx4 a[168:171], v3, s[36:39], s80 offen offset:0 sc1\n"
"v_mfma_f32_16x16x32_bf16 v[84:87], a[232:235], a[104:107], 0\n"
"ds_read_b64_tr_b16 a[252:253], v11 offset:12296\n"
"v_add_u32 v21, 98304, v19\n"
"v_mfma_f32_16x16x32_bf16 v[84:87], a[236:239], a[108:111], v[84:87]\n"
"ds_read_b64_tr_b16 a[254:255], v11 offset:18440\n"
"s_mul_i32 s7, s76, 5120\n"
"v_mfma_f32_16x16x32_bf16 v[88:91], a[232:235], a[112:115], 0\n"
"ds_read_b64_tr_b16 a[224:225], v10 offset:1024\n"
"s_mul_i32 s62, s62, 1966080\n"
"v_mfma_f32_16x16x32_bf16 v[88:91], a[236:239], a[116:119], v[88:91]\n"
"s_waitcnt lgkmcnt(5)\n"
"ds_read_b64_tr_b16 a[226:227], v10 offset:7168\n"
"v_mfma_f32_16x16x32_bf16 v[92:95], a[232:235], a[120:123], 0\n"
"buffer_load_dwordx4 a[172:175], v3, s[36:39], s80 offen offset:1024 sc1\n"
"v_mfma_f32_16x16x32_bf16 v[92:95], a[236:239], a[124:127], v[92:95]\n"
"ds_read_b64_tr_b16 a[228:229], v10 offset:13312\n"
"s_and_b32 s61, s61, 0xffff\n"
"v_mfma_f32_16x16x32_bf16 v[96:99], a[240:243], a[96:99], 0\n"
"ds_read_b64_tr_b16 a[230:231], v10 offset:19456\n"
"s_mov_b32 s63, 0x20000\n"
"v_mfma_f32_16x16x32_bf16 v[96:99], a[244:247], a[100:103], v[96:99]\n"
"ds_read_b64_tr_b16 a[232:233], v10 offset:1032\n"
"v_mul_u32_u24 v44, 384, v40\n"
"v_mfma_f32_16x16x32_bf16 v[100:103], a[240:243], a[104:107], 0\n"
"ds_read_b64_tr_b16 a[234:235], v10 offset:7176\n"
"v_add_u32 v7, v44, v43\n"
"v_mfma_f32_16x16x32_bf16 v[100:103], a[244:247], a[108:111], v[100:103]\n"
"buffer_load_dwordx4 a[176:179], v4, s[36:39], s80 offen offset:0 sc1\n"
"v_mfma_f32_16x16x32_bf16 v[104:107], a[240:243], a[112:115], 0\n"
"ds_read_b64_tr_b16 a[236:237], v10 offset:13320\n"
"v_xor_b32 v43, v42, v41\n"
"v_mfma_f32_16x16x32_bf16 v[104:107], a[244:247], a[116:119], v[104:107]\n"
"s_waitcnt lgkmcnt(7)\n"
"ds_read_b64_tr_b16 a[238:239], v10 offset:19464\n"
"v_mfma_f32_16x16x32_bf16 v[108:111], a[240:243], a[120:123], 0\n"
"s_cmp_lt_u32 s77, 240\n"
"s_cselect_b32 s62, s62, 0\n"
"s_lshl_b32 s10, s77, 5\n"
"v_mfma_f32_16x16x32_bf16 v[108:111], a[244:247], a[124:127], v[108:111]\n"
"ds_read_b64_tr_b16 a[240:241], v11 offset:1024\n"
"s_sub_u32 s87, s77, 192\n"
"v_mfma_f32_16x16x32_bf16 v[112:115], a[248:251], a[96:99], 0\n"
"buffer_load_dwordx4 a[180:183], v4, s[36:39], s80 offen offset:1024 sc1\n"
"v_mfma_f32_16x16x32_bf16 v[112:115], a[252:255], a[100:103], v[112:115]\n"
"ds_read_b64_tr_b16 a[242:243], v11 offset:7168\n"
"s_mul_i32 s87, s87, 48\n"
"v_mfma_f32_16x16x32_bf16 v[116:119], a[248:251], a[104:107], 0\n"
"ds_read_b64_tr_b16 a[244:245], v11 offset:13312\n"
"v_lshl_add_u32 v8, v43, 4, v44\n"
"v_mfma_f32_16x16x32_bf16 v[116:119], a[252:255], a[108:111], v[116:119]\n"
"ds_read_b64_tr_b16 a[246:247], v11 offset:19456\n"
"v_xor_b32 v43, 4, v43\n"
"v_mfma_f32_16x16x32_bf16 v[120:123], a[248:251], a[112:115], 0\n"
"s_cmp_lt_u32 s77, 192\n"
"s_cselect_b32 s10, s10, s87\n"
"s_sub_u32 s87, s77, 224\n"
"v_mfma_f32_16x16x32_bf16 v[120:123], a[252:255], a[116:119], v[120:123]\n"
"buffer_load_dwordx4 a[184:187], v5, s[36:39], s80 offen offset:0 sc1\n"
"v_mfma_f32_16x16x32_bf16 v[124:127], a[248:251], a[120:123], 0\n"
"s_waitcnt lgkmcnt(8)\n"
"s_mul_i32 s87, s87, 96\n"
"s_add_u32 s87, s87, 32\n"
"v_mfma_f32_16x16x32_bf16 v[124:127], a[252:255], a[124:127], v[124:127]\n"
"ds_read_b64_tr_b16 a[248:249], v11 offset:1032\n"
"v_lshl_add_u32 v9, v43, 4, v44\n"
"v_mfma_f32_16x16x32_bf16 v[128:131], a[224:227], a[96:99], 0\n"
"ds_read_b64_tr_b16 a[250:251], v11 offset:7176\n"
"v_add_u32 v54, 0, v0\n"
"v_mfma_f32_16x16x32_bf16 v[128:131], a[228:231], a[100:103], v[128:131]\n"
"ds_read_b64_tr_b16 a[252:253], v11 offset:13320\n"
"v_mul_u32_u24 v55, 3277, v54\n"
"v_mfma_f32_16x16x32_bf16 v[132:135], a[224:227], a[104:107], 0\n"
"buffer_load_dwordx4 a[188:191], v5, s[36:39], s80 offen offset:1024 sc1\n"
"v_mfma_f32_16x16x32_bf16 v[132:135], a[228:231], a[108:111], v[132:135]\n"
"s_add_u32 s80, s80, 10240\n"
"ds_read_b64_tr_b16 a[254:255], v11 offset:19464\n"
"v_mfma_f32_16x16x32_bf16 v[136:139], a[224:227], a[112:115], 0\n"
"s_cmp_lt_u32 s77, 224\n"
"s_cselect_b32 s10, s10, s87\n"
"s_cselect_b32 s88, 16, 48\n"
"v_mfma_f32_16x16x32_bf16 v[136:139], a[228:231], a[116:119], v[136:139]\n"
"s_waitcnt lgkmcnt(8)\n"
"s_mul_i32 s88, s88, 1280\n"
"s_mul_i32 s10, s10, 1280\n"
"v_mfma_f32_16x16x32_bf16 v[140:143], a[224:227], a[120:123], 0\n"
"s_mul_i32 s87, s76, 320\n"
"s_add_u32 s10, s10, s87\n"
"s_add_u32 s32, s10, s88\n"
"v_mfma_f32_16x16x32_bf16 v[140:143], a[228:231], a[124:127], v[140:143]\n"
"buffer_load_dwordx4 v16, s[52:55], s4 offen lds\n"
"v_mfma_f32_16x16x32_bf16 v[144:147], a[232:235], a[96:99], 0\n"
"s_add_u32 m0, s83, 74752\n"
"ds_read_b64_tr_b16 a[224:225], v10 offset:2048\n"
"v_mfma_f32_16x16x32_bf16 v[144:147], a[236:239], a[100:103], v[144:147]\n"
"ds_read_b64_tr_b16 a[226:227], v10 offset:8192\n"
"s_mul_i32 s33, s77, 40960\n"
"v_mfma_f32_16x16x32_bf16 v[148:151], a[232:235], a[104:107], 0\n"
"ds_read_b64_tr_b16 a[228:229], v10 offset:14336\n"
"s_mul_i32 s87, s76, 10240\n"
"v_mfma_f32_16x16x32_bf16 v[148:151], a[236:239], a[108:111], v[148:151]\n"
"ds_read_b64_tr_b16 a[230:231], v10 offset:20480\n"
"s_add_u32 s33, s33, s87\n"
"v_mfma_f32_16x16x32_bf16 v[152:155], a[232:235], a[112:115], 0\n"
"s_add_u32 s33, s33, 34344960\n"
"s_mov_b32 s46, 196608\n"
"s_mov_b32 s47, 0x20000\n"
"v_mfma_f32_16x16x32_bf16 v[152:155], a[236:239], a[116:119], v[152:155]\n"
"s_waitcnt lgkmcnt(8)\n"
"s_mov_b32 s50, 196608\n"
"s_mov_b32 s51, 0x20000\n"
"v_mfma_f32_16x16x32_bf16 v[156:159], a[232:235], a[120:123], 0\n"
"s_and_b32 s69, s69, 0xffff\n"
"s_mov_b32 s70, 2621440\n"
"s_mov_b32 s71, 0x20000\n"
"v_mfma_f32_16x16x32_bf16 v[156:159], a[236:239], a[124:127], v[156:159]\n"
"ds_read_b64_tr_b16 a[232:233], v10 offset:2056\n"
"s_and_b32 s73, s73, 0xffff\n"
"v_mfma_f32_16x16x32_bf16 v[160:163], a[240:243], a[96:99], 0\n"
"buffer_load_dwordx4 v17, s[52:55], s4 offen lds\n"
"v_mfma_f32_16x16x32_bf16 v[160:163], a[244:247], a[100:103], v[160:163]\n"
"s_add_u32 m0, s83, 75776\n"
"ds_read_b64_tr_b16 a[234:235], v10 offset:8200\n"
"v_mfma_f32_16x16x32_bf16 v[164:167], a[240:243], a[104:107], 0\n"
"ds_read_b64_tr_b16 a[236:237], v10 offset:14344\n"
"s_mov_b32 s74, 2621440\n"
"v_mfma_f32_16x16x32_bf16 v[164:167], a[244:247], a[108:111], v[164:167]\n"
"ds_read_b64_tr_b16 a[238:239], v10 offset:20488\n"
"s_mov_b32 s75, 0x20000\n"
"v_mfma_f32_16x16x32_bf16 v[168:171], a[240:243], a[112:115], 0\n"
"s_mov_b32 s85, 0x3d93cd3a\n"
"s_mov_b32 s86, 0x3fb8aa3b\n"
"s_mul_i32 s91, s89, 4096\n"
"v_mfma_f32_16x16x32_bf16 v[168:171], a[244:247], a[116:119], v[168:171]\n"
"s_waitcnt lgkmcnt(8)\n"
"s_mul_i32 s87, s78, 786432\n"
"s_add_u32 s91, s91, s87\n"
"v_mfma_f32_16x16x32_bf16 v[172:175], a[240:243], a[120:123], 0\n"
"s_add_u32 s91, s91, 2887680\n"
"s_mul_i32 s5, s89, 4096\n"
"s_mul_i32 s87, s78, 131072\n"
"v_mfma_f32_16x16x32_bf16 v[172:175], a[244:247], a[124:127], v[172:175]\n"
"ds_read_b64_tr_b16 a[240:241], v11 offset:2048\n"
"s_add_u32 s5, s5, s87\n"
"v_mfma_f32_16x16x32_bf16 v[176:179], a[248:251], a[96:99], 0\n"
"s_barrier\n"
"s_add_u32 s5, s5, 28053504\n"
"v_mfma_f32_16x16x32_bf16 v[176:179], a[252:255], a[100:103], v[176:179]\n"
"buffer_load_dwordx4 v18, s[52:55], s4 offen lds\n"
"v_mfma_f32_16x16x32_bf16 v[180:183], a[248:251], a[104:107], 0\n"
"s_add_u32 s4, s4, 98304\n"
"s_add_u32 m0, s83, 76800\n"
"s_mul_i32 s6, s89, 2048\n"
"v_mfma_f32_16x16x32_bf16 v[180:183], a[252:255], a[108:111], v[180:183]\n"
"ds_read_b64_tr_b16 a[242:243], v11 offset:8192\n"
"s_mul_i32 s87, s78, 65536\n"
"v_mfma_f32_16x16x32_bf16 v[184:187], a[248:251], a[112:115], 0\n"
"ds_read_b64_tr_b16 a[244:245], v11 offset:14336\n"
"s_add_u32 s6, s6, s87\n"
"v_mfma_f32_16x16x32_bf16 v[184:187], a[252:255], a[116:119], v[184:187]\n"
"s_waitcnt lgkmcnt(7)\n"
"ds_read_b64_tr_b16 a[246:247], v11 offset:20480\n"
"v_mfma_f32_16x16x32_bf16 v[188:191], a[248:251], a[120:123], 0\n"
"s_add_u32 s6, s6, 32247808\n"
"s_mul_i32 s14, s79, 32768\n"
"s_mul_i32 s87, s98, 8192\n"
"v_mfma_f32_16x16x32_bf16 v[188:191], a[252:255], a[124:127], v[188:191]\n"
"ds_read_b64_tr_b16 a[248:249], v11 offset:2056\n"
"s_add_u32 s14, s14, s87\n"
"v_mfma_f32_16x16x32_bf16 v[192:195], a[224:227], a[96:99], 0\n"
"ds_read_b64_tr_b16 a[250:251], v11 offset:8200\n"
"s_add_u32 s14, s14, 768\n"
"v_mfma_f32_16x16x32_bf16 v[192:195], a[228:231], a[100:103], v[192:195]\n"
"ds_read_b64_tr_b16 a[252:253], v11 offset:14344\n"
"s_and_b32 s92, s77, 4\n"
"v_mfma_f32_16x16x32_bf16 v[196:199], a[224:227], a[104:107], 0\n"
"buffer_load_dwordx4 v16, s[52:55], s4 offen lds\n"
"v_mfma_f32_16x16x32_bf16 v[196:199], a[228:231], a[108:111], v[196:199]\n"
"s_add_u32 m0, s83, 77824\n"
"ds_read_b64_tr_b16 a[254:255], v11 offset:20488\n"
"v_mfma_f32_16x16x32_bf16 v[200:203], a[224:227], a[112:115], 0\n"
"s_lshl_b32 s92, s92, 1\n"
"s_lshr_b32 s90, s77, 5\n"
"s_add_u32 s92, s92, s90\n"
"v_mfma_f32_16x16x32_bf16 v[200:203], a[228:231], a[116:119], v[200:203]\n"
"s_waitcnt lgkmcnt(8)\n"
"s_bfe_u32 s93, s77, 0x20003\n"
"s_xor_b32 s88, s76, s98\n"
"v_mfma_f32_16x16x32_bf16 v[204:207], a[224:227], a[120:123], 0\n"
"s_and_b32 s88, s88, 1\n"
"s_lshr_b32 s90, s76, 1\n"
"s_mul_i32 s34, s90, 40960\n"
"v_mfma_f32_16x16x32_bf16 v[204:207], a[228:231], a[124:127], v[204:207]\n"
"ds_read_b64_tr_b16 a[224:225], v19 offset:24576\n"
"s_mul_i32 s87, s93, 10240\n"
"v_mfma_f32_16x16x32_bf16 v[208:211], a[232:235], a[96:99], 0\n"
"ds_read_b64_tr_b16 a[226:227], v19 offset:26112\n"
"s_add_u32 s34, s34, s87\n"
"v_mfma_f32_16x16x32_bf16 v[208:211], a[236:239], a[100:103], v[208:211]\n"
"ds_read_b64_tr_b16 a[228:229], v19 offset:27648\n"
"s_mul_i32 s87, s88, 5120\n"
"v_mfma_f32_16x16x32_bf16 v[212:215], a[232:235], a[104:107], 0\n"
"ds_read_b64_tr_b16 a[230:231], v19 offset:29184\n"
"s_add_u32 s34, s34, s87\n"
"v_mfma_f32_16x16x32_bf16 v[212:215], a[236:239], a[108:111], v[212:215]\n"
"buffer_load_dwordx4 v17, s[52:55], s4 offen lds\n"
"v_mfma_f32_16x16x32_bf16 v[216:219], a[232:235], a[112:115], 0\n"
"s_add_u32 m0, s83, 78848\n"
"s_add_u32 s34, s34, 34344960\n"
"s_mul_i32 s87, s98, 1966080\n"
"v_mfma_f32_16x16x32_bf16 v[216:219], a[236:239], a[116:119], v[216:219]\n"
"s_waitcnt lgkmcnt(8)\n"
"s_add_u32 s8, s34, s87\n"
"s_mul_i32 s87, s90, 204800\n"
"v_mfma_f32_16x16x32_bf16 v[220:223], a[232:235], a[120:123], 0\n"
"s_add_u32 s8, s8, s87\n"
"s_mul_i32 s87, s98, 327680\n"
"s_add_u32 s9, s34, s87\n"
"v_mfma_f32_16x16x32_bf16 v[220:223], a[236:239], a[124:127], v[220:223]\n"
"ds_read_b64_tr_b16 a[232:233], v19 offset:36864\n"
"s_add_u32 s9, s9, 7864320\n"
"v_mfma_f32_16x16x32_bf16 v[224:227], a[240:243], a[96:99], 0\n"
"ds_read_b64_tr_b16 a[234:235], v19 offset:38400\n"
"s_mul_i32 s87, s98, 163840\n"
"v_mfma_f32_16x16x32_bf16 v[224:227], a[244:247], a[100:103], v[224:227]\n"
"ds_read_b64_tr_b16 a[236:237], v19 offset:39936\n"
"s_add_u32 s34, s34, s87\n"
"v_mfma_f32_16x16x32_bf16 v[228:231], a[240:243], a[104:107], 0\n"
"ds_read_b64_tr_b16 a[238:239], v19 offset:41472\n"
"s_add_u32 s34, s34, 9175040\n"
"v_mfma_f32_16x16x32_bf16 v[228:231], a[244:247], a[108:111], v[228:231]\n"
"s_and_b32 s88, s98, 1\n"
"s_lshr_b32 s90, s98, 1\n"
"s_mul_i32 s67, s92, 163840\n"
"v_mfma_f32_16x16x32_bf16 v[232:235], a[240:243], a[112:115], 0\n"
"buffer_load_dwordx4 v18, s[52:55], s4 offen lds\n"
"v_mfma_f32_16x16x32_bf16 v[232:235], a[244:247], a[116:119], v[232:235]\n"
"s_add_u32 s4, s4, 98304\n"
"s_add_u32 m0, s83, 98304\n"
"s_waitcnt lgkmcnt(8)\n"
"v_mfma_f32_16x16x32_bf16 v[236:239], a[240:243], a[120:123], 0\n"
"s_mul_i32 s87, s76, 40960\n"
"s_add_u32 s67, s67, s87\n"
"s_mul_i32 s87, s90, 20480\n"
"v_mfma_f32_16x16x32_bf16 v[236:239], a[244:247], a[124:127], v[236:239]\n"
"ds_read_b64_tr_b16 a[240:241], v19 offset:24608\n"
"s_add_u32 s67, s67, s87\n"
"v_mfma_f32_16x16x32_bf16 v[240:243], a[248:251], a[96:99], 0\n"
"ds_read_b64_tr_b16 a[242:243], v19 offset:26144\n"
"s_mul_i32 s87, s93, 320\n"
"v_mfma_f32_16x16x32_bf16 v[240:243], a[252:255], a[100:103], v[240:243]\n"
"ds_read_b64_tr_b16 a[244:245], v19 offset:27680\n"
"s_add_u32 s67, s67, s87\n"
"v_mfma_f32_16x16x32_bf16 v[244:247], a[248:251], a[104:107], 0\n"
"ds_read_b64_tr_b16 a[246:247], v19 offset:29216\n"
"s_mul_i32 s87, s88, 160\n"
"v_mfma_f32_16x16x32_bf16 v[244:247], a[252:255], a[108:111], v[244:247]\n"
"s_add_u32 s67, s67, s87\n"
"s_mul_i32 s12, s92, 8192\n"
"s_mul_i32 s87, s76, 2048\n"
"v_mfma_f32_16x16x32_bf16 v[248:251], a[248:251], a[112:115], 0\n"
"s_add_u32 s12, s12, s87\n"
"s_mul_i32 s87, s98, 1048576\n"
"s_add_u32 s12, s12, s87\n"
"v_mfma_f32_16x16x32_bf16 v[248:251], a[252:255], a[116:119], v[248:251]\n"
"buffer_load_dwordx4 a[192:195], v2, s[36:39], s80 offen offset:0 sc1\n"
"v_mfma_f32_16x16x32_bf16 v[252:255], a[248:251], a[120:123], 0\n"
"s_waitcnt lgkmcnt(8)\n"
"s_add_u32 s12, s12, 28053504\n"
"s_mul_i32 s13, s92, 4096\n"
"v_mfma_f32_16x16x32_bf16 v[252:255], a[252:255], a[124:127], v[252:255]\n"
"ds_read_b64_tr_b16 a[248:249], v19 offset:36896\n"
"s_mul_i32 s87, s76, 1024\n"
"v_mfma_f32_16x16x32_bf16 a[0:3], a[224:227], a[96:99], 0\n"
"ds_read_b64_tr_b16 a[250:251], v19 offset:38432\n"
"s_add_u32 s13, s13, s87\n"
"v_mfma_f32_16x16x32_bf16 a[0:3], a[228:231], a[100:103], a[0:3]\n"
"ds_read_b64_tr_b16 a[252:253], v19 offset:39968\n"
"s_mul_i32 s87, s98, 524288\n"
"v_mfma_f32_16x16x32_bf16 a[4:7], a[224:227], a[104:107], 0\n"
"buffer_load_dwordx4 a[196:199], v2, s[36:39], s80 offen offset:1024 sc1\n"
"v_mfma_f32_16x16x32_bf16 a[4:7], a[228:231], a[108:111], a[4:7]\n"
"ds_read_b64_tr_b16 a[254:255], v19 offset:41504\n"
"s_add_u32 s13, s13, s87\n"
"v_mfma_f32_16x16x32_bf16 a[8:11], a[224:227], a[112:115], 0\n"
"s_add_u32 s13, s13, 32247808\n"
"s_mul_i32 s11, s92, 8192\n"
"s_mul_i32 s87, s76, 2048\n"
"v_mfma_f32_16x16x32_bf16 a[8:11], a[228:231], a[116:119], a[8:11]\n"
"s_waitcnt lgkmcnt(8)\n"
"s_add_u32 s11, s11, s87\n"
"s_mul_i32 s87, s98, 6291456\n"
"v_mfma_f32_16x16x32_bf16 a[12:15], a[224:227], a[120:123], 0\n"
"s_add_u32 s11, s11, s87\n"
"s_add_u32 s11, s11, 2887680\n"
"s_and_b32 s90, s92, 1\n"
"v_mfma_f32_16x16x32_bf16 a[12:15], a[228:231], a[124:127], a[12:15]\n"
"buffer_load_dwordx4 a[200:203], v3, s[36:39], s80 offen offset:0 sc1\n"
"v_mfma_f32_16x16x32_bf16 a[16:19], a[232:235], a[96:99], 0\n"
"ds_read_b64_tr_b16 a[224:225], v19 offset:24640\n"
"s_mul_i32 s16, s79, 32768\n"
"v_mfma_f32_16x16x32_bf16 a[16:19], a[236:239], a[100:103], a[16:19]\n"
"ds_read_b64_tr_b16 a[226:227], v19 offset:26176\n"
"s_mul_i32 s87, s93, 8192\n"
"v_mfma_f32_16x16x32_bf16 a[20:23], a[232:235], a[104:107], 0\n"
"ds_read_b64_tr_b16 a[228:229], v19 offset:27712\n"
"s_add_u32 s16, s16, s87\n"
"v_mfma_f32_16x16x32_bf16 a[20:23], a[236:239], a[108:111], a[20:23]\n"
"ds_read_b64_tr_b16 a[230:231], v19 offset:29248\n"
"s_mul_i32 s87, s90, 1024\n"
"v_mfma_f32_16x16x32_bf16 a[24:27], a[232:235], a[112:115], 0\n"
"buffer_load_dwordx4 a[204:207], v3, s[36:39], s80 offen offset:1024 sc1\n"
"v_mfma_f32_16x16x32_bf16 a[24:27], a[236:239], a[116:119], a[24:27]\n"
"s_waitcnt lgkmcnt(8)\n"
"s_add_u32 s16, s16, s87\n"
"s_add_u32 s16, s16, 4864\n"
"v_mfma_f32_16x16x32_bf16 a[28:31], a[232:235], a[120:123], 0\n"
"s_mul_i32 s15, s79, 32768\n"
"s_add_u32 s15, s15, 512\n"
"s_mul_i32 s17, s92, 1310720\n"
"v_mfma_f32_16x16x32_bf16 a[28:31], a[236:239], a[124:127], a[28:31]\n"
"ds_read_b64_tr_b16 a[232:233], v19 offset:36928\n"
"s_mul_i32 s87, s93, 327680\n"
"v_mfma_f32_16x16x32_bf16 a[32:35], a[240:243], a[96:99], 0\n"
"ds_read_b64_tr_b16 a[234:235], v19 offset:38464\n"
"s_add_u32 s17, s17, s87\n"
"v_mfma_f32_16x16x32_bf16 a[32:35], a[244:247], a[100:103], a[32:35]\n"
"buffer_load_dwordx4 a[208:211], v4, s[36:39], s80 offen offset:0 sc1\n"
"v_mfma_f32_16x16x32_bf16 a[36:39], a[240:243], a[104:107], 0\n"
"ds_read_b64_tr_b16 a[236:237], v19 offset:40000\n"
"s_mul_i32 s87, s76, 20480\n"
"v_mfma_f32_16x16x32_bf16 a[36:39], a[244:247], a[108:111], a[36:39]\n"
"ds_read_b64_tr_b16 a[238:239], v19 offset:41536\n"
"s_add_u32 s17, s17, s87\n"
"v_mfma_f32_16x16x32_bf16 a[40:43], a[240:243], a[112:115], 0\n"
"s_add_u32 s17, s17, 44830720\n"
"s_xor_b32 s90, s98, 1\n"
"s_mul_i32 s90, s90, 81920\n"
"v_mfma_f32_16x16x32_bf16 a[40:43], a[244:247], a[116:119], a[40:43]\n"
"s_waitcnt lgkmcnt(8)\n"
"s_add_u32 s64, s17, s90\n"
"s_add_u32 s64, s64, 5120\n"
"v_mfma_f32_16x16x32_bf16 a[44:47], a[240:243], a[120:123], 0\n"
"buffer_load_dwordx4 a[212:215], v4, s[36:39], s80 offen offset:1024 sc1\n"
"v_mfma_f32_16x16x32_bf16 a[44:47], a[244:247], a[124:127], a[44:47]\n"
"ds_read_b64_tr_b16 a[240:241], v19 offset:30720\n"
"s_xor_b32 s90, s98, 2\n"
"v_mfma_f32_16x16x32_bf16 a[48:51], a[248:251], a[96:99], 0\n"
"ds_read_b64_tr_b16 a[242:243], v19 offset:32256\n"
"s_mul_i32 s90, s90, 81920\n"
"v_mfma_f32_16x16x32_bf16 a[48:51], a[252:255], a[100:103], a[48:51]\n"
"ds_read_b64_tr_b16 a[244:245], v19 offset:33792\n"
"s_add_u32 s65, s17, s90\n"
"v_mfma_f32_16x16x32_bf16 a[52:55], a[248:251], a[104:107], 0\n"
"ds_read_b64_tr_b16 a[246:247], v19 offset:35328\n"
"s_add_u32 s65, s65, 10240\n"
"v_mfma_f32_16x16x32_bf16 a[52:55], a[252:255], a[108:111], a[52:55]\n"
"buffer_load_dwordx4 a[216:219], v5, s[36:39], s80 offen offset:0 sc1\n"
"v_mfma_f32_16x16x32_bf16 a[56:59], a[248:251], a[112:115], 0\n"
"s_xor_b32 s90, s98, 3\n"
"s_mul_i32 s90, s90, 81920\n"
"s_add_u32 s66, s17, s90\n"
"v_mfma_f32_16x16x32_bf16 a[56:59], a[252:255], a[116:119], a[56:59]\n"
"s_waitcnt lgkmcnt(8)\n"
"s_add_u32 s66, s66, 15360\n"
"s_mul_i32 s90, s98, 81920\n"
"v_mfma_f32_16x16x32_bf16 a[60:63], a[248:251], a[120:123], 0\n"
"s_add_u32 s17, s17, s90\n"
"s_mov_b32 s99, 0xbdd2d3e8\n"
"s_mul_i32 s35, s78, 1024\n"
"v_mfma_f32_16x16x32_bf16 a[60:63], a[252:255], a[124:127], a[60:63]\n"
"ds_read_b64_tr_b16 a[248:249], v19 offset:43008\n"
"s_mul_i32 s87, s79, 4\n"
"v_mfma_f32_16x16x32_bf16 a[64:67], a[224:227], a[96:99], 0\n"
"buffer_load_dwordx4 a[220:223], v5, s[36:39], s80 offen offset:1024 sc1\n"
"v_mfma_f32_16x16x32_bf16 a[64:67], a[228:231], a[100:103], a[64:67]\n"
"s_sub_u32 s80, s80, 8192\n"
"ds_read_b64_tr_b16 a[250:251], v19 offset:44544\n"
"v_mfma_f32_16x16x32_bf16 a[68:71], a[224:227], a[104:107], 0\n"
"ds_read_b64_tr_b16 a[252:253], v19 offset:46080\n"
"s_add_u32 s35, s35, s87\n"
"v_mfma_f32_16x16x32_bf16 a[68:71], a[228:231], a[108:111], a[68:71]\n"
"ds_read_b64_tr_b16 a[254:255], v19 offset:47616\n"
"s_add_u32 s35, s35, 256\n"
"v_mfma_f32_16x16x32_bf16 a[72:75], a[224:227], a[112:115], 0\n"
"s_lshr_b32 s90, s98, 1\n"
"s_lshl_b32 s90, s90, 10\n"
"s_xor_b32 s88, s90, 1024\n"
"v_mfma_f32_16x16x32_bf16 a[72:75], a[228:231], a[116:119], a[72:75]\n"
"buffer_load_dwordx4 v6, s[40:43], s81 offen offset:0 lds\n"
"v_mfma_f32_16x16x32_bf16 a[76:79], a[224:227], a[120:123], 0\n"
"s_add_u32 m0, s83, 99200\n"
"s_waitcnt lgkmcnt(8)\n"
"v_lshrrev_b32 v55, 16, v55\n"
"v_mfma_f32_16x16x32_bf16 a[76:79], a[228:231], a[124:127], a[76:79]\n"
"ds_read_b64_tr_b16 a[224:225], v19 offset:30752\n"
"v_mul_u32_u24 v56, 20, v55\n"
"v_mfma_f32_16x16x32_bf16 a[80:83], a[232:235], a[96:99], 0\n"
"ds_read_b64_tr_b16 a[226:227], v19 offset:32288\n"
"v_sub_u32 v56, v54, v56\n"
"v_mfma_f32_16x16x32_bf16 a[80:83], a[236:239], a[100:103], a[80:83]\n"
"ds_read_b64_tr_b16 a[228:229], v19 offset:33824\n"
"v_bfe_u32 v57, v55, 2, 1\n"
"v_mfma_f32_16x16x32_bf16 a[84:87], a[232:235], a[104:107], 0\n"
"buffer_load_dwordx4 v6, s[40:43], s81 offen offset:128 lds\n"
"v_mfma_f32_16x16x32_bf16 a[84:87], a[236:239], a[108:111], a[84:87]\n"
"s_add_u32 vcc_hi, vcc_lo, 24576\n"
"buffer_load_dword v27, v0, s[56:59], vcc_hi idxen\n"
"s_add_u32 m0, s83, 100096\n"
"ds_read_b64_tr_b16 a[230:231], v19 offset:35360\n"
"v_mfma_f32_16x16x32_bf16 a[88:91], a[232:235], a[112:115], 0\n"
"v_lshlrev_b32 v57, 1, v57\n"
"v_xor_b32 v56, v56, v57\n"
"v_mfma_f32_16x16x32_bf16 a[88:91], a[236:239], a[116:119], a[88:91]\n"
"s_waitcnt lgkmcnt(8)\n"
"s_waitcnt vmcnt(26)\n"
"v_mul_u32_u24 v55, 1280, v55\n"
"v_mfma_f32_16x16x32_bf16 a[92:95], a[232:235], a[120:123], 0\n"
"v_lshl_add_u32 v32, v56, 4, v55\n"
"v_add_u32 v54, 64, v0\n"
"v_mfma_f32_16x16x32_bf16 a[92:95], a[236:239], a[124:127], a[92:95]\n"
"buffer_load_dwordx4 v6, s[40:43], s81 offen offset:256 lds\n"
"v_mfma_f32_16x16x32_bf16 a[0:3], a[240:243], a[128:131], a[0:3]\n"
"s_add_u32 s81, s81, 786432\n"
"s_add_u32 m0, s83, 101376\n"
"v_mul_u32_u24 v55, 3277, v54\n"
"v_mfma_f32_16x16x32_bf16 a[0:3], a[244:247], a[132:135], a[0:3]\n"
"ds_read_b64_tr_b16 a[232:233], v19 offset:43040\n"
"v_lshrrev_b32 v55, 16, v55\n"
"v_mfma_f32_16x16x32_bf16 a[4:7], a[240:243], a[136:139], a[4:7]\n"
"ds_read_b64_tr_b16 a[234:235], v19 offset:44576\n"
"v_mul_u32_u24 v56, 20, v55\n"
"v_mfma_f32_16x16x32_bf16 a[4:7], a[244:247], a[140:143], a[4:7]\n"
"ds_read_b64_tr_b16 a[236:237], v19 offset:46112\n"
"v_sub_u32 v56, v54, v56\n"
"v_mfma_f32_16x16x32_bf16 a[8:11], a[240:243], a[144:147], a[8:11]\n"
"buffer_load_dwordx4 v6, s[40:43], s82 offen offset:0 lds\n"
"v_mfma_f32_16x16x32_bf16 a[8:11], a[244:247], a[148:151], a[8:11]\n"
"s_add_u32 m0, s83, 102272\n"
"s_waitcnt lgkmcnt(7)\n"
"v_bfe_u32 v57, v55, 2, 1\n"
"v_mfma_f32_16x16x32_bf16 a[12:15], a[240:243], a[152:155], a[12:15]\n"
"ds_read_b64_tr_b16 a[238:239], v19 offset:47648\n"
"v_lshlrev_b32 v57, 1, v57\n"
"v_mfma_f32_16x16x32_bf16 a[12:15], a[244:247], a[156:159], a[12:15]\n"
"ds_read_b64_tr_b16 a[240:241], v19 offset:30784\n"
"v_xor_b32 v56, v56, v57\n"
"v_mfma_f32_16x16x32_bf16 a[16:19], a[248:251], a[128:131], a[16:19]\n"
"s_barrier\n"
"v_mul_u32_u24 v55, 1280, v55\n"
"v_mfma_f32_16x16x32_bf16 a[16:19], a[252:255], a[132:135], a[16:19]\n"
"buffer_load_dwordx4 v6, s[40:43], s82 offen offset:128 lds\n"
"v_mfma_f32_16x16x32_bf16 a[20:23], a[248:251], a[136:139], a[20:23]\n"
"s_add_u32 m0, s83, 103168\n"
"ds_read_b64_tr_b16 a[242:243], v19 offset:32320\n"
"v_mfma_f32_16x16x32_bf16 a[20:23], a[252:255], a[140:143], a[20:23]\n"
"ds_read_b64_tr_b16 a[244:245], v19 offset:33856\n"
"v_lshl_add_u32 v33, v56, 4, v55\n"
"v_mfma_f32_16x16x32_bf16 a[24:27], a[248:251], a[144:147], a[24:27]\n"
"ds_read_b64_tr_b16 a[246:247], v19 offset:35392\n"
"v_add_u32 v54, 128, v0\n"
"v_mfma_f32_16x16x32_bf16 a[24:27], a[252:255], a[148:151], a[24:27]\n"
"s_waitcnt lgkmcnt(8)\n"
"v_mul_u32_u24 v55, 3277, v54\n"
"v_lshrrev_b32 v55, 16, v55\n"
"v_mfma_f32_16x16x32_bf16 a[28:31], a[248:251], a[152:155], a[28:31]\n"
"buffer_load_dwordx4 v6, s[40:43], s82 offen offset:256 lds\n"
"v_mfma_f32_16x16x32_bf16 a[28:31], a[252:255], a[156:159], a[28:31]\n"
"s_add_u32 s82, s82, 786432\n"
"s_add_u32 m0, s83, 122880\n"
"v_mul_u32_u24 v56, 20, v55\n"
"v_mfma_f32_16x16x32_bf16 a[32:35], a[224:227], a[128:131], a[32:35]\n"
"ds_read_b64_tr_b16 a[248:249], v19 offset:43072\n"
"v_sub_u32 v56, v54, v56\n"
"v_mfma_f32_16x16x32_bf16 a[32:35], a[228:231], a[132:135], a[32:35]\n"
"ds_read_b64_tr_b16 a[250:251], v19 offset:44608\n"
"v_bfe_u32 v57, v55, 2, 1\n"
"v_mfma_f32_16x16x32_bf16 a[36:39], a[224:227], a[136:139], a[36:39]\n"
"ds_read_b64_tr_b16 a[252:253], v19 offset:46144\n"
"v_lshlrev_b32 v57, 1, v57\n"
"v_mfma_f32_16x16x32_bf16 a[36:39], a[228:231], a[140:143], a[36:39]\n"
"buffer_load_dwordx4 a[96:99], v2, s[36:39], s80 offen offset:0 sc1\n"
"v_mfma_f32_16x16x32_bf16 a[40:43], a[224:227], a[144:147], a[40:43]\n"
"ds_read_b64_tr_b16 a[254:255], v19 offset:47680\n"
"v_xor_b32 v56, v56, v57\n"
"v_mfma_f32_16x16x32_bf16 a[40:43], a[228:231], a[148:151], a[40:43]\n"
"s_waitcnt lgkmcnt(8)\n"
"v_mul_u32_u24 v55, 1280, v55\n"
"v_lshl_add_u32 v34, v56, 4, v55\n"
"v_mfma_f32_16x16x32_bf16 a[44:47], a[224:227], a[152:155], a[44:47]\n"
"v_add_u32 v54, 192, v0\n"
"v_mul_u32_u24 v55, 3277, v54\n"
"v_mfma_f32_16x16x32_bf16 a[44:47], a[228:231], a[156:159], a[44:47]\n"
"ds_read_b64_tr_b16 a[224:225], v12 offset:0\n"
"v_lshrrev_b32 v55, 16, v55\n"
"v_mfma_f32_16x16x32_bf16 a[48:51], a[232:235], a[128:131], a[48:51]\n"
"buffer_load_dwordx4 a[100:103], v2, s[36:39], s80 offen offset:1024 sc1\n"
"v_mfma_f32_16x16x32_bf16 a[48:51], a[236:239], a[132:135], a[48:51]\n"
"ds_read_b64_tr_b16 a[226:227], v12 offset:6144\n"
"v_mul_u32_u24 v56, 20, v55\n"
"v_mfma_f32_16x16x32_bf16 a[52:55], a[232:235], a[136:139], a[52:55]\n"
"ds_read_b64_tr_b16 a[228:229], v12 offset:12288\n"
"v_sub_u32 v56, v54, v56\n"
"v_mfma_f32_16x16x32_bf16 a[52:55], a[236:239], a[140:143], a[52:55]\n"
"ds_read_b64_tr_b16 a[230:231], v12 offset:18432\n"
"v_bfe_u32 v57, v55, 2, 1\n"
"v_mfma_f32_16x16x32_bf16 a[56:59], a[232:235], a[144:147], a[56:59]\n"
"v_lshlrev_b32 v57, 1, v57\n"
"v_xor_b32 v56, v56, v57\n"
"v_mfma_f32_16x16x32_bf16 a[56:59], a[236:239], a[148:151], a[56:59]\n"
"buffer_load_dwordx4 a[104:107], v3, s[36:39], s80 offen offset:0 sc1\n"
"v_mfma_f32_16x16x32_bf16 a[60:63], a[232:235], a[152:155], a[60:63]\n"
"s_waitcnt lgkmcnt(8)\n"
"v_mul_u32_u24 v55, 1280, v55\n"
"v_lshl_add_u32 v35, v56, 4, v55\n"
"v_mfma_f32_16x16x32_bf16 a[60:63], a[236:239], a[156:159], a[60:63]\n"
"ds_read_b64_tr_b16 a[232:233], v12 offset:8\n"
"v_add_u32 v54, 256, v0\n"
"v_mfma_f32_16x16x32_bf16 a[64:67], a[240:243], a[128:131], a[64:67]\n"
"ds_read_b64_tr_b16 a[234:235], v12 offset:6152\n"
"v_mul_u32_u24 v55, 3277, v54\n"
"v_mfma_f32_16x16x32_bf16 a[64:67], a[244:247], a[132:135], a[64:67]\n"
"ds_read_b64_tr_b16 a[236:237], v12 offset:12296\n"
"v_lshrrev_b32 v55, 16, v55\n"
"v_mfma_f32_16x16x32_bf16 a[68:71], a[240:243], a[136:139], a[68:71]\n"
"buffer_load_dwordx4 a[108:111], v3, s[36:39], s80 offen offset:1024 sc1\n"
"v_mfma_f32_16x16x32_bf16 a[68:71], a[244:247], a[140:143], a[68:71]\n"
"ds_read_b64_tr_b16 a[238:239], v12 offset:18440\n"
"v_mul_u32_u24 v56, 20, v55\n"
"v_mfma_f32_16x16x32_bf16 a[72:75], a[240:243], a[144:147], a[72:75]\n"
"v_sub_u32 v56, v54, v56\n"
"v_bfe_u32 v57, v55, 2, 1\n"
"v_mfma_f32_16x16x32_bf16 a[72:75], a[244:247], a[148:151], a[72:75]\n"
"s_waitcnt lgkmcnt(8)\n"
"v_lshlrev_b32 v57, 1, v57\n"
"v_xor_b32 v56, v56, v57\n"
"v_mfma_f32_16x16x32_bf16 a[76:79], a[240:243], a[152:155], a[76:79]\n"
"v_mul_u32_u24 v55, 1280, v55\n"
"v_lshl_add_u32 v36, v56, 4, v55\n"
"v_mfma_f32_16x16x32_bf16 a[76:79], a[244:247], a[156:159], a[76:79]\n"
"buffer_load_dwordx4 a[112:115], v4, s[36:39], s80 offen offset:0 sc1\n"
"v_mfma_f32_16x16x32_bf16 a[80:83], a[248:251], a[128:131], a[80:83]\n"
"ds_read_b64_tr_b16 a[240:241], v13 offset:0\n"
"v_add_u32 v58, 147456, v1\n"
"v_mfma_f32_16x16x32_bf16 a[80:83], a[252:255], a[132:135], a[80:83]\n"
"ds_read_b64_tr_b16 a[242:243], v13 offset:6144\n"
"v_lshrrev_b32 v48, 4, v0\n"
"v_mfma_f32_16x16x32_bf16 a[84:87], a[248:251], a[136:139], a[84:87]\n"
"ds_read_b64_tr_b16 a[244:245], v13 offset:12288\n"
"v_bfe_u32 v54, v0, 3, 1\n"
"v_mfma_f32_16x16x32_bf16 a[84:87], a[252:255], a[140:143], a[84:87]\n"
"ds_read_b64_tr_b16 a[246:247], v13 offset:18432\n"
"v_mul_u32_u24 v54, 3072, v54\n"
"v_mfma_f32_16x16x32_bf16 a[88:91], a[248:251], a[144:147], a[88:91]\n"
"buffer_load_dwordx4 a[116:119], v4, s[36:39], s80 offen offset:1024 sc1\n"
"v_mfma_f32_16x16x32_bf16 a[88:91], a[252:255], a[148:151], a[88:91]\n"
"s_waitcnt lgkmcnt(8)\n"
"s_waitcnt vmcnt(27)\n"
"v_and_b32 v55, 7, v0\n"
"v_mfma_f32_16x16x32_bf16 a[92:95], a[248:251], a[152:155], a[92:95]\n"
"v_lshl_add_u32 v54, v55, 7, v54\n"
"v_bfe_u32 v55, v0, 1, 3\n"
"v_mfma_f32_16x16x32_bf16 a[92:95], a[252:255], a[156:159], a[92:95]\n"
"ds_read_b64_tr_b16 a[248:249], v13 offset:8\n"
"v_xor_b32 v55, v55, v48\n"
"v_mfma_f32_16x16x32_bf16 v[64:67], a[224:227], a[160:163], v[64:67]\n"
"ds_read_b64_tr_b16 a[250:251], v13 offset:6152\n"
"v_xor_b32 v56, 0, v55\n"
"v_mfma_f32_16x16x32_bf16 v[64:67], a[228:231], a[164:167], v[64:67]\n"
"buffer_load_dwordx4 a[120:123], v5, s[36:39], s80 offen offset:0 sc1\n"
"v_mfma_f32_16x16x32_bf16 v[68:71], a[224:227], a[168:171], v[68:71]\n"
"ds_read_b64_tr_b16 a[252:253], v13 offset:12296\n"
"v_lshl_add_u32 v56, v56, 4, v54\n"
"v_mfma_f32_16x16x32_bf16 v[68:71], a[228:231], a[172:175], v[68:71]\n"
"ds_read_b64_tr_b16 a[254:255], v13 offset:18440\n"
"v_add_u32 v40, 73728, v56\n"
"v_mfma_f32_16x16x32_bf16 v[72:75], a[224:227], a[176:179], v[72:75]\n"
"v_add_u32 v42, 122880, v56\n"
"v_xor_b32 v56, 4, v55\n"
"v_mfma_f32_16x16x32_bf16 v[72:75], a[228:231], a[180:183], v[72:75]\n"
"s_waitcnt lgkmcnt(8)\n"
"v_lshl_add_u32 v56, v56, 4, v54\n"
"v_add_u32 v41, 73728, v56\n"
"v_mfma_f32_16x16x32_bf16 v[76:79], a[224:227], a[184:187], v[76:79]\n"
"buffer_load_dwordx4 a[124:127], v5, s[36:39], s80 offen offset:1024 sc1\n"
"v_mfma_f32_16x16x32_bf16 v[76:79], a[228:231], a[188:191], v[76:79]\n"
"s_add_u32 s80, s80, 10240\n"
"ds_read_b64_tr_b16 a[224:225], v12 offset:1024\n"
"v_mfma_f32_16x16x32_bf16 v[80:83], a[232:235], a[160:163], v[80:83]\n"
"ds_read_b64_tr_b16 a[226:227], v12 offset:7168\n"
"v_add_u32 v43, 122880, v56\n"
"v_mfma_f32_16x16x32_bf16 v[80:83], a[236:239], a[164:167], v[80:83]\n"
"ds_read_b64_tr_b16 a[228:229], v12 offset:13312\n"
"v_xor_b32 v54, 0, v53\n"
"v_mfma_f32_16x16x32_bf16 v[84:87], a[232:235], a[168:171], v[84:87]\n"
"ds_read_b64_tr_b16 a[230:231], v12 offset:19456\n"
"v_lshl_add_u32 v54, v54, 5, v52\n"
"v_mfma_f32_16x16x32_bf16 v[84:87], a[236:239], a[172:175], v[84:87]\n"
"buffer_load_dwordx4 v16, s[52:55], s4 offen lds\n"
"v_mfma_f32_16x16x32_bf16 v[88:91], a[232:235], a[176:179], v[88:91]\n"
"s_add_u32 m0, s83, 123904\n"
"v_lshl_add_u32 v44, v50, 3, v54\n"
"v_add_u32 v60, 49152, v44\n"
"v_mfma_f32_16x16x32_bf16 v[88:91], a[236:239], a[180:183], v[88:91]\n"
"s_waitcnt lgkmcnt(8)\n"
"v_xor_b32 v54, 1, v53\n"
"v_lshl_add_u32 v54, v54, 5, v52\n"
"v_mfma_f32_16x16x32_bf16 v[92:95], a[232:235], a[184:187], v[92:95]\n"
"v_lshl_add_u32 v45, v50, 3, v54\n"
"v_add_u32 v61, 49152, v45\n"
"v_mfma_f32_16x16x32_bf16 v[92:95], a[236:239], a[188:191], v[92:95]\n"
"ds_read_b64_tr_b16 a[232:233], v12 offset:1032\n"
"v_xor_b32 v54, 2, v53\n"
"v_mfma_f32_16x16x32_bf16 v[96:99], a[240:243], a[160:163], v[96:99]\n"
"ds_read_b64_tr_b16 a[234:235], v12 offset:7176\n"
"v_lshl_add_u32 v54, v54, 5, v52\n"
"v_mfma_f32_16x16x32_bf16 v[96:99], a[244:247], a[164:167], v[96:99]\n"
"ds_read_b64_tr_b16 a[236:237], v12 offset:13320\n"
"v_lshl_add_u32 v46, v50, 3, v54\n"
"v_mfma_f32_16x16x32_bf16 v[100:103], a[240:243], a[168:171], v[100:103]\n"
"ds_read_b64_tr_b16 a[238:239], v12 offset:19464\n"
"v_add_u32 v62, 49152, v46\n"
"v_mfma_f32_16x16x32_bf16 v[100:103], a[244:247], a[172:175], v[100:103]\n"
"v_xor_b32 v54, 3, v53\n"
"v_lshl_add_u32 v54, v54, 5, v52\n"
"v_mfma_f32_16x16x32_bf16 v[104:107], a[240:243], a[176:179], v[104:107]\n"
"buffer_load_dwordx4 v17, s[52:55], s4 offen lds\n"
"v_mfma_f32_16x16x32_bf16 v[104:107], a[244:247], a[180:183], v[104:107]\n"
"s_add_u32 m0, s83, 124928\n"
"s_waitcnt lgkmcnt(8)\n"
"v_lshl_add_u32 v47, v50, 3, v54\n"
"v_mfma_f32_16x16x32_bf16 v[108:111], a[240:243], a[184:187], v[108:111]\n"
"v_add_u32 v63, 49152, v47\n"
"v_mfma_f32_16x16x32_bf16 v[108:111], a[244:247], a[188:191], v[108:111]\n"
"ds_read_b64_tr_b16 a[240:241], v13 offset:1024\n"
"v_mfma_f32_16x16x32_bf16 v[112:115], a[248:251], a[160:163], v[112:115]\n"
"ds_read_b64_tr_b16 a[242:243], v13 offset:7168\n"
"v_mfma_f32_16x16x32_bf16 v[112:115], a[252:255], a[164:167], v[112:115]\n"
"ds_read_b64_tr_b16 a[244:245], v13 offset:13312\n"
"v_mfma_f32_16x16x32_bf16 v[116:119], a[248:251], a[168:171], v[116:119]\n"
"ds_read_b64_tr_b16 a[246:247], v13 offset:19456\n"
"v_mfma_f32_16x16x32_bf16 v[116:119], a[252:255], a[172:175], v[116:119]\n"
"ds_write_b128 v58, v[40:43]\n"
"v_mfma_f32_16x16x32_bf16 v[120:123], a[248:251], a[176:179], v[120:123]\n"
"ds_write_b128 v58, v[44:47] offset:1024\n"
"v_mfma_f32_16x16x32_bf16 v[120:123], a[252:255], a[180:183], v[120:123]\n"
"buffer_load_dwordx4 v18, s[52:55], s4 offen lds\n"
"v_mfma_f32_16x16x32_bf16 v[124:127], a[248:251], a[184:187], v[124:127]\n"
"s_add_u32 s4, s4, 98304\n"
"s_add_u32 m0, s83, 125952\n"
"s_waitcnt lgkmcnt(10)\n"
"v_mfma_f32_16x16x32_bf16 v[124:127], a[252:255], a[188:191], v[124:127]\n"
"ds_read_b64_tr_b16 a[248:249], v13 offset:1032\n"
"v_mfma_f32_16x16x32_bf16 v[128:131], a[224:227], a[160:163], v[128:131]\n"
"ds_read_b64_tr_b16 a[250:251], v13 offset:7176\n"
"v_mfma_f32_16x16x32_bf16 v[128:131], a[228:231], a[164:167], v[128:131]\n"
"ds_read_b64_tr_b16 a[252:253], v13 offset:13320\n"
"v_mfma_f32_16x16x32_bf16 v[132:135], a[224:227], a[168:171], v[132:135]\n"
"ds_read_b64_tr_b16 a[254:255], v13 offset:19464\n"
"v_mfma_f32_16x16x32_bf16 v[132:135], a[228:231], a[172:175], v[132:135]\n"
"ds_write_b128 v58, v[60:63] offset:2048\n"
"v_lshrrev_b32 v54, 2, v0\n"
"v_mfma_f32_16x16x32_bf16 v[136:139], a[224:227], a[176:179], v[136:139]\n"
"v_mul_u32_u24 v55, 320, v54\n"
"v_lshl_add_u32 v55, v50, 3, v55\n"
"v_mfma_f32_16x16x32_bf16 v[136:139], a[228:231], a[180:183], v[136:139]\n"
"s_waitcnt lgkmcnt(11)\n"
"v_add_u32 v55, s7, v55\n"
"v_and_b32 v56, 16, v0\n"
"v_mfma_f32_16x16x32_bf16 v[140:143], a[224:227], a[184:187], v[140:143]\n"
"buffer_load_dwordx4 v16, s[52:55], s4 offen lds\n"
"v_mfma_f32_16x16x32_bf16 v[140:143], a[228:231], a[188:191], v[140:143]\n"
"s_add_u32 m0, s83, 126976\n"
"ds_read_b64_tr_b16 a[224:225], v12 offset:2048\n"
"v_mfma_f32_16x16x32_bf16 v[144:147], a[232:235], a[160:163], v[144:147]\n"
"ds_read_b64_tr_b16 a[226:227], v12 offset:8192\n"
"v_lshlrev_b32 v56, 1, v56\n"
"v_mfma_f32_16x16x32_bf16 v[144:147], a[236:239], a[164:167], v[144:147]\n"
"ds_read_b64_tr_b16 a[228:229], v12 offset:14336\n"
"v_add_u32 v37, v55, v56\n"
"v_mfma_f32_16x16x32_bf16 v[148:151], a[232:235], a[168:171], v[148:151]\n"
"ds_read_b64_tr_b16 a[230:231], v12 offset:20480\n"
"v_sub_u32 v38, v55, v56\n"
"v_mfma_f32_16x16x32_bf16 v[148:151], a[236:239], a[172:175], v[148:151]\n"
"v_add_u32 v40, s90, v1\n"
"v_add_u32 v41, s88, v1\n"
"v_mfma_f32_16x16x32_bf16 v[152:155], a[232:235], a[176:179], v[152:155]\n"
"v_add_u32 v42, 786432, v40\n"
"v_add_u32 v43, 786432, v41\n"
"v_mfma_f32_16x16x32_bf16 v[152:155], a[236:239], a[180:183], v[152:155]\n"
"s_waitcnt lgkmcnt(11)\n"
"v_lshrrev_b32 v44, 1, v40\n"
"v_lshrrev_b32 v45, 1, v41\n"
"v_mfma_f32_16x16x32_bf16 v[156:159], a[232:235], a[184:187], v[156:159]\n"
"v_lshlrev_b32 v47, 3, v0\n"
"v_add_u32 v47, 2048, v47\n"
"v_mfma_f32_16x16x32_bf16 v[156:159], a[236:239], a[188:191], v[156:159]\n"
"buffer_load_dwordx4 v17, s[52:55], s4 offen lds\n"
"v_mfma_f32_16x16x32_bf16 v[160:163], a[240:243], a[160:163], v[160:163]\n"
"s_add_u32 m0, s83, 128000\n"
"ds_read_b64_tr_b16 a[232:233], v12 offset:2056\n"
"v_mfma_f32_16x16x32_bf16 v[160:163], a[244:247], a[164:167], v[160:163]\n"
"ds_read_b64_tr_b16 a[234:235], v12 offset:8200\n"
"v_and_b32 v54, 15, v0\n"
"v_mfma_f32_16x16x32_bf16 v[164:167], a[240:243], a[168:171], v[164:167]\n"
"ds_read_b64_tr_b16 a[236:237], v12 offset:14344\n"
"v_mul_u32_u24 v54, 1280, v54\n"
"v_mfma_f32_16x16x32_bf16 v[164:167], a[244:247], a[172:175], v[164:167]\n"
"ds_read_b64_tr_b16 a[238:239], v12 offset:20488\n"
"v_lshl_add_u32 v46, v48, 3, v54\n"
"v_mfma_f32_16x16x32_bf16 v[168:171], a[240:243], a[176:179], v[168:171]\n"
"ds_write_b128 v58, v[40:43] offset:3072\n"
"v_mfma_f32_16x16x32_bf16 v[168:171], a[244:247], a[180:183], v[168:171]\n"
"s_waitcnt lgkmcnt(10)\n"
"ds_write_b128 v58, v[44:47] offset:4096\n"
"v_mfma_f32_16x16x32_bf16 v[172:175], a[240:243], a[184:187], v[172:175]\n"
"v_mov_b32 v39, 0xc0135761\n"
"v_mfma_f32_16x16x32_bf16 v[172:175], a[244:247], a[188:191], v[172:175]\n"
"ds_read_b64_tr_b16 a[240:241], v13 offset:2048\n"
"v_mfma_f32_16x16x32_bf16 v[176:179], a[248:251], a[160:163], v[176:179]\n"
"s_waitcnt vmcnt(28)\n"
"s_barrier\n"
"v_mfma_f32_16x16x32_bf16 v[176:179], a[252:255], a[164:167], v[176:179]\n"
"buffer_load_dwordx4 v18, s[52:55], s4 offen lds\n"
"v_mfma_f32_16x16x32_bf16 v[180:183], a[248:251], a[168:171], v[180:183]\n"
"s_add_u32 s4, s4, 98304\n"
"s_add_u32 m0, s83, 0\n"
"v_mfma_f32_16x16x32_bf16 v[180:183], a[252:255], a[172:175], v[180:183]\n"
"ds_read_b64_tr_b16 a[242:243], v13 offset:8192\n"
"v_mfma_f32_16x16x32_bf16 v[184:187], a[248:251], a[176:179], v[184:187]\n"
"ds_read_b64_tr_b16 a[244:245], v13 offset:14336\n"
"v_mfma_f32_16x16x32_bf16 v[184:187], a[252:255], a[180:183], v[184:187]\n"
"s_waitcnt lgkmcnt(9)\n"
"ds_read_b64_tr_b16 a[246:247], v13 offset:20480\n"
"v_mfma_f32_16x16x32_bf16 v[188:191], a[248:251], a[184:187], v[188:191]\n"
"v_mfma_f32_16x16x32_bf16 v[188:191], a[252:255], a[188:191], v[188:191]\n"
"ds_read_b64_tr_b16 a[248:249], v13 offset:2056\n"
"v_mfma_f32_16x16x32_bf16 v[192:195], a[224:227], a[160:163], v[192:195]\n"
"ds_read_b64_tr_b16 a[250:251], v13 offset:8200\n"
"v_mfma_f32_16x16x32_bf16 v[192:195], a[228:231], a[164:167], v[192:195]\n"
"buffer_load_dwordx4 a[128:131], v2, s[36:39], s80 offen offset:0 sc1\n"
"v_mfma_f32_16x16x32_bf16 v[196:199], a[224:227], a[168:171], v[196:199]\n"
"ds_read_b64_tr_b16 a[252:253], v13 offset:14344\n"
"v_mfma_f32_16x16x32_bf16 v[196:199], a[228:231], a[172:175], v[196:199]\n"
"ds_read_b64_tr_b16 a[254:255], v13 offset:20488\n"
"v_mfma_f32_16x16x32_bf16 v[200:203], a[224:227], a[176:179], v[200:203]\n"
"v_mfma_f32_16x16x32_bf16 v[200:203], a[228:231], a[180:183], v[200:203]\n"
"s_waitcnt lgkmcnt(10)\n"
"v_mfma_f32_16x16x32_bf16 v[204:207], a[224:227], a[184:187], v[204:207]\n"
"buffer_load_dwordx4 a[132:135], v2, s[36:39], s80 offen offset:1024 sc1\n"
"v_mfma_f32_16x16x32_bf16 v[204:207], a[228:231], a[188:191], v[204:207]\n"
"ds_read_b64_tr_b16 a[224:225], v20 offset:24576\n"
"v_mfma_f32_16x16x32_bf16 v[208:211], a[232:235], a[160:163], v[208:211]\n"
"ds_read_b64_tr_b16 a[226:227], v20 offset:26112\n"
"v_mfma_f32_16x16x32_bf16 v[208:211], a[236:239], a[164:167], v[208:211]\n"
"ds_read_b64_tr_b16 a[228:229], v20 offset:27648\n"
"v_mfma_f32_16x16x32_bf16 v[212:215], a[232:235], a[168:171], v[212:215]\n"
"ds_read_b64_tr_b16 a[230:231], v20 offset:29184\n"
"v_mfma_f32_16x16x32_bf16 v[212:215], a[236:239], a[172:175], v[212:215]\n"
"buffer_load_dwordx4 a[136:139], v3, s[36:39], s80 offen offset:0 sc1\n"
"v_mfma_f32_16x16x32_bf16 v[216:219], a[232:235], a[176:179], v[216:219]\n"
"v_mfma_f32_16x16x32_bf16 v[216:219], a[236:239], a[180:183], v[216:219]\n"
"s_waitcnt lgkmcnt(8)\n"
"v_mfma_f32_16x16x32_bf16 v[220:223], a[232:235], a[184:187], v[220:223]\n"
"v_mfma_f32_16x16x32_bf16 v[220:223], a[236:239], a[188:191], v[220:223]\n"
"ds_read_b64_tr_b16 a[232:233], v20 offset:36864\n"
"v_mfma_f32_16x16x32_bf16 v[224:227], a[240:243], a[160:163], v[224:227]\n"
"buffer_load_dwordx4 a[140:143], v3, s[36:39], s80 offen offset:1024 sc1\n"
"v_mfma_f32_16x16x32_bf16 v[224:227], a[244:247], a[164:167], v[224:227]\n"
"ds_read_b64_tr_b16 a[234:235], v20 offset:38400\n"
"v_mfma_f32_16x16x32_bf16 v[228:231], a[240:243], a[168:171], v[228:231]\n"
"ds_read_b64_tr_b16 a[236:237], v20 offset:39936\n"
"v_mfma_f32_16x16x32_bf16 v[228:231], a[244:247], a[172:175], v[228:231]\n"
"ds_read_b64_tr_b16 a[238:239], v20 offset:41472\n"
"v_mfma_f32_16x16x32_bf16 v[232:235], a[240:243], a[176:179], v[232:235]\n"
"v_mfma_f32_16x16x32_bf16 v[232:235], a[244:247], a[180:183], v[232:235]\n"
"buffer_load_dwordx4 a[144:147], v4, s[36:39], s80 offen offset:0 sc1\n"
"v_mfma_f32_16x16x32_bf16 v[236:239], a[240:243], a[184:187], v[236:239]\n"
"s_waitcnt lgkmcnt(8)\n"
"v_mfma_f32_16x16x32_bf16 v[236:239], a[244:247], a[188:191], v[236:239]\n"
"ds_read_b64_tr_b16 a[240:241], v20 offset:24608\n"
"v_mfma_f32_16x16x32_bf16 v[240:243], a[248:251], a[160:163], v[240:243]\n"
"ds_read_b64_tr_b16 a[242:243], v20 offset:26144\n"
"v_mfma_f32_16x16x32_bf16 v[240:243], a[252:255], a[164:167], v[240:243]\n"
"ds_read_b64_tr_b16 a[244:245], v20 offset:27680\n"
"v_mfma_f32_16x16x32_bf16 v[244:247], a[248:251], a[168:171], v[244:247]\n"
"buffer_load_dwordx4 a[148:151], v4, s[36:39], s80 offen offset:1024 sc1\n"
"v_mfma_f32_16x16x32_bf16 v[244:247], a[252:255], a[172:175], v[244:247]\n"
"ds_read_b64_tr_b16 a[246:247], v20 offset:29216\n"
"v_mfma_f32_16x16x32_bf16 v[248:251], a[248:251], a[176:179], v[248:251]\n"
"v_mfma_f32_16x16x32_bf16 v[248:251], a[252:255], a[180:183], v[248:251]\n"
"s_waitcnt lgkmcnt(8)\n"
"v_mfma_f32_16x16x32_bf16 v[252:255], a[248:251], a[184:187], v[252:255]\n"
"v_mfma_f32_16x16x32_bf16 v[252:255], a[252:255], a[188:191], v[252:255]\n"
"buffer_load_dwordx4 a[152:155], v5, s[36:39], s80 offen offset:0 sc1\n"
"v_mfma_f32_16x16x32_bf16 a[0:3], a[224:227], a[160:163], a[0:3]\n"
"ds_read_b64_tr_b16 a[248:249], v20 offset:36896\n"
"v_mfma_f32_16x16x32_bf16 a[0:3], a[228:231], a[164:167], a[0:3]\n"
"ds_read_b64_tr_b16 a[250:251], v20 offset:38432\n"
"v_mfma_f32_16x16x32_bf16 a[4:7], a[224:227], a[168:171], a[4:7]\n"
"ds_read_b64_tr_b16 a[252:253], v20 offset:39968\n"
"v_mfma_f32_16x16x32_bf16 a[4:7], a[228:231], a[172:175], a[4:7]\n"
"ds_read_b64_tr_b16 a[254:255], v20 offset:41504\n"
"v_mfma_f32_16x16x32_bf16 a[8:11], a[224:227], a[176:179], a[8:11]\n"
"buffer_load_dwordx4 a[156:159], v5, s[36:39], s80 offen offset:1024 sc1\n"
"v_mfma_f32_16x16x32_bf16 a[8:11], a[228:231], a[180:183], a[8:11]\n"
"s_sub_u32 s80, s80, 8192\n"
"s_waitcnt lgkmcnt(8)\n"
"v_mfma_f32_16x16x32_bf16 a[12:15], a[224:227], a[184:187], a[12:15]\n"
"v_mfma_f32_16x16x32_bf16 a[12:15], a[228:231], a[188:191], a[12:15]\n"
"ds_read_b64_tr_b16 a[224:225], v20 offset:24640\n"
"v_mfma_f32_16x16x32_bf16 a[16:19], a[232:235], a[160:163], a[16:19]\n"
"ds_read_b64_tr_b16 a[226:227], v20 offset:26176\n"
"v_mfma_f32_16x16x32_bf16 a[16:19], a[236:239], a[164:167], a[16:19]\n"
"buffer_load_dwordx4 v6, s[40:43], s81 offen offset:0 lds\n"
"v_mfma_f32_16x16x32_bf16 a[20:23], a[232:235], a[168:171], a[20:23]\n"
"s_add_u32 m0, s83, 896\n"
"ds_read_b64_tr_b16 a[228:229], v20 offset:27712\n"
"v_mfma_f32_16x16x32_bf16 a[20:23], a[236:239], a[172:175], a[20:23]\n"
"ds_read_b64_tr_b16 a[230:231], v20 offset:29248\n"
"v_mfma_f32_16x16x32_bf16 a[24:27], a[232:235], a[176:179], a[24:27]\n"
"v_mfma_f32_16x16x32_bf16 a[24:27], a[236:239], a[180:183], a[24:27]\n"
"s_waitcnt lgkmcnt(8)\n"
"v_mfma_f32_16x16x32_bf16 a[28:31], a[232:235], a[184:187], a[28:31]\n"
"buffer_load_dwordx4 v6, s[40:43], s81 offen offset:128 lds\n"
"v_mfma_f32_16x16x32_bf16 a[28:31], a[236:239], a[188:191], a[28:31]\n"
"s_add_u32 m0, s83, 1792\n"
"ds_read_b64_tr_b16 a[232:233], v20 offset:36928\n"
"v_mfma_f32_16x16x32_bf16 a[32:35], a[240:243], a[160:163], a[32:35]\n"
"ds_read_b64_tr_b16 a[234:235], v20 offset:38464\n"
"v_mfma_f32_16x16x32_bf16 a[32:35], a[244:247], a[164:167], a[32:35]\n"
"ds_read_b64_tr_b16 a[236:237], v20 offset:40000\n"
"v_mfma_f32_16x16x32_bf16 a[36:39], a[240:243], a[168:171], a[36:39]\n"
"ds_read_b64_tr_b16 a[238:239], v20 offset:41536\n"
"v_mfma_f32_16x16x32_bf16 a[36:39], a[244:247], a[172:175], a[36:39]\n"
"buffer_load_dwordx4 v6, s[40:43], s81 offen offset:256 lds\n"
"v_mfma_f32_16x16x32_bf16 a[40:43], a[240:243], a[176:179], a[40:43]\n"
"s_add_u32 s81, s81, 786432\n"
"s_add_u32 m0, s83, 3072\n"
"v_mfma_f32_16x16x32_bf16 a[40:43], a[244:247], a[180:183], a[40:43]\n"
"s_waitcnt lgkmcnt(8)\n"
"v_mfma_f32_16x16x32_bf16 a[44:47], a[240:243], a[184:187], a[44:47]\n"
"v_mfma_f32_16x16x32_bf16 a[44:47], a[244:247], a[188:191], a[44:47]\n"
"ds_read_b64_tr_b16 a[240:241], v20 offset:30720\n"
"v_mfma_f32_16x16x32_bf16 a[48:51], a[248:251], a[160:163], a[48:51]\n"
"buffer_load_dwordx4 v6, s[40:43], s82 offen offset:0 lds\n"
"v_mfma_f32_16x16x32_bf16 a[48:51], a[252:255], a[164:167], a[48:51]\n"
"s_add_u32 m0, s83, 3968\n"
"ds_read_b64_tr_b16 a[242:243], v20 offset:32256\n"
"v_mfma_f32_16x16x32_bf16 a[52:55], a[248:251], a[168:171], a[52:55]\n"
"ds_read_b64_tr_b16 a[244:245], v20 offset:33792\n"
"v_mfma_f32_16x16x32_bf16 a[52:55], a[252:255], a[172:175], a[52:55]\n"
"ds_read_b64_tr_b16 a[246:247], v20 offset:35328\n"
"v_mfma_f32_16x16x32_bf16 a[56:59], a[248:251], a[176:179], a[56:59]\n"
"v_mfma_f32_16x16x32_bf16 a[56:59], a[252:255], a[180:183], a[56:59]\n"
"buffer_load_dwordx4 v6, s[40:43], s82 offen offset:128 lds\n"
"v_mfma_f32_16x16x32_bf16 a[60:63], a[248:251], a[184:187], a[60:63]\n"
"s_add_u32 m0, s83, 4864\n"
"s_waitcnt lgkmcnt(8)\n"
"v_mfma_f32_16x16x32_bf16 a[60:63], a[252:255], a[188:191], a[60:63]\n"
"ds_read_b64_tr_b16 a[248:249], v20 offset:43008\n"
"v_mfma_f32_16x16x32_bf16 a[64:67], a[224:227], a[160:163], a[64:67]\n"
"ds_read_b64_tr_b16 a[250:251], v20 offset:44544\n"
"v_mfma_f32_16x16x32_bf16 a[64:67], a[228:231], a[164:167], a[64:67]\n"
"ds_read_b64_tr_b16 a[252:253], v20 offset:46080\n"
"v_mfma_f32_16x16x32_bf16 a[68:71], a[224:227], a[168:171], a[68:71]\n"
"buffer_load_dwordx4 v6, s[40:43], s82 offen offset:256 lds\n"
"v_mfma_f32_16x16x32_bf16 a[68:71], a[228:231], a[172:175], a[68:71]\n"
"s_add_u32 s82, s82, 786432\n"
"s_add_u32 m0, s83, 24576\n"
"v_mfma_f32_16x16x32_bf16 a[72:75], a[224:227], a[176:179], a[72:75]\n"
"ds_read_b64_tr_b16 a[254:255], v20 offset:47616\n"
"v_mfma_f32_16x16x32_bf16 a[72:75], a[228:231], a[180:183], a[72:75]\n"
"s_waitcnt lgkmcnt(8)\n"
"v_mfma_f32_16x16x32_bf16 a[76:79], a[224:227], a[184:187], a[76:79]\n"
"v_mfma_f32_16x16x32_bf16 a[76:79], a[228:231], a[188:191], a[76:79]\n"
"buffer_load_dwordx4 v16, s[52:55], s4 offen lds\n"
"v_mfma_f32_16x16x32_bf16 a[80:83], a[232:235], a[160:163], a[80:83]\n"
"s_add_u32 m0, s83, 25600\n"
"ds_read_b64_tr_b16 a[224:225], v20 offset:30752\n"
"v_mfma_f32_16x16x32_bf16 a[80:83], a[236:239], a[164:167], a[80:83]\n"
"ds_read_b64_tr_b16 a[226:227], v20 offset:32288\n"
"v_mfma_f32_16x16x32_bf16 a[84:87], a[232:235], a[168:171], a[84:87]\n"
"ds_read_b64_tr_b16 a[228:229], v20 offset:33824\n"
"v_mfma_f32_16x16x32_bf16 a[84:87], a[236:239], a[172:175], a[84:87]\n"
"ds_read_b64_tr_b16 a[230:231], v20 offset:35360\n"
"v_mfma_f32_16x16x32_bf16 a[88:91], a[232:235], a[176:179], a[88:91]\n"
"v_mfma_f32_16x16x32_bf16 a[88:91], a[236:239], a[180:183], a[88:91]\n"
"s_waitcnt lgkmcnt(8)\n"
"s_waitcnt vmcnt(36)\n"
"v_mfma_f32_16x16x32_bf16 a[92:95], a[232:235], a[184:187], a[92:95]\n"
"v_mfma_f32_16x16x32_bf16 a[92:95], a[236:239], a[188:191], a[92:95]\n"
"ds_read_b64_tr_b16 a[232:233], v20 offset:43040\n"
"v_mfma_f32_16x16x32_bf16 a[0:3], a[240:243], a[192:195], a[0:3]\n"
"buffer_load_dwordx4 a[160:163], v2, s[36:39], s80 offen offset:0 sc1\n"
"v_mfma_f32_16x16x32_bf16 a[0:3], a[244:247], a[196:199], a[0:3]\n"
"ds_read_b64_tr_b16 a[234:235], v20 offset:44576\n"
"v_mfma_f32_16x16x32_bf16 a[4:7], a[240:243], a[200:203], a[4:7]\n"
"ds_read_b64_tr_b16 a[236:237], v20 offset:46112\n"
"v_mfma_f32_16x16x32_bf16 a[4:7], a[244:247], a[204:207], a[4:7]\n"
"ds_read_b64_tr_b16 a[238:239], v20 offset:47648\n"
"v_mfma_f32_16x16x32_bf16 a[8:11], a[240:243], a[208:211], a[8:11]\n"
"v_mfma_f32_16x16x32_bf16 a[8:11], a[244:247], a[212:215], a[8:11]\n"
"buffer_load_dwordx4 a[164:167], v2, s[36:39], s80 offen offset:1024 sc1\n"
"v_mfma_f32_16x16x32_bf16 a[12:15], a[240:243], a[216:219], a[12:15]\n"
"s_waitcnt lgkmcnt(8)\n"
"v_mfma_f32_16x16x32_bf16 a[12:15], a[244:247], a[220:223], a[12:15]\n"
"ds_read_b64_tr_b16 a[240:241], v20 offset:30784\n"
"v_mfma_f32_16x16x32_bf16 a[16:19], a[248:251], a[192:195], a[16:19]\n"
"s_waitcnt vmcnt(31)\n"
"s_barrier\n"
"v_mfma_f32_16x16x32_bf16 a[16:19], a[252:255], a[196:199], a[16:19]\n"
"ds_read_b64_tr_b16 a[242:243], v20 offset:32320\n"
"v_mfma_f32_16x16x32_bf16 a[20:23], a[248:251], a[200:203], a[20:23]\n"
"buffer_load_dwordx4 a[168:171], v3, s[36:39], s80 offen offset:0 sc1\n"
"v_mfma_f32_16x16x32_bf16 a[20:23], a[252:255], a[204:207], a[20:23]\n"
"ds_read_b64_tr_b16 a[244:245], v20 offset:33856\n"
"v_mfma_f32_16x16x32_bf16 a[24:27], a[248:251], a[208:211], a[24:27]\n"
"ds_read_b64_tr_b16 a[246:247], v20 offset:35392\n"
"v_mfma_f32_16x16x32_bf16 a[24:27], a[252:255], a[212:215], a[24:27]\n"
"s_waitcnt lgkmcnt(8)\n"
"v_mfma_f32_16x16x32_bf16 a[28:31], a[248:251], a[216:219], a[28:31]\n"
"v_mfma_f32_16x16x32_bf16 a[28:31], a[252:255], a[220:223], a[28:31]\n"
"buffer_load_dwordx4 a[172:175], v3, s[36:39], s80 offen offset:1024 sc1\n"
"v_mfma_f32_16x16x32_bf16 a[32:35], a[224:227], a[192:195], a[32:35]\n"
"ds_read_b64_tr_b16 a[248:249], v20 offset:43072\n"
"v_mfma_f32_16x16x32_bf16 a[32:35], a[228:231], a[196:199], a[32:35]\n"
"ds_read_b64_tr_b16 a[250:251], v20 offset:44608\n"
"v_mfma_f32_16x16x32_bf16 a[36:39], a[224:227], a[200:203], a[36:39]\n"
"ds_read_b64_tr_b16 a[252:253], v20 offset:46144\n"
"v_mfma_f32_16x16x32_bf16 a[36:39], a[228:231], a[204:207], a[36:39]\n"
"ds_read_b64_tr_b16 a[254:255], v20 offset:47680\n"
"v_mfma_f32_16x16x32_bf16 a[40:43], a[224:227], a[208:211], a[40:43]\n"
"buffer_load_dwordx4 a[176:179], v4, s[36:39], s80 offen offset:0 sc1\n"
"v_mfma_f32_16x16x32_bf16 a[40:43], a[228:231], a[212:215], a[40:43]\n"
"s_waitcnt lgkmcnt(8)\n"
"v_mfma_f32_16x16x32_bf16 a[44:47], a[224:227], a[216:219], a[44:47]\n"
"v_mfma_f32_16x16x32_bf16 a[44:47], a[228:231], a[220:223], a[44:47]\n"
"ds_read_b64_tr_b16 a[224:225], v14 offset:0\n"
"v_mfma_f32_16x16x32_bf16 a[48:51], a[232:235], a[192:195], a[48:51]\n"
"ds_read_b64_tr_b16 a[226:227], v14 offset:6144\n"
"v_mfma_f32_16x16x32_bf16 a[48:51], a[236:239], a[196:199], a[48:51]\n"
"buffer_load_dwordx4 a[180:183], v4, s[36:39], s80 offen offset:1024 sc1\n"
"v_mfma_f32_16x16x32_bf16 a[52:55], a[232:235], a[200:203], a[52:55]\n"
"ds_read_b64_tr_b16 a[228:229], v14 offset:12288\n"
"v_mfma_f32_16x16x32_bf16 a[52:55], a[236:239], a[204:207], a[52:55]\n"
"ds_read_b64_tr_b16 a[230:231], v14 offset:18432\n"
"v_mfma_f32_16x16x32_bf16 a[56:59], a[232:235], a[208:211], a[56:59]\n"
"v_mfma_f32_16x16x32_bf16 a[56:59], a[236:239], a[212:215], a[56:59]\n"
"s_waitcnt lgkmcnt(8)\n"
"v_mfma_f32_16x16x32_bf16 a[60:63], a[232:235], a[216:219], a[60:63]\n"
"buffer_load_dwordx4 a[184:187], v5, s[36:39], s80 offen offset:0 sc1\n"
"v_mfma_f32_16x16x32_bf16 a[60:63], a[236:239], a[220:223], a[60:63]\n"
"ds_read_b64_tr_b16 a[232:233], v14 offset:8\n"
"v_mfma_f32_16x16x32_bf16 a[64:67], a[240:243], a[192:195], a[64:67]\n"
"ds_read_b64_tr_b16 a[234:235], v14 offset:6152\n"
"v_mfma_f32_16x16x32_bf16 a[64:67], a[244:247], a[196:199], a[64:67]\n"
"ds_read_b64_tr_b16 a[236:237], v14 offset:12296\n"
"v_mfma_f32_16x16x32_bf16 a[68:71], a[240:243], a[200:203], a[68:71]\n"
"ds_read_b64_tr_b16 a[238:239], v14 offset:18440\n"
"v_mfma_f32_16x16x32_bf16 a[68:71], a[244:247], a[204:207], a[68:71]\n"
"buffer_load_dwordx4 a[188:191], v5, s[36:39], s80 offen offset:1024 sc1\n"
"v_mfma_f32_16x16x32_bf16 a[72:75], a[240:243], a[208:211], a[72:75]\n"
"s_add_u32 s80, s80, 10240\n"
"v_mfma_f32_16x16x32_bf16 a[72:75], a[244:247], a[212:215], a[72:75]\n"
"s_waitcnt lgkmcnt(8)\n"
"v_mfma_f32_16x16x32_bf16 a[76:79], a[240:243], a[216:219], a[76:79]\n"
"v_mfma_f32_16x16x32_bf16 a[76:79], a[244:247], a[220:223], a[76:79]\n"
"ds_read_b64_tr_b16 a[240:241], v15 offset:0\n"
"v_mfma_f32_16x16x32_bf16 a[80:83], a[248:251], a[192:195], a[80:83]\n"
"buffer_load_dwordx4 v17, s[52:55], s4 offen lds\n"
"v_mfma_f32_16x16x32_bf16 a[80:83], a[252:255], a[196:199], a[80:83]\n"
"s_add_u32 m0, s83, 26624\n"
"ds_read_b64_tr_b16 a[242:243], v15 offset:6144\n"
"v_mfma_f32_16x16x32_bf16 a[84:87], a[248:251], a[200:203], a[84:87]\n"
"ds_read_b64_tr_b16 a[244:245], v15 offset:12288\n"
"v_mfma_f32_16x16x32_bf16 a[84:87], a[252:255], a[204:207], a[84:87]\n"
"ds_read_b64_tr_b16 a[246:247], v15 offset:18432\n"
"v_mfma_f32_16x16x32_bf16 a[88:91], a[248:251], a[208:211], a[88:91]\n"
"v_mfma_f32_16x16x32_bf16 a[88:91], a[252:255], a[212:215], a[88:91]\n"
"s_waitcnt lgkmcnt(8)\n"
"s_waitcnt vmcnt(30)\n"
"v_mfma_f32_16x16x32_bf16 a[92:95], a[248:251], a[216:219], a[92:95]\n"
"v_mfma_f32_16x16x32_bf16 a[92:95], a[252:255], a[220:223], a[92:95]\n"
"ds_read_b64_tr_b16 a[248:249], v15 offset:8\n"
"v_mfma_f32_16x16x32_bf16 v[64:67], a[224:227], a[96:99], v[64:67]\n"
"ds_read_b64_tr_b16 a[250:251], v15 offset:6152\n"
"v_mfma_f32_16x16x32_bf16 v[64:67], a[228:231], a[100:103], v[64:67]\n"
"buffer_load_dwordx4 v18, s[52:55], s4 offen lds\n"
"v_mfma_f32_16x16x32_bf16 v[68:71], a[224:227], a[104:107], v[68:71]\n"
"s_add_u32 s4, s4, 98304\n"
"s_add_u32 m0, s83, 27648\n"
"v_mfma_f32_16x16x32_bf16 v[68:71], a[228:231], a[108:111], v[68:71]\n"
"ds_read_b64_tr_b16 a[252:253], v15 offset:12296\n"
"v_mfma_f32_16x16x32_bf16 v[72:75], a[224:227], a[112:115], v[72:75]\n"
"ds_read_b64_tr_b16 a[254:255], v15 offset:18440\n"
"v_mfma_f32_16x16x32_bf16 v[72:75], a[228:231], a[116:119], v[72:75]\n"
"s_waitcnt lgkmcnt(8)\n"
"v_mfma_f32_16x16x32_bf16 v[76:79], a[224:227], a[120:123], v[76:79]\n"
"v_mfma_f32_16x16x32_bf16 v[76:79], a[228:231], a[124:127], v[76:79]\n"
"ds_read_b64_tr_b16 a[224:225], v14 offset:1024\n"
"v_mfma_f32_16x16x32_bf16 v[80:83], a[232:235], a[96:99], v[80:83]\n"
"ds_read_b64_tr_b16 a[226:227], v14 offset:7168\n"
"v_mfma_f32_16x16x32_bf16 v[80:83], a[236:239], a[100:103], v[80:83]\n"
"ds_read_b64_tr_b16 a[228:229], v14 offset:13312\n"
"v_mfma_f32_16x16x32_bf16 v[84:87], a[232:235], a[104:107], v[84:87]\n"
"buffer_load_dwordx4 v16, s[52:55], s4 offen lds\n"
"v_mfma_f32_16x16x32_bf16 v[84:87], a[236:239], a[108:111], v[84:87]\n"
"s_add_u32 m0, s83, 28672\n"
"ds_read_b64_tr_b16 a[230:231], v14 offset:19456\n"
"v_mfma_f32_16x16x32_bf16 v[88:91], a[232:235], a[112:115], v[88:91]\n"
"v_mfma_f32_16x16x32_bf16 v[88:91], a[236:239], a[116:119], v[88:91]\n"
"s_waitcnt lgkmcnt(8)\n"
"v_mfma_f32_16x16x32_bf16 v[92:95], a[232:235], a[120:123], v[92:95]\n"
"v_mfma_f32_16x16x32_bf16 v[92:95], a[236:239], a[124:127], v[92:95]\n"
"ds_read_b64_tr_b16 a[232:233], v14 offset:1032\n"
"v_mfma_f32_16x16x32_bf16 v[96:99], a[240:243], a[96:99], v[96:99]\n"
"ds_read_b64_tr_b16 a[234:235], v14 offset:7176\n"
"v_mfma_f32_16x16x32_bf16 v[96:99], a[244:247], a[100:103], v[96:99]\n"
"ds_read_b64_tr_b16 a[236:237], v14 offset:13320\n"
"v_mfma_f32_16x16x32_bf16 v[100:103], a[240:243], a[104:107], v[100:103]\n"
"ds_read_b64_tr_b16 a[238:239], v14 offset:19464\n"
"v_mfma_f32_16x16x32_bf16 v[100:103], a[244:247], a[108:111], v[100:103]\n"
"buffer_load_dwordx4 v17, s[52:55], s4 offen lds\n"
"v_mfma_f32_16x16x32_bf16 v[104:107], a[240:243], a[112:115], v[104:107]\n"
"s_add_u32 m0, s83, 29696\n"
"v_mfma_f32_16x16x32_bf16 v[104:107], a[244:247], a[116:119], v[104:107]\n"
"s_waitcnt lgkmcnt(8)\n"
"v_mfma_f32_16x16x32_bf16 v[108:111], a[240:243], a[120:123], v[108:111]\n"
"v_mfma_f32_16x16x32_bf16 v[108:111], a[244:247], a[124:127], v[108:111]\n"
"ds_read_b64_tr_b16 a[240:241], v15 offset:1024\n"
"v_mfma_f32_16x16x32_bf16 v[112:115], a[248:251], a[96:99], v[112:115]\n"
"ds_read_b64_tr_b16 a[242:243], v15 offset:7168\n"
"v_mfma_f32_16x16x32_bf16 v[112:115], a[252:255], a[100:103], v[112:115]\n"
"ds_read_b64_tr_b16 a[244:245], v15 offset:13312\n"
"v_mfma_f32_16x16x32_bf16 v[116:119], a[248:251], a[104:107], v[116:119]\n"
"ds_read_b64_tr_b16 a[246:247], v15 offset:19456\n"
"v_mfma_f32_16x16x32_bf16 v[116:119], a[252:255], a[108:111], v[116:119]\n"
"v_mfma_f32_16x16x32_bf16 v[120:123], a[248:251], a[112:115], v[120:123]\n"
"buffer_load_dwordx4 v18, s[52:55], s4 offen lds\n"
"v_mfma_f32_16x16x32_bf16 v[120:123], a[252:255], a[116:119], v[120:123]\n"
"s_add_u32 s4, s4, 98304\n"
"s_add_u32 m0, s83, 49152\n"
"s_waitcnt lgkmcnt(8)\n"
"v_mfma_f32_16x16x32_bf16 v[124:127], a[248:251], a[120:123], v[124:127]\n"
"v_mfma_f32_16x16x32_bf16 v[124:127], a[252:255], a[124:127], v[124:127]\n"
"ds_read_b64_tr_b16 a[248:249], v15 offset:1032\n"
"v_mfma_f32_16x16x32_bf16 v[128:131], a[224:227], a[96:99], v[128:131]\n"
"ds_read_b64_tr_b16 a[250:251], v15 offset:7176\n"
"v_mfma_f32_16x16x32_bf16 v[128:131], a[228:231], a[100:103], v[128:131]\n"
"ds_read_b64_tr_b16 a[252:253], v15 offset:13320\n"
"v_mfma_f32_16x16x32_bf16 v[132:135], a[224:227], a[104:107], v[132:135]\n"
"ds_read_b64_tr_b16 a[254:255], v15 offset:19464\n"
"v_mfma_f32_16x16x32_bf16 v[132:135], a[228:231], a[108:111], v[132:135]\n"
"v_mfma_f32_16x16x32_bf16 v[136:139], a[224:227], a[112:115], v[136:139]\n"
"v_mfma_f32_16x16x32_bf16 v[136:139], a[228:231], a[116:119], v[136:139]\n"
"buffer_load_dwordx4 a[192:195], v2, s[36:39], s80 offen offset:0 sc1\n"
"v_mfma_f32_16x16x32_bf16 v[140:143], a[224:227], a[120:123], v[140:143]\n"
"s_waitcnt lgkmcnt(8)\n"
"v_mfma_f32_16x16x32_bf16 v[140:143], a[228:231], a[124:127], v[140:143]\n"
"ds_read_b64_tr_b16 a[224:225], v14 offset:2048\n"
"v_mfma_f32_16x16x32_bf16 v[144:147], a[232:235], a[96:99], v[144:147]\n"
"ds_read_b64_tr_b16 a[226:227], v14 offset:8192\n"
"v_mfma_f32_16x16x32_bf16 v[144:147], a[236:239], a[100:103], v[144:147]\n"
"ds_read_b64_tr_b16 a[228:229], v14 offset:14336\n"
"v_mfma_f32_16x16x32_bf16 v[148:151], a[232:235], a[104:107], v[148:151]\n"
"buffer_load_dwordx4 a[196:199], v2, s[36:39], s80 offen offset:1024 sc1\n"
"v_mfma_f32_16x16x32_bf16 v[148:151], a[236:239], a[108:111], v[148:151]\n"
"ds_read_b64_tr_b16 a[230:231], v14 offset:20480\n"
"v_mfma_f32_16x16x32_bf16 v[152:155], a[232:235], a[112:115], v[152:155]\n"
"v_mfma_f32_16x16x32_bf16 v[152:155], a[236:239], a[116:119], v[152:155]\n"
"s_waitcnt lgkmcnt(8)\n"
"v_mfma_f32_16x16x32_bf16 v[156:159], a[232:235], a[120:123], v[156:159]\n"
"v_mfma_f32_16x16x32_bf16 v[156:159], a[236:239], a[124:127], v[156:159]\n"
"buffer_load_dwordx4 a[200:203], v3, s[36:39], s80 offen offset:0 sc1\n"
"v_mfma_f32_16x16x32_bf16 v[160:163], a[240:243], a[96:99], v[160:163]\n"
"ds_read_b64_tr_b16 a[232:233], v14 offset:2056\n"
"v_mfma_f32_16x16x32_bf16 v[160:163], a[244:247], a[100:103], v[160:163]\n"
"ds_read_b64_tr_b16 a[234:235], v14 offset:8200\n"
"v_mfma_f32_16x16x32_bf16 v[164:167], a[240:243], a[104:107], v[164:167]\n"
"ds_read_b64_tr_b16 a[236:237], v14 offset:14344\n"
"v_mfma_f32_16x16x32_bf16 v[164:167], a[244:247], a[108:111], v[164:167]\n"
"ds_read_b64_tr_b16 a[238:239], v14 offset:20488\n"
"v_mfma_f32_16x16x32_bf16 v[168:171], a[240:243], a[112:115], v[168:171]\n"
"buffer_load_dwordx4 a[204:207], v3, s[36:39], s80 offen offset:1024 sc1\n"
"v_mfma_f32_16x16x32_bf16 v[168:171], a[244:247], a[116:119], v[168:171]\n"
"s_waitcnt lgkmcnt(8)\n"
"v_mfma_f32_16x16x32_bf16 v[172:175], a[240:243], a[120:123], v[172:175]\n"
"v_mfma_f32_16x16x32_bf16 v[172:175], a[244:247], a[124:127], v[172:175]\n"
"ds_read_b64_tr_b16 a[240:241], v15 offset:2048\n"
"v_mfma_f32_16x16x32_bf16 v[176:179], a[248:251], a[96:99], v[176:179]\n"
"s_waitcnt vmcnt(32)\n"
"s_barrier\n"
"v_mfma_f32_16x16x32_bf16 v[176:179], a[252:255], a[100:103], v[176:179]\n"
"buffer_load_dwordx4 a[208:211], v4, s[36:39], s80 offen offset:0 sc1\n"
"v_mfma_f32_16x16x32_bf16 v[180:183], a[248:251], a[104:107], v[180:183]\n"
"ds_read_b64_tr_b16 a[242:243], v15 offset:8192\n"
"v_mfma_f32_16x16x32_bf16 v[180:183], a[252:255], a[108:111], v[180:183]\n"
"ds_read_b64_tr_b16 a[244:245], v15 offset:14336\n"
"v_mfma_f32_16x16x32_bf16 v[184:187], a[248:251], a[112:115], v[184:187]\n"
"ds_read_b64_tr_b16 a[246:247], v15 offset:20480\n"
"v_mfma_f32_16x16x32_bf16 v[184:187], a[252:255], a[116:119], v[184:187]\n"
"s_waitcnt lgkmcnt(8)\n"
"v_mfma_f32_16x16x32_bf16 v[188:191], a[248:251], a[120:123], v[188:191]\n"
"buffer_load_dwordx4 a[212:215], v4, s[36:39], s80 offen offset:1024 sc1\n"
"v_mfma_f32_16x16x32_bf16 v[188:191], a[252:255], a[124:127], v[188:191]\n"
"ds_read_b64_tr_b16 a[248:249], v15 offset:2056\n"
"v_mfma_f32_16x16x32_bf16 v[192:195], a[224:227], a[96:99], v[192:195]\n"
"ds_read_b64_tr_b16 a[250:251], v15 offset:8200\n"
"v_mfma_f32_16x16x32_bf16 v[192:195], a[228:231], a[100:103], v[192:195]\n"
"ds_read_b64_tr_b16 a[252:253], v15 offset:14344\n"
"v_mfma_f32_16x16x32_bf16 v[196:199], a[224:227], a[104:107], v[196:199]\n"
"ds_read_b64_tr_b16 a[254:255], v15 offset:20488\n"
"v_mfma_f32_16x16x32_bf16 v[196:199], a[228:231], a[108:111], v[196:199]\n"
"buffer_load_dwordx4 a[216:219], v5, s[36:39], s80 offen offset:0 sc1\n"
"v_mfma_f32_16x16x32_bf16 v[200:203], a[224:227], a[112:115], v[200:203]\n"
"v_mfma_f32_16x16x32_bf16 v[200:203], a[228:231], a[116:119], v[200:203]\n"
"s_waitcnt lgkmcnt(8)\n"
"v_mfma_f32_16x16x32_bf16 v[204:207], a[224:227], a[120:123], v[204:207]\n"
"v_mfma_f32_16x16x32_bf16 v[204:207], a[228:231], a[124:127], v[204:207]\n"
"ds_read_b64_tr_b16 a[224:225], v21 offset:24576\n"
"v_mfma_f32_16x16x32_bf16 v[208:211], a[232:235], a[96:99], v[208:211]\n"
"buffer_load_dwordx4 a[220:223], v5, s[36:39], s80 offen offset:1024 sc1\n"
"v_mfma_f32_16x16x32_bf16 v[208:211], a[236:239], a[100:103], v[208:211]\n"
"s_sub_u32 s80, s80, 8192\n"
"ds_read_b64_tr_b16 a[226:227], v21 offset:26112\n"
"v_mfma_f32_16x16x32_bf16 v[212:215], a[232:235], a[104:107], v[212:215]\n"
"ds_read_b64_tr_b16 a[228:229], v21 offset:27648\n"
"v_mfma_f32_16x16x32_bf16 v[212:215], a[236:239], a[108:111], v[212:215]\n"
"ds_read_b64_tr_b16 a[230:231], v21 offset:29184\n"
"v_mfma_f32_16x16x32_bf16 v[216:219], a[232:235], a[112:115], v[216:219]\n"
"v_mfma_f32_16x16x32_bf16 v[216:219], a[236:239], a[116:119], v[216:219]\n"
"buffer_load_dwordx4 v6, s[40:43], s81 offen offset:0 lds\n"
"v_mfma_f32_16x16x32_bf16 v[220:223], a[232:235], a[120:123], v[220:223]\n"
"s_add_u32 m0, s83, 50048\n"
"s_waitcnt lgkmcnt(8)\n"
"v_mfma_f32_16x16x32_bf16 v[220:223], a[236:239], a[124:127], v[220:223]\n"
"ds_read_b64_tr_b16 a[232:233], v21 offset:36864\n"
"v_mfma_f32_16x16x32_bf16 v[224:227], a[240:243], a[96:99], v[224:227]\n"
"ds_read_b64_tr_b16 a[234:235], v21 offset:38400\n"
"v_mfma_f32_16x16x32_bf16 v[224:227], a[244:247], a[100:103], v[224:227]\n"
"ds_read_b64_tr_b16 a[236:237], v21 offset:39936\n"
"v_mfma_f32_16x16x32_bf16 v[228:231], a[240:243], a[104:107], v[228:231]\n"
"buffer_load_dwordx4 v6, s[40:43], s81 offen offset:128 lds\n"
"v_mfma_f32_16x16x32_bf16 v[228:231], a[244:247], a[108:111], v[228:231]\n"
"s_add_u32 m0, s83, 50944\n"
"ds_read_b64_tr_b16 a[238:239], v21 offset:41472\n"
"v_mfma_f32_16x16x32_bf16 v[232:235], a[240:243], a[112:115], v[232:235]\n"
"v_mfma_f32_16x16x32_bf16 v[232:235], a[244:247], a[116:119], v[232:235]\n"
"s_waitcnt lgkmcnt(8)\n"
"v_mfma_f32_16x16x32_bf16 v[236:239], a[240:243], a[120:123], v[236:239]\n"
"v_mfma_f32_16x16x32_bf16 v[236:239], a[244:247], a[124:127], v[236:239]\n"
"buffer_load_dwordx4 v6, s[40:43], s81 offen offset:256 lds\n"
"v_mfma_f32_16x16x32_bf16 v[240:243], a[248:251], a[96:99], v[240:243]\n"
"s_add_u32 s81, s81, 3932160\n"
"s_add_u32 m0, s83, 52224\n"
"v_mfma_f32_16x16x32_bf16 v[240:243], a[252:255], a[100:103], v[240:243]\n"
"ds_read_b64_tr_b16 a[240:241], v21 offset:24608\n"
"v_mfma_f32_16x16x32_bf16 v[244:247], a[248:251], a[104:107], v[244:247]\n"
"ds_read_b64_tr_b16 a[242:243], v21 offset:26144\n"
"v_mfma_f32_16x16x32_bf16 v[244:247], a[252:255], a[108:111], v[244:247]\n"
"ds_read_b64_tr_b16 a[244:245], v21 offset:27680\n"
"v_mfma_f32_16x16x32_bf16 v[248:251], a[248:251], a[112:115], v[248:251]\n"
"buffer_load_dwordx4 v6, s[40:43], s82 offen offset:0 lds\n"
"v_mfma_f32_16x16x32_bf16 v[248:251], a[252:255], a[116:119], v[248:251]\n"
"s_add_u32 m0, s83, 53120\n"
"s_waitcnt lgkmcnt(7)\n"
"v_mfma_f32_16x16x32_bf16 v[252:255], a[248:251], a[120:123], v[252:255]\n"
"ds_read_b64_tr_b16 a[246:247], v21 offset:29216\n"
"v_mfma_f32_16x16x32_bf16 v[252:255], a[252:255], a[124:127], v[252:255]\n"
"ds_read_b64_tr_b16 a[248:249], v21 offset:36896\n"
"v_mfma_f32_16x16x32_bf16 a[0:3], a[224:227], a[96:99], a[0:3]\n"
"ds_read_b64_tr_b16 a[250:251], v21 offset:38432\n"
"v_mfma_f32_16x16x32_bf16 a[0:3], a[228:231], a[100:103], a[0:3]\n"
"buffer_load_dwordx4 v6, s[40:43], s82 offen offset:128 lds\n"
"v_mfma_f32_16x16x32_bf16 a[4:7], a[224:227], a[104:107], a[4:7]\n"
"s_add_u32 m0, s83, 54016\n"
"ds_read_b64_tr_b16 a[252:253], v21 offset:39968\n"
"v_mfma_f32_16x16x32_bf16 a[4:7], a[228:231], a[108:111], a[4:7]\n"
"ds_read_b64_tr_b16 a[254:255], v21 offset:41504\n"
"v_mfma_f32_16x16x32_bf16 a[8:11], a[224:227], a[112:115], a[8:11]\n"
"v_mfma_f32_16x16x32_bf16 a[8:11], a[228:231], a[116:119], a[8:11]\n"
"s_waitcnt lgkmcnt(8)\n"
"v_mfma_f32_16x16x32_bf16 a[12:15], a[224:227], a[120:123], a[12:15]\n"
"buffer_load_dwordx4 v6, s[40:43], s82 offen offset:256 lds\n"
"v_mfma_f32_16x16x32_bf16 a[12:15], a[228:231], a[124:127], a[12:15]\n"
"s_add_u32 s82, s82, 3932160\n"
"s_add_u32 m0, s83, 73728\n"
"v_mfma_f32_16x16x32_bf16 a[16:19], a[232:235], a[96:99], a[16:19]\n"
"ds_read_b64_tr_b16 a[224:225], v21 offset:24640\n"
"v_mfma_f32_16x16x32_bf16 a[16:19], a[236:239], a[100:103], a[16:19]\n"
"ds_read_b64_tr_b16 a[226:227], v21 offset:26176\n"
"v_mfma_f32_16x16x32_bf16 a[20:23], a[232:235], a[104:107], a[20:23]\n"
"ds_read_b64_tr_b16 a[228:229], v21 offset:27712\n"
"v_mfma_f32_16x16x32_bf16 a[20:23], a[236:239], a[108:111], a[20:23]\n"
"buffer_load_dwordx4 v16, s[52:55], s4 offen lds\n"
"v_mfma_f32_16x16x32_bf16 a[24:27], a[232:235], a[112:115], a[24:27]\n"
"s_add_u32 m0, s83, 74752\n"
"ds_read_b64_tr_b16 a[230:231], v21 offset:29248\n"
"v_mfma_f32_16x16x32_bf16 a[24:27], a[236:239], a[116:119], a[24:27]\n"
"s_waitcnt lgkmcnt(8)\n"
"v_mfma_f32_16x16x32_bf16 a[28:31], a[232:235], a[120:123], a[28:31]\n"
"v_mfma_f32_16x16x32_bf16 a[28:31], a[236:239], a[124:127], a[28:31]\n"
"ds_read_b64_tr_b16 a[232:233], v21 offset:36928\n"
"v_mfma_f32_16x16x32_bf16 a[32:35], a[240:243], a[96:99], a[32:35]\n"
"ds_read_b64_tr_b16 a[234:235], v21 offset:38464\n"
"v_mfma_f32_16x16x32_bf16 a[32:35], a[244:247], a[100:103], a[32:35]\n"
"ds_read_b64_tr_b16 a[236:237], v21 offset:40000\n"
"v_mfma_f32_16x16x32_bf16 a[36:39], a[240:243], a[104:107], a[36:39]\n"
"ds_read_b64_tr_b16 a[238:239], v21 offset:41536\n"
"v_mfma_f32_16x16x32_bf16 a[36:39], a[244:247], a[108:111], a[36:39]\n"
"v_mfma_f32_16x16x32_bf16 a[40:43], a[240:243], a[112:115], a[40:43]\n"
"buffer_load_dwordx4 v17, s[52:55], s4 offen lds\n"
"v_mfma_f32_16x16x32_bf16 a[40:43], a[244:247], a[116:119], a[40:43]\n"
"s_add_u32 m0, s83, 75776\n"
"s_waitcnt lgkmcnt(8)\n"
"v_mfma_f32_16x16x32_bf16 a[44:47], a[240:243], a[120:123], a[44:47]\n"
"v_mfma_f32_16x16x32_bf16 a[44:47], a[244:247], a[124:127], a[44:47]\n"
"ds_read_b64_tr_b16 a[240:241], v21 offset:30720\n"
"v_mfma_f32_16x16x32_bf16 a[48:51], a[248:251], a[96:99], a[48:51]\n"
"ds_read_b64_tr_b16 a[242:243], v21 offset:32256\n"
"v_mfma_f32_16x16x32_bf16 a[48:51], a[252:255], a[100:103], a[48:51]\n"
"ds_read_b64_tr_b16 a[244:245], v21 offset:33792\n"
"v_mfma_f32_16x16x32_bf16 a[52:55], a[248:251], a[104:107], a[52:55]\n"
"ds_read_b64_tr_b16 a[246:247], v21 offset:35328\n"
"v_mfma_f32_16x16x32_bf16 a[52:55], a[252:255], a[108:111], a[52:55]\n"
"v_mfma_f32_16x16x32_bf16 a[56:59], a[248:251], a[112:115], a[56:59]\n"
"v_mfma_f32_16x16x32_bf16 a[56:59], a[252:255], a[116:119], a[56:59]\n"
"buffer_load_dwordx4 v18, s[52:55], s4 offen lds\n"
"v_mfma_f32_16x16x32_bf16 a[60:63], a[248:251], a[120:123], a[60:63]\n"
"s_add_u32 s4, s4, 98304\n"
"s_add_u32 m0, s83, 76800\n"
"s_waitcnt lgkmcnt(8)\n"
"v_mfma_f32_16x16x32_bf16 a[60:63], a[252:255], a[124:127], a[60:63]\n"
"ds_read_b64_tr_b16 a[248:249], v21 offset:43008\n"
"v_mfma_f32_16x16x32_bf16 a[64:67], a[224:227], a[96:99], a[64:67]\n"
"ds_read_b64_tr_b16 a[250:251], v21 offset:44544\n"
"v_mfma_f32_16x16x32_bf16 a[64:67], a[228:231], a[100:103], a[64:67]\n"
"ds_read_b64_tr_b16 a[252:253], v21 offset:46080\n"
"v_mfma_f32_16x16x32_bf16 a[68:71], a[224:227], a[104:107], a[68:71]\n"
"ds_read_b64_tr_b16 a[254:255], v21 offset:47616\n"
"v_mfma_f32_16x16x32_bf16 a[68:71], a[228:231], a[108:111], a[68:71]\n"
"v_mfma_f32_16x16x32_bf16 a[72:75], a[224:227], a[112:115], a[72:75]\n"
"v_mfma_f32_16x16x32_bf16 a[72:75], a[228:231], a[116:119], a[72:75]\n"
"s_waitcnt lgkmcnt(8)\n"
"v_mfma_f32_16x16x32_bf16 a[76:79], a[224:227], a[120:123], a[76:79]\n"
"buffer_load_dwordx4 v16, s[52:55], s4 offen lds\n"
"v_mfma_f32_16x16x32_bf16 a[76:79], a[228:231], a[124:127], a[76:79]\n"
"s_add_u32 m0, s83, 77824\n"
"ds_read_b64_tr_b16 a[224:225], v21 offset:30752\n"
"v_mfma_f32_16x16x32_bf16 a[80:83], a[232:235], a[96:99], a[80:83]\n"
"ds_read_b64_tr_b16 a[226:227], v21 offset:32288\n"
"v_mfma_f32_16x16x32_bf16 a[80:83], a[236:239], a[100:103], a[80:83]\n"
"ds_read_b64_tr_b16 a[228:229], v21 offset:33824\n"
"v_mfma_f32_16x16x32_bf16 a[84:87], a[232:235], a[104:107], a[84:87]\n"
"ds_read_b64_tr_b16 a[230:231], v21 offset:35360\n"
"v_mfma_f32_16x16x32_bf16 a[84:87], a[236:239], a[108:111], a[84:87]\n"
"v_mfma_f32_16x16x32_bf16 a[88:91], a[232:235], a[112:115], a[88:91]\n"
"v_mfma_f32_16x16x32_bf16 a[88:91], a[236:239], a[116:119], a[88:91]\n"
"s_waitcnt lgkmcnt(8)\n"
"s_waitcnt vmcnt(38)\n"
"v_mfma_f32_16x16x32_bf16 a[92:95], a[232:235], a[120:123], a[92:95]\n"
"v_mfma_f32_16x16x32_bf16 a[92:95], a[236:239], a[124:127], a[92:95]\n"
"buffer_load_dwordx4 a[96:99], v2, s[36:39], s80 offen offset:0 sc1\n"
"v_mfma_f32_16x16x32_bf16 a[0:3], a[240:243], a[128:131], a[0:3]\n"
"ds_read_b64_tr_b16 a[232:233], v21 offset:43040\n"
"v_mfma_f32_16x16x32_bf16 a[0:3], a[244:247], a[132:135], a[0:3]\n"
"ds_read_b64_tr_b16 a[234:235], v21 offset:44576\n"
"v_mfma_f32_16x16x32_bf16 a[4:7], a[240:243], a[136:139], a[4:7]\n"
"ds_read_b64_tr_b16 a[236:237], v21 offset:46112\n"
"v_mfma_f32_16x16x32_bf16 a[4:7], a[244:247], a[140:143], a[4:7]\n"
"ds_read_b64_tr_b16 a[238:239], v21 offset:47648\n"
"v_mfma_f32_16x16x32_bf16 a[8:11], a[240:243], a[144:147], a[8:11]\n"
"buffer_load_dwordx4 a[100:103], v2, s[36:39], s80 offen offset:1024 sc1\n"
"v_mfma_f32_16x16x32_bf16 a[8:11], a[244:247], a[148:151], a[8:11]\n"
"s_waitcnt lgkmcnt(8)\n"
"v_mfma_f32_16x16x32_bf16 a[12:15], a[240:243], a[152:155], a[12:15]\n"
"v_mfma_f32_16x16x32_bf16 a[12:15], a[244:247], a[156:159], a[12:15]\n"
"ds_read_b64_tr_b16 a[240:241], v21 offset:30784\n"
"v_mfma_f32_16x16x32_bf16 a[16:19], a[248:251], a[128:131], a[16:19]\n"
"s_waitcnt vmcnt(34)\n"
"s_barrier\n"
"v_mfma_f32_16x16x32_bf16 a[16:19], a[252:255], a[132:135], a[16:19]\n"
"buffer_load_dwordx4 a[104:107], v3, s[36:39], s80 offen offset:0 sc1\n"
"v_mfma_f32_16x16x32_bf16 a[20:23], a[248:251], a[136:139], a[20:23]\n"
"ds_read_b64_tr_b16 a[242:243], v21 offset:32320\n"
"v_mfma_f32_16x16x32_bf16 a[20:23], a[252:255], a[140:143], a[20:23]\n"
"ds_read_b64_tr_b16 a[244:245], v21 offset:33856\n"
"v_mfma_f32_16x16x32_bf16 a[24:27], a[248:251], a[144:147], a[24:27]\n"
"ds_read_b64_tr_b16 a[246:247], v21 offset:35392\n"
"v_mfma_f32_16x16x32_bf16 a[24:27], a[252:255], a[148:151], a[24:27]\n"
"s_waitcnt lgkmcnt(8)\n"
"v_mfma_f32_16x16x32_bf16 a[28:31], a[248:251], a[152:155], a[28:31]\n"
"buffer_load_dwordx4 a[108:111], v3, s[36:39], s80 offen offset:1024 sc1\n"
"v_mfma_f32_16x16x32_bf16 a[28:31], a[252:255], a[156:159], a[28:31]\n"
"ds_read_b64_tr_b16 a[248:249], v21 offset:43072\n"
"v_mfma_f32_16x16x32_bf16 a[32:35], a[224:227], a[128:131], a[32:35]\n"
"ds_read_b64_tr_b16 a[250:251], v21 offset:44608\n"
"v_mfma_f32_16x16x32_bf16 a[32:35], a[228:231], a[132:135], a[32:35]\n"
"ds_read_b64_tr_b16 a[252:253], v21 offset:46144\n"
"v_mfma_f32_16x16x32_bf16 a[36:39], a[224:227], a[136:139], a[36:39]\n"
"ds_read_b64_tr_b16 a[254:255], v21 offset:47680\n"
"v_mfma_f32_16x16x32_bf16 a[36:39], a[228:231], a[140:143], a[36:39]\n"
"buffer_load_dwordx4 a[112:115], v4, s[36:39], s80 offen offset:0 sc1\n"
"v_mfma_f32_16x16x32_bf16 a[40:43], a[224:227], a[144:147], a[40:43]\n"
"v_mfma_f32_16x16x32_bf16 a[40:43], a[228:231], a[148:151], a[40:43]\n"
"s_waitcnt lgkmcnt(8)\n"
"v_mfma_f32_16x16x32_bf16 a[44:47], a[224:227], a[152:155], a[44:47]\n"
"v_mfma_f32_16x16x32_bf16 a[44:47], a[228:231], a[156:159], a[44:47]\n"
"ds_read_b64_tr_b16 a[224:225], v10 offset:0\n"
"v_mfma_f32_16x16x32_bf16 a[48:51], a[232:235], a[128:131], a[48:51]\n"
"buffer_load_dwordx4 a[116:119], v4, s[36:39], s80 offen offset:1024 sc1\n"
"v_mfma_f32_16x16x32_bf16 a[48:51], a[236:239], a[132:135], a[48:51]\n"
"ds_read_b64_tr_b16 a[226:227], v10 offset:6144\n"
"v_mfma_f32_16x16x32_bf16 a[52:55], a[232:235], a[136:139], a[52:55]\n"
"ds_read_b64_tr_b16 a[228:229], v10 offset:12288\n"
"v_mfma_f32_16x16x32_bf16 a[52:55], a[236:239], a[140:143], a[52:55]\n"
"ds_read_b64_tr_b16 a[230:231], v10 offset:18432\n"
"v_mfma_f32_16x16x32_bf16 a[56:59], a[232:235], a[144:147], a[56:59]\n"
"v_mfma_f32_16x16x32_bf16 a[56:59], a[236:239], a[148:151], a[56:59]\n"
"buffer_load_dwordx4 a[120:123], v5, s[36:39], s80 offen offset:0 sc1\n"
"v_mfma_f32_16x16x32_bf16 a[60:63], a[232:235], a[152:155], a[60:63]\n"
"s_waitcnt lgkmcnt(8)\n"
"v_mfma_f32_16x16x32_bf16 a[60:63], a[236:239], a[156:159], a[60:63]\n"
"ds_read_b64_tr_b16 a[232:233], v10 offset:8\n"
"v_mfma_f32_16x16x32_bf16 a[64:67], a[240:243], a[128:131], a[64:67]\n"
"ds_read_b64_tr_b16 a[234:235], v10 offset:6152\n"
"v_mfma_f32_16x16x32_bf16 a[64:67], a[244:247], a[132:135], a[64:67]\n"
"ds_read_b64_tr_b16 a[236:237], v10 offset:12296\n"
"v_mfma_f32_16x16x32_bf16 a[68:71], a[240:243], a[136:139], a[68:71]\n"
"buffer_load_dwordx4 a[124:127], v5, s[36:39], s80 offen offset:1024 sc1\n"
"v_mfma_f32_16x16x32_bf16 a[68:71], a[244:247], a[140:143], a[68:71]\n"
"s_add_u32 s80, s80, 10240\n"
"ds_read_b64_tr_b16 a[238:239], v10 offset:18440\n"
"v_mfma_f32_16x16x32_bf16 a[72:75], a[240:243], a[144:147], a[72:75]\n"
"v_mfma_f32_16x16x32_bf16 a[72:75], a[244:247], a[148:151], a[72:75]\n"
"s_waitcnt lgkmcnt(8)\n"
"v_mfma_f32_16x16x32_bf16 a[76:79], a[240:243], a[152:155], a[76:79]\n"
"v_mfma_f32_16x16x32_bf16 a[76:79], a[244:247], a[156:159], a[76:79]\n"
"buffer_load_dwordx4 v17, s[52:55], s4 offen lds\n"
"v_mfma_f32_16x16x32_bf16 a[80:83], a[248:251], a[128:131], a[80:83]\n"
"s_add_u32 m0, s83, 78848\n"
"ds_read_b64_tr_b16 a[240:241], v11 offset:0\n"
"v_mfma_f32_16x16x32_bf16 a[80:83], a[252:255], a[132:135], a[80:83]\n"
"ds_read_b64_tr_b16 a[242:243], v11 offset:6144\n"
"v_mfma_f32_16x16x32_bf16 a[84:87], a[248:251], a[136:139], a[84:87]\n"
"ds_read_b64_tr_b16 a[244:245], v11 offset:12288\n"
"v_mfma_f32_16x16x32_bf16 a[84:87], a[252:255], a[140:143], a[84:87]\n"
"ds_read_b64_tr_b16 a[246:247], v11 offset:18432\n"
"v_mfma_f32_16x16x32_bf16 a[88:91], a[248:251], a[144:147], a[88:91]\n"
"v_mfma_f32_16x16x32_bf16 a[88:91], a[252:255], a[148:151], a[88:91]\n"
"s_waitcnt lgkmcnt(8)\n"
"s_waitcnt vmcnt(32)\n"
"v_mfma_f32_16x16x32_bf16 a[92:95], a[248:251], a[152:155], a[92:95]\n"
"v_mfma_f32_16x16x32_bf16 a[92:95], a[252:255], a[156:159], a[92:95]\n"
"ds_read_b64_tr_b16 a[248:249], v11 offset:8\n"
"v_mfma_f32_16x16x32_bf16 v[64:67], a[224:227], a[160:163], v[64:67]\n"
"buffer_load_dwordx4 v18, s[52:55], s4 offen lds\n"
"v_mfma_f32_16x16x32_bf16 v[64:67], a[228:231], a[164:167], v[64:67]\n"
"s_add_u32 s4, s4, 98304\n"
"s_add_u32 m0, s83, 98304\n"
"v_mfma_f32_16x16x32_bf16 v[68:71], a[224:227], a[168:171], v[68:71]\n"
"ds_read_b64_tr_b16 a[250:251], v11 offset:6152\n"
"v_mfma_f32_16x16x32_bf16 v[68:71], a[228:231], a[172:175], v[68:71]\n"
"ds_read_b64_tr_b16 a[252:253], v11 offset:12296\n"
"v_mfma_f32_16x16x32_bf16 v[72:75], a[224:227], a[176:179], v[72:75]\n"
"ds_read_b64_tr_b16 a[254:255], v11 offset:18440\n"
"v_mfma_f32_16x16x32_bf16 v[72:75], a[228:231], a[180:183], v[72:75]\n"
"s_waitcnt lgkmcnt(8)\n"
"v_mfma_f32_16x16x32_bf16 v[76:79], a[224:227], a[184:187], v[76:79]\n"
"v_mfma_f32_16x16x32_bf16 v[76:79], a[228:231], a[188:191], v[76:79]\n"
"ds_read_b64_tr_b16 a[224:225], v10 offset:1024\n"
"v_mfma_f32_16x16x32_bf16 v[80:83], a[232:235], a[160:163], v[80:83]\n"
"ds_read_b64_tr_b16 a[226:227], v10 offset:7168\n"
"v_mfma_f32_16x16x32_bf16 v[80:83], a[236:239], a[164:167], v[80:83]\n"
"buffer_load_dwordx4 a[128:131], v2, s[36:39], s80 offen offset:0 sc1\n"
"v_mfma_f32_16x16x32_bf16 v[84:87], a[232:235], a[168:171], v[84:87]\n"
"ds_read_b64_tr_b16 a[228:229], v10 offset:13312\n"
"v_mfma_f32_16x16x32_bf16 v[84:87], a[236:239], a[172:175], v[84:87]\n"
"ds_read_b64_tr_b16 a[230:231], v10 offset:19456\n"
"v_mfma_f32_16x16x32_bf16 v[88:91], a[232:235], a[176:179], v[88:91]\n"
"v_mfma_f32_16x16x32_bf16 v[88:91], a[236:239], a[180:183], v[88:91]\n"
"s_waitcnt lgkmcnt(8)\n"
"v_mfma_f32_16x16x32_bf16 v[92:95], a[232:235], a[184:187], v[92:95]\n"
"buffer_load_dwordx4 a[132:135], v2, s[36:39], s80 offen offset:1024 sc1\n"
"v_mfma_f32_16x16x32_bf16 v[92:95], a[236:239], a[188:191], v[92:95]\n"
"ds_read_b64_tr_b16 a[232:233], v10 offset:1032\n"
"v_mfma_f32_16x16x32_bf16 v[96:99], a[240:243], a[160:163], v[96:99]\n"
"ds_read_b64_tr_b16 a[234:235], v10 offset:7176\n"
"v_mfma_f32_16x16x32_bf16 v[96:99], a[244:247], a[164:167], v[96:99]\n"
"ds_read_b64_tr_b16 a[236:237], v10 offset:13320\n"
"v_mfma_f32_16x16x32_bf16 v[100:103], a[240:243], a[168:171], v[100:103]\n"
"ds_read_b64_tr_b16 a[238:239], v10 offset:19464\n"
"v_mfma_f32_16x16x32_bf16 v[100:103], a[244:247], a[172:175], v[100:103]\n"
"buffer_load_dwordx4 a[136:139], v3, s[36:39], s80 offen offset:0 sc1\n"
"v_mfma_f32_16x16x32_bf16 v[104:107], a[240:243], a[176:179], v[104:107]\n"
"v_mfma_f32_16x16x32_bf16 v[104:107], a[244:247], a[180:183], v[104:107]\n"
"s_waitcnt lgkmcnt(8)\n"
"v_mfma_f32_16x16x32_bf16 v[108:111], a[240:243], a[184:187], v[108:111]\n"
"v_mfma_f32_16x16x32_bf16 v[108:111], a[244:247], a[188:191], v[108:111]\n"
"ds_read_b64_tr_b16 a[240:241], v11 offset:1024\n"
"v_mfma_f32_16x16x32_bf16 v[112:115], a[248:251], a[160:163], v[112:115]\n"
"buffer_load_dwordx4 a[140:143], v3, s[36:39], s80 offen offset:1024 sc1\n"
"v_mfma_f32_16x16x32_bf16 v[112:115], a[252:255], a[164:167], v[112:115]\n"
"ds_read_b64_tr_b16 a[242:243], v11 offset:7168\n"
"v_mfma_f32_16x16x32_bf16 v[116:119], a[248:251], a[168:171], v[116:119]\n"
"ds_read_b64_tr_b16 a[244:245], v11 offset:13312\n"
"v_mfma_f32_16x16x32_bf16 v[116:119], a[252:255], a[172:175], v[116:119]\n"
"ds_read_b64_tr_b16 a[246:247], v11 offset:19456\n"
"v_mfma_f32_16x16x32_bf16 v[120:123], a[248:251], a[176:179], v[120:123]\n"
"v_mfma_f32_16x16x32_bf16 v[120:123], a[252:255], a[180:183], v[120:123]\n"
"buffer_load_dwordx4 a[144:147], v4, s[36:39], s80 offen offset:0 sc1\n"
"v_mfma_f32_16x16x32_bf16 v[124:127], a[248:251], a[184:187], v[124:127]\n"
"s_waitcnt lgkmcnt(8)\n"
"v_mfma_f32_16x16x32_bf16 v[124:127], a[252:255], a[188:191], v[124:127]\n"
"ds_read_b64_tr_b16 a[248:249], v11 offset:1032\n"
"v_mfma_f32_16x16x32_bf16 v[128:131], a[224:227], a[160:163], v[128:131]\n"
"ds_read_b64_tr_b16 a[250:251], v11 offset:7176\n"
"v_mfma_f32_16x16x32_bf16 v[128:131], a[228:231], a[164:167], v[128:131]\n"
"ds_read_b64_tr_b16 a[252:253], v11 offset:13320\n"
"v_mfma_f32_16x16x32_bf16 v[132:135], a[224:227], a[168:171], v[132:135]\n"
"buffer_load_dwordx4 a[148:151], v4, s[36:39], s80 offen offset:1024 sc1\n"
"v_mfma_f32_16x16x32_bf16 v[132:135], a[228:231], a[172:175], v[132:135]\n"
"ds_read_b64_tr_b16 a[254:255], v11 offset:19464\n"
"v_mfma_f32_16x16x32_bf16 v[136:139], a[224:227], a[176:179], v[136:139]\n"
"v_mfma_f32_16x16x32_bf16 v[136:139], a[228:231], a[180:183], v[136:139]\n"
"s_waitcnt lgkmcnt(8)\n"
"v_mfma_f32_16x16x32_bf16 v[140:143], a[224:227], a[184:187], v[140:143]\n"
"v_mfma_f32_16x16x32_bf16 v[140:143], a[228:231], a[188:191], v[140:143]\n"
"buffer_load_dwordx4 a[152:155], v5, s[36:39], s80 offen offset:0 sc1\n"
"v_mfma_f32_16x16x32_bf16 v[144:147], a[232:235], a[160:163], v[144:147]\n"
"ds_read_b64_tr_b16 a[224:225], v10 offset:2048\n"
"v_mfma_f32_16x16x32_bf16 v[144:147], a[236:239], a[164:167], v[144:147]\n"
"ds_read_b64_tr_b16 a[226:227], v10 offset:8192\n"
"v_mfma_f32_16x16x32_bf16 v[148:151], a[232:235], a[168:171], v[148:151]\n"
"ds_read_b64_tr_b16 a[228:229], v10 offset:14336\n"
"v_mfma_f32_16x16x32_bf16 v[148:151], a[236:239], a[172:175], v[148:151]\n"
"ds_read_b64_tr_b16 a[230:231], v10 offset:20480\n"
"v_mfma_f32_16x16x32_bf16 v[152:155], a[232:235], a[176:179], v[152:155]\n"
"buffer_load_dwordx4 a[156:159], v5, s[36:39], s80 offen offset:1024 sc1\n"
"v_mfma_f32_16x16x32_bf16 v[152:155], a[236:239], a[180:183], v[152:155]\n"
"s_sub_u32 s80, s80, 8192\n"
"s_waitcnt lgkmcnt(8)\n"
"v_mfma_f32_16x16x32_bf16 v[156:159], a[232:235], a[184:187], v[156:159]\n"
"v_mfma_f32_16x16x32_bf16 v[156:159], a[236:239], a[188:191], v[156:159]\n"
"ds_read_b64_tr_b16 a[232:233], v10 offset:2056\n"
"v_mfma_f32_16x16x32_bf16 v[160:163], a[240:243], a[160:163], v[160:163]\n"
"ds_read_b64_tr_b16 a[234:235], v10 offset:8200\n"
"v_mfma_f32_16x16x32_bf16 v[160:163], a[244:247], a[164:167], v[160:163]\n"
"buffer_load_dwordx4 v6, s[40:43], s81 offen offset:0 lds\n"
"v_mfma_f32_16x16x32_bf16 v[164:167], a[240:243], a[168:171], v[164:167]\n"
"s_add_u32 m0, s83, 99200\n"
"ds_read_b64_tr_b16 a[236:237], v10 offset:14344\n"
"v_mfma_f32_16x16x32_bf16 v[164:167], a[244:247], a[172:175], v[164:167]\n"
"ds_read_b64_tr_b16 a[238:239], v10 offset:20488\n"
"v_mfma_f32_16x16x32_bf16 v[168:171], a[240:243], a[176:179], v[168:171]\n"
"v_mfma_f32_16x16x32_bf16 v[168:171], a[244:247], a[180:183], v[168:171]\n"
"s_waitcnt lgkmcnt(8)\n"
"v_mfma_f32_16x16x32_bf16 v[172:175], a[240:243], a[184:187], v[172:175]\n"
"buffer_load_dwordx4 v6, s[40:43], s81 offen offset:128 lds\n"
"v_mfma_f32_16x16x32_bf16 v[172:175], a[244:247], a[188:191], v[172:175]\n"
"s_add_u32 m0, s83, 100096\n"
"ds_read_b64_tr_b16 a[240:241], v11 offset:2048\n"
"v_mfma_f32_16x16x32_bf16 v[176:179], a[248:251], a[160:163], v[176:179]\n"
"s_waitcnt vmcnt(38)\n"
"s_barrier\n"
"v_mfma_f32_16x16x32_bf16 v[176:179], a[252:255], a[164:167], v[176:179]\n"
"ds_read_b64_tr_b16 a[242:243], v11 offset:8192\n"
"v_mfma_f32_16x16x32_bf16 v[180:183], a[248:251], a[168:171], v[180:183]\n"
"ds_read_b64_tr_b16 a[244:245], v11 offset:14336\n"
"v_mfma_f32_16x16x32_bf16 v[180:183], a[252:255], a[172:175], v[180:183]\n"
"buffer_load_dwordx4 v6, s[40:43], s81 offen offset:256 lds\n"
"v_mfma_f32_16x16x32_bf16 v[184:187], a[248:251], a[176:179], v[184:187]\n"
"s_sub_u32 s81, s81, 3145728\n"
"s_add_u32 m0, s83, 101376\n"
"v_mfma_f32_16x16x32_bf16 v[184:187], a[252:255], a[180:183], v[184:187]\n"
"s_waitcnt lgkmcnt(7)\n"
"ds_read_b64_tr_b16 a[246:247], v11 offset:20480\n"
"v_mfma_f32_16x16x32_bf16 v[188:191], a[248:251], a[184:187], v[188:191]\n"
"v_mfma_f32_16x16x32_bf16 v[188:191], a[252:255], a[188:191], v[188:191]\n"
"ds_read_b64_tr_b16 a[248:249], v11 offset:2056\n"
"v_mfma_f32_16x16x32_bf16 v[192:195], a[224:227], a[160:163], v[192:195]\n"
"buffer_load_dwordx4 v6, s[40:43], s82 offen offset:0 lds\n"
"v_mfma_f32_16x16x32_bf16 v[192:195], a[228:231], a[164:167], v[192:195]\n"
"s_add_u32 m0, s83, 102272\n"
"ds_read_b64_tr_b16 a[250:251], v11 offset:8200\n"
"v_mfma_f32_16x16x32_bf16 v[196:199], a[224:227], a[168:171], v[196:199]\n"
"ds_read_b64_tr_b16 a[252:253], v11 offset:14344\n"
"v_mfma_f32_16x16x32_bf16 v[196:199], a[228:231], a[172:175], v[196:199]\n"
"s_add_u32 vcc_hi, vcc_lo, 32768\n"
"buffer_load_dword v27, v0, s[56:59], vcc_hi idxen\n"
"ds_read_b64_tr_b16 a[254:255], v11 offset:20488\n"
"v_mfma_f32_16x16x32_bf16 v[200:203], a[224:227], a[176:179], v[200:203]\n"
"v_mfma_f32_16x16x32_bf16 v[200:203], a[228:231], a[180:183], v[200:203]\n"
"buffer_load_dwordx4 v6, s[40:43], s82 offen offset:128 lds\n"
"v_mfma_f32_16x16x32_bf16 v[204:207], a[224:227], a[184:187], v[204:207]\n"
"s_add_u32 m0, s83, 103168\n"
"s_waitcnt lgkmcnt(8)\n"
"v_mfma_f32_16x16x32_bf16 v[204:207], a[228:231], a[188:191], v[204:207]\n"
"ds_read_b64_tr_b16 a[224:225], v19 offset:24576\n"
"v_mfma_f32_16x16x32_bf16 v[208:211], a[232:235], a[160:163], v[208:211]\n"
"ds_read_b64_tr_b16 a[226:227], v19 offset:26112\n"
"v_mfma_f32_16x16x32_bf16 v[208:211], a[236:239], a[164:167], v[208:211]\n"
"ds_read_b64_tr_b16 a[228:229], v19 offset:27648\n"
"v_mfma_f32_16x16x32_bf16 v[212:215], a[232:235], a[168:171], v[212:215]\n"
"buffer_load_dwordx4 v6, s[40:43], s82 offen offset:256 lds\n"
"v_mfma_f32_16x16x32_bf16 v[212:215], a[236:239], a[172:175], v[212:215]\n"
"s_sub_u32 s82, s82, 3145728\n"
"s_add_u32 m0, s83, 122880\n"
"v_mfma_f32_16x16x32_bf16 v[216:219], a[232:235], a[176:179], v[216:219]\n"
"ds_read_b64_tr_b16 a[230:231], v19 offset:29184\n"
"v_mfma_f32_16x16x32_bf16 v[216:219], a[236:239], a[180:183], v[216:219]\n"
"s_waitcnt lgkmcnt(8)\n"
"v_mfma_f32_16x16x32_bf16 v[220:223], a[232:235], a[184:187], v[220:223]\n"
"v_mfma_f32_16x16x32_bf16 v[220:223], a[236:239], a[188:191], v[220:223]\n"
"buffer_load_dwordx4 v6, s[40:43], s81 offen offset:0 lds\n"
"v_mfma_f32_16x16x32_bf16 v[224:227], a[240:243], a[160:163], v[224:227]\n"
"s_add_u32 m0, s83, 123776\n"
"ds_read_b64_tr_b16 a[232:233], v19 offset:36864\n"
"v_mfma_f32_16x16x32_bf16 v[224:227], a[244:247], a[164:167], v[224:227]\n"
"ds_read_b64_tr_b16 a[234:235], v19 offset:38400\n"
"v_mfma_f32_16x16x32_bf16 v[228:231], a[240:243], a[168:171], v[228:231]\n"
"ds_read_b64_tr_b16 a[236:237], v19 offset:39936\n"
"v_mfma_f32_16x16x32_bf16 v[228:231], a[244:247], a[172:175], v[228:231]\n"
"ds_read_b64_tr_b16 a[238:239], v19 offset:41472\n"
"v_mfma_f32_16x16x32_bf16 v[232:235], a[240:243], a[176:179], v[232:235]\n"
"buffer_load_dwordx4 v6, s[40:43], s81 offen offset:128 lds\n"
"v_mfma_f32_16x16x32_bf16 v[232:235], a[244:247], a[180:183], v[232:235]\n"
"s_add_u32 m0, s83, 124672\n"
"s_waitcnt lgkmcnt(8)\n"
"v_mfma_f32_16x16x32_bf16 v[236:239], a[240:243], a[184:187], v[236:239]\n"
"v_mfma_f32_16x16x32_bf16 v[236:239], a[244:247], a[188:191], v[236:239]\n"
"ds_read_b64_tr_b16 a[240:241], v19 offset:24608\n"
"v_mfma_f32_16x16x32_bf16 v[240:243], a[248:251], a[160:163], v[240:243]\n"
"ds_read_b64_tr_b16 a[242:243], v19 offset:26144\n"
"v_mfma_f32_16x16x32_bf16 v[240:243], a[252:255], a[164:167], v[240:243]\n"
"buffer_load_dwordx4 v6, s[40:43], s81 offen offset:256 lds\n"
"v_mfma_f32_16x16x32_bf16 v[244:247], a[248:251], a[168:171], v[244:247]\n"
"s_add_u32 s81, s81, 786432\n"
"s_add_u32 m0, s83, 125952\n"
"v_mfma_f32_16x16x32_bf16 v[244:247], a[252:255], a[172:175], v[244:247]\n"
"ds_read_b64_tr_b16 a[244:245], v19 offset:27680\n"
"v_mfma_f32_16x16x32_bf16 v[248:251], a[248:251], a[176:179], v[248:251]\n"
"ds_read_b64_tr_b16 a[246:247], v19 offset:29216\n"
"v_mfma_f32_16x16x32_bf16 v[248:251], a[252:255], a[180:183], v[248:251]\n"
"s_waitcnt lgkmcnt(8)\n"
"v_mfma_f32_16x16x32_bf16 v[252:255], a[248:251], a[184:187], v[252:255]\n"
"buffer_load_dwordx4 v6, s[40:43], s82 offen offset:0 lds\n"
"v_mfma_f32_16x16x32_bf16 v[252:255], a[252:255], a[188:191], v[252:255]\n"
"s_add_u32 m0, s83, 126848\n"
"ds_read_b64_tr_b16 a[248:249], v19 offset:36896\n"
"v_mfma_f32_16x16x32_bf16 a[0:3], a[224:227], a[160:163], a[0:3]\n"
"ds_read_b64_tr_b16 a[250:251], v19 offset:38432\n"
"v_mfma_f32_16x16x32_bf16 a[0:3], a[228:231], a[164:167], a[0:3]\n"
"ds_read_b64_tr_b16 a[252:253], v19 offset:39968\n"
"v_mfma_f32_16x16x32_bf16 a[4:7], a[224:227], a[168:171], a[4:7]\n"
"ds_read_b64_tr_b16 a[254:255], v19 offset:41504\n"
"v_mfma_f32_16x16x32_bf16 a[4:7], a[228:231], a[172:175], a[4:7]\n"
"buffer_load_dwordx4 v6, s[40:43], s82 offen offset:128 lds\n"
"v_mfma_f32_16x16x32_bf16 a[8:11], a[224:227], a[176:179], a[8:11]\n"
"s_add_u32 m0, s83, 127744\n"
"v_mfma_f32_16x16x32_bf16 a[8:11], a[228:231], a[180:183], a[8:11]\n"
"s_waitcnt lgkmcnt(8)\n"
"v_mfma_f32_16x16x32_bf16 a[12:15], a[224:227], a[184:187], a[12:15]\n"
"v_mfma_f32_16x16x32_bf16 a[12:15], a[228:231], a[188:191], a[12:15]\n"
"ds_read_b64_tr_b16 a[224:225], v19 offset:24640\n"
"v_mfma_f32_16x16x32_bf16 a[16:19], a[232:235], a[160:163], a[16:19]\n"
"buffer_load_dwordx4 v6, s[40:43], s82 offen offset:256 lds\n"
"v_mfma_f32_16x16x32_bf16 a[16:19], a[236:239], a[164:167], a[16:19]\n"
"s_add_u32 s82, s82, 786432\n"
"ds_read_b64_tr_b16 a[226:227], v19 offset:26176\n"
"v_mfma_f32_16x16x32_bf16 a[20:23], a[232:235], a[168:171], a[20:23]\n"
"ds_read_b64_tr_b16 a[228:229], v19 offset:27712\n"
"v_mfma_f32_16x16x32_bf16 a[20:23], a[236:239], a[172:175], a[20:23]\n"
"ds_read_b64_tr_b16 a[230:231], v19 offset:29248\n"
"v_mfma_f32_16x16x32_bf16 a[24:27], a[232:235], a[176:179], a[24:27]\n"
"v_mfma_f32_16x16x32_bf16 a[24:27], a[236:239], a[180:183], a[24:27]\n"
"s_waitcnt lgkmcnt(8)\n"
"v_mfma_f32_16x16x32_bf16 a[28:31], a[232:235], a[184:187], a[28:31]\n"
"v_mfma_f32_16x16x32_bf16 a[28:31], a[236:239], a[188:191], a[28:31]\n"
"ds_read_b64_tr_b16 a[232:233], v19 offset:36928\n"
"v_mfma_f32_16x16x32_bf16 a[32:35], a[240:243], a[160:163], a[32:35]\n"
"ds_read_b64_tr_b16 a[234:235], v19 offset:38464\n"
"v_mfma_f32_16x16x32_bf16 a[32:35], a[244:247], a[164:167], a[32:35]\n"
"ds_read_b64_tr_b16 a[236:237], v19 offset:40000\n"
"v_mfma_f32_16x16x32_bf16 a[36:39], a[240:243], a[168:171], a[36:39]\n"
"ds_read_b64_tr_b16 a[238:239], v19 offset:41536\n"
"v_mfma_f32_16x16x32_bf16 a[36:39], a[244:247], a[172:175], a[36:39]\n"
"v_mfma_f32_16x16x32_bf16 a[40:43], a[240:243], a[176:179], a[40:43]\n"
"v_mfma_f32_16x16x32_bf16 a[40:43], a[244:247], a[180:183], a[40:43]\n"
"s_waitcnt lgkmcnt(8)\n"
"v_mfma_f32_16x16x32_bf16 a[44:47], a[240:243], a[184:187], a[44:47]\n"
"v_mfma_f32_16x16x32_bf16 a[44:47], a[244:247], a[188:191], a[44:47]\n"
"ds_read_b64_tr_b16 a[240:241], v19 offset:30720\n"
"v_mfma_f32_16x16x32_bf16 a[48:51], a[248:251], a[160:163], a[48:51]\n"
"ds_read_b64_tr_b16 a[242:243], v19 offset:32256\n"
"v_mfma_f32_16x16x32_bf16 a[48:51], a[252:255], a[164:167], a[48:51]\n"
"ds_read_b64_tr_b16 a[244:245], v19 offset:33792\n"
"v_mfma_f32_16x16x32_bf16 a[52:55], a[248:251], a[168:171], a[52:55]\n"
"ds_read_b64_tr_b16 a[246:247], v19 offset:35328\n"
"v_mfma_f32_16x16x32_bf16 a[52:55], a[252:255], a[172:175], a[52:55]\n"
"v_mfma_f32_16x16x32_bf16 a[56:59], a[248:251], a[176:179], a[56:59]\n"
"v_mfma_f32_16x16x32_bf16 a[56:59], a[252:255], a[180:183], a[56:59]\n"
"s_waitcnt lgkmcnt(8)\n"
"v_mfma_f32_16x16x32_bf16 a[60:63], a[248:251], a[184:187], a[60:63]\n"
"v_mfma_f32_16x16x32_bf16 a[60:63], a[252:255], a[188:191], a[60:63]\n"
"ds_read_b64_tr_b16 a[248:249], v19 offset:43008\n"
"v_mfma_f32_16x16x32_bf16 a[64:67], a[224:227], a[160:163], a[64:67]\n"
"ds_read_b64_tr_b16 a[250:251], v19 offset:44544\n"
"v_mfma_f32_16x16x32_bf16 a[64:67], a[228:231], a[164:167], a[64:67]\n"
"ds_read_b64_tr_b16 a[252:253], v19 offset:46080\n"
"v_mfma_f32_16x16x32_bf16 a[68:71], a[224:227], a[168:171], a[68:71]\n"
"ds_read_b64_tr_b16 a[254:255], v19 offset:47616\n"
"v_mfma_f32_16x16x32_bf16 a[68:71], a[228:231], a[172:175], a[68:71]\n"
"v_mfma_f32_16x16x32_bf16 a[72:75], a[224:227], a[176:179], a[72:75]\n"
"v_mfma_f32_16x16x32_bf16 a[72:75], a[228:231], a[180:183], a[72:75]\n"
"s_waitcnt lgkmcnt(8)\n"
"v_mfma_f32_16x16x32_bf16 a[76:79], a[224:227], a[184:187], a[76:79]\n"
"v_mfma_f32_16x16x32_bf16 a[76:79], a[228:231], a[188:191], a[76:79]\n"
"ds_read_b64_tr_b16 a[224:225], v19 offset:30752\n"
"v_mfma_f32_16x16x32_bf16 a[80:83], a[232:235], a[160:163], a[80:83]\n"
"ds_read_b64_tr_b16 a[226:227], v19 offset:32288\n"
"v_mfma_f32_16x16x32_bf16 a[80:83], a[236:239], a[164:167], a[80:83]\n"
"ds_read_b64_tr_b16 a[228:229], v19 offset:33824\n"
"v_mfma_f32_16x16x32_bf16 a[84:87], a[232:235], a[168:171], a[84:87]\n"
"ds_read_b64_tr_b16 a[230:231], v19 offset:35360\n"
"v_mfma_f32_16x16x32_bf16 a[84:87], a[236:239], a[172:175], a[84:87]\n"
"v_mfma_f32_16x16x32_bf16 a[88:91], a[232:235], a[176:179], a[88:91]\n"
"v_mfma_f32_16x16x32_bf16 a[88:91], a[236:239], a[180:183], a[88:91]\n"
"s_waitcnt lgkmcnt(8)\n"
"s_waitcnt vmcnt(41)\n"
"v_mfma_f32_16x16x32_bf16 a[92:95], a[232:235], a[184:187], a[92:95]\n"
"v_mfma_f32_16x16x32_bf16 a[92:95], a[236:239], a[188:191], a[92:95]\n"
"buffer_load_dwordx4 a[160:163], v2, s[36:39], s80 offen offset:0 sc1\n"
"v_mfma_f32_16x16x32_bf16 a[0:3], a[240:243], a[192:195], a[0:3]\n"
"ds_read_b64_tr_b16 a[232:233], v19 offset:43040\n"
"v_mfma_f32_16x16x32_bf16 a[0:3], a[244:247], a[196:199], a[0:3]\n"
"ds_read_b64_tr_b16 a[234:235], v19 offset:44576\n"
"v_mfma_f32_16x16x32_bf16 a[4:7], a[240:243], a[200:203], a[4:7]\n"
"ds_read_b64_tr_b16 a[236:237], v19 offset:46112\n"
"v_mfma_f32_16x16x32_bf16 a[4:7], a[244:247], a[204:207], a[4:7]\n"
"buffer_load_dwordx4 a[164:167], v2, s[36:39], s80 offen offset:1024 sc1\n"
"v_mfma_f32_16x16x32_bf16 a[8:11], a[240:243], a[208:211], a[8:11]\n"
"ds_read_b64_tr_b16 a[238:239], v19 offset:47648\n"
"v_mfma_f32_16x16x32_bf16 a[8:11], a[244:247], a[212:215], a[8:11]\n"
"s_waitcnt lgkmcnt(8)\n"
"v_mfma_f32_16x16x32_bf16 a[12:15], a[240:243], a[216:219], a[12:15]\n"
"v_mfma_f32_16x16x32_bf16 a[12:15], a[244:247], a[220:223], a[12:15]\n"
"ds_read_b64_tr_b16 a[240:241], v19 offset:30784\n"
"v_mfma_f32_16x16x32_bf16 a[16:19], a[248:251], a[192:195], a[16:19]\n"
"s_waitcnt vmcnt(37)\n"
"s_barrier\n"
"s_add_u32 m0, s7, 0\n"
"v_mfma_f32_16x16x32_bf16 a[16:19], a[252:255], a[196:199], a[16:19]\n"
"buffer_load_dwordx4 v32, s[60:63], s10 offen lds\n"
"v_mfma_f32_16x16x32_bf16 a[20:23], a[248:251], a[200:203], a[20:23]\n"
"s_add_u32 m0, s7, 1024\n"
"ds_read_b64_tr_b16 a[242:243], v19 offset:32320\n"
"v_mfma_f32_16x16x32_bf16 a[20:23], a[252:255], a[204:207], a[20:23]\n"
"ds_read_b64_tr_b16 a[244:245], v19 offset:33856\n"
"v_mfma_f32_16x16x32_bf16 a[24:27], a[248:251], a[208:211], a[24:27]\n"
"ds_read_b64_tr_b16 a[246:247], v19 offset:35392\n"
"v_mfma_f32_16x16x32_bf16 a[24:27], a[252:255], a[212:215], a[24:27]\n"
"s_waitcnt lgkmcnt(8)\n"
"v_mfma_f32_16x16x32_bf16 a[28:31], a[248:251], a[216:219], a[28:31]\n"
"buffer_load_dwordx4 v33, s[60:63], s10 offen lds\n"
"v_mfma_f32_16x16x32_bf16 a[28:31], a[252:255], a[220:223], a[28:31]\n"
"s_add_u32 m0, s7, 2048\n"
"ds_read_b64_tr_b16 a[248:249], v19 offset:43072\n"
"v_mfma_f32_16x16x32_bf16 a[32:35], a[224:227], a[192:195], a[32:35]\n"
"ds_read_b64_tr_b16 a[250:251], v19 offset:44608\n"
"v_mfma_f32_16x16x32_bf16 a[32:35], a[228:231], a[196:199], a[32:35]\n"
"ds_read_b64_tr_b16 a[252:253], v19 offset:46144\n"
"v_mfma_f32_16x16x32_bf16 a[36:39], a[224:227], a[200:203], a[36:39]\n"
"ds_read_b64_tr_b16 a[254:255], v19 offset:47680\n"
"v_mfma_f32_16x16x32_bf16 a[36:39], a[228:231], a[204:207], a[36:39]\n"
"v_mfma_f32_16x16x32_bf16 a[40:43], a[224:227], a[208:211], a[40:43]\n"
"buffer_load_dwordx4 v34, s[60:63], s10 offen lds\n"
"v_mfma_f32_16x16x32_bf16 a[40:43], a[228:231], a[212:215], a[40:43]\n"
"s_add_u32 m0, s7, 3072\n"
"s_waitcnt lgkmcnt(8)\n"
"v_mfma_f32_16x16x32_bf16 a[44:47], a[224:227], a[216:219], a[44:47]\n"
"v_mfma_f32_16x16x32_bf16 a[44:47], a[228:231], a[220:223], a[44:47]\n"
"ds_read_b64_tr_b16 a[224:225], v12 offset:0\n"
"v_mfma_f32_16x16x32_bf16 a[48:51], a[232:235], a[192:195], a[48:51]\n"
"ds_read_b64_tr_b16 a[226:227], v12 offset:6144\n"
"v_mfma_f32_16x16x32_bf16 a[48:51], a[236:239], a[196:199], a[48:51]\n"
"ds_read_b64_tr_b16 a[228:229], v12 offset:12288\n"
"v_mfma_f32_16x16x32_bf16 a[52:55], a[232:235], a[200:203], a[52:55]\n"
"buffer_load_dwordx4 v35, s[60:63], s10 offen lds\n"
"v_mfma_f32_16x16x32_bf16 a[52:55], a[236:239], a[204:207], a[52:55]\n"
"s_add_u32 m0, s7, 4096\n"
"ds_read_b64_tr_b16 a[230:231], v12 offset:18432\n"
"v_mfma_f32_16x16x32_bf16 a[56:59], a[232:235], a[208:211], a[56:59]\n"
"v_mfma_f32_16x16x32_bf16 a[56:59], a[236:239], a[212:215], a[56:59]\n"
"s_waitcnt lgkmcnt(8)\n"
"v_mfma_f32_16x16x32_bf16 a[60:63], a[232:235], a[216:219], a[60:63]\n"
"v_mfma_f32_16x16x32_bf16 a[60:63], a[236:239], a[220:223], a[60:63]\n"
"ds_read_b64_tr_b16 a[232:233], v12 offset:8\n"
"v_mfma_f32_16x16x32_bf16 a[64:67], a[240:243], a[192:195], a[64:67]\n"
"buffer_load_dwordx4 v36, s[60:63], s10 offen lds\n"
"v_mfma_f32_16x16x32_bf16 a[64:67], a[244:247], a[196:199], a[64:67]\n"
"ds_read_b64_tr_b16 a[234:235], v12 offset:6152\n"
"v_mfma_f32_16x16x32_bf16 a[68:71], a[240:243], a[200:203], a[68:71]\n"
"ds_read_b64_tr_b16 a[236:237], v12 offset:12296\n"
"v_mfma_f32_16x16x32_bf16 a[68:71], a[244:247], a[204:207], a[68:71]\n"
"ds_read_b64_tr_b16 a[238:239], v12 offset:18440\n"
"v_mfma_f32_16x16x32_bf16 a[72:75], a[240:243], a[208:211], a[72:75]\n"
"v_mfma_f32_16x16x32_bf16 a[72:75], a[244:247], a[212:215], a[72:75]\n"
"s_waitcnt lgkmcnt(8)\n"
"v_mfma_f32_16x16x32_bf16 a[76:79], a[240:243], a[216:219], a[76:79]\n"
"buffer_load_dwordx4 a[168:171], v3, s[36:39], s80 offen offset:0 sc1\n"
"v_mfma_f32_16x16x32_bf16 a[76:79], a[244:247], a[220:223], a[76:79]\n"
"ds_read_b64_tr_b16 a[240:241], v13 offset:0\n"
"v_mfma_f32_16x16x32_bf16 a[80:83], a[248:251], a[192:195], a[80:83]\n"
"ds_read_b64_tr_b16 a[242:243], v13 offset:6144\n"
"v_mfma_f32_16x16x32_bf16 a[80:83], a[252:255], a[196:199], a[80:83]\n"
"ds_read_b64_tr_b16 a[244:245], v13 offset:12288\n"
"v_mfma_f32_16x16x32_bf16 a[84:87], a[248:251], a[200:203], a[84:87]\n"
"ds_read_b64_tr_b16 a[246:247], v13 offset:18432\n"
"v_mfma_f32_16x16x32_bf16 a[84:87], a[252:255], a[204:207], a[84:87]\n"
"buffer_load_dwordx4 a[172:175], v3, s[36:39], s80 offen offset:1024 sc1\n"
"v_mfma_f32_16x16x32_bf16 a[88:91], a[248:251], a[208:211], a[88:91]\n"
"v_mfma_f32_16x16x32_bf16 a[88:91], a[252:255], a[212:215], a[88:91]\n"
"s_waitcnt lgkmcnt(8)\n"
"s_waitcnt vmcnt(32)\n"
"v_mfma_f32_16x16x32_bf16 a[92:95], a[248:251], a[216:219], a[92:95]\n"
"v_mfma_f32_16x16x32_bf16 a[92:95], a[252:255], a[220:223], a[92:95]\n"
"ds_read_b64_tr_b16 a[248:249], v13 offset:8\n"
"v_mfma_f32_16x16x32_bf16 v[64:67], a[224:227], a[96:99], v[64:67]\n"
"buffer_load_dwordx4 a[176:179], v4, s[36:39], s80 offen offset:0 sc1\n"
"v_mfma_f32_16x16x32_bf16 v[64:67], a[228:231], a[100:103], v[64:67]\n"
"ds_read_b64_tr_b16 a[250:251], v13 offset:6152\n"
"v_mfma_f32_16x16x32_bf16 v[68:71], a[224:227], a[104:107], v[68:71]\n"
"ds_read_b64_tr_b16 a[252:253], v13 offset:12296\n"
"v_mfma_f32_16x16x32_bf16 v[68:71], a[228:231], a[108:111], v[68:71]\n"
"ds_read_b64_tr_b16 a[254:255], v13 offset:18440\n"
"v_mfma_f32_16x16x32_bf16 v[72:75], a[224:227], a[112:115], v[72:75]\n"
"v_mfma_f32_16x16x32_bf16 v[72:75], a[228:231], a[116:119], v[72:75]\n"
"buffer_load_dwordx4 a[180:183], v4, s[36:39], s80 offen offset:1024 sc1\n"
"v_mfma_f32_16x16x32_bf16 v[76:79], a[224:227], a[120:123], v[76:79]\n"
"s_waitcnt lgkmcnt(8)\n"
"v_mfma_f32_16x16x32_bf16 v[76:79], a[228:231], a[124:127], v[76:79]\n"
"ds_read_b64_tr_b16 a[224:225], v12 offset:1024\n"
"v_mfma_f32_16x16x32_bf16 v[80:83], a[232:235], a[96:99], v[80:83]\n"
"ds_read_b64_tr_b16 a[226:227], v12 offset:7168\n"
"v_mfma_f32_16x16x32_bf16 v[80:83], a[236:239], a[100:103], v[80:83]\n"
"ds_read_b64_tr_b16 a[228:229], v12 offset:13312\n"
"v_mfma_f32_16x16x32_bf16 v[84:87], a[232:235], a[104:107], v[84:87]\n"
"buffer_load_dwordx4 a[184:187], v5, s[36:39], s80 offen offset:0 sc1\n"
"v_mfma_f32_16x16x32_bf16 v[84:87], a[236:239], a[108:111], v[84:87]\n"
"ds_read_b64_tr_b16 a[230:231], v12 offset:19456\n"
"v_mfma_f32_16x16x32_bf16 v[88:91], a[232:235], a[112:115], v[88:91]\n"
"v_mfma_f32_16x16x32_bf16 v[88:91], a[236:239], a[116:119], v[88:91]\n"
"s_waitcnt lgkmcnt(8)\n"
"v_mfma_f32_16x16x32_bf16 v[92:95], a[232:235], a[120:123], v[92:95]\n"
"v_mfma_f32_16x16x32_bf16 v[92:95], a[236:239], a[124:127], v[92:95]\n"
"buffer_load_dwordx4 a[188:191], v5, s[36:39], s80 offen offset:1024 sc1\n"
"v_mfma_f32_16x16x32_bf16 v[96:99], a[240:243], a[96:99], v[96:99]\n"
"s_add_u32 s80, s80, 2048\n"
"ds_read_b64_tr_b16 a[232:233], v12 offset:1032\n"
"v_mfma_f32_16x16x32_bf16 v[96:99], a[244:247], a[100:103], v[96:99]\n"
"ds_read_b64_tr_b16 a[234:235], v12 offset:7176\n"
"v_mfma_f32_16x16x32_bf16 v[100:103], a[240:243], a[104:107], v[100:103]\n"
"ds_read_b64_tr_b16 a[236:237], v12 offset:13320\n"
"v_mfma_f32_16x16x32_bf16 v[100:103], a[244:247], a[108:111], v[100:103]\n"
"ds_read_b64_tr_b16 a[238:239], v12 offset:19464\n"
"v_mfma_f32_16x16x32_bf16 v[104:107], a[240:243], a[112:115], v[104:107]\n"
"buffer_load_dwordx4 a[192:195], v2, s[36:39], s80 offen offset:0 sc1\n"
"v_mfma_f32_16x16x32_bf16 v[104:107], a[244:247], a[116:119], v[104:107]\n"
"s_waitcnt lgkmcnt(8)\n"
"v_mfma_f32_16x16x32_bf16 v[108:111], a[240:243], a[120:123], v[108:111]\n"
"v_mfma_f32_16x16x32_bf16 v[108:111], a[244:247], a[124:127], v[108:111]\n"
"ds_read_b64_tr_b16 a[240:241], v13 offset:1024\n"
"v_mfma_f32_16x16x32_bf16 v[112:115], a[248:251], a[96:99], v[112:115]\n"
"ds_read_b64_tr_b16 a[242:243], v13 offset:7168\n"
"v_mfma_f32_16x16x32_bf16 v[112:115], a[252:255], a[100:103], v[112:115]\n"
"buffer_load_dwordx4 a[196:199], v2, s[36:39], s80 offen offset:1024 sc1\n"
"v_mfma_f32_16x16x32_bf16 v[116:119], a[248:251], a[104:107], v[116:119]\n"
"ds_read_b64_tr_b16 a[244:245], v13 offset:13312\n"
"v_mfma_f32_16x16x32_bf16 v[116:119], a[252:255], a[108:111], v[116:119]\n"
"ds_read_b64_tr_b16 a[246:247], v13 offset:19456\n"
"v_mfma_f32_16x16x32_bf16 v[120:123], a[248:251], a[112:115], v[120:123]\n"
"v_mfma_f32_16x16x32_bf16 v[120:123], a[252:255], a[116:119], v[120:123]\n"
"s_waitcnt lgkmcnt(8)\n"
"v_mfma_f32_16x16x32_bf16 v[124:127], a[248:251], a[120:123], v[124:127]\n"
"buffer_load_dwordx4 a[200:203], v3, s[36:39], s80 offen offset:0 sc1\n"
"v_mfma_f32_16x16x32_bf16 v[124:127], a[252:255], a[124:127], v[124:127]\n"
"ds_read_b64_tr_b16 a[248:249], v13 offset:1032\n"
"v_mfma_f32_16x16x32_bf16 v[128:131], a[224:227], a[96:99], v[128:131]\n"
"ds_read_b64_tr_b16 a[250:251], v13 offset:7176\n"
"v_mfma_f32_16x16x32_bf16 v[128:131], a[228:231], a[100:103], v[128:131]\n"
"ds_read_b64_tr_b16 a[252:253], v13 offset:13320\n"
"v_mfma_f32_16x16x32_bf16 v[132:135], a[224:227], a[104:107], v[132:135]\n"
"ds_read_b64_tr_b16 a[254:255], v13 offset:19464\n"
"v_mfma_f32_16x16x32_bf16 v[132:135], a[228:231], a[108:111], v[132:135]\n"
"buffer_load_dwordx4 a[204:207], v3, s[36:39], s80 offen offset:1024 sc1\n"
"v_mfma_f32_16x16x32_bf16 v[136:139], a[224:227], a[112:115], v[136:139]\n"
"v_mfma_f32_16x16x32_bf16 v[136:139], a[228:231], a[116:119], v[136:139]\n"
"s_waitcnt lgkmcnt(8)\n"
"v_mfma_f32_16x16x32_bf16 v[140:143], a[224:227], a[120:123], v[140:143]\n"
"v_mfma_f32_16x16x32_bf16 v[140:143], a[228:231], a[124:127], v[140:143]\n"
"ds_read_b64_tr_b16 a[224:225], v12 offset:2048\n"
"v_mfma_f32_16x16x32_bf16 v[144:147], a[232:235], a[96:99], v[144:147]\n"
"buffer_load_dwordx4 a[208:211], v4, s[36:39], s80 offen offset:0 sc1\n"
"v_mfma_f32_16x16x32_bf16 v[144:147], a[236:239], a[100:103], v[144:147]\n"
"ds_read_b64_tr_b16 a[226:227], v12 offset:8192\n"
"v_mfma_f32_16x16x32_bf16 v[148:151], a[232:235], a[104:107], v[148:151]\n"
"ds_read_b64_tr_b16 a[228:229], v12 offset:14336\n"
"v_mfma_f32_16x16x32_bf16 v[148:151], a[236:239], a[108:111], v[148:151]\n"
"ds_read_b64_tr_b16 a[230:231], v12 offset:20480\n"
"v_mfma_f32_16x16x32_bf16 v[152:155], a[232:235], a[112:115], v[152:155]\n"
"v_mfma_f32_16x16x32_bf16 v[152:155], a[236:239], a[116:119], v[152:155]\n"
"buffer_load_dwordx4 a[212:215], v4, s[36:39], s80 offen offset:1024 sc1\n"
"v_mfma_f32_16x16x32_bf16 v[156:159], a[232:235], a[120:123], v[156:159]\n"
"s_waitcnt lgkmcnt(8)\n"
"v_mfma_f32_16x16x32_bf16 v[156:159], a[236:239], a[124:127], v[156:159]\n"
"ds_read_b64_tr_b16 a[232:233], v12 offset:2056\n"
"v_mfma_f32_16x16x32_bf16 v[160:163], a[240:243], a[96:99], v[160:163]\n"
"ds_read_b64_tr_b16 a[234:235], v12 offset:8200\n"
"v_mfma_f32_16x16x32_bf16 v[160:163], a[244:247], a[100:103], v[160:163]\n"
"ds_read_b64_tr_b16 a[236:237], v12 offset:14344\n"
"v_mfma_f32_16x16x32_bf16 v[164:167], a[240:243], a[104:107], v[164:167]\n"
"buffer_load_dwordx4 a[216:219], v5, s[36:39], s80 offen offset:0 sc1\n"
"v_mfma_f32_16x16x32_bf16 v[164:167], a[244:247], a[108:111], v[164:167]\n"
"ds_read_b64_tr_b16 a[238:239], v12 offset:20488\n"
"v_mfma_f32_16x16x32_bf16 v[168:171], a[240:243], a[112:115], v[168:171]\n"
"v_mfma_f32_16x16x32_bf16 v[168:171], a[244:247], a[116:119], v[168:171]\n"
"s_waitcnt lgkmcnt(8)\n"
"v_mfma_f32_16x16x32_bf16 v[172:175], a[240:243], a[120:123], v[172:175]\n"
"v_mfma_f32_16x16x32_bf16 v[172:175], a[244:247], a[124:127], v[172:175]\n"
"buffer_load_dwordx4 a[220:223], v5, s[36:39], s80 offen offset:1024 sc1\n"
"v_mfma_f32_16x16x32_bf16 v[176:179], a[248:251], a[96:99], v[176:179]\n"
"s_waitcnt vmcnt(42)\n"
"s_barrier\n"
"s_add_u32 m0, s7, 24576\n"
"v_mfma_f32_16x16x32_bf16 v[176:179], a[252:255], a[100:103], v[176:179]\n"
"s_add_u32 s80, s80, 2048\n"
"ds_read_b64_tr_b16 a[240:241], v13 offset:2048\n"
"v_mfma_f32_16x16x32_bf16 v[180:183], a[248:251], a[104:107], v[180:183]\n"
"ds_read_b64_tr_b16 a[242:243], v13 offset:8192\n"
"v_mfma_f32_16x16x32_bf16 v[180:183], a[252:255], a[108:111], v[180:183]\n"
"ds_read_b64_tr_b16 a[244:245], v13 offset:14336\n"
"v_mfma_f32_16x16x32_bf16 v[184:187], a[248:251], a[112:115], v[184:187]\n"
"buffer_load_dwordx4 v32, s[60:63], s32 offen lds\n"
"v_mfma_f32_16x16x32_bf16 v[184:187], a[252:255], a[116:119], v[184:187]\n"
"s_add_u32 m0, s7, 25600\n"
"s_waitcnt lgkmcnt(7)\n"
"v_mfma_f32_16x16x32_bf16 v[188:191], a[248:251], a[120:123], v[188:191]\n"
"ds_read_b64_tr_b16 a[246:247], v13 offset:20480\n"
"v_mfma_f32_16x16x32_bf16 v[188:191], a[252:255], a[124:127], v[188:191]\n"
"ds_read_b64_tr_b16 a[248:249], v13 offset:2056\n"
"v_mfma_f32_16x16x32_bf16 v[192:195], a[224:227], a[96:99], v[192:195]\n"
"ds_read_b64_tr_b16 a[250:251], v13 offset:8200\n"
"v_mfma_f32_16x16x32_bf16 v[192:195], a[228:231], a[100:103], v[192:195]\n"
"ds_read_b64_tr_b16 a[252:253], v13 offset:14344\n"
"v_mfma_f32_16x16x32_bf16 v[196:199], a[224:227], a[104:107], v[196:199]\n"
"buffer_load_dwordx4 v33, s[60:63], s32 offen lds\n"
"v_mfma_f32_16x16x32_bf16 v[196:199], a[228:231], a[108:111], v[196:199]\n"
"s_add_u32 m0, s7, 26624\n"
"ds_read_b64_tr_b16 a[254:255], v13 offset:20488\n"
"v_mfma_f32_16x16x32_bf16 v[200:203], a[224:227], a[112:115], v[200:203]\n"
"v_mfma_f32_16x16x32_bf16 v[200:203], a[228:231], a[116:119], v[200:203]\n"
"s_waitcnt lgkmcnt(8)\n"
"v_mfma_f32_16x16x32_bf16 v[204:207], a[224:227], a[120:123], v[204:207]\n"
"v_mfma_f32_16x16x32_bf16 v[204:207], a[228:231], a[124:127], v[204:207]\n"
"ds_read_b64_tr_b16 a[224:225], v20 offset:24576\n"
"v_mfma_f32_16x16x32_bf16 v[208:211], a[232:235], a[96:99], v[208:211]\n"
"buffer_load_dwordx4 v34, s[60:63], s32 offen lds\n"
"v_mfma_f32_16x16x32_bf16 v[208:211], a[236:239], a[100:103], v[208:211]\n"
"s_add_u32 m0, s7, 27648\n"
"ds_read_b64_tr_b16 a[226:227], v20 offset:26112\n"
"v_mfma_f32_16x16x32_bf16 v[212:215], a[232:235], a[104:107], v[212:215]\n"
"ds_read_b64_tr_b16 a[228:229], v20 offset:27648\n"
"v_mfma_f32_16x16x32_bf16 v[212:215], a[236:239], a[108:111], v[212:215]\n"
"ds_read_b64_tr_b16 a[230:231], v20 offset:29184\n"
"v_mfma_f32_16x16x32_bf16 v[216:219], a[232:235], a[112:115], v[216:219]\n"
"v_mfma_f32_16x16x32_bf16 v[216:219], a[236:239], a[116:119], v[216:219]\n"
"s_waitcnt lgkmcnt(8)\n"
"v_mfma_f32_16x16x32_bf16 v[220:223], a[232:235], a[120:123], v[220:223]\n"
"buffer_load_dwordx4 v35, s[60:63], s32 offen lds\n"
"v_mfma_f32_16x16x32_bf16 v[220:223], a[236:239], a[124:127], v[220:223]\n"
"s_add_u32 m0, s7, 28672\n"
"ds_read_b64_tr_b16 a[232:233], v20 offset:36864\n"
"v_mfma_f32_16x16x32_bf16 v[224:227], a[240:243], a[96:99], v[224:227]\n"
"ds_read_b64_tr_b16 a[234:235], v20 offset:38400\n"
"v_mfma_f32_16x16x32_bf16 v[224:227], a[244:247], a[100:103], v[224:227]\n"
"ds_read_b64_tr_b16 a[236:237], v20 offset:39936\n"
"v_mfma_f32_16x16x32_bf16 v[228:231], a[240:243], a[104:107], v[228:231]\n"
"ds_read_b64_tr_b16 a[238:239], v20 offset:41472\n"
"v_mfma_f32_16x16x32_bf16 v[228:231], a[244:247], a[108:111], v[228:231]\n"
"v_mfma_f32_16x16x32_bf16 v[232:235], a[240:243], a[112:115], v[232:235]\n"
"buffer_load_dwordx4 v36, s[60:63], s32 offen lds\n"
"v_mfma_f32_16x16x32_bf16 v[232:235], a[244:247], a[116:119], v[232:235]\n"
"s_waitcnt lgkmcnt(8)\n"
"v_mfma_f32_16x16x32_bf16 v[236:239], a[240:243], a[120:123], v[236:239]\n"
"v_mfma_f32_16x16x32_bf16 v[236:239], a[244:247], a[124:127], v[236:239]\n"
"ds_read_b64_tr_b16 a[240:241], v20 offset:24608\n"
"v_mfma_f32_16x16x32_bf16 v[240:243], a[248:251], a[96:99], v[240:243]\n"
"ds_read_b64_tr_b16 a[242:243], v20 offset:26144\n"
"v_mfma_f32_16x16x32_bf16 v[240:243], a[252:255], a[100:103], v[240:243]\n"
"ds_read_b64_tr_b16 a[244:245], v20 offset:27680\n"
"v_mfma_f32_16x16x32_bf16 v[244:247], a[248:251], a[104:107], v[244:247]\n"
"ds_read_b64_tr_b16 a[246:247], v20 offset:29216\n"
"v_mfma_f32_16x16x32_bf16 v[244:247], a[252:255], a[108:111], v[244:247]\n"
"v_mfma_f32_16x16x32_bf16 v[248:251], a[248:251], a[112:115], v[248:251]\n"
"v_mfma_f32_16x16x32_bf16 v[248:251], a[252:255], a[116:119], v[248:251]\n"
"s_waitcnt lgkmcnt(8)\n"
"v_mfma_f32_16x16x32_bf16 v[252:255], a[248:251], a[120:123], v[252:255]\n"
"v_mfma_f32_16x16x32_bf16 v[252:255], a[252:255], a[124:127], v[252:255]\n"
"ds_read_b64_tr_b16 a[248:249], v20 offset:36896\n"
"v_mfma_f32_16x16x32_bf16 a[0:3], a[224:227], a[96:99], a[0:3]\n"
"ds_read_b64_tr_b16 a[250:251], v20 offset:38432\n"
"v_mfma_f32_16x16x32_bf16 a[0:3], a[228:231], a[100:103], a[0:3]\n"
"ds_read_b64_tr_b16 a[252:253], v20 offset:39968\n"
"v_mfma_f32_16x16x32_bf16 a[4:7], a[224:227], a[104:107], a[4:7]\n"
"ds_read_b64_tr_b16 a[254:255], v20 offset:41504\n"
"v_mfma_f32_16x16x32_bf16 a[4:7], a[228:231], a[108:111], a[4:7]\n"
"v_mfma_f32_16x16x32_bf16 a[8:11], a[224:227], a[112:115], a[8:11]\n"
"v_mfma_f32_16x16x32_bf16 a[8:11], a[228:231], a[116:119], a[8:11]\n"
"s_waitcnt lgkmcnt(8)\n"
"v_mfma_f32_16x16x32_bf16 a[12:15], a[224:227], a[120:123], a[12:15]\n"
"v_mfma_f32_16x16x32_bf16 a[12:15], a[228:231], a[124:127], a[12:15]\n"
"ds_read_b64_tr_b16 a[224:225], v20 offset:24640\n"
"v_mfma_f32_16x16x32_bf16 a[16:19], a[232:235], a[96:99], a[16:19]\n"
"ds_read_b64_tr_b16 a[226:227], v20 offset:26176\n"
"v_mfma_f32_16x16x32_bf16 a[16:19], a[236:239], a[100:103], a[16:19]\n"
"ds_read_b64_tr_b16 a[228:229], v20 offset:27712\n"
"v_mfma_f32_16x16x32_bf16 a[20:23], a[232:235], a[104:107], a[20:23]\n"
"ds_read_b64_tr_b16 a[230:231], v20 offset:29248\n"
"v_mfma_f32_16x16x32_bf16 a[20:23], a[236:239], a[108:111], a[20:23]\n"
"v_mfma_f32_16x16x32_bf16 a[24:27], a[232:235], a[112:115], a[24:27]\n"
"v_mfma_f32_16x16x32_bf16 a[24:27], a[236:239], a[116:119], a[24:27]\n"
"s_waitcnt lgkmcnt(8)\n"
"v_mfma_f32_16x16x32_bf16 a[28:31], a[232:235], a[120:123], a[28:31]\n"
"v_mfma_f32_16x16x32_bf16 a[28:31], a[236:239], a[124:127], a[28:31]\n"
"ds_read_b64_tr_b16 a[232:233], v20 offset:36928\n"
"v_mfma_f32_16x16x32_bf16 a[32:35], a[240:243], a[96:99], a[32:35]\n"
"ds_read_b64_tr_b16 a[234:235], v20 offset:38464\n"
"v_mfma_f32_16x16x32_bf16 a[32:35], a[244:247], a[100:103], a[32:35]\n"
"ds_read_b64_tr_b16 a[236:237], v20 offset:40000\n"
"v_mfma_f32_16x16x32_bf16 a[36:39], a[240:243], a[104:107], a[36:39]\n"
"ds_read_b64_tr_b16 a[238:239], v20 offset:41536\n"
"v_mfma_f32_16x16x32_bf16 a[36:39], a[244:247], a[108:111], a[36:39]\n"
"v_mfma_f32_16x16x32_bf16 a[40:43], a[240:243], a[112:115], a[40:43]\n"
"v_mfma_f32_16x16x32_bf16 a[40:43], a[244:247], a[116:119], a[40:43]\n"
"s_waitcnt lgkmcnt(8)\n"
"v_mfma_f32_16x16x32_bf16 a[44:47], a[240:243], a[120:123], a[44:47]\n"
"v_mfma_f32_16x16x32_bf16 a[44:47], a[244:247], a[124:127], a[44:47]\n"
"ds_read_b64_tr_b16 a[240:241], v20 offset:30720\n"
"v_mfma_f32_16x16x32_bf16 a[48:51], a[248:251], a[96:99], a[48:51]\n"
"ds_read_b64_tr_b16 a[242:243], v20 offset:32256\n"
"v_mfma_f32_16x16x32_bf16 a[48:51], a[252:255], a[100:103], a[48:51]\n"
"ds_read_b64_tr_b16 a[244:245], v20 offset:33792\n"
"v_mfma_f32_16x16x32_bf16 a[52:55], a[248:251], a[104:107], a[52:55]\n"
"ds_read_b64_tr_b16 a[246:247], v20 offset:35328\n"
"v_mfma_f32_16x16x32_bf16 a[52:55], a[252:255], a[108:111], a[52:55]\n"
"v_mfma_f32_16x16x32_bf16 a[56:59], a[248:251], a[112:115], a[56:59]\n"
"v_mfma_f32_16x16x32_bf16 a[56:59], a[252:255], a[116:119], a[56:59]\n"
"s_waitcnt lgkmcnt(8)\n"
"v_mfma_f32_16x16x32_bf16 a[60:63], a[248:251], a[120:123], a[60:63]\n"
"v_mfma_f32_16x16x32_bf16 a[60:63], a[252:255], a[124:127], a[60:63]\n"
"ds_read_b64_tr_b16 a[248:249], v20 offset:43008\n"
"v_mfma_f32_16x16x32_bf16 a[64:67], a[224:227], a[96:99], a[64:67]\n"
"ds_read_b64_tr_b16 a[250:251], v20 offset:44544\n"
"v_mfma_f32_16x16x32_bf16 a[64:67], a[228:231], a[100:103], a[64:67]\n"
"ds_read_b64_tr_b16 a[252:253], v20 offset:46080\n"
"v_mfma_f32_16x16x32_bf16 a[68:71], a[224:227], a[104:107], a[68:71]\n"
"ds_read_b64_tr_b16 a[254:255], v20 offset:47616\n"
"v_mfma_f32_16x16x32_bf16 a[68:71], a[228:231], a[108:111], a[68:71]\n"
"v_mfma_f32_16x16x32_bf16 a[72:75], a[224:227], a[112:115], a[72:75]\n"
"s_add_u32 vcc_hi, vcc_lo, 49152\n"
"buffer_load_dword v27, v0, s[56:59], vcc_hi idxen\n"
"v_mfma_f32_16x16x32_bf16 a[72:75], a[228:231], a[116:119], a[72:75]\n"
"s_waitcnt lgkmcnt(8)\n"
"v_mfma_f32_16x16x32_bf16 a[76:79], a[224:227], a[120:123], a[76:79]\n"
"v_mfma_f32_16x16x32_bf16 a[76:79], a[228:231], a[124:127], a[76:79]\n"
"ds_read_b64_tr_b16 a[224:225], v20 offset:30752\n"
"v_mfma_f32_16x16x32_bf16 a[80:83], a[232:235], a[96:99], a[80:83]\n"
"ds_read_b64_tr_b16 a[226:227], v20 offset:32288\n"
"v_mfma_f32_16x16x32_bf16 a[80:83], a[236:239], a[100:103], a[80:83]\n"
"ds_read_b64_tr_b16 a[228:229], v20 offset:33824\n"
"v_mfma_f32_16x16x32_bf16 a[84:87], a[232:235], a[104:107], a[84:87]\n"
"ds_read_b64_tr_b16 a[230:231], v20 offset:35360\n"
"v_mfma_f32_16x16x32_bf16 a[84:87], a[236:239], a[108:111], a[84:87]\n"
"v_mfma_f32_16x16x32_bf16 a[88:91], a[232:235], a[112:115], a[88:91]\n"
"s_add_u32 vcc_hi, vcc_lo, 40960\n"
"buffer_load_dword v27, v0, s[56:59], vcc_hi idxen\n"
"v_mfma_f32_16x16x32_bf16 a[88:91], a[236:239], a[116:119], a[88:91]\n"
"s_waitcnt lgkmcnt(8)\n"
"s_waitcnt vmcnt(41)\n"
"v_mfma_f32_16x16x32_bf16 a[92:95], a[232:235], a[120:123], a[92:95]\n"
"v_mfma_f32_16x16x32_bf16 a[92:95], a[236:239], a[124:127], a[92:95]\n"
"buffer_load_dwordx4 a[96:99], v2, s[36:39], s80 offen offset:0 sc1\n"
"v_mfma_f32_16x16x32_bf16 a[0:3], a[240:243], a[128:131], a[0:3]\n"
"ds_read_b64_tr_b16 a[232:233], v20 offset:43040\n"
"v_mfma_f32_16x16x32_bf16 a[0:3], a[244:247], a[132:135], a[0:3]\n"
"ds_read_b64_tr_b16 a[234:235], v20 offset:44576\n"
"v_mfma_f32_16x16x32_bf16 a[4:7], a[240:243], a[136:139], a[4:7]\n"
"ds_read_b64_tr_b16 a[236:237], v20 offset:46112\n"
"v_mfma_f32_16x16x32_bf16 a[4:7], a[244:247], a[140:143], a[4:7]\n"
"buffer_load_dwordx4 a[100:103], v2, s[36:39], s80 offen offset:1024 sc1\n"
"v_mfma_f32_16x16x32_bf16 a[8:11], a[240:243], a[144:147], a[8:11]\n"
"ds_read_b64_tr_b16 a[238:239], v20 offset:47648\n"
"v_mfma_f32_16x16x32_bf16 a[8:11], a[244:247], a[148:151], a[8:11]\n"
"s_waitcnt lgkmcnt(8)\n"
"v_mfma_f32_16x16x32_bf16 a[12:15], a[240:243], a[152:155], a[12:15]\n"
"v_mfma_f32_16x16x32_bf16 a[12:15], a[244:247], a[156:159], a[12:15]\n"
"ds_read_b64_tr_b16 a[240:241], v20 offset:30784\n"
"v_mfma_f32_16x16x32_bf16 a[16:19], a[248:251], a[128:131], a[16:19]\n"
"s_waitcnt vmcnt(36)\n"
"s_barrier\n"
"s_add_u32 m0, s83, 49152\n"
"v_mfma_f32_16x16x32_bf16 a[16:19], a[252:255], a[132:135], a[16:19]\n"
"buffer_load_dwordx4 v6, s[40:43], s81 offen offset:0 lds\n"
"v_mfma_f32_16x16x32_bf16 a[20:23], a[248:251], a[136:139], a[20:23]\n"
"s_add_u32 m0, s83, 50048\n"
"ds_read_b64_tr_b16 a[242:243], v20 offset:32320\n"
"v_mfma_f32_16x16x32_bf16 a[20:23], a[252:255], a[140:143], a[20:23]\n"
"ds_read_b64_tr_b16 a[244:245], v20 offset:33856\n"
"v_mfma_f32_16x16x32_bf16 a[24:27], a[248:251], a[144:147], a[24:27]\n"
"ds_read_b64_tr_b16 a[246:247], v20 offset:35392\n"
"v_mfma_f32_16x16x32_bf16 a[24:27], a[252:255], a[148:151], a[24:27]\n"
"buffer_load_dwordx4 v6, s[40:43], s81 offen offset:128 lds\n"
"v_mfma_f32_16x16x32_bf16 a[28:31], a[248:251], a[152:155], a[28:31]\n"
"s_add_u32 m0, s83, 50944\n"
"s_waitcnt lgkmcnt(8)\n"
"v_accvgpr_read_b32 v40, a0\n"
"v_mfma_f32_16x16x32_bf16 a[28:31], a[252:255], a[156:159], a[28:31]\n"
"ds_read_b64_tr_b16 a[248:249], v20 offset:43072\n"
"v_accvgpr_read_b32 v41, a1\n"
"v_mfma_f32_16x16x32_bf16 a[32:35], a[224:227], a[128:131], a[32:35]\n"
"ds_read_b64_tr_b16 a[250:251], v20 offset:44608\n"
"v_accvgpr_read_b32 v42, a2\n"
"v_mfma_f32_16x16x32_bf16 a[32:35], a[228:231], a[132:135], a[32:35]\n"
"ds_read_b64_tr_b16 a[252:253], v20 offset:46144\n"
"v_accvgpr_read_b32 v43, a3\n"
"v_mfma_f32_16x16x32_bf16 a[36:39], a[224:227], a[136:139], a[36:39]\n"
"buffer_load_dwordx4 v6, s[40:43], s81 offen offset:256 lds\n"
"v_mfma_f32_16x16x32_bf16 a[36:39], a[228:231], a[140:143], a[36:39]\n"
"s_add_u32 s81, s81, 786432\n"
"s_add_u32 m0, s83, 52224\n"
"v_mul_f32 v44, v40, v40\n"
"v_mfma_f32_16x16x32_bf16 a[40:43], a[224:227], a[144:147], a[40:43]\n"
"ds_read_b64_tr_b16 a[254:255], v20 offset:47680\n"
"v_mul_f32 v45, v41, v41\n"
"v_mfma_f32_16x16x32_bf16 a[40:43], a[228:231], a[148:151], a[40:43]\n"
"s_waitcnt lgkmcnt(8)\n"
"v_mul_f32 v46, v42, v42\n"
"v_mul_f32 v47, v43, v43\n"
"v_mfma_f32_16x16x32_bf16 a[44:47], a[224:227], a[152:155], a[44:47]\n"
"v_fma_f32 v44, v44, s99, v39\n"
"v_fma_f32 v45, v45, s99, v39\n"
"v_mfma_f32_16x16x32_bf16 a[44:47], a[228:231], a[156:159], a[44:47]\n"
"buffer_load_dwordx4 v6, s[40:43], s82 offen offset:0 lds\n"
"v_mfma_f32_16x16x32_bf16 a[48:51], a[232:235], a[128:131], a[48:51]\n"
"s_add_u32 m0, s83, 53120\n"
"ds_read_b64_tr_b16 a[224:225], v14 offset:0\n"
"v_mfma_f32_16x16x32_bf16 a[48:51], a[236:239], a[132:135], a[48:51]\n"
"ds_read_b64_tr_b16 a[226:227], v14 offset:6144\n"
"v_fma_f32 v46, v46, s99, v39\n"
"v_mfma_f32_16x16x32_bf16 a[52:55], a[232:235], a[136:139], a[52:55]\n"
"ds_read_b64_tr_b16 a[228:229], v14 offset:12288\n"
"v_fma_f32 v47, v47, s99, v39\n"
"v_mfma_f32_16x16x32_bf16 a[52:55], a[236:239], a[140:143], a[52:55]\n"
"ds_read_b64_tr_b16 a[230:231], v14 offset:18432\n"
"v_mul_f32 v44, v44, v40\n"
"v_mfma_f32_16x16x32_bf16 a[56:59], a[232:235], a[144:147], a[56:59]\n"
"buffer_load_dwordx4 v6, s[40:43], s82 offen offset:128 lds\n"
"v_mfma_f32_16x16x32_bf16 a[56:59], a[236:239], a[148:151], a[56:59]\n"
"s_add_u32 m0, s83, 54016\n"
"s_waitcnt lgkmcnt(8)\n"
"v_mul_f32 v45, v45, v41\n"
"v_mfma_f32_16x16x32_bf16 a[60:63], a[232:235], a[152:155], a[60:63]\n"
"v_mul_f32 v46, v46, v42\n"
"v_mul_f32 v47, v47, v43\n"
"v_mfma_f32_16x16x32_bf16 a[60:63], a[236:239], a[156:159], a[60:63]\n"
"ds_read_b64_tr_b16 a[232:233], v14 offset:8\n"
"v_accvgpr_read_b32 v48, a32\n"
"v_mfma_f32_16x16x32_bf16 a[64:67], a[240:243], a[128:131], a[64:67]\n"
"ds_read_b64_tr_b16 a[234:235], v14 offset:6152\n"
"v_accvgpr_read_b32 v49, a33\n"
"v_mfma_f32_16x16x32_bf16 a[64:67], a[244:247], a[132:135], a[64:67]\n"
"buffer_load_dwordx4 v6, s[40:43], s82 offen offset:256 lds\n"
"v_mfma_f32_16x16x32_bf16 a[68:71], a[240:243], a[136:139], a[68:71]\n"
"s_add_u32 s82, s82, 786432\n"
"ds_read_b64_tr_b16 a[236:237], v14 offset:12296\n"
"v_mfma_f32_16x16x32_bf16 a[68:71], a[244:247], a[140:143], a[68:71]\n"
"ds_read_b64_tr_b16 a[238:239], v14 offset:18440\n"
"v_accvgpr_read_b32 v50, a34\n"
"v_mfma_f32_16x16x32_bf16 a[72:75], a[240:243], a[144:147], a[72:75]\n"
"v_exp_f32 v44, v44\n"
"v_mfma_f32_16x16x32_bf16 a[72:75], a[244:247], a[148:151], a[72:75]\n"
"s_waitcnt lgkmcnt(8)\n"
"v_exp_f32 v45, v45\n"
"v_mfma_f32_16x16x32_bf16 a[76:79], a[240:243], a[152:155], a[76:79]\n"
"buffer_load_dwordx4 a[104:107], v3, s[36:39], s80 offen offset:0 sc1\n"
"v_mfma_f32_16x16x32_bf16 a[76:79], a[244:247], a[156:159], a[76:79]\n"
"ds_read_b64_tr_b16 a[240:241], v15 offset:0\n"
"v_add_f32 v44, 1.0, v44\n"
"v_mfma_f32_16x16x32_bf16 a[80:83], a[248:251], a[128:131], a[80:83]\n"
"ds_read_b64_tr_b16 a[242:243], v15 offset:6144\n"
"v_add_f32 v45, 1.0, v45\n"
"v_mfma_f32_16x16x32_bf16 a[80:83], a[252:255], a[132:135], a[80:83]\n"
"ds_read_b64_tr_b16 a[244:245], v15 offset:12288\n"
"v_accvgpr_read_b32 v51, a35\n"
"v_mfma_f32_16x16x32_bf16 a[84:87], a[248:251], a[136:139], a[84:87]\n"
"ds_read_b64_tr_b16 a[246:247], v15 offset:18432\n"
"v_mul_f32 v52, v48, v48\n"
"v_mfma_f32_16x16x32_bf16 a[84:87], a[252:255], a[140:143], a[84:87]\n"
"buffer_load_dwordx4 a[108:111], v3, s[36:39], s80 offen offset:1024 sc1\n"
"v_mfma_f32_16x16x32_bf16 a[88:91], a[248:251], a[144:147], a[88:91]\n"
"v_exp_f32 v46, v46\n"
"v_mfma_f32_16x16x32_bf16 a[88:91], a[252:255], a[148:151], a[88:91]\n"
"s_waitcnt lgkmcnt(8)\n"
"v_exp_f32 v47, v47\n"
"v_mfma_f32_16x16x32_bf16 a[92:95], a[248:251], a[152:155], a[92:95]\n"
"v_add_f32 v46, 1.0, v46\n"
"v_add_f32 v47, 1.0, v47\n"
"v_mfma_f32_16x16x32_bf16 a[92:95], a[252:255], a[156:159], a[92:95]\n"
"ds_read_b64_tr_b16 a[248:249], v15 offset:8\n"
"v_mul_f32 v53, v49, v49\n"
"v_mfma_f32_16x16x32_bf16 v[64:67], a[224:227], a[128:131], v[64:67]\n"
"buffer_load_dwordx4 a[112:115], v4, s[36:39], s80 offen offset:0 sc1\n"
"v_mfma_f32_16x16x32_bf16 v[64:67], a[228:231], a[132:135], v[64:67]\n"
"ds_read_b64_tr_b16 a[250:251], v15 offset:6152\n"
"v_mul_f32 v54, v50, v50\n"
"v_mfma_f32_16x16x32_bf16 v[68:71], a[224:227], a[136:139], v[68:71]\n"
"ds_read_b64_tr_b16 a[252:253], v15 offset:12296\n"
"v_mul_f32 v55, v51, v51\n"
"v_mfma_f32_16x16x32_bf16 v[68:71], a[228:231], a[140:143], v[68:71]\n"
"ds_read_b64_tr_b16 a[254:255], v15 offset:18440\n"
"v_fma_f32 v52, v52, s99, v39\n"
"v_mfma_f32_16x16x32_bf16 v[72:75], a[224:227], a[144:147], v[72:75]\n"
"v_rcp_f32 v44, v44\n"
"v_mfma_f32_16x16x32_bf16 v[72:75], a[228:231], a[148:151], v[72:75]\n"
"buffer_load_dwordx4 a[116:119], v4, s[36:39], s80 offen offset:1024 sc1\n"
"v_mfma_f32_16x16x32_bf16 v[76:79], a[224:227], a[152:155], v[76:79]\n"
"s_waitcnt lgkmcnt(8)\n"
"v_rcp_f32 v45, v45\n"
"v_mfma_f32_16x16x32_bf16 v[76:79], a[228:231], a[156:159], v[76:79]\n"
"ds_read_b64_tr_b16 a[224:225], v14 offset:1024\n"
"v_mul_f32 v40, v40, v44\n"
"v_mfma_f32_16x16x32_bf16 v[80:83], a[232:235], a[128:131], v[80:83]\n"
"ds_read_b64_tr_b16 a[226:227], v14 offset:7168\n"
"v_mul_f32 v41, v41, v45\n"
"v_mfma_f32_16x16x32_bf16 v[80:83], a[236:239], a[132:135], v[80:83]\n"
"ds_read_b64_tr_b16 a[228:229], v14 offset:13312\n"
"v_accvgpr_read_b32 v44, a16\n"
"v_mfma_f32_16x16x32_bf16 v[84:87], a[232:235], a[136:139], v[84:87]\n"
"buffer_load_dwordx4 a[120:123], v5, s[36:39], s80 offen offset:0 sc1\n"
"v_mfma_f32_16x16x32_bf16 v[84:87], a[236:239], a[140:143], v[84:87]\n"
"ds_read_b64_tr_b16 a[230:231], v14 offset:19456\n"
"v_accvgpr_read_b32 v45, a17\n"
"v_mfma_f32_16x16x32_bf16 v[88:91], a[232:235], a[144:147], v[88:91]\n"
"v_rcp_f32 v46, v46\n"
"v_mfma_f32_16x16x32_bf16 v[88:91], a[236:239], a[148:151], v[88:91]\n"
"s_waitcnt lgkmcnt(8)\n"
"v_rcp_f32 v47, v47\n"
"v_mfma_f32_16x16x32_bf16 v[92:95], a[232:235], a[152:155], v[92:95]\n"
"v_mul_f32 v42, v42, v46\n"
"v_mul_f32 v43, v43, v47\n"
"v_mfma_f32_16x16x32_bf16 v[92:95], a[236:239], a[156:159], v[92:95]\n"
"buffer_load_dwordx4 a[124:127], v5, s[36:39], s80 offen offset:1024 sc1\n"
"v_mfma_f32_16x16x32_bf16 v[96:99], a[240:243], a[128:131], v[96:99]\n"
"s_add_u32 s80, s80, 2048\n"
"ds_read_b64_tr_b16 a[232:233], v14 offset:1032\n"
"v_mfma_f32_16x16x32_bf16 v[96:99], a[244:247], a[132:135], v[96:99]\n"
"ds_read_b64_tr_b16 a[234:235], v14 offset:7176\n"
"v_accvgpr_read_b32 v46, a18\n"
"v_mfma_f32_16x16x32_bf16 v[100:103], a[240:243], a[136:139], v[100:103]\n"
"ds_read_b64_tr_b16 a[236:237], v14 offset:13320\n"
"v_accvgpr_read_b32 v47, a19\n"
"v_mfma_f32_16x16x32_bf16 v[100:103], a[244:247], a[140:143], v[100:103]\n"
"ds_read_b64_tr_b16 a[238:239], v14 offset:19464\n"
"v_mul_f32 v40, v40, v44\n"
"v_mfma_f32_16x16x32_bf16 v[104:107], a[240:243], a[144:147], v[104:107]\n"
"v_mul_f32 v41, v41, v45\n"
"v_mul_f32 v42, v42, v46\n"
"v_mfma_f32_16x16x32_bf16 v[104:107], a[244:247], a[148:151], v[104:107]\n"
"s_waitcnt lgkmcnt(8)\n"
"v_mul_f32 v43, v43, v47\n"
"v_fma_f32 v53, v53, s99, v39\n"
"v_mfma_f32_16x16x32_bf16 v[108:111], a[240:243], a[152:155], v[108:111]\n"
"v_cvt_pk_bf16_f32 v28, v40, v41\n"
"v_cvt_pk_bf16_f32 v29, v42, v43\n"
"v_mfma_f32_16x16x32_bf16 v[108:111], a[244:247], a[156:159], v[108:111]\n"
"ds_read_b64_tr_b16 a[240:241], v15 offset:1024\n"
"v_fma_f32 v54, v54, s99, v39\n"
"v_mfma_f32_16x16x32_bf16 v[112:115], a[248:251], a[128:131], v[112:115]\n"
"ds_read_b64_tr_b16 a[242:243], v15 offset:7168\n"
"v_fma_f32 v55, v55, s99, v39\n"
"v_mfma_f32_16x16x32_bf16 v[112:115], a[252:255], a[132:135], v[112:115]\n"
"ds_read_b64_tr_b16 a[244:245], v15 offset:13312\n"
"v_mul_f32 v52, v52, v48\n"
"v_mfma_f32_16x16x32_bf16 v[116:119], a[248:251], a[136:139], v[116:119]\n"
"ds_read_b64_tr_b16 a[246:247], v15 offset:19456\n"
"v_mul_f32 v53, v53, v49\n"
"v_mfma_f32_16x16x32_bf16 v[116:119], a[252:255], a[140:143], v[116:119]\n"
"v_mul_f32 v54, v54, v50\n"
"v_mul_f32 v55, v55, v51\n"
"v_mfma_f32_16x16x32_bf16 v[120:123], a[248:251], a[144:147], v[120:123]\n"
"v_exp_f32 v52, v52\n"
"v_mfma_f32_16x16x32_bf16 v[120:123], a[252:255], a[148:151], v[120:123]\n"
"s_waitcnt lgkmcnt(8)\n"
"v_exp_f32 v53, v53\n"
"v_mfma_f32_16x16x32_bf16 v[124:127], a[248:251], a[152:155], v[124:127]\n"
"v_exp_f32 v54, v54\n"
"v_mfma_f32_16x16x32_bf16 v[124:127], a[252:255], a[156:159], v[124:127]\n"
"ds_read_b64_tr_b16 a[248:249], v15 offset:1032\n"
"v_add_f32 v52, 1.0, v52\n"
"v_mfma_f32_16x16x32_bf16 v[128:131], a[224:227], a[128:131], v[128:131]\n"
"ds_read_b64_tr_b16 a[250:251], v15 offset:7176\n"
"v_add_f32 v53, 1.0, v53\n"
"v_mfma_f32_16x16x32_bf16 v[128:131], a[228:231], a[132:135], v[128:131]\n"
"ds_read_b64_tr_b16 a[252:253], v15 offset:13320\n"
"v_add_f32 v54, 1.0, v54\n"
"v_mfma_f32_16x16x32_bf16 v[132:135], a[224:227], a[136:139], v[132:135]\n"
"ds_read_b64_tr_b16 a[254:255], v15 offset:19464\n"
"v_add_u32 v26, 147456, v1\n"
"v_mfma_f32_16x16x32_bf16 v[132:135], a[228:231], a[140:143], v[132:135]\n"
"v_exp_f32 v55, v55\n"
"v_mfma_f32_16x16x32_bf16 v[136:139], a[224:227], a[144:147], v[136:139]\n"
"ds_read_b128 v[16:19], v26\n"
"v_add_f32 v55, 1.0, v55\n"
"v_mfma_f32_16x16x32_bf16 v[136:139], a[228:231], a[148:151], v[136:139]\n"
"s_waitcnt lgkmcnt(9)\n"
"v_rcp_f32 v52, v52\n"
"v_mfma_f32_16x16x32_bf16 v[140:143], a[224:227], a[152:155], v[140:143]\n"
"v_rcp_f32 v53, v53\n"
"v_mfma_f32_16x16x32_bf16 v[140:143], a[228:231], a[156:159], v[140:143]\n"
"ds_read_b64_tr_b16 a[224:225], v14 offset:2048\n"
"v_mul_f32 v48, v48, v52\n"
"v_mfma_f32_16x16x32_bf16 v[144:147], a[232:235], a[128:131], v[144:147]\n"
"ds_read_b64_tr_b16 a[226:227], v14 offset:8192\n"
"v_mul_f32 v49, v49, v53\n"
"v_mfma_f32_16x16x32_bf16 v[144:147], a[236:239], a[132:135], v[144:147]\n"
"ds_read_b64_tr_b16 a[228:229], v14 offset:14336\n"
"v_accvgpr_read_b32 v52, a48\n"
"v_mfma_f32_16x16x32_bf16 v[148:151], a[232:235], a[136:139], v[148:151]\n"
"ds_read_b64_tr_b16 a[230:231], v14 offset:20480\n"
"v_accvgpr_read_b32 v53, a49\n"
"v_mfma_f32_16x16x32_bf16 v[148:151], a[236:239], a[140:143], v[148:151]\n"
"v_rcp_f32 v54, v54\n"
"v_mfma_f32_16x16x32_bf16 v[152:155], a[232:235], a[144:147], v[152:155]\n"
"v_rcp_f32 v55, v55\n"
"v_mfma_f32_16x16x32_bf16 v[152:155], a[236:239], a[148:151], v[152:155]\n"
"s_waitcnt lgkmcnt(9)\n"
"v_mul_f32 v50, v50, v54\n"
"v_mul_f32 v51, v51, v55\n"
"v_mfma_f32_16x16x32_bf16 v[156:159], a[232:235], a[152:155], v[156:159]\n"
"ds_read_b128 v[20:23], v26 offset:1024\n"
"v_accvgpr_read_b32 v54, a50\n"
"v_mfma_f32_16x16x32_bf16 v[156:159], a[236:239], a[156:159], v[156:159]\n"
"ds_read_b64_tr_b16 a[232:233], v14 offset:2056\n"
"v_accvgpr_read_b32 v55, a51\n"
"v_mfma_f32_16x16x32_bf16 v[160:163], a[240:243], a[128:131], v[160:163]\n"
"ds_read_b64_tr_b16 a[234:235], v14 offset:8200\n"
"v_mul_f32 v48, v48, v52\n"
"v_mfma_f32_16x16x32_bf16 v[160:163], a[244:247], a[132:135], v[160:163]\n"
"ds_read_b64_tr_b16 a[236:237], v14 offset:14344\n"
"v_mul_f32 v49, v49, v53\n"
"v_mfma_f32_16x16x32_bf16 v[164:167], a[240:243], a[136:139], v[164:167]\n"
"s_waitcnt vmcnt(16)\n"
"ds_read_b64_tr_b16 a[0:1], v37 offset:0\n"
"v_mfma_f32_16x16x32_bf16 v[164:167], a[244:247], a[140:143], v[164:167]\n"
"ds_read_b64_tr_b16 a[2:3], v37 offset:24576\n"
"v_mul_f32 v50, v50, v54\n"
"v_mfma_f32_16x16x32_bf16 v[168:171], a[240:243], a[144:147], v[168:171]\n"
"ds_read_b64_tr_b16 a[238:239], v14 offset:20488\n"
"v_mul_f32 v51, v51, v55\n"
"v_mfma_f32_16x16x32_bf16 v[168:171], a[244:247], a[148:151], v[168:171]\n"
"s_waitcnt lgkmcnt(12)\n"
"ds_read_b64_tr_b16 a[16:17], v38 offset:32\n"
"v_mfma_f32_16x16x32_bf16 v[172:175], a[240:243], a[152:155], v[172:175]\n"
"ds_read_b64_tr_b16 a[18:19], v38 offset:24608\n"
"v_cvt_pk_bf16_f32 v30, v48, v49\n"
"v_mfma_f32_16x16x32_bf16 v[172:175], a[244:247], a[156:159], v[172:175]\n"
"ds_read_b64_tr_b16 a[240:241], v15 offset:2048\n"
"v_cvt_pk_bf16_f32 v31, v50, v51\n"
"v_mfma_f32_16x16x32_bf16 v[176:179], a[248:251], a[128:131], v[176:179]\n"
"s_waitcnt vmcnt(14)\n"
"s_barrier\n"
"s_add_u32 m0, s83, 73728\n"
"v_mfma_f32_16x16x32_bf16 v[176:179], a[252:255], a[132:135], v[176:179]\n"
"buffer_store_dwordx4 v[28:31], v1, s[36:39], s5 offen offset:0 sc1\n"
"v_mfma_f32_16x16x32_bf16 v[180:183], a[248:251], a[136:139], v[180:183]\n"
"ds_read_b64_tr_b16 a[242:243], v15 offset:8192\n"
"v_mfma_f32_16x16x32_bf16 v[180:183], a[252:255], a[140:143], v[180:183]\n"
"ds_read_b64_tr_b16 a[244:245], v15 offset:14336\n"
"v_accvgpr_read_b32 v56, a64\n"
"v_mfma_f32_16x16x32_bf16 v[184:187], a[248:251], a[144:147], v[184:187]\n"
"ds_read_b64_tr_b16 a[246:247], v15 offset:20480\n"
"v_mfma_f32_16x16x32_bf16 v[184:187], a[252:255], a[148:151], v[184:187]\n"
"buffer_load_dwordx4 v6, s[40:43], s81 offen offset:0 lds\n"
"v_mfma_f32_16x16x32_bf16 v[188:191], a[248:251], a[152:155], v[188:191]\n"
"s_add_u32 m0, s83, 74624\n"
"s_waitcnt lgkmcnt(13)\n"
"v_accvgpr_read_b32 v57, a65\n"
"v_mfma_f32_16x16x32_bf16 v[188:191], a[252:255], a[156:159], v[188:191]\n"
"ds_read_b64_tr_b16 a[32:33], v37 offset:64\n"
"v_accvgpr_read_b32 v58, a66\n"
"v_mfma_f32_16x16x32_bf16 v[192:195], a[224:227], a[128:131], v[192:195]\n"
"ds_read_b64_tr_b16 a[34:35], v37 offset:24640\n"
"v_accvgpr_read_b32 v59, a67\n"
"v_mfma_f32_16x16x32_bf16 v[192:195], a[228:231], a[132:135], v[192:195]\n"
"ds_read_b64_tr_b16 a[248:249], v15 offset:2056\n"
"v_mul_f32 v60, v56, v56\n"
"v_mfma_f32_16x16x32_bf16 v[196:199], a[224:227], a[136:139], v[196:199]\n"
"buffer_load_dwordx4 v6, s[40:43], s81 offen offset:128 lds\n"
"v_mfma_f32_16x16x32_bf16 v[196:199], a[228:231], a[140:143], v[196:199]\n"
"s_add_u32 m0, s83, 75520\n"
"ds_read_b64_tr_b16 a[250:251], v15 offset:8200\n"
"v_mfma_f32_16x16x32_bf16 v[200:203], a[224:227], a[144:147], v[200:203]\n"
"ds_read_b64_tr_b16 a[252:253], v15 offset:14344\n"
"v_mul_f32 v61, v57, v57\n"
"v_mfma_f32_16x16x32_bf16 v[200:203], a[228:231], a[148:151], v[200:203]\n"
"ds_read_b64_tr_b16 a[254:255], v15 offset:20488\n"
"v_mfma_f32_16x16x32_bf16 v[204:207], a[224:227], a[152:155], v[204:207]\n"
"ds_read_b64_tr_b16 a[48:49], v38 offset:96\n"
"s_waitcnt lgkmcnt(13)\n"
"v_mfma_f32_16x16x32_bf16 v[204:207], a[228:231], a[156:159], v[204:207]\n"
"buffer_store_dwordx4 a[0:3], v1, s[36:39], s33 offen offset:0 sc1\n"
"v_mfma_f32_16x16x32_bf16 v[208:211], a[232:235], a[128:131], v[208:211]\n"
"ds_read_b64_tr_b16 a[50:51], v38 offset:24672\n"
"v_mul_f32 v62, v58, v58\n"
"v_mfma_f32_16x16x32_bf16 v[208:211], a[236:239], a[132:135], v[208:211]\n"
"ds_read_b64_tr_b16 a[224:225], v14 offset:24576\n"
"v_mul_f32 v63, v59, v59\n"
"v_mfma_f32_16x16x32_bf16 v[212:215], a[232:235], a[136:139], v[212:215]\n"
"ds_read_b64_tr_b16 a[226:227], v14 offset:30720\n"
"v_fma_f32 v60, v60, s99, v39\n"
"v_mfma_f32_16x16x32_bf16 v[212:215], a[236:239], a[140:143], v[212:215]\n"
"ds_read_b64_tr_b16 a[228:229], v14 offset:36864\n"
"v_fma_f32 v61, v61, s99, v39\n"
"v_mfma_f32_16x16x32_bf16 v[216:219], a[232:235], a[144:147], v[216:219]\n"
"buffer_load_dwordx4 v6, s[40:43], s81 offen offset:256 lds\n"
"v_mfma_f32_16x16x32_bf16 v[216:219], a[236:239], a[148:151], v[216:219]\n"
"s_add_u32 s81, s81, 786432\n"
"s_add_u32 m0, s83, 76800\n"
"v_mfma_f32_16x16x32_bf16 v[220:223], a[232:235], a[152:155], v[220:223]\n"
"ds_read_b64_tr_b16 a[230:231], v14 offset:43008\n"
"s_waitcnt lgkmcnt(12)\n"
"v_mfma_f32_16x16x32_bf16 v[220:223], a[236:239], a[156:159], v[220:223]\n"
"ds_read_b128 v[24:27], v26 offset:2048\n"
"v_fma_f32 v62, v62, s99, v39\n"
"v_mfma_f32_16x16x32_bf16 v[224:227], a[240:243], a[128:131], v[224:227]\n"
"buffer_store_dwordx4 a[16:19], v1, s[36:39], s33 offen offset:1024 sc1\n"
"v_mfma_f32_16x16x32_bf16 v[224:227], a[244:247], a[132:135], v[224:227]\n"
"ds_read_b64_tr_b16 a[0:1], v37 offset:128\n"
"v_fma_f32 v63, v63, s99, v39\n"
"v_mfma_f32_16x16x32_bf16 v[228:231], a[240:243], a[136:139], v[228:231]\n"
"ds_read_b64_tr_b16 a[2:3], v37 offset:24704\n"
"v_mul_f32 v60, v60, v56\n"
"v_mfma_f32_16x16x32_bf16 v[228:231], a[244:247], a[140:143], v[228:231]\n"
"ds_read_b64_tr_b16 a[232:233], v14 offset:24584\n"
"v_mul_f32 v61, v61, v57\n"
"v_mfma_f32_16x16x32_bf16 v[232:235], a[240:243], a[144:147], v[232:235]\n"
"ds_read_b64_tr_b16 a[234:235], v14 offset:30728\n"
"v_mul_f32 v62, v62, v58\n"
"v_mfma_f32_16x16x32_bf16 v[232:235], a[244:247], a[148:151], v[232:235]\n"
"s_waitcnt lgkmcnt(15)\n"
"ds_read_b64_tr_b16 a[236:237], v14 offset:36872\n"
"v_mfma_f32_16x16x32_bf16 v[236:239], a[240:243], a[152:155], v[236:239]\n"
"buffer_store_dwordx4 a[32:35], v1, s[36:39], s33 offen offset:2048 sc1\n"
"v_mfma_f32_16x16x32_bf16 v[236:239], a[244:247], a[156:159], v[236:239]\n"
"ds_read_b64_tr_b16 a[238:239], v14 offset:43016\n"
"s_waitcnt lgkmcnt(0)\n"
"v_mfma_f32_16x16x32_bf16 v[240:243], a[248:251], a[128:131], v[240:243]\n"
"ds_read_b64_tr_b16 a[16:17], v38 offset:160\n"
"v_mfma_f32_16x16x32_bf16 v[240:243], a[252:255], a[132:135], v[240:243]\n"
"ds_read_b64_tr_b16 a[18:19], v38 offset:24736\n"
"v_mul_f32 v63, v63, v59\n"
"v_mfma_f32_16x16x32_bf16 v[244:247], a[248:251], a[136:139], v[244:247]\n"
"ds_read_b64_tr_b16 a[240:241], v15 offset:24576\n"
"v_accvgpr_read_b32 v40, a4\n"
"v_mfma_f32_16x16x32_bf16 v[244:247], a[252:255], a[140:143], v[244:247]\n"
"buffer_load_dwordx4 v6, s[40:43], s82 offen offset:0 lds\n"
"v_mfma_f32_16x16x32_bf16 v[248:251], a[248:251], a[144:147], v[248:251]\n"
"s_add_u32 m0, s83, 77696\n"
"ds_read_b64_tr_b16 a[242:243], v15 offset:30720\n"
"v_mfma_f32_16x16x32_bf16 v[248:251], a[252:255], a[148:151], v[248:251]\n"
"v_accvgpr_read_b32 v41, a5\n"
"v_mfma_f32_16x16x32_bf16 v[252:255], a[248:251], a[152:155], v[252:255]\n"
"ds_read_b64_tr_b16 a[244:245], v15 offset:36864\n"
"v_accvgpr_read_b32 v42, a6\n"
"v_mfma_f32_16x16x32_bf16 v[252:255], a[252:255], a[156:159], v[252:255]\n"
"ds_read_b64_tr_b16 a[246:247], v15 offset:43008\n"
"v_mfma_f32_16x16x32_bf16 v[64:67], a[224:227], a[160:163], v[64:67]\n"
"buffer_store_dwordx4 a[48:51], v1, s[36:39], s33 offen offset:3072 sc1\n"
"v_mfma_f32_16x16x32_bf16 v[64:67], a[228:231], a[164:167], v[64:67]\n"
"s_add_u32 s33, s33, 4096\n"
"ds_read_b64_tr_b16 a[32:33], v37 offset:192\n"
"v_mfma_f32_16x16x32_bf16 v[68:71], a[224:227], a[168:171], v[68:71]\n"
"ds_read_b64_tr_b16 a[34:35], v37 offset:24768\n"
"v_accvgpr_read_b32 v43, a7\n"
"v_mfma_f32_16x16x32_bf16 v[68:71], a[228:231], a[172:175], v[68:71]\n"
"ds_read_b64_tr_b16 a[248:249], v15 offset:24584\n"
"v_mul_f32 v44, v40, v40\n"
"v_mfma_f32_16x16x32_bf16 v[72:75], a[224:227], a[176:179], v[72:75]\n"
"ds_read_b64_tr_b16 a[250:251], v15 offset:30728\n"
"v_mul_f32 v45, v41, v41\n"
"v_mfma_f32_16x16x32_bf16 v[72:75], a[228:231], a[180:183], v[72:75]\n"
"buffer_store_dwordx4 a[0:3], v1, s[36:39], s33 offen offset:0 sc1\n"
"v_mfma_f32_16x16x32_bf16 v[76:79], a[224:227], a[184:187], v[76:79]\n"
"ds_read_b64_tr_b16 a[252:253], v15 offset:36872\n"
"v_mfma_f32_16x16x32_bf16 v[76:79], a[228:231], a[188:191], v[76:79]\n"
"ds_read_b64_tr_b16 a[254:255], v15 offset:43016\n"
"v_mul_f32 v46, v42, v42\n"
"v_mfma_f32_16x16x32_bf16 v[80:83], a[232:235], a[160:163], v[80:83]\n"
"v_exp_f32 v60, v60\n"
"v_mfma_f32_16x16x32_bf16 v[80:83], a[236:239], a[164:167], v[80:83]\n"
"v_exp_f32 v61, v61\n"
"v_mfma_f32_16x16x32_bf16 v[84:87], a[232:235], a[168:171], v[84:87]\n"
"buffer_load_dwordx4 v6, s[40:43], s82 offen offset:128 lds\n"
"v_mfma_f32_16x16x32_bf16 v[84:87], a[236:239], a[172:175], v[84:87]\n"
"s_add_u32 m0, s83, 78592\n"
"v_exp_f32 v62, v62\n"
"v_mfma_f32_16x16x32_bf16 v[88:91], a[232:235], a[176:179], v[88:91]\n"
"v_exp_f32 v63, v63\n"
"v_mfma_f32_16x16x32_bf16 v[88:91], a[236:239], a[180:183], v[88:91]\n"
"s_waitcnt lgkmcnt(6)\n"
"v_add_f32 v60, 1.0, v60\n"
"v_add_f32 v61, 1.0, v61\n"
"v_mfma_f32_16x16x32_bf16 v[92:95], a[232:235], a[184:187], v[92:95]\n"
"ds_read_b64_tr_b16 a[224:225], v14 offset:25600\n"
"v_add_f32 v62, 1.0, v62\n"
"v_mfma_f32_16x16x32_bf16 v[92:95], a[236:239], a[188:191], v[92:95]\n"
"buffer_store_dwordx4 a[16:19], v1, s[36:39], s33 offen offset:1024 sc1\n"
"v_mfma_f32_16x16x32_bf16 v[96:99], a[240:243], a[160:163], v[96:99]\n"
"ds_read_b64_tr_b16 a[226:227], v14 offset:31744\n"
"v_add_f32 v63, 1.0, v63\n"
"v_mfma_f32_16x16x32_bf16 v[96:99], a[244:247], a[164:167], v[96:99]\n"
"v_rcp_f32 v60, v60\n"
"v_mfma_f32_16x16x32_bf16 v[100:103], a[240:243], a[168:171], v[100:103]\n"
"v_rcp_f32 v61, v61\n"
"v_mfma_f32_16x16x32_bf16 v[100:103], a[244:247], a[172:175], v[100:103]\n"
"v_rcp_f32 v62, v62\n"
"v_mfma_f32_16x16x32_bf16 v[104:107], a[240:243], a[176:179], v[104:107]\n"
"v_rcp_f32 v63, v63\n"
"v_mfma_f32_16x16x32_bf16 v[104:107], a[244:247], a[180:183], v[104:107]\n"
"s_waitcnt lgkmcnt(2)\n"
"v_mul_f32 v56, v56, v60\n"
"v_mul_f32 v57, v57, v61\n"
"v_mul_f32 v58, v58, v62\n"
"v_mul_f32 v59, v59, v63\n"
"v_mfma_f32_16x16x32_bf16 v[108:111], a[240:243], a[184:187], v[108:111]\n"
"v_accvgpr_read_b32 v60, a80\n"
"v_accvgpr_read_b32 v61, a81\n"
"v_accvgpr_read_b32 v62, a82\n"
"v_accvgpr_read_b32 v63, a83\n"
"v_mfma_f32_16x16x32_bf16 v[108:111], a[244:247], a[188:191], v[108:111]\n"
"v_mul_f32 v56, v56, v60\n"
"v_mul_f32 v57, v57, v61\n"
"v_mul_f32 v58, v58, v62\n"
"v_mul_f32 v59, v59, v63\n"
"v_mfma_f32_16x16x32_bf16 v[112:115], a[248:251], a[160:163], v[112:115]\n"
"v_cvt_pk_bf16_f32 v56, v56, v57\n"
"v_cvt_pk_bf16_f32 v57, v58, v59\n"
"v_mfma_f32_16x16x32_bf16 v[112:115], a[252:255], a[164:167], v[112:115]\n"
"ds_read_b64_tr_b16 a[228:229], v14 offset:37888\n"
"ds_read_b64_tr_b16 a[230:231], v14 offset:44032\n"
"ds_read_b64_tr_b16 a[48:49], v38 offset:224\n"
"ds_read_b64_tr_b16 a[50:51], v38 offset:24800\n"
"v_mfma_f32_16x16x32_bf16 v[116:119], a[248:251], a[168:171], v[116:119]\n"
"buffer_store_dwordx4 a[32:35], v1, s[36:39], s33 offen offset:2048 sc1\n"
"v_mfma_f32_16x16x32_bf16 v[116:119], a[252:255], a[172:175], v[116:119]\n"
"ds_read_b64_tr_b16 a[232:233], v14 offset:25608\n"
"v_lshlrev_b32 v60, 3, v0\n"
"v_mfma_f32_16x16x32_bf16 v[120:123], a[248:251], a[176:179], v[120:123]\n"
"ds_read_b64_tr_b16 a[234:235], v14 offset:31752\n"
"v_mul_f32 v47, v43, v43\n"
"v_mfma_f32_16x16x32_bf16 v[120:123], a[252:255], a[180:183], v[120:123]\n"
"ds_read_b64_tr_b16 a[236:237], v14 offset:37896\n"
"v_fma_f32 v44, v44, s99, v39\n"
"v_mfma_f32_16x16x32_bf16 v[124:127], a[248:251], a[184:187], v[124:127]\n"
"buffer_store_dwordx2 v[56:57], v60, s[36:39], s6 offen offset:0 sc1\n"
"v_mfma_f32_16x16x32_bf16 v[124:127], a[252:255], a[188:191], v[124:127]\n"
"s_waitcnt lgkmcnt(7)\n"
"ds_read_b64_tr_b16 a[238:239], v14 offset:44040\n"
"v_mfma_f32_16x16x32_bf16 v[128:131], a[224:227], a[160:163], v[128:131]\n"
"ds_read_b64_tr_b16 a[0:1], v37 offset:256\n"
"s_waitcnt lgkmcnt(5)\n"
"v_mfma_f32_16x16x32_bf16 v[128:131], a[228:231], a[164:167], v[128:131]\n"
"buffer_load_dwordx4 v6, s[40:43], s82 offen offset:256 lds\n"
"s_add_u32 s82, s82, 786432\n"
"ds_read_b64_tr_b16 a[2:3], v37 offset:24832\n"
"v_mfma_f32_16x16x32_bf16 v[132:135], a[224:227], a[168:171], v[132:135]\n"
"ds_read_b64_tr_b16 a[240:241], v15 offset:25600\n"
"v_fma_f32 v45, v45, s99, v39\n"
"v_mfma_f32_16x16x32_bf16 v[132:135], a[228:231], a[172:175], v[132:135]\n"
"buffer_store_dwordx4 a[48:51], v1, s[36:39], s33 offen offset:3072 sc1\n"
"v_mfma_f32_16x16x32_bf16 v[136:139], a[224:227], a[176:179], v[136:139]\n"
"ds_read_b64_tr_b16 a[242:243], v15 offset:31744\n"
"s_add_u32 s33, s33, 4096\n"
"v_mfma_f32_16x16x32_bf16 v[136:139], a[228:231], a[180:183], v[136:139]\n"
"ds_read_b64_tr_b16 a[244:245], v15 offset:37888\n"
"v_mfma_f32_16x16x32_bf16 v[140:143], a[224:227], a[184:187], v[140:143]\n"
"ds_read_b64_tr_b16 a[246:247], v15 offset:44032\n"
"v_fma_f32 v46, v46, s99, v39\n"
"v_mfma_f32_16x16x32_bf16 v[140:143], a[228:231], a[188:191], v[140:143]\n"
"ds_read_b64_tr_b16 a[16:17], v38 offset:288\n"
"s_waitcnt lgkmcnt(5)\n"
"v_mfma_f32_16x16x32_bf16 v[144:147], a[232:235], a[160:163], v[144:147]\n"
"ds_read_b64_tr_b16 a[18:19], v38 offset:24864\n"
"v_fma_f32 v47, v47, s99, v39\n"
"v_mfma_f32_16x16x32_bf16 v[144:147], a[236:239], a[164:167], v[144:147]\n"
"ds_read_b64_tr_b16 a[248:249], v15 offset:25608\n"
"v_mul_f32 v44, v44, v40\n"
"v_mfma_f32_16x16x32_bf16 v[148:151], a[232:235], a[168:171], v[148:151]\n"
"ds_read_b64_tr_b16 a[250:251], v15 offset:31752\n"
"v_mul_f32 v45, v45, v41\n"
"v_mfma_f32_16x16x32_bf16 v[148:151], a[236:239], a[172:175], v[148:151]\n"
"buffer_store_dwordx4 a[0:3], v1, s[36:39], s33 offen offset:0 sc1\n"
"v_mfma_f32_16x16x32_bf16 v[152:155], a[232:235], a[176:179], v[152:155]\n"
"ds_read_b64_tr_b16 a[252:253], v15 offset:37896\n"
"v_mul_f32 v46, v46, v42\n"
"v_mfma_f32_16x16x32_bf16 v[152:155], a[236:239], a[180:183], v[152:155]\n"
"v_mul_f32 v47, v47, v43\n"
"v_accvgpr_read_b32 v48, a36\n"
"v_mfma_f32_16x16x32_bf16 v[156:159], a[232:235], a[184:187], v[156:159]\n"
"s_waitcnt lgkmcnt(3)\n"
"v_exp_f32 v44, v44\n"
"v_mfma_f32_16x16x32_bf16 v[156:159], a[236:239], a[188:191], v[156:159]\n"
"v_exp_f32 v45, v45\n"
"v_mfma_f32_16x16x32_bf16 v[160:163], a[240:243], a[160:163], v[160:163]\n"
"v_exp_f32 v46, v46\n"
"v_exp_f32 v47, v47\n"
"v_mfma_f32_16x16x32_bf16 v[160:163], a[244:247], a[164:167], v[160:163]\n"
"ds_read_b64_tr_b16 a[254:255], v15 offset:44040\n"
"v_add_f32 v44, 1.0, v44\n"
"v_add_f32 v45, 1.0, v45\n"
"v_add_f32 v46, 1.0, v46\n"
"v_add_f32 v47, 1.0, v47\n"
"v_mfma_f32_16x16x32_bf16 v[164:167], a[240:243], a[168:171], v[164:167]\n"
"v_rcp_f32 v44, v44\n"
"v_rcp_f32 v45, v45\n"
"v_rcp_f32 v46, v46\n"
"v_rcp_f32 v47, v47\n"
"v_mfma_f32_16x16x32_bf16 v[164:167], a[244:247], a[172:175], v[164:167]\n"
"v_mul_f32 v40, v40, v44\n"
"v_mul_f32 v41, v41, v45\n"
"v_mul_f32 v42, v42, v46\n"
"v_mul_f32 v43, v43, v47\n"
"v_mfma_f32_16x16x32_bf16 v[168:171], a[240:243], a[176:179], v[168:171]\n"
"buffer_store_dwordx4 a[16:19], v1, s[36:39], s33 offen offset:1024 sc1\n"
"v_accvgpr_read_b32 v44, a20\n"
"v_accvgpr_read_b32 v45, a21\n"
"v_accvgpr_read_b32 v46, a22\n"
"v_accvgpr_read_b32 v47, a23\n"
"v_mfma_f32_16x16x32_bf16 v[168:171], a[244:247], a[180:183], v[168:171]\n"
"v_mul_f32 v40, v40, v44\n"
"v_mul_f32 v41, v41, v45\n"
"v_mul_f32 v42, v42, v46\n"
"v_mul_f32 v43, v43, v47\n"
"v_mfma_f32_16x16x32_bf16 v[172:175], a[240:243], a[184:187], v[172:175]\n"
"v_cvt_pk_bf16_f32 v28, v40, v41\n"
"v_cvt_pk_bf16_f32 v29, v42, v43\n"
"v_mfma_f32_16x16x32_bf16 v[172:175], a[244:247], a[188:191], v[172:175]\n"
"s_waitcnt lgkmcnt(0)\n"
"ds_read_b64_tr_b16 a[224:225], v14 offset:26624\n"
"v_mfma_f32_16x16x32_bf16 v[176:179], a[248:251], a[160:163], v[176:179]\n"
"s_waitcnt vmcnt(24)\n"
"s_barrier\n"
"s_add_u32 m0, s83, 0\n"
"v_mfma_f32_16x16x32_bf16 v[176:179], a[252:255], a[164:167], v[176:179]\n"
"ds_read_b64_tr_b16 a[226:227], v14 offset:32768\n"
"ds_read_b64_tr_b16 a[228:229], v14 offset:38912\n"
"ds_read_b64_tr_b16 a[230:231], v14 offset:45056\n"
"v_mfma_f32_16x16x32_bf16 v[180:183], a[248:251], a[168:171], v[180:183]\n"
"buffer_load_dwordx4 v6, s[40:43], s81 offen offset:0 lds\n"
"v_mfma_f32_16x16x32_bf16 v[180:183], a[252:255], a[172:175], v[180:183]\n"
"s_add_u32 m0, s83, 896\n"
"ds_read_b64_tr_b16 a[232:233], v14 offset:26632\n"
"v_mfma_f32_16x16x32_bf16 v[184:187], a[248:251], a[176:179], v[184:187]\n"
"ds_read_b64_tr_b16 a[234:235], v14 offset:32776\n"
"v_accvgpr_read_b32 v49, a37\n"
"v_mfma_f32_16x16x32_bf16 v[184:187], a[252:255], a[180:183], v[184:187]\n"
"ds_read_b64_tr_b16 a[236:237], v14 offset:38920\n"
"v_accvgpr_read_b32 v50, a38\n"
"v_mfma_f32_16x16x32_bf16 v[188:191], a[248:251], a[184:187], v[188:191]\n"
"buffer_load_dwordx4 v6, s[40:43], s81 offen offset:128 lds\n"
"v_mfma_f32_16x16x32_bf16 v[188:191], a[252:255], a[188:191], v[188:191]\n"
"s_waitcnt lgkmcnt(3)\n"
"s_add_u32 m0, s83, 1792\n"
"v_accvgpr_read_b32 v51, a39\n"
"v_mfma_f32_16x16x32_bf16 v[192:195], a[224:227], a[160:163], v[192:195]\n"
"ds_read_b64_tr_b16 a[238:239], v14 offset:45064\n"
"v_mul_f32 v52, v48, v48\n"
"v_mfma_f32_16x16x32_bf16 v[192:195], a[228:231], a[164:167], v[192:195]\n"
"ds_read_b64_tr_b16 a[240:241], v15 offset:26624\n"
"v_mul_f32 v53, v49, v49\n"
"v_mfma_f32_16x16x32_bf16 v[196:199], a[224:227], a[168:171], v[196:199]\n"
"ds_read_b64_tr_b16 a[242:243], v15 offset:32768\n"
"v_mul_f32 v54, v50, v50\n"
"v_mfma_f32_16x16x32_bf16 v[196:199], a[228:231], a[172:175], v[196:199]\n"
"buffer_load_dwordx4 v6, s[40:43], s81 offen offset:256 lds\n"
"v_mfma_f32_16x16x32_bf16 v[200:203], a[224:227], a[176:179], v[200:203]\n"
"s_add_u32 m0, s83, 3072\n"
"ds_read_b64_tr_b16 a[244:245], v15 offset:38912\n"
"v_mfma_f32_16x16x32_bf16 v[200:203], a[228:231], a[180:183], v[200:203]\n"
"ds_read_b64_tr_b16 a[246:247], v15 offset:45056\n"
"v_mul_f32 v55, v51, v51\n"
"v_mfma_f32_16x16x32_bf16 v[204:207], a[224:227], a[184:187], v[204:207]\n"
"s_waitcnt lgkmcnt(4)\n"
"v_fma_f32 v52, v52, s99, v39\n"
"v_fma_f32 v53, v53, s99, v39\n"
"v_mfma_f32_16x16x32_bf16 v[204:207], a[228:231], a[188:191], v[204:207]\n"
"ds_read_b64_tr_b16 a[248:249], v15 offset:26632\n"
"v_fma_f32 v54, v54, s99, v39\n"
"v_mfma_f32_16x16x32_bf16 v[208:211], a[232:235], a[160:163], v[208:211]\n"
"buffer_load_dwordx4 v6, s[40:43], s82 offen offset:0 lds\n"
"v_mfma_f32_16x16x32_bf16 v[208:211], a[236:239], a[164:167], v[208:211]\n"
"s_add_u32 m0, s83, 3968\n"
"v_fma_f32 v55, v55, s99, v39\n"
"v_mul_f32 v52, v52, v48\n"
"v_mfma_f32_16x16x32_bf16 v[212:215], a[232:235], a[168:171], v[212:215]\n"
"ds_read_b64_tr_b16 a[250:251], v15 offset:32776\n"
"v_mul_f32 v53, v53, v49\n"
"v_mfma_f32_16x16x32_bf16 v[212:215], a[236:239], a[172:175], v[212:215]\n"
"ds_read_b64_tr_b16 a[252:253], v15 offset:38920\n"
"v_mul_f32 v54, v54, v50\n"
"v_mfma_f32_16x16x32_bf16 v[216:219], a[232:235], a[176:179], v[216:219]\n"
"ds_read_b64_tr_b16 a[254:255], v15 offset:45064\n"
"v_mul_f32 v55, v55, v51\n"
"v_mfma_f32_16x16x32_bf16 v[216:219], a[236:239], a[180:183], v[216:219]\n"
"buffer_load_dwordx4 v6, s[40:43], s82 offen offset:128 lds\n"
"v_mfma_f32_16x16x32_bf16 v[220:223], a[232:235], a[184:187], v[220:223]\n"
"s_waitcnt lgkmcnt(4)\n"
"v_exp_f32 v52, v52\n"
"v_exp_f32 v53, v53\n"
"v_exp_f32 v54, v54\n"
"v_exp_f32 v55, v55\n"
"v_mfma_f32_16x16x32_bf16 v[220:223], a[236:239], a[188:191], v[220:223]\n"
"s_add_u32 m0, s83, 4864\n"
"v_add_f32 v52, 1.0, v52\n"
"v_add_f32 v53, 1.0, v53\n"
"v_add_f32 v54, 1.0, v54\n"
"v_add_f32 v55, 1.0, v55\n"
"v_mfma_f32_16x16x32_bf16 v[224:227], a[240:243], a[160:163], v[224:227]\n"
"v_rcp_f32 v52, v52\n"
"v_rcp_f32 v53, v53\n"
"v_rcp_f32 v54, v54\n"
"v_rcp_f32 v55, v55\n"
"v_mfma_f32_16x16x32_bf16 v[224:227], a[244:247], a[164:167], v[224:227]\n"
"v_mul_f32 v48, v48, v52\n"
"v_mul_f32 v49, v49, v53\n"
"v_mul_f32 v50, v50, v54\n"
"v_mul_f32 v51, v51, v55\n"
"v_mfma_f32_16x16x32_bf16 v[228:231], a[240:243], a[168:171], v[228:231]\n"
"v_accvgpr_read_b32 v52, a52\n"
"v_accvgpr_read_b32 v53, a53\n"
"v_accvgpr_read_b32 v54, a54\n"
"v_accvgpr_read_b32 v55, a55\n"
"v_mfma_f32_16x16x32_bf16 v[228:231], a[244:247], a[172:175], v[228:231]\n"
"v_mul_f32 v48, v48, v52\n"
"v_mul_f32 v49, v49, v53\n"
"v_mul_f32 v50, v50, v54\n"
"v_mul_f32 v51, v51, v55\n"
"v_mfma_f32_16x16x32_bf16 v[232:235], a[240:243], a[176:179], v[232:235]\n"
"v_cvt_pk_bf16_f32 v30, v48, v49\n"
"v_cvt_pk_bf16_f32 v31, v50, v51\n"
"v_mfma_f32_16x16x32_bf16 v[232:235], a[244:247], a[180:183], v[232:235]\n"
"buffer_store_dwordx4 v[28:31], v1, s[36:39], s5 offen offset:1024 sc1\n"
"v_mfma_f32_16x16x32_bf16 v[236:239], a[240:243], a[184:187], v[236:239]\n"
"s_waitcnt lgkmcnt(0)\n"
"ds_read_b64_tr_b16 a[224:225], v12 offset:0\n"
"v_mfma_f32_16x16x32_bf16 v[236:239], a[244:247], a[188:191], v[236:239]\n"
"ds_read_b64_tr_b16 a[226:227], v12 offset:6144\n"
"v_accvgpr_read_b32 v56, a68\n"
"v_mfma_f32_16x16x32_bf16 v[240:243], a[248:251], a[160:163], v[240:243]\n"
"ds_read_b64_tr_b16 a[228:229], v12 offset:12288\n"
"v_accvgpr_read_b32 v57, a69\n"
"v_mfma_f32_16x16x32_bf16 v[240:243], a[252:255], a[164:167], v[240:243]\n"
"ds_read_b64_tr_b16 a[230:231], v12 offset:18432\n"
"v_accvgpr_read_b32 v58, a70\n"
"v_mfma_f32_16x16x32_bf16 v[244:247], a[248:251], a[168:171], v[244:247]\n"
"buffer_load_dwordx4 v6, s[40:43], s82 offen offset:256 lds\n"
"v_mfma_f32_16x16x32_bf16 v[244:247], a[252:255], a[172:175], v[244:247]\n"
"s_add_u32 s84, s83, 0\n"
"s_add_u32 m0, s83, 98304\n"
"v_accvgpr_read_b32 v59, a71\n"
"v_mfma_f32_16x16x32_bf16 v[248:251], a[248:251], a[176:179], v[248:251]\n"
"ds_read_b64_tr_b16 a[232:233], v12 offset:8\n"
"v_mul_f32 v60, v56, v56\n"
"v_mfma_f32_16x16x32_bf16 v[248:251], a[252:255], a[180:183], v[248:251]\n"
"ds_read_b64_tr_b16 a[234:235], v12 offset:6152\n"
"v_mfma_f32_16x16x32_bf16 v[252:255], a[248:251], a[184:187], v[252:255]\n"
"buffer_load_dwordx4 a[128:131], v2, s[36:39], s80 offen offset:0 sc1\n"
"v_mfma_f32_16x16x32_bf16 v[252:255], a[252:255], a[188:191], v[252:255]\n"
"s_waitcnt lgkmcnt(2)\n"
"ds_read_b64_tr_b16 a[236:237], v12 offset:12296\n"
"v_mfma_f32_16x16x32_bf16 v[64:67], a[224:227], a[192:195], v[64:67]\n"
"ds_read_b64_tr_b16 a[238:239], v12 offset:18440\n"
"v_mul_f32 v61, v57, v57\n"
"v_mfma_f32_16x16x32_bf16 v[64:67], a[228:231], a[196:199], v[64:67]\n"
"ds_read_b64_tr_b16 a[240:241], v13 offset:0\n"
"v_mul_f32 v62, v58, v58\n"
"v_mfma_f32_16x16x32_bf16 v[68:71], a[224:227], a[200:203], v[68:71]\n"
"ds_read_b64_tr_b16 a[242:243], v13 offset:6144\n"
"v_mul_f32 v63, v59, v59\n"
"v_mfma_f32_16x16x32_bf16 v[68:71], a[228:231], a[204:207], v[68:71]\n"
"buffer_load_dwordx4 a[132:135], v2, s[36:39], s80 offen offset:1024 sc1\n"
"v_mfma_f32_16x16x32_bf16 v[72:75], a[224:227], a[208:211], v[72:75]\n"
"ds_read_b64_tr_b16 a[244:245], v13 offset:12288\n"
"v_fma_f32 v60, v60, s99, v39\n"
"v_mfma_f32_16x16x32_bf16 v[72:75], a[228:231], a[212:215], v[72:75]\n"
"ds_read_b64_tr_b16 a[246:247], v13 offset:18432\n"
"v_fma_f32 v61, v61, s99, v39\n"
"v_mfma_f32_16x16x32_bf16 v[76:79], a[224:227], a[216:219], v[76:79]\n"
"s_waitcnt lgkmcnt(4)\n"
"v_fma_f32 v62, v62, s99, v39\n"
"v_fma_f32 v63, v63, s99, v39\n"
"v_mfma_f32_16x16x32_bf16 v[76:79], a[228:231], a[220:223], v[76:79]\n"
"ds_read_b64_tr_b16 a[248:249], v13 offset:8\n"
"v_mul_f32 v60, v60, v56\n"
"v_mfma_f32_16x16x32_bf16 v[80:83], a[232:235], a[192:195], v[80:83]\n"
"buffer_load_dwordx4 a[136:139], v3, s[36:39], s80 offen offset:0 sc1\n"
"v_mfma_f32_16x16x32_bf16 v[80:83], a[236:239], a[196:199], v[80:83]\n"
"ds_read_b64_tr_b16 a[250:251], v13 offset:6152\n"
"v_mul_f32 v61, v61, v57\n"
"v_mfma_f32_16x16x32_bf16 v[84:87], a[232:235], a[200:203], v[84:87]\n"
"ds_read_b64_tr_b16 a[252:253], v13 offset:12296\n"
"v_mul_f32 v62, v62, v58\n"
"v_mfma_f32_16x16x32_bf16 v[84:87], a[236:239], a[204:207], v[84:87]\n"
"ds_read_b64_tr_b16 a[254:255], v13 offset:18440\n"
"v_mul_f32 v63, v63, v59\n"
"v_mfma_f32_16x16x32_bf16 v[88:91], a[232:235], a[208:211], v[88:91]\n"
"v_exp_f32 v60, v60\n"
"v_mfma_f32_16x16x32_bf16 v[88:91], a[236:239], a[212:215], v[88:91]\n"
"s_waitcnt lgkmcnt(4)\n"
"v_exp_f32 v61, v61\n"
"v_exp_f32 v62, v62\n"
"v_exp_f32 v63, v63\n"
"v_mfma_f32_16x16x32_bf16 v[92:95], a[232:235], a[216:219], v[92:95]\n"
"v_add_f32 v60, 1.0, v60\n"
"v_add_f32 v61, 1.0, v61\n"
"v_add_f32 v62, 1.0, v62\n"
"v_add_f32 v63, 1.0, v63\n"
"v_mfma_f32_16x16x32_bf16 v[92:95], a[236:239], a[220:223], v[92:95]\n"
"v_rcp_f32 v60, v60\n"
"v_rcp_f32 v61, v61\n"
"v_rcp_f32 v62, v62\n"
"v_rcp_f32 v63, v63\n"
"v_mfma_f32_16x16x32_bf16 v[96:99], a[240:243], a[192:195], v[96:99]\n"
"v_mul_f32 v56, v56, v60\n"
"v_mul_f32 v57, v57, v61\n"
"v_mul_f32 v58, v58, v62\n"
"v_mul_f32 v59, v59, v63\n"
"v_mfma_f32_16x16x32_bf16 v[96:99], a[244:247], a[196:199], v[96:99]\n"
"v_accvgpr_read_b32 v60, a84\n"
"v_accvgpr_read_b32 v61, a85\n"
"v_accvgpr_read_b32 v62, a86\n"
"v_accvgpr_read_b32 v63, a87\n"
"v_mfma_f32_16x16x32_bf16 v[100:103], a[240:243], a[200:203], v[100:103]\n"
"v_mul_f32 v56, v56, v60\n"
"v_mul_f32 v57, v57, v61\n"
"v_mul_f32 v58, v58, v62\n"
"v_mul_f32 v59, v59, v63\n"
"v_mfma_f32_16x16x32_bf16 v[100:103], a[244:247], a[204:207], v[100:103]\n"
"v_cvt_pk_bf16_f32 v56, v56, v57\n"
"v_cvt_pk_bf16_f32 v57, v58, v59\n"
"v_mfma_f32_16x16x32_bf16 v[104:107], a[240:243], a[208:211], v[104:107]\n"
"buffer_load_dwordx4 a[140:143], v3, s[36:39], s80 offen offset:1024 sc1\n"
"v_mfma_f32_16x16x32_bf16 v[104:107], a[244:247], a[212:215], v[104:107]\n"
"s_waitcnt lgkmcnt(0)\n"
"v_lshlrev_b32 v60, 3, v0\n"
"v_accvgpr_read_b32 v40, a8\n"
"v_mfma_f32_16x16x32_bf16 v[108:111], a[240:243], a[216:219], v[108:111]\n"
"ds_read_b64_tr_b16 a[224:225], v12 offset:1024\n"
"v_accvgpr_read_b32 v41, a9\n"
"v_mfma_f32_16x16x32_bf16 v[108:111], a[244:247], a[220:223], v[108:111]\n"
"ds_read_b64_tr_b16 a[226:227], v12 offset:7168\n"
"v_accvgpr_read_b32 v42, a10\n"
"v_mfma_f32_16x16x32_bf16 v[112:115], a[248:251], a[192:195], v[112:115]\n"
"buffer_store_dwordx2 v[56:57], v60, s[36:39], s6 offen offset:512 sc1\n"
"v_mfma_f32_16x16x32_bf16 v[112:115], a[252:255], a[196:199], v[112:115]\n"
"ds_read_b64_tr_b16 a[228:229], v12 offset:13312\n"
"ds_read_b64_tr_b16 a[230:231], v12 offset:19456\n"
"v_mfma_f32_16x16x32_bf16 v[116:119], a[248:251], a[200:203], v[116:119]\n"
"ds_read_b64_tr_b16 a[232:233], v12 offset:1032\n"
"v_accvgpr_read_b32 v43, a11\n"
"v_mfma_f32_16x16x32_bf16 v[116:119], a[252:255], a[204:207], v[116:119]\n"
"buffer_load_dwordx4 a[144:147], v4, s[36:39], s80 offen offset:0 sc1\n"
"v_mfma_f32_16x16x32_bf16 v[120:123], a[248:251], a[208:211], v[120:123]\n"
"ds_read_b64_tr_b16 a[234:235], v12 offset:7176\n"
"v_mul_f32 v44, v40, v40\n"
"v_mfma_f32_16x16x32_bf16 v[120:123], a[252:255], a[212:215], v[120:123]\n"
"ds_read_b64_tr_b16 a[236:237], v12 offset:13320\n"
"v_mul_f32 v45, v41, v41\n"
"v_mfma_f32_16x16x32_bf16 v[124:127], a[248:251], a[216:219], v[124:127]\n"
"ds_read_b64_tr_b16 a[238:239], v12 offset:19464\n"
"v_mul_f32 v46, v42, v42\n"
"v_mfma_f32_16x16x32_bf16 v[124:127], a[252:255], a[220:223], v[124:127]\n"
"s_waitcnt lgkmcnt(4)\n"
"v_mul_f32 v47, v43, v43\n"
"v_fma_f32 v44, v44, s99, v39\n"
"v_mfma_f32_16x16x32_bf16 v[128:131], a[224:227], a[192:195], v[128:131]\n"
"buffer_load_dwordx4 a[148:151], v4, s[36:39], s80 offen offset:1024 sc1\n"
"v_mfma_f32_16x16x32_bf16 v[128:131], a[228:231], a[196:199], v[128:131]\n"
"ds_read_b64_tr_b16 a[240:241], v13 offset:1024\n"
"v_fma_f32 v45, v45, s99, v39\n"
"v_mfma_f32_16x16x32_bf16 v[132:135], a[224:227], a[200:203], v[132:135]\n"
"ds_read_b64_tr_b16 a[242:243], v13 offset:7168\n"
"v_fma_f32 v46, v46, s99, v39\n"
"v_mfma_f32_16x16x32_bf16 v[132:135], a[228:231], a[204:207], v[132:135]\n"
"ds_read_b64_tr_b16 a[244:245], v13 offset:13312\n"
"v_fma_f32 v47, v47, s99, v39\n"
"v_mfma_f32_16x16x32_bf16 v[136:139], a[224:227], a[208:211], v[136:139]\n"
"ds_read_b64_tr_b16 a[246:247], v13 offset:19456\n"
"v_mul_f32 v44, v44, v40\n"
"v_mfma_f32_16x16x32_bf16 v[136:139], a[228:231], a[212:215], v[136:139]\n"
"buffer_load_dwordx4 a[152:155], v5, s[36:39], s80 offen offset:0 sc1\n"
"v_mfma_f32_16x16x32_bf16 v[140:143], a[224:227], a[216:219], v[140:143]\n"
"s_waitcnt lgkmcnt(4)\n"
"v_mul_f32 v45, v45, v41\n"
"v_mul_f32 v46, v46, v42\n"
"v_mfma_f32_16x16x32_bf16 v[140:143], a[228:231], a[220:223], v[140:143]\n"
"ds_read_b64_tr_b16 a[248:249], v13 offset:1032\n"
"v_mul_f32 v47, v47, v43\n"
"v_mfma_f32_16x16x32_bf16 v[144:147], a[232:235], a[192:195], v[144:147]\n"
"v_exp_f32 v44, v44\n"
"v_mfma_f32_16x16x32_bf16 v[144:147], a[236:239], a[196:199], v[144:147]\n"
"v_exp_f32 v45, v45\n"
"v_mfma_f32_16x16x32_bf16 v[148:151], a[232:235], a[200:203], v[148:151]\n"
"buffer_load_dwordx4 a[156:159], v5, s[36:39], s80 offen offset:1024 sc1\n"
"v_mfma_f32_16x16x32_bf16 v[148:151], a[236:239], a[204:207], v[148:151]\n"
"v_exp_f32 v46, v46\n"
"v_exp_f32 v47, v47\n"
"v_mfma_f32_16x16x32_bf16 v[152:155], a[232:235], a[208:211], v[152:155]\n"
"v_add_f32 v44, 1.0, v44\n"
"v_add_f32 v45, 1.0, v45\n"
"v_add_f32 v46, 1.0, v46\n"
"v_add_f32 v47, 1.0, v47\n"
"v_mfma_f32_16x16x32_bf16 v[152:155], a[236:239], a[212:215], v[152:155]\n"
"s_waitcnt lgkmcnt(1)\n"
"v_rcp_f32 v44, v44\n"
"v_rcp_f32 v45, v45\n"
"v_rcp_f32 v46, v46\n"
"v_rcp_f32 v47, v47\n"
"v_mfma_f32_16x16x32_bf16 v[156:159], a[232:235], a[216:219], v[156:159]\n"
"v_mul_f32 v40, v40, v44\n"
"v_mul_f32 v41, v41, v45\n"
"v_mul_f32 v42, v42, v46\n"
"v_mul_f32 v43, v43, v47\n"
"v_mfma_f32_16x16x32_bf16 v[156:159], a[236:239], a[220:223], v[156:159]\n"
"v_accvgpr_read_b32 v44, a24\n"
"v_accvgpr_read_b32 v45, a25\n"
"v_accvgpr_read_b32 v46, a26\n"
"v_accvgpr_read_b32 v47, a27\n"
"v_mfma_f32_16x16x32_bf16 v[160:163], a[240:243], a[192:195], v[160:163]\n"
"v_mul_f32 v40, v40, v44\n"
"v_mul_f32 v41, v41, v45\n"
"v_mul_f32 v42, v42, v46\n"
"v_mul_f32 v43, v43, v47\n"
"v_mfma_f32_16x16x32_bf16 v[160:163], a[244:247], a[196:199], v[160:163]\n"
"ds_read_b64_tr_b16 a[250:251], v13 offset:7176\n"
"ds_read_b64_tr_b16 a[252:253], v13 offset:13320\n"
"ds_read_b64_tr_b16 a[254:255], v13 offset:19464\n"
"v_cvt_pk_bf16_f32 v28, v40, v41\n"
"v_cvt_pk_bf16_f32 v29, v42, v43\n"
"v_mfma_f32_16x16x32_bf16 v[164:167], a[240:243], a[200:203], v[164:167]\n"
"buffer_load_dwordx4 v8, s[44:47], s84 offen offset:0 lds\n"
"v_mfma_f32_16x16x32_bf16 v[164:167], a[244:247], a[204:207], v[164:167]\n"
"ds_read_b64_tr_b16 a[224:225], v12 offset:2048\n"
"v_accvgpr_read_b32 v48, a40\n"
"v_mfma_f32_16x16x32_bf16 v[168:171], a[240:243], a[208:211], v[168:171]\n"
"ds_read_b64_tr_b16 a[226:227], v12 offset:8192\n"
"v_accvgpr_read_b32 v49, a41\n"
"v_mfma_f32_16x16x32_bf16 v[168:171], a[244:247], a[212:215], v[168:171]\n"
"ds_read_b64_tr_b16 a[228:229], v12 offset:14336\n"
"v_accvgpr_read_b32 v50, a42\n"
"v_mfma_f32_16x16x32_bf16 v[172:175], a[240:243], a[216:219], v[172:175]\n"
"buffer_load_dwordx4 v9, s[44:47], s84 offen offset:3072 lds\n"
"v_mfma_f32_16x16x32_bf16 v[172:175], a[244:247], a[220:223], v[172:175]\n"
"s_waitcnt lgkmcnt(3)\n"
"s_add_u32 m0, s83, 99200\n"
"v_accvgpr_read_b32 v51, a43\n"
"v_mfma_f32_16x16x32_bf16 v[176:179], a[248:251], a[192:195], v[176:179]\n"
"s_waitcnt vmcnt(21)\n"
"s_barrier\n"
"v_mul_f32 v52, v48, v48\n"
"v_mfma_f32_16x16x32_bf16 v[176:179], a[252:255], a[196:199], v[176:179]\n"
"ds_read_b64_tr_b16 a[230:231], v12 offset:20480\n"
"v_mul_f32 v53, v49, v49\n"
"v_mfma_f32_16x16x32_bf16 v[180:183], a[248:251], a[200:203], v[180:183]\n"
"ds_read_b64_tr_b16 a[232:233], v12 offset:2056\n"
"v_mul_f32 v54, v50, v50\n"
"v_mfma_f32_16x16x32_bf16 v[180:183], a[252:255], a[204:207], v[180:183]\n"
"buffer_load_dwordx4 v8, s[44:47], s84 offen offset:128 lds\n"
"v_mfma_f32_16x16x32_bf16 v[184:187], a[248:251], a[208:211], v[184:187]\n"
"ds_read_b64_tr_b16 a[234:235], v12 offset:8200\n"
"v_mul_f32 v55, v51, v51\n"
"v_mfma_f32_16x16x32_bf16 v[184:187], a[252:255], a[212:215], v[184:187]\n"
"ds_read_b64_tr_b16 a[236:237], v12 offset:14344\n"
"v_fma_f32 v52, v52, s99, v39\n"
"v_mfma_f32_16x16x32_bf16 v[188:191], a[248:251], a[216:219], v[188:191]\n"
"ds_read_b64_tr_b16 a[238:239], v12 offset:20488\n"
"v_fma_f32 v53, v53, s99, v39\n"
"v_mfma_f32_16x16x32_bf16 v[188:191], a[252:255], a[220:223], v[188:191]\n"
"s_waitcnt lgkmcnt(4)\n"
"v_fma_f32 v54, v54, s99, v39\n"
"v_fma_f32 v55, v55, s99, v39\n"
"v_mfma_f32_16x16x32_bf16 v[192:195], a[224:227], a[192:195], v[192:195]\n"
"buffer_load_dwordx4 v9, s[44:47], s84 offen offset:3200 lds\n"
"v_mfma_f32_16x16x32_bf16 v[192:195], a[228:231], a[196:199], v[192:195]\n"
"s_add_u32 m0, s83, 100096\n"
"v_mul_f32 v52, v52, v48\n"
"v_mul_f32 v53, v53, v49\n"
"v_mfma_f32_16x16x32_bf16 v[196:199], a[224:227], a[200:203], v[196:199]\n"
"ds_read_b64_tr_b16 a[240:241], v13 offset:2048\n"
"v_mul_f32 v54, v54, v50\n"
"v_mfma_f32_16x16x32_bf16 v[196:199], a[228:231], a[204:207], v[196:199]\n"
"ds_read_b64_tr_b16 a[242:243], v13 offset:8192\n"
"v_mul_f32 v55, v55, v51\n"
"v_mfma_f32_16x16x32_bf16 v[200:203], a[224:227], a[208:211], v[200:203]\n"
"v_exp_f32 v52, v52\n"
"v_mfma_f32_16x16x32_bf16 v[200:203], a[228:231], a[212:215], v[200:203]\n"
"buffer_load_dwordx4 v8, s[44:47], s84 offen offset:256 lds\n"
"v_mfma_f32_16x16x32_bf16 v[204:207], a[224:227], a[216:219], v[204:207]\n"
"s_waitcnt lgkmcnt(2)\n"
"v_exp_f32 v53, v53\n"
"v_mfma_f32_16x16x32_bf16 v[204:207], a[228:231], a[220:223], v[204:207]\n"
"v_exp_f32 v54, v54\n"
"v_mfma_f32_16x16x32_bf16 v[208:211], a[232:235], a[192:195], v[208:211]\n"
"v_exp_f32 v55, v55\n"
"v_mfma_f32_16x16x32_bf16 v[208:211], a[236:239], a[196:199], v[208:211]\n"
"ds_read_b64_tr_b16 a[244:245], v13 offset:14336\n"
"ds_read_b64_tr_b16 a[246:247], v13 offset:20480\n"
"v_mfma_f32_16x16x32_bf16 v[212:215], a[232:235], a[200:203], v[212:215]\n"
"v_add_f32 v52, 1.0, v52\n"
"v_add_f32 v53, 1.0, v53\n"
"v_add_f32 v54, 1.0, v54\n"
"v_add_f32 v55, 1.0, v55\n"
"v_mfma_f32_16x16x32_bf16 v[212:215], a[236:239], a[204:207], v[212:215]\n"
"v_rcp_f32 v52, v52\n"
"v_rcp_f32 v53, v53\n"
"v_rcp_f32 v54, v54\n"
"v_rcp_f32 v55, v55\n"
"v_mfma_f32_16x16x32_bf16 v[216:219], a[232:235], a[208:211], v[216:219]\n"
"v_mul_f32 v48, v48, v52\n"
"v_mul_f32 v49, v49, v53\n"
"v_mul_f32 v50, v50, v54\n"
"v_mul_f32 v51, v51, v55\n"
"v_mfma_f32_16x16x32_bf16 v[216:219], a[236:239], a[212:215], v[216:219]\n"
"v_accvgpr_read_b32 v52, a56\n"
"v_accvgpr_read_b32 v53, a57\n"
"v_accvgpr_read_b32 v54, a58\n"
"v_accvgpr_read_b32 v55, a59\n"
"v_mfma_f32_16x16x32_bf16 v[220:223], a[232:235], a[216:219], v[220:223]\n"
"v_mul_f32 v48, v48, v52\n"
"v_mul_f32 v49, v49, v53\n"
"v_mul_f32 v50, v50, v54\n"
"v_mul_f32 v51, v51, v55\n"
"v_mfma_f32_16x16x32_bf16 v[220:223], a[236:239], a[220:223], v[220:223]\n"
"s_waitcnt lgkmcnt(0)\n"
"v_cvt_pk_bf16_f32 v30, v48, v49\n"
"v_cvt_pk_bf16_f32 v31, v50, v51\n"
"v_mfma_f32_16x16x32_bf16 v[224:227], a[240:243], a[192:195], v[224:227]\n"
"buffer_store_dwordx4 v[28:31], v1, s[36:39], s5 offen offset:2048 sc1\n"
"v_mfma_f32_16x16x32_bf16 v[224:227], a[244:247], a[196:199], v[224:227]\n"
"ds_read_b64_tr_b16 a[248:249], v13 offset:2056\n"
"ds_read_b64_tr_b16 a[250:251], v13 offset:8200\n"
"ds_read_b64_tr_b16 a[252:253], v13 offset:14344\n"
"ds_read_b64_tr_b16 a[254:255], v13 offset:20488\n"
"v_mfma_f32_16x16x32_bf16 v[228:231], a[240:243], a[200:203], v[228:231]\n"
"ds_read_b64_tr_b16 a[224:225], v12 offset:24576\n"
"v_accvgpr_read_b32 v56, a72\n"
"v_mfma_f32_16x16x32_bf16 v[228:231], a[244:247], a[204:207], v[228:231]\n"
"ds_read_b64_tr_b16 a[226:227], v12 offset:30720\n"
"v_accvgpr_read_b32 v57, a73\n"
"v_mfma_f32_16x16x32_bf16 v[232:235], a[240:243], a[208:211], v[232:235]\n"
"buffer_load_dwordx4 v9, s[44:47], s84 offen offset:3328 lds\n"
"v_mfma_f32_16x16x32_bf16 v[232:235], a[244:247], a[212:215], v[232:235]\n"
"s_add_u32 s84, s83, 24576\n"
"s_add_u32 m0, s83, 122880\n"
"v_accvgpr_read_b32 v58, a74\n"
"v_mfma_f32_16x16x32_bf16 v[236:239], a[240:243], a[216:219], v[236:239]\n"
"ds_read_b64_tr_b16 a[228:229], v12 offset:36864\n"
"v_accvgpr_read_b32 v59, a75\n"
"v_mfma_f32_16x16x32_bf16 v[236:239], a[244:247], a[220:223], v[236:239]\n"
"s_waitcnt lgkmcnt(3)\n"
"ds_read_b64_tr_b16 a[230:231], v12 offset:43008\n"
"v_mfma_f32_16x16x32_bf16 v[240:243], a[248:251], a[192:195], v[240:243]\n"
"ds_read_b64_tr_b16 a[232:233], v12 offset:24584\n"
"v_mul_f32 v60, v56, v56\n"
"v_mfma_f32_16x16x32_bf16 v[240:243], a[252:255], a[196:199], v[240:243]\n"
"buffer_load_dwordx4 v8, s[44:47], s84 offen offset:0 lds\n"
"v_mfma_f32_16x16x32_bf16 v[244:247], a[248:251], a[200:203], v[244:247]\n"
"ds_read_b64_tr_b16 a[234:235], v12 offset:30728\n"
"v_mul_f32 v61, v57, v57\n"
"v_mfma_f32_16x16x32_bf16 v[244:247], a[252:255], a[204:207], v[244:247]\n"
"ds_read_b64_tr_b16 a[236:237], v12 offset:36872\n"
"v_mul_f32 v62, v58, v58\n"
"v_mfma_f32_16x16x32_bf16 v[248:251], a[248:251], a[208:211], v[248:251]\n"
"ds_read_b64_tr_b16 a[238:239], v12 offset:43016\n"
"v_mul_f32 v63, v59, v59\n"
"v_mfma_f32_16x16x32_bf16 v[248:251], a[252:255], a[212:215], v[248:251]\n"
"s_waitcnt lgkmcnt(4)\n"
"v_fma_f32 v60, v60, s99, v39\n"
"v_mfma_f32_16x16x32_bf16 v[252:255], a[248:251], a[216:219], v[252:255]\n"
"buffer_load_dwordx4 v9, s[44:47], s84 offen offset:3072 lds\n"
"v_mfma_f32_16x16x32_bf16 v[252:255], a[252:255], a[220:223], v[252:255]\n"
"s_add_u32 m0, s83, 123776\n"
"v_fma_f32 v61, v61, s99, v39\n"
"v_fma_f32 v62, v62, s99, v39\n"
"v_mfma_f32_16x16x32_bf16 v[64:67], a[224:227], a[96:99], v[64:67]\n"
"ds_read_b64_tr_b16 a[240:241], v13 offset:24576\n"
"v_fma_f32 v63, v63, s99, v39\n"
"v_mfma_f32_16x16x32_bf16 v[64:67], a[228:231], a[100:103], v[64:67]\n"
"ds_read_b64_tr_b16 a[242:243], v13 offset:30720\n"
"v_mul_f32 v60, v60, v56\n"
"v_mfma_f32_16x16x32_bf16 v[68:71], a[224:227], a[104:107], v[68:71]\n"
"ds_read_b64_tr_b16 a[244:245], v13 offset:36864\n"
"v_mul_f32 v61, v61, v57\n"
"v_mfma_f32_16x16x32_bf16 v[68:71], a[228:231], a[108:111], v[68:71]\n"
"buffer_load_dwordx4 v8, s[44:47], s84 offen offset:128 lds\n"
"v_mfma_f32_16x16x32_bf16 v[72:75], a[224:227], a[112:115], v[72:75]\n"
"ds_read_b64_tr_b16 a[246:247], v13 offset:43008\n"
"v_mul_f32 v62, v62, v58\n"
"v_mfma_f32_16x16x32_bf16 v[72:75], a[228:231], a[116:119], v[72:75]\n"
"s_waitcnt lgkmcnt(4)\n"
"v_mul_f32 v63, v63, v59\n"
"v_accvgpr_read_b32 v40, a12\n"
"v_mfma_f32_16x16x32_bf16 v[76:79], a[224:227], a[120:123], v[76:79]\n"
"v_exp_f32 v60, v60\n"
"v_mfma_f32_16x16x32_bf16 v[76:79], a[228:231], a[124:127], v[76:79]\n"
"v_exp_f32 v61, v61\n"
"v_exp_f32 v62, v62\n"
"v_exp_f32 v63, v63\n"
"v_mfma_f32_16x16x32_bf16 v[80:83], a[232:235], a[96:99], v[80:83]\n"
"v_add_f32 v60, 1.0, v60\n"
"v_add_f32 v61, 1.0, v61\n"
"v_add_f32 v62, 1.0, v62\n"
"v_add_f32 v63, 1.0, v63\n"
"v_mfma_f32_16x16x32_bf16 v[80:83], a[236:239], a[100:103], v[80:83]\n"
"v_rcp_f32 v60, v60\n"
"v_rcp_f32 v61, v61\n"
"v_rcp_f32 v62, v62\n"
"v_rcp_f32 v63, v63\n"
"v_mfma_f32_16x16x32_bf16 v[84:87], a[232:235], a[104:107], v[84:87]\n"
"v_mul_f32 v56, v56, v60\n"
"v_mul_f32 v57, v57, v61\n"
"v_mul_f32 v58, v58, v62\n"
"v_mul_f32 v59, v59, v63\n"
"v_mfma_f32_16x16x32_bf16 v[84:87], a[236:239], a[108:111], v[84:87]\n"
"v_accvgpr_read_b32 v60, a88\n"
"v_accvgpr_read_b32 v61, a89\n"
"v_accvgpr_read_b32 v62, a90\n"
"v_accvgpr_read_b32 v63, a91\n"
"v_mfma_f32_16x16x32_bf16 v[88:91], a[232:235], a[112:115], v[88:91]\n"
"v_mul_f32 v56, v56, v60\n"
"v_mul_f32 v57, v57, v61\n"
"v_mul_f32 v58, v58, v62\n"
"v_mul_f32 v59, v59, v63\n"
"v_mfma_f32_16x16x32_bf16 v[88:91], a[236:239], a[116:119], v[88:91]\n"
"s_waitcnt lgkmcnt(0)\n"
"v_cvt_pk_bf16_f32 v56, v56, v57\n"
"v_cvt_pk_bf16_f32 v57, v58, v59\n"
"v_mfma_f32_16x16x32_bf16 v[92:95], a[232:235], a[120:123], v[92:95]\n"
"buffer_load_dwordx4 v9, s[44:47], s84 offen offset:3200 lds\n"
"v_mfma_f32_16x16x32_bf16 v[92:95], a[236:239], a[124:127], v[92:95]\n"
"s_add_u32 m0, s83, 124672\n"
"ds_read_b64_tr_b16 a[248:249], v13 offset:24584\n"
"v_mfma_f32_16x16x32_bf16 v[96:99], a[240:243], a[96:99], v[96:99]\n"
"ds_read_b64_tr_b16 a[250:251], v13 offset:30728\n"
"v_lshlrev_b32 v60, 3, v0\n"
"v_mfma_f32_16x16x32_bf16 v[96:99], a[244:247], a[100:103], v[96:99]\n"
"ds_read_b64_tr_b16 a[252:253], v13 offset:36872\n"
"ds_read_b64_tr_b16 a[254:255], v13 offset:43016\n"
"v_mfma_f32_16x16x32_bf16 v[100:103], a[240:243], a[104:107], v[100:103]\n"
"buffer_store_dwordx2 v[56:57], v60, s[36:39], s6 offen offset:1024 sc1\n"
"v_mfma_f32_16x16x32_bf16 v[100:103], a[244:247], a[108:111], v[100:103]\n"
"ds_read_b64_tr_b16 a[224:225], v12 offset:25600\n"
"v_accvgpr_read_b32 v41, a13\n"
"v_mfma_f32_16x16x32_bf16 v[104:107], a[240:243], a[112:115], v[104:107]\n"
"ds_read_b64_tr_b16 a[226:227], v12 offset:31744\n"
"v_accvgpr_read_b32 v42, a14\n"
"v_mfma_f32_16x16x32_bf16 v[104:107], a[244:247], a[116:119], v[104:107]\n"
"buffer_load_dwordx4 v8, s[44:47], s84 offen offset:256 lds\n"
"v_mfma_f32_16x16x32_bf16 v[108:111], a[240:243], a[120:123], v[108:111]\n"
"ds_read_b64_tr_b16 a[228:229], v12 offset:37888\n"
"v_accvgpr_read_b32 v43, a15\n"
"v_mfma_f32_16x16x32_bf16 v[108:111], a[244:247], a[124:127], v[108:111]\n"
"s_waitcnt lgkmcnt(3)\n"
"ds_read_b64_tr_b16 a[230:231], v12 offset:44032\n"
"v_mfma_f32_16x16x32_bf16 v[112:115], a[248:251], a[96:99], v[112:115]\n"
"ds_read_b64_tr_b16 a[232:233], v12 offset:25608\n"
"v_mul_f32 v44, v40, v40\n"
"v_mfma_f32_16x16x32_bf16 v[112:115], a[252:255], a[100:103], v[112:115]\n"
"ds_read_b64_tr_b16 a[234:235], v12 offset:31752\n"
"v_mul_f32 v45, v41, v41\n"
"v_mfma_f32_16x16x32_bf16 v[116:119], a[248:251], a[104:107], v[116:119]\n"
"buffer_load_dwordx4 v9, s[44:47], s84 offen offset:3328 lds\n"
"v_mfma_f32_16x16x32_bf16 v[116:119], a[252:255], a[108:111], v[116:119]\n"
"s_add_u32 s84, s83, 0\n"
"s_add_u32 m0, s83, 24576\n"
"v_mul_f32 v46, v42, v42\n"
"v_mfma_f32_16x16x32_bf16 v[120:123], a[248:251], a[112:115], v[120:123]\n"
"ds_read_b64_tr_b16 a[236:237], v12 offset:37896\n"
"v_mul_f32 v47, v43, v43\n"
"v_mfma_f32_16x16x32_bf16 v[120:123], a[252:255], a[116:119], v[120:123]\n"
"s_waitcnt lgkmcnt(3)\n"
"ds_read_b64_tr_b16 a[238:239], v12 offset:44040\n"
"v_mfma_f32_16x16x32_bf16 v[124:127], a[248:251], a[120:123], v[124:127]\n"
"ds_read_b64_tr_b16 a[240:241], v13 offset:25600\n"
"v_fma_f32 v44, v44, s99, v39\n"
"v_mfma_f32_16x16x32_bf16 v[124:127], a[252:255], a[124:127], v[124:127]\n"
"buffer_load_dwordx4 v7, s[48:51], s84 offen offset:0 lds\n"
"v_mfma_f32_16x16x32_bf16 v[128:131], a[224:227], a[96:99], v[128:131]\n"
"ds_read_b64_tr_b16 a[242:243], v13 offset:31744\n"
"v_fma_f32 v45, v45, s99, v39\n"
"v_mfma_f32_16x16x32_bf16 v[128:131], a[228:231], a[100:103], v[128:131]\n"
"ds_read_b64_tr_b16 a[244:245], v13 offset:37888\n"
"v_fma_f32 v46, v46, s99, v39\n"
"v_mfma_f32_16x16x32_bf16 v[132:135], a[224:227], a[104:107], v[132:135]\n"
"ds_read_b64_tr_b16 a[246:247], v13 offset:44032\n"
"v_fma_f32 v47, v47, s99, v39\n"
"v_mfma_f32_16x16x32_bf16 v[132:135], a[228:231], a[108:111], v[132:135]\n"
"ds_read_b64_tr_b16 a[248:249], v13 offset:25608\n"
"v_mul_f32 v44, v44, v40\n"
"v_mfma_f32_16x16x32_bf16 v[136:139], a[224:227], a[112:115], v[136:139]\n"
"v_mul_f32 v45, v45, v41\n"
"v_mul_f32 v46, v46, v42\n"
"v_mul_f32 v47, v47, v43\n"
"v_mfma_f32_16x16x32_bf16 v[136:139], a[228:231], a[116:119], v[136:139]\n"
"s_waitcnt lgkmcnt(5)\n"
"v_exp_f32 v44, v44\n"
"v_exp_f32 v45, v45\n"
"v_exp_f32 v46, v46\n"
"v_exp_f32 v47, v47\n"
"v_mfma_f32_16x16x32_bf16 v[140:143], a[224:227], a[120:123], v[140:143]\n"
"v_add_f32 v44, 1.0, v44\n"
"v_add_f32 v45, 1.0, v45\n"
"v_add_f32 v46, 1.0, v46\n"
"v_add_f32 v47, 1.0, v47\n"
"v_mfma_f32_16x16x32_bf16 v[140:143], a[228:231], a[124:127], v[140:143]\n"
"v_rcp_f32 v44, v44\n"
"v_rcp_f32 v45, v45\n"
"v_rcp_f32 v46, v46\n"
"v_rcp_f32 v47, v47\n"
"v_mfma_f32_16x16x32_bf16 v[144:147], a[232:235], a[96:99], v[144:147]\n"
"v_mul_f32 v40, v40, v44\n"
"v_mul_f32 v41, v41, v45\n"
"v_mul_f32 v42, v42, v46\n"
"v_mul_f32 v43, v43, v47\n"
"v_mfma_f32_16x16x32_bf16 v[144:147], a[236:239], a[100:103], v[144:147]\n"
"v_accvgpr_read_b32 v44, a28\n"
"v_accvgpr_read_b32 v45, a29\n"
"v_accvgpr_read_b32 v46, a30\n"
"v_accvgpr_read_b32 v47, a31\n"
"v_mfma_f32_16x16x32_bf16 v[148:151], a[232:235], a[104:107], v[148:151]\n"
"v_mul_f32 v40, v40, v44\n"
"v_mul_f32 v41, v41, v45\n"
"v_mul_f32 v42, v42, v46\n"
"v_mul_f32 v43, v43, v47\n"
"v_mfma_f32_16x16x32_bf16 v[148:151], a[236:239], a[108:111], v[148:151]\n"
"v_cvt_pk_bf16_f32 v28, v40, v41\n"
"v_cvt_pk_bf16_f32 v29, v42, v43\n"
"v_mfma_f32_16x16x32_bf16 v[152:155], a[232:235], a[112:115], v[152:155]\n"
"buffer_load_dwordx4 v7, s[48:51], s84 offen offset:3072 lds\n"
"v_mfma_f32_16x16x32_bf16 v[152:155], a[236:239], a[116:119], v[152:155]\n"
"s_add_u32 m0, s83, 25472\n"
"s_waitcnt lgkmcnt(1)\n"
"v_accvgpr_read_b32 v48, a44\n"
"v_mfma_f32_16x16x32_bf16 v[156:159], a[232:235], a[120:123], v[156:159]\n"
"ds_read_b64_tr_b16 a[250:251], v13 offset:31752\n"
"v_accvgpr_read_b32 v49, a45\n"
"v_mfma_f32_16x16x32_bf16 v[156:159], a[236:239], a[124:127], v[156:159]\n"
"ds_read_b64_tr_b16 a[252:253], v13 offset:37896\n"
"v_accvgpr_read_b32 v50, a46\n"
"v_mfma_f32_16x16x32_bf16 v[160:163], a[240:243], a[96:99], v[160:163]\n"
"buffer_load_dwordx4 v7, s[48:51], s84 offen offset:128 lds\n"
"v_mfma_f32_16x16x32_bf16 v[160:163], a[244:247], a[100:103], v[160:163]\n"
"ds_read_b64_tr_b16 a[254:255], v13 offset:44040\n"
"v_accvgpr_read_b32 v51, a47\n"
"v_mfma_f32_16x16x32_bf16 v[164:167], a[240:243], a[104:107], v[164:167]\n"
"ds_read_b64_tr_b16 a[224:225], v12 offset:26624\n"
"v_mul_f32 v52, v48, v48\n"
"v_mfma_f32_16x16x32_bf16 v[164:167], a[244:247], a[108:111], v[164:167]\n"
"ds_read_b64_tr_b16 a[226:227], v12 offset:32768\n"
"v_mul_f32 v53, v49, v49\n"
"v_mfma_f32_16x16x32_bf16 v[168:171], a[240:243], a[112:115], v[168:171]\n"
"ds_read_b64_tr_b16 a[228:229], v12 offset:38912\n"
"v_mul_f32 v54, v50, v50\n"
"v_mfma_f32_16x16x32_bf16 v[168:171], a[244:247], a[116:119], v[168:171]\n"
"buffer_load_dwordx4 v7, s[48:51], s84 offen offset:3200 lds\n"
"v_mfma_f32_16x16x32_bf16 v[172:175], a[240:243], a[120:123], v[172:175]\n"
"s_add_u32 m0, s83, 26368\n"
"ds_read_b64_tr_b16 a[230:231], v12 offset:45056\n"
"v_mfma_f32_16x16x32_bf16 v[172:175], a[244:247], a[124:127], v[172:175]\n"
"s_waitcnt lgkmcnt(4)\n"
"v_mul_f32 v55, v51, v51\n"
"v_fma_f32 v52, v52, s99, v39\n"
"v_mfma_f32_16x16x32_bf16 v[176:179], a[248:251], a[96:99], v[176:179]\n"
"s_waitcnt vmcnt(27)\n"
"s_barrier\n"
"v_fma_f32 v53, v53, s99, v39\n"
"v_mfma_f32_16x16x32_bf16 v[176:179], a[252:255], a[100:103], v[176:179]\n"
"ds_read_b64_tr_b16 a[232:233], v12 offset:26632\n"
"v_fma_f32 v54, v54, s99, v39\n"
"v_mfma_f32_16x16x32_bf16 v[180:183], a[248:251], a[104:107], v[180:183]\n"
"buffer_load_dwordx4 v7, s[48:51], s84 offen offset:256 lds\n"
"v_mfma_f32_16x16x32_bf16 v[180:183], a[252:255], a[108:111], v[180:183]\n"
"ds_read_b64_tr_b16 a[234:235], v12 offset:32776\n"
"v_fma_f32 v55, v55, s99, v39\n"
"v_mfma_f32_16x16x32_bf16 v[184:187], a[248:251], a[112:115], v[184:187]\n"
"ds_read_b64_tr_b16 a[236:237], v12 offset:38920\n"
"v_mul_f32 v52, v52, v48\n"
"v_mfma_f32_16x16x32_bf16 v[184:187], a[252:255], a[116:119], v[184:187]\n"
"s_waitcnt lgkmcnt(3)\n"
"ds_read_b64_tr_b16 a[238:239], v12 offset:45064\n"
"v_mfma_f32_16x16x32_bf16 v[188:191], a[248:251], a[120:123], v[188:191]\n"
"ds_read_b64_tr_b16 a[240:241], v13 offset:26624\n"
"v_mul_f32 v53, v53, v49\n"
"v_mfma_f32_16x16x32_bf16 v[188:191], a[252:255], a[124:127], v[188:191]\n"
"buffer_load_dwordx4 v7, s[48:51], s84 offen offset:3328 lds\n"
"v_mfma_f32_16x16x32_bf16 v[192:195], a[224:227], a[96:99], v[192:195]\n"
"ds_read_b64_tr_b16 a[242:243], v13 offset:32768\n"
"v_mul_f32 v54, v54, v50\n"
"v_mfma_f32_16x16x32_bf16 v[192:195], a[228:231], a[100:103], v[192:195]\n"
"ds_read_b64_tr_b16 a[244:245], v13 offset:38912\n"
"v_mul_f32 v55, v55, v51\n"
"v_mfma_f32_16x16x32_bf16 v[196:199], a[224:227], a[104:107], v[196:199]\n"
"v_exp_f32 v52, v52\n"
"v_mfma_f32_16x16x32_bf16 v[196:199], a[228:231], a[108:111], v[196:199]\n"
"v_exp_f32 v53, v53\n"
"v_exp_f32 v54, v54\n"
"v_exp_f32 v55, v55\n"
"v_mfma_f32_16x16x32_bf16 v[200:203], a[224:227], a[112:115], v[200:203]\n"
"v_add_f32 v52, 1.0, v52\n"
"v_add_f32 v53, 1.0, v53\n"
"v_add_f32 v54, 1.0, v54\n"
"v_add_f32 v55, 1.0, v55\n"
"v_mfma_f32_16x16x32_bf16 v[200:203], a[228:231], a[116:119], v[200:203]\n"
"s_waitcnt lgkmcnt(3)\n"
"v_rcp_f32 v52, v52\n"
"v_rcp_f32 v53, v53\n"
"v_rcp_f32 v54, v54\n"
"v_rcp_f32 v55, v55\n"
"v_mfma_f32_16x16x32_bf16 v[204:207], a[224:227], a[120:123], v[204:207]\n"
"v_mul_f32 v48, v48, v52\n"
"v_mul_f32 v49, v49, v53\n"
"v_mul_f32 v50, v50, v54\n"
"v_mul_f32 v51, v51, v55\n"
"v_mfma_f32_16x16x32_bf16 v[204:207], a[228:231], a[124:127], v[204:207]\n"
"v_accvgpr_read_b32 v52, a60\n"
"v_accvgpr_read_b32 v53, a61\n"
"v_accvgpr_read_b32 v54, a62\n"
"v_accvgpr_read_b32 v55, a63\n"
"v_mfma_f32_16x16x32_bf16 v[208:211], a[232:235], a[96:99], v[208:211]\n"
"v_mul_f32 v48, v48, v52\n"
"v_mul_f32 v49, v49, v53\n"
"v_mul_f32 v50, v50, v54\n"
"v_mul_f32 v51, v51, v55\n"
"v_mfma_f32_16x16x32_bf16 v[208:211], a[236:239], a[100:103], v[208:211]\n"
"ds_read_b64_tr_b16 a[246:247], v13 offset:45056\n"
"v_cvt_pk_bf16_f32 v30, v48, v49\n"
"v_cvt_pk_bf16_f32 v31, v50, v51\n"
"v_mfma_f32_16x16x32_bf16 v[212:215], a[232:235], a[104:107], v[212:215]\n"
"buffer_store_dwordx4 v[28:31], v1, s[36:39], s5 offen offset:3072 sc1\n"
"v_mfma_f32_16x16x32_bf16 v[212:215], a[236:239], a[108:111], v[212:215]\n"
"ds_read_b64_tr_b16 a[248:249], v13 offset:26632\n"
"v_accvgpr_read_b32 v56, a76\n"
"v_mfma_f32_16x16x32_bf16 v[216:219], a[232:235], a[112:115], v[216:219]\n"
"ds_read_b64_tr_b16 a[250:251], v13 offset:32776\n"
"v_accvgpr_read_b32 v57, a77\n"
"v_mfma_f32_16x16x32_bf16 v[216:219], a[236:239], a[116:119], v[216:219]\n"
"ds_read_b64_tr_b16 a[252:253], v13 offset:38920\n"
"v_accvgpr_read_b32 v58, a78\n"
"v_mfma_f32_16x16x32_bf16 v[220:223], a[232:235], a[120:123], v[220:223]\n"
"ds_read_b64_tr_b16 a[254:255], v13 offset:45064\n"
"v_accvgpr_read_b32 v59, a79\n"
"v_mfma_f32_16x16x32_bf16 v[220:223], a[236:239], a[124:127], v[220:223]\n"
"s_waitcnt lgkmcnt(4)\n"
"ds_read_b64_tr_b16 a[160:161], v10 offset:0\n"
"v_mfma_f32_16x16x32_bf16 v[224:227], a[240:243], a[96:99], v[224:227]\n"
"ds_read_b64_tr_b16 a[162:163], v10 offset:6144\n"
"v_mul_f32 v60, v56, v56\n"
"v_mfma_f32_16x16x32_bf16 v[224:227], a[244:247], a[100:103], v[224:227]\n"
"ds_read_b64_tr_b16 a[164:165], v10 offset:12288\n"
"v_mul_f32 v61, v57, v57\n"
"v_mfma_f32_16x16x32_bf16 v[228:231], a[240:243], a[104:107], v[228:231]\n"
"ds_read_b64_tr_b16 a[166:167], v10 offset:18432\n"
"v_mul_f32 v62, v58, v58\n"
"v_mfma_f32_16x16x32_bf16 v[228:231], a[244:247], a[108:111], v[228:231]\n"
"ds_read_b64_tr_b16 a[168:169], v10 offset:8\n"
"v_mul_f32 v63, v59, v59\n"
"v_mfma_f32_16x16x32_bf16 v[232:235], a[240:243], a[112:115], v[232:235]\n"
"ds_read_b64_tr_b16 a[170:171], v10 offset:6152\n"
"v_fma_f32 v60, v60, s99, v39\n"
"v_mfma_f32_16x16x32_bf16 v[232:235], a[244:247], a[116:119], v[232:235]\n"
"s_waitcnt lgkmcnt(6)\n"
"v_fma_f32 v61, v61, s99, v39\n"
"v_fma_f32 v62, v62, s99, v39\n"
"v_mfma_f32_16x16x32_bf16 v[236:239], a[240:243], a[120:123], v[236:239]\n"
"ds_read_b64_tr_b16 a[172:173], v10 offset:12296\n"
"v_fma_f32 v63, v63, s99, v39\n"
"v_mfma_f32_16x16x32_bf16 v[236:239], a[244:247], a[124:127], v[236:239]\n"
"ds_read_b64_tr_b16 a[174:175], v10 offset:18440\n"
"v_mul_f32 v60, v60, v56\n"
"v_mfma_f32_16x16x32_bf16 v[240:243], a[248:251], a[96:99], v[240:243]\n"
"ds_read_b64_tr_b16 a[176:177], v11 offset:0\n"
"v_mul_f32 v61, v61, v57\n"
"v_mfma_f32_16x16x32_bf16 v[240:243], a[252:255], a[100:103], v[240:243]\n"
"ds_read_b64_tr_b16 a[178:179], v11 offset:6144\n"
"v_mul_f32 v62, v62, v58\n"
"v_mfma_f32_16x16x32_bf16 v[244:247], a[248:251], a[104:107], v[244:247]\n"
"ds_read_b64_tr_b16 a[180:181], v11 offset:12288\n"
"v_mul_f32 v63, v63, v59\n"
"v_mfma_f32_16x16x32_bf16 v[244:247], a[252:255], a[108:111], v[244:247]\n"
"v_exp_f32 v60, v60\n"
"v_mfma_f32_16x16x32_bf16 v[248:251], a[248:251], a[112:115], v[248:251]\n"
"v_exp_f32 v61, v61\n"
"v_mfma_f32_16x16x32_bf16 v[248:251], a[252:255], a[116:119], v[248:251]\n"
"s_waitcnt lgkmcnt(7)\n"
"s_waitcnt vmcnt(21)\n"
"v_add_f32 v60, 1.0, v60\n"
"v_mfma_f32_16x16x32_bf16 v[252:255], a[248:251], a[120:123], v[252:255]\n"
"v_exp_f32 v62, v62\n"
"v_mfma_f32_16x16x32_bf16 v[252:255], a[252:255], a[124:127], v[252:255]\n"
"v_exp_f32 v63, v63\n"
"v_mfma_f32_16x16x32_bf16 v[64:67], a[160:163], a[128:131], v[64:67]\n"
"ds_read_b64_tr_b16 a[182:183], v11 offset:18432\n"
"v_add_f32 v61, 1.0, v61\n"
"v_mfma_f32_16x16x32_bf16 v[64:67], a[164:167], a[132:135], v[64:67]\n"
"ds_read_b64_tr_b16 a[184:185], v11 offset:8\n"
"v_add_f32 v62, 1.0, v62\n"
"v_mfma_f32_16x16x32_bf16 v[68:71], a[160:163], a[136:139], v[68:71]\n"
"ds_read_b64_tr_b16 a[186:187], v11 offset:6152\n"
"v_add_f32 v63, 1.0, v63\n"
"v_mfma_f32_16x16x32_bf16 v[68:71], a[164:167], a[140:143], v[68:71]\n"
"v_rcp_f32 v60, v60\n"
"v_mfma_f32_16x16x32_bf16 v[72:75], a[160:163], a[144:147], v[72:75]\n"
"v_rcp_f32 v61, v61\n"
"v_rcp_f32 v62, v62\n"
"v_rcp_f32 v63, v63\n"
"v_mfma_f32_16x16x32_bf16 v[72:75], a[164:167], a[148:151], v[72:75]\n"
"s_waitcnt lgkmcnt(6)\n"
"v_mul_f32 v56, v56, v60\n"
"v_mul_f32 v57, v57, v61\n"
"v_mul_f32 v58, v58, v62\n"
"v_mul_f32 v59, v59, v63\n"
"v_mfma_f32_16x16x32_bf16 v[76:79], a[160:163], a[152:155], v[76:79]\n"
"v_accvgpr_read_b32 v60, a92\n"
"v_accvgpr_read_b32 v61, a93\n"
"v_accvgpr_read_b32 v62, a94\n"
"v_accvgpr_read_b32 v63, a95\n"
"v_mfma_f32_16x16x32_bf16 v[76:79], a[164:167], a[156:159], v[76:79]\n"
"v_mul_f32 v56, v56, v60\n"
"v_mul_f32 v57, v57, v61\n"
"v_mul_f32 v58, v58, v62\n"
"v_mul_f32 v59, v59, v63\n"
"v_mfma_f32_16x16x32_bf16 v[80:83], a[168:171], a[128:131], v[80:83]\n"
"v_cvt_pk_bf16_f32 v56, v56, v57\n"
"v_cvt_pk_bf16_f32 v57, v58, v59\n"
"v_mfma_f32_16x16x32_bf16 v[80:83], a[172:175], a[132:135], v[80:83]\n"
"ds_read_b64_tr_b16 a[188:189], v11 offset:12296\n"
"v_lshlrev_b32 v60, 3, v0\n"
"v_mfma_f32_16x16x32_bf16 v[84:87], a[168:171], a[136:139], v[84:87]\n"
"buffer_store_dwordx2 v[56:57], v60, s[36:39], s6 offen offset:1536 sc1\n"
"v_mfma_f32_16x16x32_bf16 v[84:87], a[172:175], a[140:143], v[84:87]\n"
"ds_read_b64_tr_b16 a[190:191], v11 offset:18440\n"
"v_mfma_f32_16x16x32_bf16 v[88:91], a[168:171], a[144:147], v[88:91]\n"
"ds_read_b64_tr_b16 a[160:161], v10 offset:1024\n"
"v_cvt_pk_bf16_f32 v32, v64, v65\n"
"v_mfma_f32_16x16x32_bf16 v[88:91], a[172:175], a[148:151], v[88:91]\n"
"s_waitcnt lgkmcnt(5)\n"
"ds_read_b64_tr_b16 a[162:163], v10 offset:7168\n"
"v_mfma_f32_16x16x32_bf16 v[92:95], a[168:171], a[152:155], v[92:95]\n"
"ds_read_b64_tr_b16 a[164:165], v10 offset:13312\n"
"v_cvt_pk_bf16_f32 v33, v66, v67\n"
"v_mfma_f32_16x16x32_bf16 v[92:95], a[172:175], a[156:159], v[92:95]\n"
"ds_read_b64_tr_b16 a[166:167], v10 offset:19456\n"
"v_cvt_pk_bf16_f32 v34, v80, v81\n"
"v_mfma_f32_16x16x32_bf16 v[96:99], a[176:179], a[128:131], v[96:99]\n"
"ds_read_b64_tr_b16 a[168:169], v10 offset:1032\n"
"v_cvt_pk_bf16_f32 v35, v82, v83\n"
"v_mfma_f32_16x16x32_bf16 v[96:99], a[180:183], a[132:135], v[96:99]\n"
"ds_read_b64_tr_b16 a[170:171], v10 offset:7176\n"
"v_cvt_pk_bf16_f32 v56, v68, v69\n"
"v_mfma_f32_16x16x32_bf16 v[100:103], a[176:179], a[136:139], v[100:103]\n"
"ds_read_b64_tr_b16 a[172:173], v10 offset:13320\n"
"v_cvt_pk_bf16_f32 v57, v70, v71\n"
"v_mfma_f32_16x16x32_bf16 v[100:103], a[180:183], a[140:143], v[100:103]\n"
"ds_read_b64_tr_b16 a[174:175], v10 offset:19464\n"
"v_cvt_pk_bf16_f32 v58, v84, v85\n"
"v_mfma_f32_16x16x32_bf16 v[104:107], a[176:179], a[144:147], v[104:107]\n"
"v_cvt_pk_bf16_f32 v59, v86, v87\n"
"v_cvt_pk_bf16_f32 v72, v72, v73\n"
"v_mfma_f32_16x16x32_bf16 v[104:107], a[180:183], a[148:151], v[104:107]\n"
"s_waitcnt lgkmcnt(8)\n"
"v_cvt_pk_bf16_f32 v73, v74, v75\n"
"v_cvt_pk_bf16_f32 v88, v88, v89\n"
"v_mfma_f32_16x16x32_bf16 v[108:111], a[176:179], a[152:155], v[108:111]\n"
"v_cvt_pk_bf16_f32 v89, v90, v91\n"
"v_accvgpr_write_b32 a208, v72\n"
"v_mfma_f32_16x16x32_bf16 v[108:111], a[180:183], a[156:159], v[108:111]\n"
"ds_read_b64_tr_b16 a[176:177], v11 offset:1024\n"
"v_accvgpr_write_b32 a209, v73\n"
"v_mfma_f32_16x16x32_bf16 v[112:115], a[184:187], a[128:131], v[112:115]\n"
"ds_read_b64_tr_b16 a[178:179], v11 offset:7168\n"
"v_accvgpr_write_b32 a210, v88\n"
"v_mfma_f32_16x16x32_bf16 v[112:115], a[188:191], a[132:135], v[112:115]\n"
"ds_read_b64_tr_b16 a[180:181], v11 offset:13312\n"
"v_accvgpr_write_b32 a211, v89\n"
"v_mfma_f32_16x16x32_bf16 v[116:119], a[184:187], a[136:139], v[116:119]\n"
"ds_read_b64_tr_b16 a[182:183], v11 offset:19456\n"
"v_cvt_pk_bf16_f32 v76, v76, v77\n"
"v_mfma_f32_16x16x32_bf16 v[116:119], a[188:191], a[140:143], v[116:119]\n"
"v_cvt_pk_bf16_f32 v77, v78, v79\n"
"v_cvt_pk_bf16_f32 v92, v92, v93\n"
"v_mfma_f32_16x16x32_bf16 v[120:123], a[184:187], a[144:147], v[120:123]\n"
"v_cvt_pk_bf16_f32 v93, v94, v95\n"
"v_accvgpr_write_b32 a232, v76\n"
"v_mfma_f32_16x16x32_bf16 v[120:123], a[188:191], a[148:151], v[120:123]\n"
"s_waitcnt lgkmcnt(8)\n"
"v_accvgpr_write_b32 a233, v77\n"
"v_accvgpr_write_b32 a234, v92\n"
"v_mfma_f32_16x16x32_bf16 v[124:127], a[184:187], a[152:155], v[124:127]\n"
"v_accvgpr_write_b32 a235, v93\n"
"v_cvt_pk_bf16_f32 v36, v96, v97\n"
"v_mfma_f32_16x16x32_bf16 v[124:127], a[188:191], a[156:159], v[124:127]\n"
"ds_read_b64_tr_b16 a[184:185], v11 offset:1032\n"
"v_cvt_pk_bf16_f32 v37, v98, v99\n"
"v_mfma_f32_16x16x32_bf16 v[128:131], a[160:163], a[128:131], v[128:131]\n"
"ds_read_b64_tr_b16 a[186:187], v11 offset:7176\n"
"v_cvt_pk_bf16_f32 v38, v112, v113\n"
"v_mfma_f32_16x16x32_bf16 v[128:131], a[164:167], a[132:135], v[128:131]\n"
"ds_read_b64_tr_b16 a[188:189], v11 offset:13320\n"
"v_cvt_pk_bf16_f32 v39, v114, v115\n"
"v_mfma_f32_16x16x32_bf16 v[132:135], a[160:163], a[136:139], v[132:135]\n"
"ds_read_b64_tr_b16 a[190:191], v11 offset:19464\n"
"v_cvt_pk_bf16_f32 v60, v100, v101\n"
"v_mfma_f32_16x16x32_bf16 v[132:135], a[164:167], a[140:143], v[132:135]\n"
"v_cvt_pk_bf16_f32 v61, v102, v103\n"
"v_cvt_pk_bf16_f32 v62, v116, v117\n"
"v_mfma_f32_16x16x32_bf16 v[136:139], a[160:163], a[144:147], v[136:139]\n"
"v_cvt_pk_bf16_f32 v63, v118, v119\n"
"v_cvt_pk_bf16_f32 v104, v104, v105\n"
"v_mfma_f32_16x16x32_bf16 v[136:139], a[164:167], a[148:151], v[136:139]\n"
"s_waitcnt lgkmcnt(8)\n"
"v_cvt_pk_bf16_f32 v105, v106, v107\n"
"v_cvt_pk_bf16_f32 v120, v120, v121\n"
"v_mfma_f32_16x16x32_bf16 v[140:143], a[160:163], a[152:155], v[140:143]\n"
"v_cvt_pk_bf16_f32 v121, v122, v123\n"
"v_accvgpr_write_b32 a212, v104\n"
"v_mfma_f32_16x16x32_bf16 v[140:143], a[164:167], a[156:159], v[140:143]\n"
"ds_read_b64_tr_b16 a[160:161], v10 offset:2048\n"
"v_accvgpr_write_b32 a213, v105\n"
"v_mfma_f32_16x16x32_bf16 v[144:147], a[168:171], a[128:131], v[144:147]\n"
"ds_read_b64_tr_b16 a[162:163], v10 offset:8192\n"
"v_accvgpr_write_b32 a214, v120\n"
"v_mfma_f32_16x16x32_bf16 v[144:147], a[172:175], a[132:135], v[144:147]\n"
"ds_read_b64_tr_b16 a[164:165], v10 offset:14336\n"
"v_accvgpr_write_b32 a215, v121\n"
"v_mfma_f32_16x16x32_bf16 v[148:151], a[168:171], a[136:139], v[148:151]\n"
"ds_read_b64_tr_b16 a[166:167], v10 offset:20480\n"
"v_cvt_pk_bf16_f32 v108, v108, v109\n"
"v_mfma_f32_16x16x32_bf16 v[148:151], a[172:175], a[140:143], v[148:151]\n"
"v_cvt_pk_bf16_f32 v109, v110, v111\n"
"v_cvt_pk_bf16_f32 v124, v124, v125\n"
"v_mfma_f32_16x16x32_bf16 v[152:155], a[168:171], a[144:147], v[152:155]\n"
"v_cvt_pk_bf16_f32 v125, v126, v127\n"
"v_accvgpr_write_b32 a236, v108\n"
"v_mfma_f32_16x16x32_bf16 v[152:155], a[172:175], a[148:151], v[152:155]\n"
"s_waitcnt lgkmcnt(8)\n"
"v_accvgpr_write_b32 a237, v109\n"
"v_accvgpr_write_b32 a238, v124\n"
"v_mfma_f32_16x16x32_bf16 v[156:159], a[168:171], a[152:155], v[156:159]\n"
"v_accvgpr_write_b32 a239, v125\n"
"v_cvt_pk_bf16_f32 v40, v128, v129\n"
"v_mfma_f32_16x16x32_bf16 v[156:159], a[172:175], a[156:159], v[156:159]\n"
"ds_read_b64_tr_b16 a[168:169], v10 offset:2056\n"
"v_cvt_pk_bf16_f32 v41, v130, v131\n"
"v_mfma_f32_16x16x32_bf16 v[160:163], a[176:179], a[128:131], v[160:163]\n"
"ds_read_b64_tr_b16 a[170:171], v10 offset:8200\n"
"v_cvt_pk_bf16_f32 v42, v144, v145\n"
"v_mfma_f32_16x16x32_bf16 v[160:163], a[180:183], a[132:135], v[160:163]\n"
"ds_read_b64_tr_b16 a[172:173], v10 offset:14344\n"
"v_cvt_pk_bf16_f32 v43, v146, v147\n"
"v_mfma_f32_16x16x32_bf16 v[164:167], a[176:179], a[136:139], v[164:167]\n"
"ds_read_b64_tr_b16 a[174:175], v10 offset:20488\n"
"v_cvt_pk_bf16_f32 v64, v132, v133\n"
"v_mfma_f32_16x16x32_bf16 v[164:167], a[180:183], a[140:143], v[164:167]\n"
"v_cvt_pk_bf16_f32 v65, v134, v135\n"
"v_cvt_pk_bf16_f32 v66, v148, v149\n"
"v_mfma_f32_16x16x32_bf16 v[168:171], a[176:179], a[144:147], v[168:171]\n"
"v_cvt_pk_bf16_f32 v67, v150, v151\n"
"v_cvt_pk_bf16_f32 v136, v136, v137\n"
"v_mfma_f32_16x16x32_bf16 v[168:171], a[180:183], a[148:151], v[168:171]\n"
"s_waitcnt lgkmcnt(8)\n"
"v_cvt_pk_bf16_f32 v137, v138, v139\n"
"v_cvt_pk_bf16_f32 v152, v152, v153\n"
"v_mfma_f32_16x16x32_bf16 v[172:175], a[176:179], a[152:155], v[172:175]\n"
"v_cvt_pk_bf16_f32 v153, v154, v155\n"
"v_accvgpr_write_b32 a216, v136\n"
"v_mfma_f32_16x16x32_bf16 v[172:175], a[180:183], a[156:159], v[172:175]\n"
"ds_read_b64_tr_b16 a[176:177], v11 offset:2048\n"
"v_accvgpr_write_b32 a217, v137\n"
"v_mfma_f32_16x16x32_bf16 v[176:179], a[184:187], a[128:131], v[176:179]\n"
"ds_read_b64_tr_b16 a[178:179], v11 offset:8192\n"
"v_accvgpr_write_b32 a218, v152\n"
"v_mfma_f32_16x16x32_bf16 v[176:179], a[188:191], a[132:135], v[176:179]\n"
"ds_read_b64_tr_b16 a[180:181], v11 offset:14336\n"
"v_accvgpr_write_b32 a219, v153\n"
"v_mfma_f32_16x16x32_bf16 v[180:183], a[184:187], a[136:139], v[180:183]\n"
"ds_read_b64_tr_b16 a[182:183], v11 offset:20480\n"
"v_cvt_pk_bf16_f32 v140, v140, v141\n"
"v_mfma_f32_16x16x32_bf16 v[180:183], a[188:191], a[140:143], v[180:183]\n"
"v_cvt_pk_bf16_f32 v141, v142, v143\n"
"v_cvt_pk_bf16_f32 v156, v156, v157\n"
"v_mfma_f32_16x16x32_bf16 v[184:187], a[184:187], a[144:147], v[184:187]\n"
"v_cvt_pk_bf16_f32 v157, v158, v159\n"
"v_accvgpr_write_b32 a240, v140\n"
"v_mfma_f32_16x16x32_bf16 v[184:187], a[188:191], a[148:151], v[184:187]\n"
"s_waitcnt lgkmcnt(8)\n"
"v_accvgpr_write_b32 a241, v141\n"
"v_accvgpr_write_b32 a242, v156\n"
"v_mfma_f32_16x16x32_bf16 v[188:191], a[184:187], a[152:155], v[188:191]\n"
"v_accvgpr_write_b32 a243, v157\n"
"v_cvt_pk_bf16_f32 v44, v160, v161\n"
"v_mfma_f32_16x16x32_bf16 v[188:191], a[188:191], a[156:159], v[188:191]\n"
"ds_read_b64_tr_b16 a[184:185], v11 offset:2056\n"
"v_cvt_pk_bf16_f32 v45, v162, v163\n"
"v_mfma_f32_16x16x32_bf16 v[192:195], a[160:163], a[128:131], v[192:195]\n"
"ds_read_b64_tr_b16 a[186:187], v11 offset:8200\n"
"v_cvt_pk_bf16_f32 v46, v176, v177\n"
"v_mfma_f32_16x16x32_bf16 v[192:195], a[164:167], a[132:135], v[192:195]\n"
"ds_read_b64_tr_b16 a[188:189], v11 offset:14344\n"
"v_cvt_pk_bf16_f32 v47, v178, v179\n"
"v_mfma_f32_16x16x32_bf16 v[196:199], a[160:163], a[136:139], v[196:199]\n"
"s_waitcnt vmcnt(15)\n"
"s_barrier\n"
"s_add_u32 s84, s83, 49152\n"
"v_mfma_f32_16x16x32_bf16 v[196:199], a[164:167], a[140:143], v[196:199]\n"
"s_add_u32 m0, s83, 73728\n"
"ds_read_b64_tr_b16 a[190:191], v11 offset:20488\n"
"v_mfma_f32_16x16x32_bf16 v[200:203], a[160:163], a[144:147], v[200:203]\n"
"buffer_load_dwordx4 v8, s[44:47], s84 offen offset:0 lds\n"
"v_mfma_f32_16x16x32_bf16 v[200:203], a[164:167], a[148:151], v[200:203]\n"
"s_waitcnt lgkmcnt(8)\n"
"v_cvt_pk_bf16_f32 v68, v164, v165\n"
"v_cvt_pk_bf16_f32 v69, v166, v167\n"
"v_mfma_f32_16x16x32_bf16 v[204:207], a[160:163], a[152:155], v[204:207]\n"
"ds_read_b128 v[144:147], v16 offset:24576\n"
"v_cvt_pk_bf16_f32 v70, v180, v181\n"
"v_mfma_f32_16x16x32_bf16 v[204:207], a[164:167], a[156:159], v[204:207]\n"
"ds_read_b128 v[148:151], v17 offset:24576\n"
"v_cvt_pk_bf16_f32 v71, v182, v183\n"
"v_mfma_f32_16x16x32_bf16 v[208:211], a[168:171], a[128:131], v[208:211]\n"
"buffer_load_dwordx4 v9, s[44:47], s84 offen offset:3072 lds\n"
"v_mfma_f32_16x16x32_bf16 v[208:211], a[172:175], a[132:135], v[208:211]\n"
"s_add_u32 m0, s83, 74624\n"
"v_cvt_pk_bf16_f32 v168, v168, v169\n"
"v_cvt_pk_bf16_f32 v169, v170, v171\n"
"v_mfma_f32_16x16x32_bf16 v[212:215], a[168:171], a[136:139], v[212:215]\n"
"ds_read_b128 v[152:155], v16 offset:25600\n"
"v_cvt_pk_bf16_f32 v184, v184, v185\n"
"v_mfma_f32_16x16x32_bf16 v[212:215], a[172:175], a[140:143], v[212:215]\n"
"ds_read_b128 v[156:159], v17 offset:25600\n"
"v_cvt_pk_bf16_f32 v185, v186, v187\n"
"v_mfma_f32_16x16x32_bf16 v[216:219], a[168:171], a[144:147], v[216:219]\n"
"v_accvgpr_write_b32 a220, v168\n"
"v_accvgpr_write_b32 a221, v169\n"
"v_mfma_f32_16x16x32_bf16 v[216:219], a[172:175], a[148:151], v[216:219]\n"
"buffer_load_dwordx4 v8, s[44:47], s84 offen offset:128 lds\n"
"v_mfma_f32_16x16x32_bf16 v[220:223], a[168:171], a[152:155], v[220:223]\n"
"s_waitcnt lgkmcnt(8)\n"
"v_accvgpr_write_b32 a222, v184\n"
"v_accvgpr_write_b32 a223, v185\n"
"v_mfma_f32_16x16x32_bf16 v[220:223], a[172:175], a[156:159], v[220:223]\n"
"v_cvt_pk_bf16_f32 v172, v172, v173\n"
"v_cvt_pk_bf16_f32 v173, v174, v175\n"
"v_mfma_f32_16x16x32_bf16 v[224:227], a[176:179], a[128:131], v[224:227]\n"
"v_cvt_pk_bf16_f32 v188, v188, v189\n"
"v_cvt_pk_bf16_f32 v189, v190, v191\n"
"v_mfma_f32_16x16x32_bf16 v[224:227], a[180:183], a[132:135], v[224:227]\n"
"v_accvgpr_write_b32 a244, v172\n"
"v_accvgpr_write_b32 a245, v173\n"
"v_mfma_f32_16x16x32_bf16 v[228:231], a[176:179], a[136:139], v[228:231]\n"
"buffer_load_dwordx4 v9, s[44:47], s84 offen offset:3200 lds\n"
"v_mfma_f32_16x16x32_bf16 v[228:231], a[180:183], a[140:143], v[228:231]\n"
"s_add_u32 m0, s83, 75520\n"
"v_accvgpr_write_b32 a246, v188\n"
"v_accvgpr_write_b32 a247, v189\n"
"v_mfma_f32_16x16x32_bf16 v[232:235], a[176:179], a[144:147], v[232:235]\n"
"ds_read_b128 v[160:163], v16 offset:26624\n"
"v_cvt_pk_bf16_f32 v48, v192, v193\n"
"v_mfma_f32_16x16x32_bf16 v[232:235], a[180:183], a[148:151], v[232:235]\n"
"s_waitcnt lgkmcnt(5)\n"
"ds_read_b128 v[164:167], v17 offset:26624\n"
"v_mfma_f32_16x16x32_bf16 v[236:239], a[176:179], a[152:155], v[236:239]\n"
"ds_read_b128 v[168:171], v16 offset:30720\n"
"v_cvt_pk_bf16_f32 v49, v194, v195\n"
"v_mfma_f32_16x16x32_bf16 v[236:239], a[180:183], a[156:159], v[236:239]\n"
"buffer_load_dwordx4 v8, s[44:47], s84 offen offset:256 lds\n"
"v_mfma_f32_16x16x32_bf16 v[240:243], a[184:187], a[128:131], v[240:243]\n"
"ds_read_b128 v[172:175], v17 offset:30720\n"
"v_cvt_pk_bf16_f32 v50, v208, v209\n"
"v_mfma_f32_16x16x32_bf16 v[240:243], a[188:191], a[132:135], v[240:243]\n"
"ds_read_b128 v[176:179], v16 offset:31744\n"
"v_cvt_pk_bf16_f32 v51, v210, v211\n"
"v_mfma_f32_16x16x32_bf16 v[244:247], a[184:187], a[136:139], v[244:247]\n"
"ds_read_b128 v[180:183], v17 offset:31744\n"
"v_cvt_pk_bf16_f32 v72, v196, v197\n"
"v_mfma_f32_16x16x32_bf16 v[244:247], a[188:191], a[140:143], v[244:247]\n"
"ds_read_b128 v[184:187], v16 offset:32768\n"
"v_cvt_pk_bf16_f32 v73, v198, v199\n"
"v_mfma_f32_16x16x32_bf16 v[248:251], a[184:187], a[144:147], v[248:251]\n"
"buffer_load_dwordx4 v9, s[44:47], s84 offen offset:3328 lds\n"
"v_mfma_f32_16x16x32_bf16 v[248:251], a[188:191], a[148:151], v[248:251]\n"
"s_add_u32 s84, s83, 24576\n"
"s_add_u32 m0, s83, 49152\n"
"v_cvt_pk_bf16_f32 v74, v212, v213\n"
"v_mfma_f32_16x16x32_bf16 v[252:255], a[184:187], a[152:155], v[252:255]\n"
"s_waitcnt lgkmcnt(5)\n"
"v_cvt_pk_bf16_f32 v75, v214, v215\n"
"v_cvt_pk_bf16_f32 v200, v200, v201\n"
"v_mfma_f32_16x16x32_bf16 v[252:255], a[188:191], a[156:159], v[252:255]\n"
"v_cvt_pk_bf16_f32 v201, v202, v203\n"
"v_cvt_pk_bf16_f32 v216, v216, v217\n"
"v_cvt_pk_bf16_f32 v217, v218, v219\n"
"v_accvgpr_write_b32 a224, v200\n"
"v_accvgpr_write_b32 a225, v201\n"
"v_accvgpr_write_b32 a226, v216\n"
"v_accvgpr_write_b32 a227, v217\n"
"v_cvt_pk_bf16_f32 v204, v204, v205\n"
"v_cvt_pk_bf16_f32 v205, v206, v207\n"
"v_cvt_pk_bf16_f32 v220, v220, v221\n"
"v_cvt_pk_bf16_f32 v221, v222, v223\n"
"v_accvgpr_write_b32 a248, v204\n"
"v_accvgpr_write_b32 a249, v205\n"
"v_accvgpr_write_b32 a250, v220\n"
"v_accvgpr_write_b32 a251, v221\n"
"v_mfma_f32_16x16x32_bf16 v[216:219], v[144:147], v[32:35], 0\n"
"ds_read_b128 v[188:191], v17 offset:32768\n"
"v_cvt_pk_bf16_f32 v52, v224, v225\n"
"v_mfma_f32_16x16x32_bf16 v[216:219], v[148:151], v[36:39], v[216:219]\n"
"buffer_load_dwordx4 v7, s[48:51], s84 offen offset:0 lds\n"
"v_mfma_f32_16x16x32_bf16 v[216:219], v[152:155], v[40:43], v[216:219]\n"
"v_cvt_pk_bf16_f32 v53, v226, v227\n"
"v_cvt_pk_bf16_f32 v54, v240, v241\n"
"v_mfma_f32_16x16x32_bf16 v[216:219], v[156:159], v[44:47], v[216:219]\n"
"v_cvt_pk_bf16_f32 v55, v242, v243\n"
"v_cvt_pk_bf16_f32 v76, v228, v229\n"
"v_mfma_f32_16x16x32_bf16 v[216:219], v[160:163], v[48:51], v[216:219]\n"
"v_cvt_pk_bf16_f32 v77, v230, v231\n"
"v_cvt_pk_bf16_f32 v78, v244, v245\n"
"v_mfma_f32_16x16x32_bf16 v[216:219], v[164:167], v[52:55], v[216:219]\n"
"v_cvt_pk_bf16_f32 v79, v246, v247\n"
"v_cvt_pk_bf16_f32 v232, v232, v233\n"
"v_mfma_f32_16x16x32_bf16 v[220:223], v[144:147], v[56:59], 0\n"
"buffer_load_dwordx4 v7, s[48:51], s84 offen offset:3072 lds\n"
"v_mfma_f32_16x16x32_bf16 v[220:223], v[148:151], v[60:63], v[220:223]\n"
"s_add_u32 m0, s83, 50048\n"
"v_cvt_pk_bf16_f32 v233, v234, v235\n"
"v_cvt_pk_bf16_f32 v248, v248, v249\n"
"v_mfma_f32_16x16x32_bf16 v[220:223], v[152:155], v[64:67], v[220:223]\n"
"v_cvt_pk_bf16_f32 v249, v250, v251\n"
"v_accvgpr_write_b32 a228, v232\n"
"v_accvgpr_write_b32 a229, v233\n"
"v_accvgpr_write_b32 a230, v248\n"
"v_accvgpr_write_b32 a231, v249\n"
"v_cvt_pk_bf16_f32 v236, v236, v237\n"
"v_cvt_pk_bf16_f32 v237, v238, v239\n"
"v_cvt_pk_bf16_f32 v252, v252, v253\n"
"v_cvt_pk_bf16_f32 v253, v254, v255\n"
"v_accvgpr_write_b32 a252, v236\n"
"v_accvgpr_write_b32 a253, v237\n"
"v_accvgpr_write_b32 a254, v252\n"
"v_accvgpr_write_b32 a255, v253\n"
"v_max3_f32 v244, v216, v217, v218\n"
"v_mfma_f32_16x16x32_bf16 v[220:223], v[156:159], v[68:71], v[220:223]\n"
"v_max_f32 v244, v244, v219\n"
"v_cvt_pk_bf16_f32 v216, 0, v216\n"
"v_mfma_f32_16x16x32_bf16 v[220:223], v[160:163], v[72:75], v[220:223]\n"
"v_mov_b32 v245, v244\n"
"v_cvt_pk_bf16_f32 v217, 0, v217\n"
"v_mfma_f32_16x16x32_bf16 v[220:223], v[164:167], v[76:79], v[220:223]\n"
"v_permlane32_swap_b32 v244, v245\n"
"v_cvt_pk_bf16_f32 v218, 0, v218\n"
"v_mfma_f32_16x16x32_bf16 v[224:227], v[144:147], a[208:211], 0\n"
"v_max_f32 v244, v244, v245\n"
"v_cvt_pk_bf16_f32 v219, 0, v219\n"
"v_mfma_f32_16x16x32_bf16 v[224:227], v[148:151], a[212:215], v[224:227]\n"
"v_mov_b32 v245, v244\n"
"v_mul_f32 v216, s85, v216\n"
"v_mfma_f32_16x16x32_bf16 v[224:227], v[152:155], a[216:219], v[224:227]\n"
"v_permlane16_swap_b32 v244, v245\n"
"v_max3_f32 v246, v220, v221, v222\n"
"v_mfma_f32_16x16x32_bf16 v[224:227], v[156:159], a[220:223], v[224:227]\n"
"v_max_f32 v244, v244, v245\n"
"v_max_f32 v246, v246, v223\n"
"v_mfma_f32_16x16x32_bf16 v[224:227], v[160:163], a[224:227], v[224:227]\n"
"v_cvt_pk_bf16_f32 v244, 0, v244\n"
"v_mov_b32 v247, v246\n"
"v_mfma_f32_16x16x32_bf16 v[224:227], v[164:167], a[228:231], v[224:227]\n"
"v_mul_f32 v244, s85, v244\n"
"v_permlane32_swap_b32 v246, v247\n"
"v_mfma_f32_16x16x32_bf16 v[228:231], v[144:147], a[232:235], 0\n"
"v_cvt_pk_bf16_f32 v244, 0, v244\n"
"v_max_f32 v246, v246, v247\n"
"v_mfma_f32_16x16x32_bf16 v[228:231], v[148:151], a[236:239], v[228:231]\n"
"v_mul_f32 v244, s86, v244\n"
"v_mov_b32 v247, v246\n"
"v_mfma_f32_16x16x32_bf16 v[228:231], v[152:155], a[240:243], v[228:231]\n"
"v_add_f32 v28, 0x42800000, v244\n"
"v_permlane16_swap_b32 v246, v247\n"
"v_max3_f32 v248, v224, v225, v226\n"
"v_mfma_f32_16x16x32_bf16 v[228:231], v[156:159], a[244:247], v[228:231]\n"
"s_waitcnt lgkmcnt(0)\n"
"v_max_f32 v246, v246, v247\n"
"v_max_f32 v248, v248, v227\n"
"v_mfma_f32_16x16x32_bf16 v[228:231], v[160:163], a[248:251], v[228:231]\n"
"v_cvt_pk_bf16_f32 v246, 0, v246\n"
"v_mov_b32 v249, v248\n"
"v_mfma_f32_16x16x32_bf16 v[228:231], v[164:167], a[252:255], v[228:231]\n"
"v_mul_f32 v246, s85, v246\n"
"v_permlane32_swap_b32 v248, v249\n"
"v_mfma_f32_16x16x32_bf16 v[232:235], v[168:171], v[32:35], 0\n"
"v_cvt_pk_bf16_f32 v246, 0, v246\n"
"v_max_f32 v248, v248, v249\n"
"v_mfma_f32_16x16x32_bf16 v[232:235], v[172:175], v[36:39], v[232:235]\n"
"v_mul_f32 v246, s86, v246\n"
"v_mov_b32 v249, v248\n"
"v_mfma_f32_16x16x32_bf16 v[232:235], v[176:179], v[40:43], v[232:235]\n"
"v_add_f32 v29, 0x42800000, v246\n"
"v_permlane16_swap_b32 v248, v249\n"
"v_max3_f32 v250, v228, v229, v230\n"
"v_mfma_f32_16x16x32_bf16 v[232:235], v[180:183], v[44:47], v[232:235]\n"
"v_max_f32 v248, v248, v249\n"
"v_max_f32 v250, v250, v231\n"
"v_mfma_f32_16x16x32_bf16 v[232:235], v[184:187], v[48:51], v[232:235]\n"
"v_cvt_pk_bf16_f32 v248, 0, v248\n"
"v_mov_b32 v251, v250\n"
"v_mfma_f32_16x16x32_bf16 v[232:235], v[188:191], v[52:55], v[232:235]\n"
"v_mul_f32 v248, s85, v248\n"
"v_permlane32_swap_b32 v250, v251\n"
"v_mfma_f32_16x16x32_bf16 v[236:239], v[168:171], v[56:59], 0\n"
"v_cvt_pk_bf16_f32 v248, 0, v248\n"
"v_max_f32 v250, v250, v251\n"
"v_mfma_f32_16x16x32_bf16 v[236:239], v[172:175], v[60:63], v[236:239]\n"
"v_mul_f32 v248, s86, v248\n"
"v_mov_b32 v251, v250\n"
"v_mul_f32 v217, s85, v217\n"
"v_mul_f32 v218, s85, v218\n"
"v_mul_f32 v219, s85, v219\n"
"v_mfma_f32_16x16x32_bf16 v[236:239], v[176:179], v[64:67], v[236:239]\n"
"v_add_f32 v30, 0x42800000, v248\n"
"v_permlane16_swap_b32 v250, v251\n"
"v_cvt_pk_bf16_f32 v216, 0, v216\n"
"v_cvt_pk_bf16_f32 v217, 0, v217\n"
"v_cvt_pk_bf16_f32 v218, 0, v218\n"
"v_cvt_pk_bf16_f32 v219, 0, v219\n"
"v_mfma_f32_16x16x32_bf16 v[236:239], v[180:183], v[68:71], v[236:239]\n"
"v_max_f32 v250, v250, v251\n"
"v_fma_f32 v216, v216, s86, -v28\n"
"v_fma_f32 v217, v217, s86, -v28\n"
"v_fma_f32 v218, v218, s86, -v28\n"
"v_fma_f32 v219, v219, s86, -v28\n"
"v_mfma_f32_16x16x32_bf16 v[236:239], v[184:187], v[72:75], v[236:239]\n"
"v_cvt_pk_bf16_f32 v250, 0, v250\n"
"v_exp_f32 v216, v216\n"
"v_exp_f32 v217, v217\n"
"v_exp_f32 v218, v218\n"
"v_exp_f32 v219, v219\n"
"v_mfma_f32_16x16x32_bf16 v[236:239], v[188:191], v[76:79], v[236:239]\n"
"v_mul_f32 v250, s85, v250\n"
"v_cvt_pk_bf16_f32 v80, v216, v217\n"
"v_cvt_pk_bf16_f32 v81, v218, v219\n"
"v_mfma_f32_16x16x32_bf16 v[216:219], v[168:171], a[208:211], 0\n"
"v_cvt_pk_bf16_f32 v250, 0, v250\n"
"v_cvt_pk_bf16_f32 v220, 0, v220\n"
"v_cvt_pk_bf16_f32 v221, 0, v221\n"
"v_cvt_pk_bf16_f32 v222, 0, v222\n"
"v_cvt_pk_bf16_f32 v223, 0, v223\n"
"v_mfma_f32_16x16x32_bf16 v[216:219], v[172:175], a[212:215], v[216:219]\n"
"v_mul_f32 v250, s86, v250\n"
"v_mul_f32 v220, s85, v220\n"
"v_mul_f32 v221, s85, v221\n"
"v_mul_f32 v222, s85, v222\n"
"v_mul_f32 v223, s85, v223\n"
"v_mfma_f32_16x16x32_bf16 v[216:219], v[176:179], a[216:219], v[216:219]\n"
"v_add_f32 v31, 0x42800000, v250\n"
"v_cvt_pk_bf16_f32 v220, 0, v220\n"
"v_cvt_pk_bf16_f32 v221, 0, v221\n"
"v_cvt_pk_bf16_f32 v222, 0, v222\n"
"v_cvt_pk_bf16_f32 v223, 0, v223\n"
"v_mfma_f32_16x16x32_bf16 v[216:219], v[180:183], a[220:223], v[216:219]\n"
"v_fma_f32 v220, v220, s86, -v29\n"
"v_fma_f32 v221, v221, s86, -v29\n"
"v_fma_f32 v222, v222, s86, -v29\n"
"v_fma_f32 v223, v223, s86, -v29\n"
"v_mfma_f32_16x16x32_bf16 v[216:219], v[184:187], a[224:227], v[216:219]\n"
"s_waitcnt vmcnt(10)\n"
"s_barrier\n"
"v_exp_f32 v220, v220\n"
"v_exp_f32 v221, v221\n"
"v_exp_f32 v222, v222\n"
"v_exp_f32 v223, v223\n"
"v_mfma_f32_16x16x32_bf16 v[216:219], v[188:191], a[228:231], v[216:219]\n"
"ds_read_b128 v[144:147], v16 offset:36864\n"
"ds_read_b128 v[148:151], v17 offset:36864\n"
"ds_read_b128 v[152:155], v16 offset:37888\n"
"ds_read_b128 v[156:159], v17 offset:37888\n"
"ds_read_b128 v[160:163], v16 offset:38912\n"
"ds_read_b128 v[164:167], v17 offset:38912\n"
"v_cvt_pk_bf16_f32 v84, v220, v221\n"
"v_cvt_pk_bf16_f32 v85, v222, v223\n"
"v_mfma_f32_16x16x32_bf16 v[220:223], v[168:171], a[232:235], 0\n"
"v_cvt_pk_bf16_f32 v224, 0, v224\n"
"v_cvt_pk_bf16_f32 v225, 0, v225\n"
"v_cvt_pk_bf16_f32 v226, 0, v226\n"
"v_cvt_pk_bf16_f32 v227, 0, v227\n"
"v_mfma_f32_16x16x32_bf16 v[220:223], v[172:175], a[236:239], v[220:223]\n"
"v_mul_f32 v224, s85, v224\n"
"v_mul_f32 v225, s85, v225\n"
"v_mul_f32 v226, s85, v226\n"
"v_mul_f32 v227, s85, v227\n"
"v_mfma_f32_16x16x32_bf16 v[220:223], v[176:179], a[240:243], v[220:223]\n"
"v_cvt_pk_bf16_f32 v224, 0, v224\n"
"v_cvt_pk_bf16_f32 v225, 0, v225\n"
"v_cvt_pk_bf16_f32 v226, 0, v226\n"
"v_cvt_pk_bf16_f32 v227, 0, v227\n"
"v_mfma_f32_16x16x32_bf16 v[220:223], v[180:183], a[244:247], v[220:223]\n"
"v_fma_f32 v224, v224, s86, -v30\n"
"v_fma_f32 v225, v225, s86, -v30\n"
"v_fma_f32 v226, v226, s86, -v30\n"
"v_fma_f32 v227, v227, s86, -v30\n"
"v_mfma_f32_16x16x32_bf16 v[220:223], v[184:187], a[248:251], v[220:223]\n"
"v_exp_f32 v224, v224\n"
"v_exp_f32 v225, v225\n"
"v_exp_f32 v226, v226\n"
"v_exp_f32 v227, v227\n"
"v_mfma_f32_16x16x32_bf16 v[220:223], v[188:191], a[252:255], v[220:223]\n"
"s_waitcnt lgkmcnt(0)\n"
"v_cvt_pk_bf16_f32 v88, v224, v225\n"
"v_cvt_pk_bf16_f32 v89, v226, v227\n"
"v_mfma_f32_16x16x32_bf16 v[224:227], v[144:147], v[32:35], 0\n"
"v_cvt_pk_bf16_f32 v228, 0, v228\n"
"v_cvt_pk_bf16_f32 v229, 0, v229\n"
"v_cvt_pk_bf16_f32 v230, 0, v230\n"
"v_cvt_pk_bf16_f32 v231, 0, v231\n"
"v_mfma_f32_16x16x32_bf16 v[224:227], v[148:151], v[36:39], v[224:227]\n"
"v_mul_f32 v228, s85, v228\n"
"v_mul_f32 v229, s85, v229\n"
"v_mul_f32 v230, s85, v230\n"
"v_mul_f32 v231, s85, v231\n"
"v_mfma_f32_16x16x32_bf16 v[224:227], v[152:155], v[40:43], v[224:227]\n"
"v_cvt_pk_bf16_f32 v228, 0, v228\n"
"v_cvt_pk_bf16_f32 v229, 0, v229\n"
"v_cvt_pk_bf16_f32 v230, 0, v230\n"
"v_cvt_pk_bf16_f32 v231, 0, v231\n"
"v_mfma_f32_16x16x32_bf16 v[224:227], v[156:159], v[44:47], v[224:227]\n"
"v_fma_f32 v228, v228, s86, -v31\n"
"v_fma_f32 v229, v229, s86, -v31\n"
"v_fma_f32 v230, v230, s86, -v31\n"
"v_fma_f32 v231, v231, s86, -v31\n"
"v_mfma_f32_16x16x32_bf16 v[224:227], v[160:163], v[48:51], v[224:227]\n"
"v_exp_f32 v228, v228\n"
"v_exp_f32 v229, v229\n"
"v_exp_f32 v230, v230\n"
"v_exp_f32 v231, v231\n"
"v_mfma_f32_16x16x32_bf16 v[224:227], v[164:167], v[52:55], v[224:227]\n"
"v_cvt_pk_bf16_f32 v92, v228, v229\n"
"v_cvt_pk_bf16_f32 v93, v230, v231\n"
"v_mfma_f32_16x16x32_bf16 v[228:231], v[144:147], v[56:59], 0\n"
"v_cvt_pk_bf16_f32 v232, 0, v232\n"
"v_cvt_pk_bf16_f32 v233, 0, v233\n"
"v_cvt_pk_bf16_f32 v234, 0, v234\n"
"v_cvt_pk_bf16_f32 v235, 0, v235\n"
"v_mfma_f32_16x16x32_bf16 v[228:231], v[148:151], v[60:63], v[228:231]\n"
"v_mul_f32 v232, s85, v232\n"
"v_mul_f32 v233, s85, v233\n"
"v_mul_f32 v234, s85, v234\n"
"v_mul_f32 v235, s85, v235\n"
"v_mfma_f32_16x16x32_bf16 v[228:231], v[152:155], v[64:67], v[228:231]\n"
"v_cvt_pk_bf16_f32 v232, 0, v232\n"
"v_cvt_pk_bf16_f32 v233, 0, v233\n"
"v_cvt_pk_bf16_f32 v234, 0, v234\n"
"v_cvt_pk_bf16_f32 v235, 0, v235\n"
"v_mfma_f32_16x16x32_bf16 v[228:231], v[156:159], v[68:71], v[228:231]\n"
"v_fma_f32 v232, v232, s86, -v28\n"
"v_fma_f32 v233, v233, s86, -v28\n"
"v_fma_f32 v234, v234, s86, -v28\n"
"v_fma_f32 v235, v235, s86, -v28\n"
"v_mfma_f32_16x16x32_bf16 v[228:231], v[160:163], v[72:75], v[228:231]\n"
"v_exp_f32 v232, v232\n"
"v_exp_f32 v233, v233\n"
"v_exp_f32 v234, v234\n"
"v_exp_f32 v235, v235\n"
"v_mfma_f32_16x16x32_bf16 v[228:231], v[164:167], v[76:79], v[228:231]\n"
"v_cvt_pk_bf16_f32 v82, v232, v233\n"
"v_cvt_pk_bf16_f32 v83, v234, v235\n"
"v_mfma_f32_16x16x32_bf16 v[232:235], v[144:147], a[208:211], 0\n"
"v_cvt_pk_bf16_f32 v236, 0, v236\n"
"v_cvt_pk_bf16_f32 v237, 0, v237\n"
"v_cvt_pk_bf16_f32 v238, 0, v238\n"
"v_cvt_pk_bf16_f32 v239, 0, v239\n"
"v_mfma_f32_16x16x32_bf16 v[232:235], v[148:151], a[212:215], v[232:235]\n"
"v_mul_f32 v236, s85, v236\n"
"v_mul_f32 v237, s85, v237\n"
"v_mul_f32 v238, s85, v238\n"
"v_mul_f32 v239, s85, v239\n"
"v_mfma_f32_16x16x32_bf16 v[232:235], v[152:155], a[216:219], v[232:235]\n"
"v_cvt_pk_bf16_f32 v236, 0, v236\n"
"v_cvt_pk_bf16_f32 v237, 0, v237\n"
"v_cvt_pk_bf16_f32 v238, 0, v238\n"
"v_cvt_pk_bf16_f32 v239, 0, v239\n"
"v_mfma_f32_16x16x32_bf16 v[232:235], v[156:159], a[220:223], v[232:235]\n"
"v_fma_f32 v236, v236, s86, -v29\n"
"v_fma_f32 v237, v237, s86, -v29\n"
"v_fma_f32 v238, v238, s86, -v29\n"
"v_fma_f32 v239, v239, s86, -v29\n"
"v_mfma_f32_16x16x32_bf16 v[232:235], v[160:163], a[224:227], v[232:235]\n"
"v_exp_f32 v236, v236\n"
"v_exp_f32 v237, v237\n"
"v_exp_f32 v238, v238\n"
"v_exp_f32 v239, v239\n"
"v_mfma_f32_16x16x32_bf16 v[232:235], v[164:167], a[228:231], v[232:235]\n"
"ds_read_b128 v[168:171], v16 offset:43008\n"
"ds_read_b128 v[172:175], v17 offset:43008\n"
"ds_read_b128 v[176:179], v16 offset:44032\n"
"ds_read_b128 v[180:183], v17 offset:44032\n"
"ds_read_b128 v[184:187], v16 offset:45056\n"
"ds_read_b128 v[188:191], v17 offset:45056\n"
"v_cvt_pk_bf16_f32 v86, v236, v237\n"
"v_cvt_pk_bf16_f32 v87, v238, v239\n"
"v_mfma_f32_16x16x32_bf16 v[236:239], v[144:147], a[232:235], 0\n"
"v_cvt_pk_bf16_f32 v216, 0, v216\n"
"v_cvt_pk_bf16_f32 v217, 0, v217\n"
"v_cvt_pk_bf16_f32 v218, 0, v218\n"
"v_cvt_pk_bf16_f32 v219, 0, v219\n"
"v_mfma_f32_16x16x32_bf16 v[236:239], v[148:151], a[236:239], v[236:239]\n"
"v_mul_f32 v216, s85, v216\n"
"v_mul_f32 v217, s85, v217\n"
"v_mul_f32 v218, s85, v218\n"
"v_mul_f32 v219, s85, v219\n"
"v_mfma_f32_16x16x32_bf16 v[236:239], v[152:155], a[240:243], v[236:239]\n"
"v_cvt_pk_bf16_f32 v216, 0, v216\n"
"v_cvt_pk_bf16_f32 v217, 0, v217\n"
"v_cvt_pk_bf16_f32 v218, 0, v218\n"
"v_cvt_pk_bf16_f32 v219, 0, v219\n"
"v_mfma_f32_16x16x32_bf16 v[236:239], v[156:159], a[244:247], v[236:239]\n"
"v_fma_f32 v216, v216, s86, -v30\n"
"v_fma_f32 v217, v217, s86, -v30\n"
"v_fma_f32 v218, v218, s86, -v30\n"
"v_fma_f32 v219, v219, s86, -v30\n"
"v_mfma_f32_16x16x32_bf16 v[236:239], v[160:163], a[248:251], v[236:239]\n"
"v_exp_f32 v216, v216\n"
"v_exp_f32 v217, v217\n"
"v_exp_f32 v218, v218\n"
"v_exp_f32 v219, v219\n"
"v_mfma_f32_16x16x32_bf16 v[236:239], v[164:167], a[252:255], v[236:239]\n"
"s_waitcnt lgkmcnt(0)\n"
"v_cvt_pk_bf16_f32 v90, v216, v217\n"
"v_cvt_pk_bf16_f32 v91, v218, v219\n"
"v_mfma_f32_16x16x32_bf16 v[216:219], v[168:171], v[32:35], 0\n"
"v_cvt_pk_bf16_f32 v220, 0, v220\n"
"v_cvt_pk_bf16_f32 v221, 0, v221\n"
"v_cvt_pk_bf16_f32 v222, 0, v222\n"
"v_cvt_pk_bf16_f32 v223, 0, v223\n"
"v_mfma_f32_16x16x32_bf16 v[216:219], v[172:175], v[36:39], v[216:219]\n"
"v_mul_f32 v220, s85, v220\n"
"v_mul_f32 v221, s85, v221\n"
"v_mul_f32 v222, s85, v222\n"
"v_mul_f32 v223, s85, v223\n"
"v_mfma_f32_16x16x32_bf16 v[216:219], v[176:179], v[40:43], v[216:219]\n"
"v_cvt_pk_bf16_f32 v220, 0, v220\n"
"v_cvt_pk_bf16_f32 v221, 0, v221\n"
"v_cvt_pk_bf16_f32 v222, 0, v222\n"
"v_cvt_pk_bf16_f32 v223, 0, v223\n"
"v_mfma_f32_16x16x32_bf16 v[216:219], v[180:183], v[44:47], v[216:219]\n"
"v_fma_f32 v220, v220, s86, -v31\n"
"v_fma_f32 v221, v221, s86, -v31\n"
"v_fma_f32 v222, v222, s86, -v31\n"
"v_fma_f32 v223, v223, s86, -v31\n"
"v_mfma_f32_16x16x32_bf16 v[216:219], v[184:187], v[48:51], v[216:219]\n"
"v_exp_f32 v220, v220\n"
"v_exp_f32 v221, v221\n"
"v_exp_f32 v222, v222\n"
"v_exp_f32 v223, v223\n"
"v_mfma_f32_16x16x32_bf16 v[216:219], v[188:191], v[52:55], v[216:219]\n"
"v_cvt_pk_bf16_f32 v94, v220, v221\n"
"v_cvt_pk_bf16_f32 v95, v222, v223\n"
"v_mfma_f32_16x16x32_bf16 v[220:223], v[168:171], v[56:59], 0\n"
"v_cvt_pk_bf16_f32 v224, 0, v224\n"
"v_cvt_pk_bf16_f32 v225, 0, v225\n"
"v_cvt_pk_bf16_f32 v226, 0, v226\n"
"v_cvt_pk_bf16_f32 v227, 0, v227\n"
"v_mfma_f32_16x16x32_bf16 v[220:223], v[172:175], v[60:63], v[220:223]\n"
"v_mul_f32 v224, s85, v224\n"
"v_mul_f32 v225, s85, v225\n"
"v_mul_f32 v226, s85, v226\n"
"v_mul_f32 v227, s85, v227\n"
"v_mfma_f32_16x16x32_bf16 v[220:223], v[176:179], v[64:67], v[220:223]\n"
"v_cvt_pk_bf16_f32 v224, 0, v224\n"
"v_cvt_pk_bf16_f32 v225, 0, v225\n"
"v_cvt_pk_bf16_f32 v226, 0, v226\n"
"v_cvt_pk_bf16_f32 v227, 0, v227\n"
"v_mfma_f32_16x16x32_bf16 v[220:223], v[180:183], v[68:71], v[220:223]\n"
"v_fma_f32 v224, v224, s86, -v28\n"
"v_fma_f32 v225, v225, s86, -v28\n"
"v_fma_f32 v226, v226, s86, -v28\n"
"v_fma_f32 v227, v227, s86, -v28\n"
"v_mfma_f32_16x16x32_bf16 v[220:223], v[184:187], v[72:75], v[220:223]\n"
"v_exp_f32 v224, v224\n"
"v_exp_f32 v225, v225\n"
"v_exp_f32 v226, v226\n"
"v_exp_f32 v227, v227\n"
"v_mfma_f32_16x16x32_bf16 v[220:223], v[188:191], v[76:79], v[220:223]\n"
"v_cvt_pk_bf16_f32 v96, v224, v225\n"
"v_cvt_pk_bf16_f32 v97, v226, v227\n"
"v_mfma_f32_16x16x32_bf16 v[224:227], v[168:171], a[208:211], 0\n"
"v_cvt_pk_bf16_f32 v228, 0, v228\n"
"v_cvt_pk_bf16_f32 v229, 0, v229\n"
"v_cvt_pk_bf16_f32 v230, 0, v230\n"
"v_cvt_pk_bf16_f32 v231, 0, v231\n"
"v_mfma_f32_16x16x32_bf16 v[224:227], v[172:175], a[212:215], v[224:227]\n"
"v_mul_f32 v228, s85, v228\n"
"v_mul_f32 v229, s85, v229\n"
"v_mul_f32 v230, s85, v230\n"
"v_mul_f32 v231, s85, v231\n"
"v_mfma_f32_16x16x32_bf16 v[224:227], v[176:179], a[216:219], v[224:227]\n"
"v_cvt_pk_bf16_f32 v228, 0, v228\n"
"v_cvt_pk_bf16_f32 v229, 0, v229\n"
"v_cvt_pk_bf16_f32 v230, 0, v230\n"
"v_cvt_pk_bf16_f32 v231, 0, v231\n"
"v_mfma_f32_16x16x32_bf16 v[224:227], v[180:183], a[220:223], v[224:227]\n"
"v_fma_f32 v228, v228, s86, -v29\n"
"v_fma_f32 v229, v229, s86, -v29\n"
"v_fma_f32 v230, v230, s86, -v29\n"
"v_fma_f32 v231, v231, s86, -v29\n"
"v_mfma_f32_16x16x32_bf16 v[224:227], v[184:187], a[224:227], v[224:227]\n"
"v_exp_f32 v228, v228\n"
"v_exp_f32 v229, v229\n"
"v_exp_f32 v230, v230\n"
"v_exp_f32 v231, v231\n"
"v_mfma_f32_16x16x32_bf16 v[224:227], v[188:191], a[228:231], v[224:227]\n"
"ds_read_b128 v[144:147], v18 offset:0\n"
"ds_read_b128 v[148:151], v19 offset:0\n"
"ds_read_b128 v[152:155], v18 offset:1024\n"
"ds_read_b128 v[156:159], v19 offset:1024\n"
"ds_read_b128 v[160:163], v18 offset:2048\n"
"ds_read_b128 v[164:167], v19 offset:2048\n"
"v_cvt_pk_bf16_f32 v100, v228, v229\n"
"v_cvt_pk_bf16_f32 v101, v230, v231\n"
"v_mfma_f32_16x16x32_bf16 v[228:231], v[168:171], a[232:235], 0\n"
"v_mov_b32 v240, 0x3f803f80\n"
"v_mov_b32 v241, 0x3f803f80\n"
"v_mov_b32 v242, 0x3f803f80\n"
"v_mov_b32 v243, 0x3f803f80\n"
"v_cvt_pk_bf16_f32 v232, 0, v232\n"
"v_cvt_pk_bf16_f32 v233, 0, v233\n"
"v_cvt_pk_bf16_f32 v234, 0, v234\n"
"v_cvt_pk_bf16_f32 v235, 0, v235\n"
"v_mfma_f32_16x16x32_bf16 v[228:231], v[172:175], a[236:239], v[228:231]\n"
"v_mul_f32 v232, s85, v232\n"
"v_mul_f32 v233, s85, v233\n"
"v_mul_f32 v234, s85, v234\n"
"v_mul_f32 v235, s85, v235\n"
"v_mfma_f32_16x16x32_bf16 v[228:231], v[176:179], a[240:243], v[228:231]\n"
"v_cvt_pk_bf16_f32 v232, 0, v232\n"
"v_cvt_pk_bf16_f32 v233, 0, v233\n"
"v_cvt_pk_bf16_f32 v234, 0, v234\n"
"v_cvt_pk_bf16_f32 v235, 0, v235\n"
"v_mfma_f32_16x16x32_bf16 v[228:231], v[180:183], a[244:247], v[228:231]\n"
"v_fma_f32 v232, v232, s86, -v30\n"
"v_fma_f32 v233, v233, s86, -v30\n"
"v_fma_f32 v234, v234, s86, -v30\n"
"v_fma_f32 v235, v235, s86, -v30\n"
"v_mfma_f32_16x16x32_bf16 v[228:231], v[184:187], a[248:251], v[228:231]\n"
"v_exp_f32 v232, v232\n"
"v_exp_f32 v233, v233\n"
"v_exp_f32 v234, v234\n"
"v_exp_f32 v235, v235\n"
"v_mfma_f32_16x16x32_bf16 v[228:231], v[188:191], a[252:255], v[228:231]\n"
"s_waitcnt lgkmcnt(0)\n"
"v_cvt_pk_bf16_f32 v104, v232, v233\n"
"v_cvt_pk_bf16_f32 v105, v234, v235\n"
"v_mfma_f32_16x16x32_bf16 v[232:235], v[144:147], v[32:35], 0\n"
"v_cvt_pk_bf16_f32 v236, 0, v236\n"
"v_cvt_pk_bf16_f32 v237, 0, v237\n"
"v_cvt_pk_bf16_f32 v238, 0, v238\n"
"v_cvt_pk_bf16_f32 v239, 0, v239\n"
"v_mfma_f32_16x16x32_bf16 v[232:235], v[148:151], v[36:39], v[232:235]\n"
"v_mul_f32 v236, s85, v236\n"
"v_mul_f32 v237, s85, v237\n"
"v_mul_f32 v238, s85, v238\n"
"v_mul_f32 v239, s85, v239\n"
"v_mfma_f32_16x16x32_bf16 v[232:235], v[152:155], v[40:43], v[232:235]\n"
"v_cvt_pk_bf16_f32 v236, 0, v236\n"
"v_cvt_pk_bf16_f32 v237, 0, v237\n"
"v_cvt_pk_bf16_f32 v238, 0, v238\n"
"v_cvt_pk_bf16_f32 v239, 0, v239\n"
"v_mfma_f32_16x16x32_bf16 v[232:235], v[156:159], v[44:47], v[232:235]\n"
"v_fma_f32 v236, v236, s86, -v31\n"
"v_fma_f32 v237, v237, s86, -v31\n"
"v_fma_f32 v238, v238, s86, -v31\n"
"v_fma_f32 v239, v239, s86, -v31\n"
"v_mfma_f32_16x16x32_bf16 v[232:235], v[160:163], v[48:51], v[232:235]\n"
"v_exp_f32 v236, v236\n"
"v_exp_f32 v237, v237\n"
"v_exp_f32 v238, v238\n"
"v_exp_f32 v239, v239\n"
"v_mfma_f32_16x16x32_bf16 v[232:235], v[164:167], v[52:55], v[232:235]\n"
"ds_read_b64_tr_b16 v[192:193], v20 offset:24576\n"
"ds_read_b64_tr_b16 v[194:195], v20 offset:30720\n"
"ds_read_b64_tr_b16 v[196:197], v20 offset:36864\n"
"ds_read_b64_tr_b16 v[198:199], v20 offset:43008\n"
"v_cvt_pk_bf16_f32 v108, v236, v237\n"
"v_cvt_pk_bf16_f32 v109, v238, v239\n"
"v_mfma_f32_16x16x32_bf16 v[236:239], v[144:147], v[56:59], 0\n"
"v_cvt_pk_bf16_f32 v216, 0, v216\n"
"v_cvt_pk_bf16_f32 v217, 0, v217\n"
"v_cvt_pk_bf16_f32 v218, 0, v218\n"
"v_cvt_pk_bf16_f32 v219, 0, v219\n"
"v_cvt_pk_bf16_f32 v220, 0, v220\n"
"v_cvt_pk_bf16_f32 v221, 0, v221\n"
"v_cvt_pk_bf16_f32 v222, 0, v222\n"
"v_cvt_pk_bf16_f32 v223, 0, v223\n"
"v_cvt_pk_bf16_f32 v224, 0, v224\n"
"v_cvt_pk_bf16_f32 v225, 0, v225\n"
"v_cvt_pk_bf16_f32 v226, 0, v226\n"
"v_cvt_pk_bf16_f32 v227, 0, v227\n"
"v_cvt_pk_bf16_f32 v228, 0, v228\n"
"v_cvt_pk_bf16_f32 v229, 0, v229\n"
"v_cvt_pk_bf16_f32 v230, 0, v230\n"
"v_cvt_pk_bf16_f32 v231, 0, v231\n"
"v_mfma_f32_16x16x32_bf16 v[236:239], v[148:151], v[60:63], v[236:239]\n"
"v_mul_f32 v216, s85, v216\n"
"v_mul_f32 v217, s85, v217\n"
"v_mul_f32 v218, s85, v218\n"
"v_mul_f32 v219, s85, v219\n"
"v_mul_f32 v220, s85, v220\n"
"v_mul_f32 v221, s85, v221\n"
"v_mul_f32 v222, s85, v222\n"
"v_mul_f32 v223, s85, v223\n"
"v_mul_f32 v224, s85, v224\n"
"v_mul_f32 v225, s85, v225\n"
"v_mul_f32 v226, s85, v226\n"
"v_mul_f32 v227, s85, v227\n"
"v_mul_f32 v228, s85, v228\n"
"v_mul_f32 v229, s85, v229\n"
"v_mul_f32 v230, s85, v230\n"
"v_mul_f32 v231, s85, v231\n"
"v_mfma_f32_16x16x32_bf16 v[236:239], v[152:155], v[64:67], v[236:239]\n"
"v_cvt_pk_bf16_f32 v216, 0, v216\n"
"v_cvt_pk_bf16_f32 v217, 0, v217\n"
"v_cvt_pk_bf16_f32 v218, 0, v218\n"
"v_cvt_pk_bf16_f32 v219, 0, v219\n"
"v_cvt_pk_bf16_f32 v220, 0, v220\n"
"v_cvt_pk_bf16_f32 v221, 0, v221\n"
"v_cvt_pk_bf16_f32 v222, 0, v222\n"
"v_cvt_pk_bf16_f32 v223, 0, v223\n"
"v_cvt_pk_bf16_f32 v224, 0, v224\n"
"v_cvt_pk_bf16_f32 v225, 0, v225\n"
"v_cvt_pk_bf16_f32 v226, 0, v226\n"
"v_cvt_pk_bf16_f32 v227, 0, v227\n"
"v_cvt_pk_bf16_f32 v228, 0, v228\n"
"v_cvt_pk_bf16_f32 v229, 0, v229\n"
"v_cvt_pk_bf16_f32 v230, 0, v230\n"
"v_cvt_pk_bf16_f32 v231, 0, v231\n"
"v_mfma_f32_16x16x32_bf16 v[236:239], v[156:159], v[68:71], v[236:239]\n"
"v_fma_f32 v216, v216, s86, -v28\n"
"v_fma_f32 v217, v217, s86, -v28\n"
"v_fma_f32 v218, v218, s86, -v28\n"
"v_fma_f32 v219, v219, s86, -v28\n"
"v_fma_f32 v220, v220, s86, -v29\n"
"v_fma_f32 v221, v221, s86, -v29\n"
"v_fma_f32 v222, v222, s86, -v29\n"
"v_fma_f32 v223, v223, s86, -v29\n"
"v_fma_f32 v224, v224, s86, -v30\n"
"v_fma_f32 v225, v225, s86, -v30\n"
"v_fma_f32 v226, v226, s86, -v30\n"
"v_fma_f32 v227, v227, s86, -v30\n"
"v_fma_f32 v228, v228, s86, -v31\n"
"v_fma_f32 v229, v229, s86, -v31\n"
"v_fma_f32 v230, v230, s86, -v31\n"
"v_fma_f32 v231, v231, s86, -v31\n"
"v_mfma_f32_16x16x32_bf16 v[236:239], v[160:163], v[72:75], v[236:239]\n"
"v_exp_f32 v216, v216\n"
"v_exp_f32 v217, v217\n"
"v_exp_f32 v218, v218\n"
"v_exp_f32 v219, v219\n"
"v_exp_f32 v220, v220\n"
"v_exp_f32 v221, v221\n"
"v_exp_f32 v222, v222\n"
"v_exp_f32 v223, v223\n"
"v_exp_f32 v224, v224\n"
"v_exp_f32 v225, v225\n"
"v_exp_f32 v226, v226\n"
"v_exp_f32 v227, v227\n"
"v_exp_f32 v228, v228\n"
"v_exp_f32 v229, v229\n"
"v_exp_f32 v230, v230\n"
"v_exp_f32 v231, v231\n"
"v_mfma_f32_16x16x32_bf16 v[236:239], v[164:167], v[76:79], v[236:239]\n"
"s_waitcnt lgkmcnt(0)\n"
"v_cvt_pk_bf16_f32 v98, v216, v217\n"
"v_cvt_pk_bf16_f32 v99, v218, v219\n"
"v_cvt_pk_bf16_f32 v102, v220, v221\n"
"v_cvt_pk_bf16_f32 v103, v222, v223\n"
"v_cvt_pk_bf16_f32 v106, v224, v225\n"
"v_cvt_pk_bf16_f32 v107, v226, v227\n"
"v_cvt_pk_bf16_f32 v110, v228, v229\n"
"v_cvt_pk_bf16_f32 v111, v230, v231\n"
"v_mfma_f32_16x16x32_bf16 a[0:3], v[192:195], v[80:83], 0\n"
"buffer_load_dwordx4 v7, s[48:51], s84 offen offset:128 lds\n"
"v_mfma_f32_16x16x32_bf16 a[0:3], v[196:199], v[96:99], a[0:3]\n"
"ds_read_b64_tr_b16 v[200:201], v21 offset:24576\n"
"v_cvt_pk_bf16_f32 v232, 0, v232\n"
"v_mfma_f32_16x16x32_bf16 a[4:7], v[192:195], v[84:87], 0\n"
"s_waitcnt vmcnt(9)\n"
"s_barrier\n"
"v_cvt_pk_bf16_f32 v233, 0, v233\n"
"v_mfma_f32_16x16x32_bf16 a[4:7], v[196:199], v[100:103], a[4:7]\n"
"ds_read_b64_tr_b16 v[202:203], v21 offset:30720\n"
"v_cvt_pk_bf16_f32 v234, 0, v234\n"
"v_mfma_f32_16x16x32_bf16 a[8:11], v[192:195], v[88:91], 0\n"
"buffer_load_dwordx4 v7, s[48:51], s84 offen offset:3200 lds\n"
"v_mfma_f32_16x16x32_bf16 a[8:11], v[196:199], v[104:107], a[8:11]\n"
"s_cmp_lg_u32 s76, 0\n"
"s_cbranch_scc1 .Lsig0\n"
"s_mov_b64 exec, 1\n"
"v_mov_b32 v252, 1\n"
"buffer_atomic_add v252, off, s[36:39], s15 sc1\n"
"buffer_atomic_add v252, off, s[36:39], s14 offset:3072 sc1\n"
"s_mov_b64 exec, -1\n"
".Lsig0:\n"
"v_mfma_f32_16x16x32_bf16 v[216:219], v[144:147], a[208:211], 0\n"
"s_add_u32 m0, s83, 50944\n"
"ds_read_b64_tr_b16 v[204:205], v21 offset:36864\n"
"v_mfma_f32_16x16x32_bf16 v[216:219], v[148:151], a[212:215], v[216:219]\n"
"ds_read_b64_tr_b16 v[206:207], v21 offset:43008\n"
"v_cvt_pk_bf16_f32 v235, 0, v235\n"
"v_mfma_f32_16x16x32_bf16 v[216:219], v[152:155], a[216:219], v[216:219]\n"
"ds_read_b64_tr_b16 v[208:209], v22 offset:24576\n"
"v_mul_f32 v232, s85, v232\n"
"v_mfma_f32_16x16x32_bf16 v[216:219], v[156:159], a[220:223], v[216:219]\n"
"buffer_load_dwordx4 v7, s[48:51], s84 offen offset:256 lds\n"
"v_mfma_f32_16x16x32_bf16 v[216:219], v[160:163], a[224:227], v[216:219]\n"
"ds_read_b64_tr_b16 v[210:211], v22 offset:30720\n"
"v_mul_f32 v233, s85, v233\n"
"v_mfma_f32_16x16x32_bf16 v[216:219], v[164:167], a[228:231], v[216:219]\n"
"ds_read_b64_tr_b16 v[212:213], v22 offset:36864\n"
"v_mul_f32 v234, s85, v234\n"
"v_mfma_f32_16x16x32_bf16 a[12:15], v[192:195], v[92:95], 0\n"
"ds_read_b64_tr_b16 v[214:215], v22 offset:43008\n"
"v_mul_f32 v235, s85, v235\n"
"v_mfma_f32_16x16x32_bf16 a[12:15], v[196:199], v[108:111], a[12:15]\n"
"s_waitcnt lgkmcnt(4)\n"
"ds_read_b128 v[168:171], v18 offset:6144\n"
"v_mfma_f32_16x16x32_bf16 a[16:19], v[200:203], v[80:83], 0\n"
"buffer_load_dwordx4 v7, s[48:51], s84 offen offset:3328 lds\n"
"v_mfma_f32_16x16x32_bf16 a[16:19], v[204:207], v[96:99], a[16:19]\n"
"s_add_u32 s84, s83, 73728\n"
"s_add_u32 m0, s83, 98304\n"
"v_cvt_pk_bf16_f32 v216, 0, v216\n"
"v_mfma_f32_16x16x32_bf16 a[20:23], v[200:203], v[84:87], 0\n"
"ds_read_b128 v[172:175], v19 offset:6144\n"
"v_cvt_pk_bf16_f32 v217, 0, v217\n"
"v_mfma_f32_16x16x32_bf16 a[20:23], v[204:207], v[100:103], a[20:23]\n"
"ds_read_b128 v[176:179], v18 offset:7168\n"
"v_cvt_pk_bf16_f32 v218, 0, v218\n"
"v_mfma_f32_16x16x32_bf16 v[220:223], v[144:147], a[232:235], 0\n"
"ds_read_b128 v[180:183], v19 offset:7168\n"
"v_cvt_pk_bf16_f32 v219, 0, v219\n"
"v_mfma_f32_16x16x32_bf16 v[220:223], v[148:151], a[236:239], v[220:223]\n"
"buffer_load_dwordx4 v8, s[44:47], s84 offen offset:0 lds\n"
"v_mfma_f32_16x16x32_bf16 v[220:223], v[152:155], a[240:243], v[220:223]\n"
"ds_read_b128 v[184:187], v18 offset:8192\n"
"v_mul_f32 v216, s85, v216\n"
"v_mfma_f32_16x16x32_bf16 v[220:223], v[156:159], a[244:247], v[220:223]\n"
"ds_read_b128 v[188:191], v19 offset:8192\n"
"v_mul_f32 v217, s85, v217\n"
"v_mfma_f32_16x16x32_bf16 v[220:223], v[160:163], a[248:251], v[220:223]\n"
"ds_read_b64_tr_b16 v[192:193], v23 offset:24576\n"
"v_mul_f32 v218, s85, v218\n"
"v_mfma_f32_16x16x32_bf16 v[220:223], v[164:167], a[252:255], v[220:223]\n"
"ds_read_b64_tr_b16 v[194:195], v23 offset:30720\n"
"v_mul_f32 v219, s85, v219\n"
"v_mfma_f32_16x16x32_bf16 a[24:27], v[200:203], v[88:91], 0\n"
"buffer_load_dwordx4 v9, s[44:47], s84 offen offset:3072 lds\n"
"v_mfma_f32_16x16x32_bf16 a[24:27], v[204:207], v[104:107], a[24:27]\n"
"s_add_u32 m0, s83, 99200\n"
"v_cvt_pk_bf16_f32 v216, 0, v216\n"
"v_mfma_f32_16x16x32_bf16 a[28:31], v[200:203], v[92:95], 0\n"
"ds_read_b64_tr_b16 v[196:197], v23 offset:36864\n"
"v_cvt_pk_bf16_f32 v217, 0, v217\n"
"v_mfma_f32_16x16x32_bf16 a[28:31], v[204:207], v[108:111], a[28:31]\n"
"s_waitcnt lgkmcnt(3)\n"
"v_cvt_pk_bf16_f32 v218, 0, v218\n"
"v_cvt_pk_bf16_f32 v219, 0, v219\n"
"v_mfma_f32_16x16x32_bf16 a[32:35], v[208:211], v[80:83], 0\n"
"ds_read_b64_tr_b16 v[198:199], v23 offset:43008\n"
"v_fma_f32 v216, v216, s86, -v30\n"
"v_mfma_f32_16x16x32_bf16 a[32:35], v[212:215], v[96:99], a[32:35]\n"
"v_fma_f32 v217, v217, s86, -v30\n"
"v_fma_f32 v218, v218, s86, -v30\n"
"v_fma_f32 v219, v219, s86, -v30\n"
"v_mfma_f32_16x16x32_bf16 v[224:227], v[168:171], v[32:35], 0\n"
"v_exp_f32 v216, v216\n"
"v_exp_f32 v217, v217\n"
"v_exp_f32 v218, v218\n"
"v_exp_f32 v219, v219\n"
"v_mfma_f32_16x16x32_bf16 v[224:227], v[172:175], v[36:39], v[224:227]\n"
"v_cvt_pk_bf16_f32 v120, v216, v217\n"
"v_cvt_pk_bf16_f32 v121, v218, v219\n"
"v_mfma_f32_16x16x32_bf16 v[224:227], v[176:179], v[40:43], v[224:227]\n"
"buffer_load_dwordx4 v8, s[44:47], s84 offen offset:128 lds\n"
"v_mfma_f32_16x16x32_bf16 v[224:227], v[180:183], v[44:47], v[224:227]\n"
"ds_read_b64_tr_b16 v[200:201], v20 offset:25600\n"
"v_cvt_pk_bf16_f32 v220, 0, v220\n"
"v_mfma_f32_16x16x32_bf16 v[224:227], v[184:187], v[48:51], v[224:227]\n"
"ds_read_b64_tr_b16 v[202:203], v20 offset:31744\n"
"v_cvt_pk_bf16_f32 v221, 0, v221\n"
"v_mfma_f32_16x16x32_bf16 v[224:227], v[188:191], v[52:55], v[224:227]\n"
"ds_read_b64_tr_b16 v[204:205], v20 offset:37888\n"
"v_cvt_pk_bf16_f32 v222, 0, v222\n"
"v_mfma_f32_16x16x32_bf16 a[36:39], v[208:211], v[84:87], 0\n"
"buffer_load_dwordx4 v9, s[44:47], s84 offen offset:3200 lds\n"
"v_mfma_f32_16x16x32_bf16 a[36:39], v[212:215], v[100:103], a[36:39]\n"
"s_add_u32 m0, s83, 100096\n"
"v_cvt_pk_bf16_f32 v223, 0, v223\n"
"v_mul_f32 v220, s85, v220\n"
"v_mfma_f32_16x16x32_bf16 a[40:43], v[208:211], v[88:91], 0\n"
"ds_read_b64_tr_b16 v[206:207], v20 offset:44032\n"
"v_mul_f32 v221, s85, v221\n"
"v_mfma_f32_16x16x32_bf16 a[40:43], v[212:215], v[104:107], a[40:43]\n"
"s_waitcnt lgkmcnt(4)\n"
"v_mul_f32 v222, s85, v222\n"
"v_mul_f32 v223, s85, v223\n"
"v_mfma_f32_16x16x32_bf16 a[44:47], v[208:211], v[92:95], 0\n"
"v_cvt_pk_bf16_f32 v220, 0, v220\n"
"v_cvt_pk_bf16_f32 v221, 0, v221\n"
"v_cvt_pk_bf16_f32 v222, 0, v222\n"
"v_cvt_pk_bf16_f32 v223, 0, v223\n"
"v_mfma_f32_16x16x32_bf16 a[44:47], v[212:215], v[108:111], a[44:47]\n"
"v_fma_f32 v220, v220, s86, -v31\n"
"v_fma_f32 v221, v221, s86, -v31\n"
"v_fma_f32 v222, v222, s86, -v31\n"
"v_fma_f32 v223, v223, s86, -v31\n"
"v_mfma_f32_16x16x32_bf16 a[48:51], v[192:195], v[80:83], 0\n"
"v_exp_f32 v220, v220\n"
"v_exp_f32 v221, v221\n"
"v_exp_f32 v222, v222\n"
"v_exp_f32 v223, v223\n"
"v_mfma_f32_16x16x32_bf16 a[48:51], v[196:199], v[96:99], a[48:51]\n"
"v_cvt_pk_bf16_f32 v124, v220, v221\n"
"v_cvt_pk_bf16_f32 v125, v222, v223\n"
"v_mfma_f32_16x16x32_bf16 v[228:231], v[168:171], v[56:59], 0\n"
"buffer_load_dwordx4 v8, s[44:47], s84 offen offset:256 lds\n"
"v_mfma_f32_16x16x32_bf16 v[228:231], v[172:175], v[60:63], v[228:231]\n"
"ds_read_b64_tr_b16 v[208:209], v21 offset:25600\n"
"v_cvt_pk_bf16_f32 v224, 0, v224\n"
"v_mfma_f32_16x16x32_bf16 v[228:231], v[176:179], v[64:67], v[228:231]\n"
"ds_read_b64_tr_b16 v[210:211], v21 offset:31744\n"
"v_cvt_pk_bf16_f32 v225, 0, v225\n"
"v_mfma_f32_16x16x32_bf16 v[228:231], v[180:183], v[68:71], v[228:231]\n"
"ds_read_b64_tr_b16 v[212:213], v21 offset:37888\n"
"v_cvt_pk_bf16_f32 v226, 0, v226\n"
"v_mfma_f32_16x16x32_bf16 v[228:231], v[184:187], v[72:75], v[228:231]\n"
"buffer_load_dwordx4 v9, s[44:47], s84 offen offset:3328 lds\n"
"v_mfma_f32_16x16x32_bf16 v[228:231], v[188:191], v[76:79], v[228:231]\n"
"s_add_u32 s84, s83, 49152\n"
"s_add_u32 m0, s83, 0\n"
"v_cvt_pk_bf16_f32 v227, 0, v227\n"
"v_mfma_f32_16x16x32_bf16 a[52:55], v[192:195], v[84:87], 0\n"
"ds_read_b64_tr_b16 v[214:215], v21 offset:44032\n"
"v_mul_f32 v224, s85, v224\n"
"v_mfma_f32_16x16x32_bf16 a[52:55], v[196:199], v[100:103], a[52:55]\n"
"v_mul_f32 v225, s85, v225\n"
"v_mul_f32 v226, s85, v226\n"
"v_mul_f32 v227, s85, v227\n"
"v_mfma_f32_16x16x32_bf16 a[56:59], v[192:195], v[88:91], 0\n"
"v_cvt_pk_bf16_f32 v232, 0, v232\n"
"v_cvt_pk_bf16_f32 v233, 0, v233\n"
"v_cvt_pk_bf16_f32 v234, 0, v234\n"
"v_cvt_pk_bf16_f32 v235, 0, v235\n"
"v_cvt_pk_bf16_f32 v224, 0, v224\n"
"v_cvt_pk_bf16_f32 v225, 0, v225\n"
"v_cvt_pk_bf16_f32 v226, 0, v226\n"
"v_cvt_pk_bf16_f32 v227, 0, v227\n"
"v_mfma_f32_16x16x32_bf16 a[56:59], v[196:199], v[104:107], a[56:59]\n"
"v_fma_f32 v232, v232, s86, -v28\n"
"v_fma_f32 v233, v233, s86, -v28\n"
"v_fma_f32 v234, v234, s86, -v28\n"
"v_fma_f32 v235, v235, s86, -v28\n"
"v_fma_f32 v224, v224, s86, -v28\n"
"v_fma_f32 v225, v225, s86, -v28\n"
"v_fma_f32 v226, v226, s86, -v28\n"
"v_fma_f32 v227, v227, s86, -v28\n"
"v_mfma_f32_16x16x32_bf16 a[60:63], v[192:195], v[92:95], 0\n"
"v_exp_f32 v232, v232\n"
"v_exp_f32 v233, v233\n"
"v_exp_f32 v234, v234\n"
"v_exp_f32 v235, v235\n"
"v_exp_f32 v224, v224\n"
"v_exp_f32 v225, v225\n"
"v_exp_f32 v226, v226\n"
"v_exp_f32 v227, v227\n"
"v_mfma_f32_16x16x32_bf16 a[60:63], v[196:199], v[108:111], a[60:63]\n"
"v_cvt_pk_bf16_f32 v112, v232, v233\n"
"v_cvt_pk_bf16_f32 v113, v234, v235\n"
"v_cvt_pk_bf16_f32 v114, v224, v225\n"
"v_cvt_pk_bf16_f32 v115, v226, v227\n"
"v_mfma_f32_16x16x32_bf16 v[232:235], v[168:171], a[208:211], 0\n"
"buffer_load_dwordx4 v7, s[48:51], s84 offen offset:0 lds\n"
"v_mfma_f32_16x16x32_bf16 v[232:235], v[172:175], a[212:215], v[232:235]\n"
"ds_read_b128 v[144:147], v18 offset:12288\n"
"v_cvt_pk_bf16_f32 v236, 0, v236\n"
"v_mfma_f32_16x16x32_bf16 v[232:235], v[176:179], a[216:219], v[232:235]\n"
"ds_read_b128 v[148:151], v19 offset:12288\n"
"v_cvt_pk_bf16_f32 v237, 0, v237\n"
"v_mfma_f32_16x16x32_bf16 v[232:235], v[180:183], a[220:223], v[232:235]\n"
"s_waitcnt lgkmcnt(6)\n"
"v_cvt_pk_bf16_f32 v238, 0, v238\n"
"v_cvt_pk_bf16_f32 v239, 0, v239\n"
"v_mfma_f32_16x16x32_bf16 v[232:235], v[184:187], a[224:227], v[232:235]\n"
"buffer_load_dwordx4 v7, s[48:51], s84 offen offset:3072 lds\n"
"v_mfma_f32_16x16x32_bf16 v[232:235], v[188:191], a[228:231], v[232:235]\n"
"s_add_u32 m0, s83, 896\n"
"v_mul_f32 v236, s85, v236\n"
"v_mul_f32 v237, s85, v237\n"
"v_mfma_f32_16x16x32_bf16 a[64:67], v[200:203], v[80:83], 0\n"
"ds_read_b128 v[152:155], v18 offset:13312\n"
"v_mul_f32 v238, s85, v238\n"
"v_mfma_f32_16x16x32_bf16 a[64:67], v[204:207], v[96:99], a[64:67]\n"
"v_mul_f32 v239, s85, v239\n"
"v_cvt_pk_bf16_f32 v236, 0, v236\n"
"v_mfma_f32_16x16x32_bf16 a[68:71], v[200:203], v[84:87], 0\n"
"v_cvt_pk_bf16_f32 v237, 0, v237\n"
"v_cvt_pk_bf16_f32 v238, 0, v238\n"
"v_cvt_pk_bf16_f32 v239, 0, v239\n"
"v_cvt_pk_bf16_f32 v228, 0, v228\n"
"v_cvt_pk_bf16_f32 v229, 0, v229\n"
"v_cvt_pk_bf16_f32 v230, 0, v230\n"
"v_cvt_pk_bf16_f32 v231, 0, v231\n"
"v_mfma_f32_16x16x32_bf16 a[68:71], v[204:207], v[100:103], a[68:71]\n"
"v_fma_f32 v236, v236, s86, -v29\n"
"v_fma_f32 v237, v237, s86, -v29\n"
"v_fma_f32 v238, v238, s86, -v29\n"
"v_fma_f32 v239, v239, s86, -v29\n"
"v_mul_f32 v228, s85, v228\n"
"v_mul_f32 v229, s85, v229\n"
"v_mul_f32 v230, s85, v230\n"
"v_mul_f32 v231, s85, v231\n"
"v_mfma_f32_16x16x32_bf16 a[72:75], v[200:203], v[88:91], 0\n"
"v_exp_f32 v236, v236\n"
"v_exp_f32 v237, v237\n"
"v_exp_f32 v238, v238\n"
"v_exp_f32 v239, v239\n"
"v_cvt_pk_bf16_f32 v228, 0, v228\n"
"v_cvt_pk_bf16_f32 v229, 0, v229\n"
"v_cvt_pk_bf16_f32 v230, 0, v230\n"
"v_cvt_pk_bf16_f32 v231, 0, v231\n"
"v_mfma_f32_16x16x32_bf16 a[72:75], v[204:207], v[104:107], a[72:75]\n"
"v_cvt_pk_bf16_f32 v116, v236, v237\n"
"v_cvt_pk_bf16_f32 v117, v238, v239\n"
"v_fma_f32 v228, v228, s86, -v29\n"
"v_fma_f32 v229, v229, s86, -v29\n"
"v_fma_f32 v230, v230, s86, -v29\n"
"v_fma_f32 v231, v231, s86, -v29\n"
"v_mfma_f32_16x16x32_bf16 v[236:239], v[168:171], a[232:235], 0\n"
"v_exp_f32 v228, v228\n"
"v_exp_f32 v229, v229\n"
"v_exp_f32 v230, v230\n"
"v_exp_f32 v231, v231\n"
"v_mfma_f32_16x16x32_bf16 v[236:239], v[172:175], a[236:239], v[236:239]\n"
"v_cvt_pk_bf16_f32 v118, v228, v229\n"
"v_cvt_pk_bf16_f32 v119, v230, v231\n"
"v_mfma_f32_16x16x32_bf16 v[236:239], v[176:179], a[240:243], v[236:239]\n"
"buffer_load_dwordx4 v7, s[48:51], s84 offen offset:128 lds\n"
"v_mfma_f32_16x16x32_bf16 v[236:239], v[180:183], a[244:247], v[236:239]\n"
"ds_read_b128 v[156:159], v19 offset:13312\n"
"v_cvt_pk_bf16_f32 v232, 0, v232\n"
"v_mfma_f32_16x16x32_bf16 v[236:239], v[184:187], a[248:251], v[236:239]\n"
"ds_read_b128 v[160:163], v18 offset:14336\n"
"v_cvt_pk_bf16_f32 v233, 0, v233\n"
"v_mfma_f32_16x16x32_bf16 v[236:239], v[188:191], a[252:255], v[236:239]\n"
"ds_read_b128 v[164:167], v19 offset:14336\n"
"s_waitcnt lgkmcnt(6)\n"
"v_mfma_f32_16x16x32_bf16 a[76:79], v[200:203], v[92:95], 0\n"
"buffer_load_dwordx4 v7, s[48:51], s84 offen offset:3200 lds\n"
"v_mfma_f32_16x16x32_bf16 a[76:79], v[204:207], v[108:111], a[76:79]\n"
"s_add_u32 m0, s83, 1792\n"
"v_cvt_pk_bf16_f32 v234, 0, v234\n"
"v_cvt_pk_bf16_f32 v235, 0, v235\n"
"v_mfma_f32_16x16x32_bf16 a[80:83], v[208:211], v[80:83], 0\n"
"ds_read_b64_tr_b16 v[192:193], v22 offset:25600\n"
"v_mul_f32 v232, s85, v232\n"
"v_mfma_f32_16x16x32_bf16 a[80:83], v[212:215], v[96:99], a[80:83]\n"
"v_mul_f32 v233, s85, v233\n"
"v_mul_f32 v234, s85, v234\n"
"v_mul_f32 v235, s85, v235\n"
"v_mfma_f32_16x16x32_bf16 a[84:87], v[208:211], v[84:87], 0\n"
"v_cvt_pk_bf16_f32 v232, 0, v232\n"
"v_cvt_pk_bf16_f32 v233, 0, v233\n"
"v_cvt_pk_bf16_f32 v234, 0, v234\n"
"v_cvt_pk_bf16_f32 v235, 0, v235\n"
"v_mfma_f32_16x16x32_bf16 a[84:87], v[212:215], v[100:103], a[84:87]\n"
"s_waitcnt lgkmcnt(1)\n"
"v_fma_f32 v232, v232, s86, -v30\n"
"v_fma_f32 v233, v233, s86, -v30\n"
"v_fma_f32 v234, v234, s86, -v30\n"
"v_fma_f32 v235, v235, s86, -v30\n"
"v_mfma_f32_16x16x32_bf16 v[216:219], v[144:147], v[32:35], 0\n"
"v_exp_f32 v232, v232\n"
"v_exp_f32 v233, v233\n"
"v_exp_f32 v234, v234\n"
"v_exp_f32 v235, v235\n"
"v_mfma_f32_16x16x32_bf16 v[216:219], v[148:151], v[36:39], v[216:219]\n"
"v_cvt_pk_bf16_f32 v122, v232, v233\n"
"v_cvt_pk_bf16_f32 v123, v234, v235\n"
"v_mfma_f32_16x16x32_bf16 v[216:219], v[152:155], v[40:43], v[216:219]\n"
"buffer_load_dwordx4 v7, s[48:51], s84 offen offset:256 lds\n"
"v_mfma_f32_16x16x32_bf16 v[216:219], v[156:159], v[44:47], v[216:219]\n"
"ds_read_b64_tr_b16 v[194:195], v22 offset:31744\n"
"ds_read_b64_tr_b16 v[196:197], v22 offset:37888\n"
"ds_read_b64_tr_b16 v[198:199], v22 offset:44032\n"
"v_mfma_f32_16x16x32_bf16 v[216:219], v[160:163], v[48:51], v[216:219]\n"
"ds_read_b64_tr_b16 v[200:201], v23 offset:25600\n"
"v_cvt_pk_bf16_f32 v236, 0, v236\n"
"v_mfma_f32_16x16x32_bf16 v[216:219], v[164:167], v[52:55], v[216:219]\n"
"ds_read_b64_tr_b16 v[202:203], v23 offset:31744\n"
"v_cvt_pk_bf16_f32 v237, 0, v237\n"
"v_mfma_f32_16x16x32_bf16 a[88:91], v[208:211], v[88:91], 0\n"
"buffer_load_dwordx4 v7, s[48:51], s84 offen offset:3328 lds\n"
"v_mfma_f32_16x16x32_bf16 a[88:91], v[212:215], v[104:107], a[88:91]\n"
"ds_read_b64_tr_b16 v[204:205], v23 offset:37888\n"
"v_cvt_pk_bf16_f32 v238, 0, v238\n"
"v_mfma_f32_16x16x32_bf16 a[92:95], v[208:211], v[92:95], 0\n"
"v_cvt_pk_bf16_f32 v239, 0, v239\n"
"v_mul_f32 v236, s85, v236\n"
"v_mfma_f32_16x16x32_bf16 a[92:95], v[212:215], v[108:111], a[92:95]\n"
"s_waitcnt lgkmcnt(3)\n"
"v_mul_f32 v237, s85, v237\n"
"v_mul_f32 v238, s85, v238\n"
"v_mul_f32 v239, s85, v239\n"
"v_mfma_f32_16x16x32_bf16 a[96:99], v[192:195], v[80:83], 0\n"
"v_cvt_pk_bf16_f32 v236, 0, v236\n"
"v_cvt_pk_bf16_f32 v237, 0, v237\n"
"v_cvt_pk_bf16_f32 v238, 0, v238\n"
"v_cvt_pk_bf16_f32 v239, 0, v239\n"
"v_mfma_f32_16x16x32_bf16 a[96:99], v[196:199], v[96:99], a[96:99]\n"
"v_fma_f32 v236, v236, s86, -v31\n"
"v_fma_f32 v237, v237, s86, -v31\n"
"v_fma_f32 v238, v238, s86, -v31\n"
"v_fma_f32 v239, v239, s86, -v31\n"
"v_mfma_f32_16x16x32_bf16 a[100:103], v[192:195], v[84:87], 0\n"
"v_exp_f32 v236, v236\n"
"v_exp_f32 v237, v237\n"
"v_exp_f32 v238, v238\n"
"v_exp_f32 v239, v239\n"
"v_mfma_f32_16x16x32_bf16 a[100:103], v[196:199], v[100:103], a[100:103]\n"
"v_cvt_pk_bf16_f32 v126, v236, v237\n"
"v_cvt_pk_bf16_f32 v127, v238, v239\n"
"v_mfma_f32_16x16x32_bf16 v[220:223], v[144:147], v[56:59], 0\n"
"ds_read_b64_tr_b16 v[206:207], v23 offset:44032\n"
"v_cvt_pk_bf16_f32 v216, 0, v216\n"
"v_mfma_f32_16x16x32_bf16 v[220:223], v[148:151], v[60:63], v[220:223]\n"
"ds_read_b64_tr_b16 v[208:209], v20 offset:26624\n"
"v_cvt_pk_bf16_f32 v217, 0, v217\n"
"v_mfma_f32_16x16x32_bf16 v[220:223], v[152:155], v[64:67], v[220:223]\n"
"ds_read_b64_tr_b16 v[210:211], v20 offset:32768\n"
"v_cvt_pk_bf16_f32 v218, 0, v218\n"
"v_mfma_f32_16x16x32_bf16 v[220:223], v[156:159], v[68:71], v[220:223]\n"
"ds_read_b64_tr_b16 v[212:213], v20 offset:38912\n"
"v_cvt_pk_bf16_f32 v219, 0, v219\n"
"v_mfma_f32_16x16x32_bf16 v[220:223], v[160:163], v[72:75], v[220:223]\n"
"ds_read_b64_tr_b16 v[214:215], v20 offset:45056\n"
"v_mul_f32 v216, s85, v216\n"
"v_mfma_f32_16x16x32_bf16 v[220:223], v[164:167], v[76:79], v[220:223]\n"
"ds_read_b128 v[168:171], v18 offset:18432\n"
"v_mul_f32 v217, s85, v217\n"
"v_mfma_f32_16x16x32_bf16 a[104:107], v[192:195], v[88:91], 0\n"
"ds_read_b128 v[172:175], v19 offset:18432\n"
"v_mul_f32 v218, s85, v218\n"
"v_mfma_f32_16x16x32_bf16 a[104:107], v[196:199], v[104:107], a[104:107]\n"
"s_waitcnt lgkmcnt(6)\n"
"v_mul_f32 v219, s85, v219\n"
"v_cvt_pk_bf16_f32 v216, 0, v216\n"
"v_mfma_f32_16x16x32_bf16 a[108:111], v[192:195], v[92:95], 0\n"
"v_cvt_pk_bf16_f32 v217, 0, v217\n"
"v_cvt_pk_bf16_f32 v218, 0, v218\n"
"v_cvt_pk_bf16_f32 v219, 0, v219\n"
"v_mfma_f32_16x16x32_bf16 a[108:111], v[196:199], v[108:111], a[108:111]\n"
"v_fma_f32 v216, v216, s86, -v28\n"
"v_fma_f32 v217, v217, s86, -v28\n"
"v_fma_f32 v218, v218, s86, -v28\n"
"v_fma_f32 v219, v219, s86, -v28\n"
"v_mfma_f32_16x16x32_bf16 a[112:115], v[200:203], v[80:83], 0\n"
"v_exp_f32 v216, v216\n"
"v_exp_f32 v217, v217\n"
"v_exp_f32 v218, v218\n"
"v_exp_f32 v219, v219\n"
"v_mfma_f32_16x16x32_bf16 a[112:115], v[204:207], v[96:99], a[112:115]\n"
"v_cvt_pk_bf16_f32 v128, v216, v217\n"
"v_cvt_pk_bf16_f32 v129, v218, v219\n"
"v_mfma_f32_16x16x32_bf16 v[224:227], v[144:147], a[208:211], 0\n"
"ds_read_b128 v[176:179], v18 offset:19456\n"
"v_cvt_pk_bf16_f32 v220, 0, v220\n"
"v_mfma_f32_16x16x32_bf16 v[224:227], v[148:151], a[212:215], v[224:227]\n"
"ds_read_b128 v[180:183], v19 offset:19456\n"
"v_cvt_pk_bf16_f32 v221, 0, v221\n"
"v_mfma_f32_16x16x32_bf16 v[224:227], v[152:155], a[216:219], v[224:227]\n"
"ds_read_b128 v[184:187], v18 offset:20480\n"
"v_cvt_pk_bf16_f32 v222, 0, v222\n"
"v_mfma_f32_16x16x32_bf16 v[224:227], v[156:159], a[220:223], v[224:227]\n"
"ds_read_b128 v[188:191], v19 offset:20480\n"
"v_cvt_pk_bf16_f32 v223, 0, v223\n"
"v_mfma_f32_16x16x32_bf16 v[224:227], v[160:163], a[224:227], v[224:227]\n"
"ds_read_b64_tr_b16 v[192:193], v21 offset:26624\n"
"v_mul_f32 v220, s85, v220\n"
"v_mfma_f32_16x16x32_bf16 v[224:227], v[164:167], a[228:231], v[224:227]\n"
"ds_read_b64_tr_b16 v[194:195], v21 offset:32768\n"
"v_mul_f32 v221, s85, v221\n"
"v_mfma_f32_16x16x32_bf16 a[116:119], v[200:203], v[84:87], 0\n"
"ds_read_b64_tr_b16 v[196:197], v21 offset:38912\n"
"v_mul_f32 v222, s85, v222\n"
"v_mfma_f32_16x16x32_bf16 a[116:119], v[204:207], v[100:103], a[116:119]\n"
"ds_read_b64_tr_b16 v[198:199], v21 offset:45056\n"
"v_mul_f32 v223, s85, v223\n"
"v_mfma_f32_16x16x32_bf16 a[120:123], v[200:203], v[88:91], 0\n"
"v_cvt_pk_bf16_f32 v220, 0, v220\n"
"v_cvt_pk_bf16_f32 v221, 0, v221\n"
"v_mfma_f32_16x16x32_bf16 a[120:123], v[204:207], v[104:107], a[120:123]\n"
"v_cvt_pk_bf16_f32 v222, 0, v222\n"
"v_cvt_pk_bf16_f32 v223, 0, v223\n"
"v_mfma_f32_16x16x32_bf16 a[124:127], v[200:203], v[92:95], 0\n"
"v_fma_f32 v220, v220, s86, -v29\n"
"v_fma_f32 v221, v221, s86, -v29\n"
"v_mfma_f32_16x16x32_bf16 a[124:127], v[204:207], v[108:111], a[124:127]\n"
"v_fma_f32 v222, v222, s86, -v29\n"
"v_fma_f32 v223, v223, s86, -v29\n"
"v_mfma_f32_16x16x32_bf16 v[228:231], v[144:147], a[232:235], 0\n"
"v_exp_f32 v220, v220\n"
"v_exp_f32 v221, v221\n"
"v_exp_f32 v222, v222\n"
"v_exp_f32 v223, v223\n"
"v_mfma_f32_16x16x32_bf16 v[228:231], v[148:151], a[236:239], v[228:231]\n"
"v_cvt_pk_bf16_f32 v132, v220, v221\n"
"v_cvt_pk_bf16_f32 v133, v222, v223\n"
"v_mfma_f32_16x16x32_bf16 v[228:231], v[152:155], a[240:243], v[228:231]\n"
"ds_read_b64_tr_b16 v[200:201], v22 offset:26624\n"
"v_cvt_pk_bf16_f32 v224, 0, v224\n"
"v_mfma_f32_16x16x32_bf16 v[228:231], v[156:159], a[244:247], v[228:231]\n"
"s_waitcnt lgkmcnt(11)\n"
"v_cvt_pk_bf16_f32 v225, 0, v225\n"
"v_cvt_pk_bf16_f32 v226, 0, v226\n"
"v_mfma_f32_16x16x32_bf16 v[228:231], v[160:163], a[248:251], v[228:231]\n"
"ds_read_b64_tr_b16 v[202:203], v22 offset:32768\n"
"v_cvt_pk_bf16_f32 v227, 0, v227\n"
"v_mfma_f32_16x16x32_bf16 v[228:231], v[164:167], a[252:255], v[228:231]\n"
"ds_read_b64_tr_b16 v[204:205], v22 offset:38912\n"
"v_mul_f32 v224, s85, v224\n"
"v_mfma_f32_16x16x32_bf16 a[128:131], v[208:211], v[80:83], 0\n"
"ds_read_b64_tr_b16 v[206:207], v22 offset:45056\n"
"v_mul_f32 v225, s85, v225\n"
"v_mfma_f32_16x16x32_bf16 a[128:131], v[212:215], v[96:99], a[128:131]\n"
"v_mul_f32 v226, s85, v226\n"
"v_mul_f32 v227, s85, v227\n"
"v_mfma_f32_16x16x32_bf16 a[132:135], v[208:211], v[84:87], 0\n"
"v_cvt_pk_bf16_f32 v224, 0, v224\n"
"v_cvt_pk_bf16_f32 v225, 0, v225\n"
"v_mfma_f32_16x16x32_bf16 a[132:135], v[212:215], v[100:103], a[132:135]\n"
"s_waitcnt lgkmcnt(8)\n"
"v_cvt_pk_bf16_f32 v226, 0, v226\n"
"v_cvt_pk_bf16_f32 v227, 0, v227\n"
"v_mfma_f32_16x16x32_bf16 a[136:139], v[208:211], v[88:91], 0\n"
"v_fma_f32 v224, v224, s86, -v30\n"
"v_fma_f32 v225, v225, s86, -v30\n"
"v_mfma_f32_16x16x32_bf16 a[136:139], v[212:215], v[104:107], a[136:139]\n"
"v_fma_f32 v226, v226, s86, -v30\n"
"v_fma_f32 v227, v227, s86, -v30\n"
"v_mfma_f32_16x16x32_bf16 v[232:235], v[168:171], v[32:35], 0\n"
"v_exp_f32 v224, v224\n"
"v_exp_f32 v225, v225\n"
"v_exp_f32 v226, v226\n"
"v_exp_f32 v227, v227\n"
"v_mfma_f32_16x16x32_bf16 v[232:235], v[172:175], v[36:39], v[232:235]\n"
"v_cvt_pk_bf16_f32 v136, v224, v225\n"
"v_cvt_pk_bf16_f32 v137, v226, v227\n"
"v_mfma_f32_16x16x32_bf16 v[232:235], v[176:179], v[40:43], v[232:235]\n"
"v_cvt_pk_bf16_f32 v228, 0, v228\n"
"v_cvt_pk_bf16_f32 v229, 0, v229\n"
"v_mfma_f32_16x16x32_bf16 v[232:235], v[180:183], v[44:47], v[232:235]\n"
"v_cvt_pk_bf16_f32 v230, 0, v230\n"
"v_cvt_pk_bf16_f32 v231, 0, v231\n"
"v_mfma_f32_16x16x32_bf16 v[232:235], v[184:187], v[48:51], v[232:235]\n"
"v_mul_f32 v228, s85, v228\n"
"v_mul_f32 v229, s85, v229\n"
"v_mfma_f32_16x16x32_bf16 v[232:235], v[188:191], v[52:55], v[232:235]\n"
"s_waitcnt lgkmcnt(4)\n"
"v_mul_f32 v230, s85, v230\n"
"v_mul_f32 v231, s85, v231\n"
"v_mfma_f32_16x16x32_bf16 a[140:143], v[208:211], v[92:95], 0\n"
"v_cvt_pk_bf16_f32 v228, 0, v228\n"
"v_cvt_pk_bf16_f32 v229, 0, v229\n"
"v_mfma_f32_16x16x32_bf16 a[140:143], v[212:215], v[108:111], a[140:143]\n"
"ds_read_b64_tr_b16 v[208:209], v23 offset:26624\n"
"v_cvt_pk_bf16_f32 v230, 0, v230\n"
"v_mfma_f32_16x16x32_bf16 a[144:147], v[192:195], v[80:83], 0\n"
"ds_read_b64_tr_b16 v[210:211], v23 offset:32768\n"
"v_cvt_pk_bf16_f32 v231, 0, v231\n"
"v_mfma_f32_16x16x32_bf16 a[144:147], v[196:199], v[96:99], a[144:147]\n"
"ds_read_b64_tr_b16 v[212:213], v23 offset:38912\n"
"v_fma_f32 v228, v228, s86, -v31\n"
"v_mfma_f32_16x16x32_bf16 a[148:151], v[192:195], v[84:87], 0\n"
"s_waitcnt vmcnt(12)\n"
"s_barrier\n"
"s_add_u32 s84, s83, 98304\n"
"v_mfma_f32_16x16x32_bf16 a[148:151], v[196:199], v[100:103], a[148:151]\n"
"s_add_u32 m0, s83, 122880\n"
"v_fma_f32 v229, v229, s86, -v31\n"
"v_fma_f32 v230, v230, s86, -v31\n"
"v_fma_f32 v231, v231, s86, -v31\n"
"v_mfma_f32_16x16x32_bf16 a[152:155], v[192:195], v[88:91], 0\n"
"v_exp_f32 v228, v228\n"
"v_exp_f32 v229, v229\n"
"v_exp_f32 v230, v230\n"
"v_exp_f32 v231, v231\n"
"v_mfma_f32_16x16x32_bf16 a[152:155], v[196:199], v[104:107], a[152:155]\n"
"v_cvt_pk_bf16_f32 v140, v228, v229\n"
"v_cvt_pk_bf16_f32 v141, v230, v231\n"
"v_mfma_f32_16x16x32_bf16 v[236:239], v[168:171], v[56:59], 0\n"
"buffer_load_dwordx4 v8, s[44:47], s84 offen offset:0 lds\n"
"v_mfma_f32_16x16x32_bf16 v[236:239], v[172:175], v[60:63], v[236:239]\n"
"ds_read_b64_tr_b16 v[214:215], v23 offset:45056\n"
"v_cvt_pk_bf16_f32 v232, 0, v232\n"
"v_mfma_f32_16x16x32_bf16 v[236:239], v[176:179], v[64:67], v[236:239]\n"
"ds_read_b128 v[144:147], v16 offset:0\n"
"v_cvt_pk_bf16_f32 v233, 0, v233\n"
"v_mfma_f32_16x16x32_bf16 v[236:239], v[180:183], v[68:71], v[236:239]\n"
"ds_read_b128 v[148:151], v17 offset:0\n"
"v_cvt_pk_bf16_f32 v234, 0, v234\n"
"v_mfma_f32_16x16x32_bf16 v[236:239], v[184:187], v[72:75], v[236:239]\n"
"buffer_load_dwordx4 v9, s[44:47], s84 offen offset:3072 lds\n"
"v_mfma_f32_16x16x32_bf16 v[236:239], v[188:191], v[76:79], v[236:239]\n"
"s_add_u32 m0, s83, 123776\n"
"v_cvt_pk_bf16_f32 v235, 0, v235\n"
"v_mul_f32 v232, s85, v232\n"
"v_mfma_f32_16x16x32_bf16 a[156:159], v[192:195], v[92:95], 0\n"
"s_waitcnt lgkmcnt(6)\n"
"v_mul_f32 v233, s85, v233\n"
"v_mul_f32 v234, s85, v234\n"
"v_mfma_f32_16x16x32_bf16 a[156:159], v[196:199], v[108:111], a[156:159]\n"
"v_mul_f32 v235, s85, v235\n"
"v_cvt_pk_bf16_f32 v232, 0, v232\n"
"v_mfma_f32_16x16x32_bf16 a[160:163], v[200:203], v[80:83], 0\n"
"v_cvt_pk_bf16_f32 v233, 0, v233\n"
"v_cvt_pk_bf16_f32 v234, 0, v234\n"
"v_cvt_pk_bf16_f32 v235, 0, v235\n"
"v_mfma_f32_16x16x32_bf16 a[160:163], v[204:207], v[96:99], a[160:163]\n"
"v_fma_f32 v232, v232, s86, -v28\n"
"v_fma_f32 v233, v233, s86, -v28\n"
"v_fma_f32 v234, v234, s86, -v28\n"
"v_fma_f32 v235, v235, s86, -v28\n"
"v_mfma_f32_16x16x32_bf16 a[164:167], v[200:203], v[84:87], 0\n"
"v_exp_f32 v232, v232\n"
"v_exp_f32 v233, v233\n"
"v_exp_f32 v234, v234\n"
"v_exp_f32 v235, v235\n"
"v_mfma_f32_16x16x32_bf16 a[164:167], v[204:207], v[100:103], a[164:167]\n"
"v_cvt_pk_bf16_f32 v130, v232, v233\n"
"v_cvt_pk_bf16_f32 v131, v234, v235\n"
"v_mfma_f32_16x16x32_bf16 v[216:219], v[168:171], a[208:211], 0\n"
"buffer_load_dwordx4 v8, s[44:47], s84 offen offset:128 lds\n"
"v_mfma_f32_16x16x32_bf16 v[216:219], v[172:175], a[212:215], v[216:219]\n"
"ds_read_b128 v[152:155], v16 offset:1024\n"
"v_cvt_pk_bf16_f32 v236, 0, v236\n"
"v_mfma_f32_16x16x32_bf16 v[216:219], v[176:179], a[216:219], v[216:219]\n"
"ds_read_b128 v[156:159], v17 offset:1024\n"
"v_cvt_pk_bf16_f32 v237, 0, v237\n"
"v_mfma_f32_16x16x32_bf16 v[216:219], v[180:183], a[220:223], v[216:219]\n"
"ds_read_b128 v[160:163], v16 offset:2048\n"
"v_cvt_pk_bf16_f32 v238, 0, v238\n"
"v_mfma_f32_16x16x32_bf16 v[216:219], v[184:187], a[224:227], v[216:219]\n"
"buffer_load_dwordx4 v9, s[44:47], s84 offen offset:3200 lds\n"
"v_mfma_f32_16x16x32_bf16 v[216:219], v[188:191], a[228:231], v[216:219]\n"
"s_add_u32 m0, s83, 124672\n"
"v_cvt_pk_bf16_f32 v239, 0, v239\n"
"v_mul_f32 v236, s85, v236\n"
"v_mfma_f32_16x16x32_bf16 a[168:171], v[200:203], v[88:91], 0\n"
"ds_read_b128 v[164:167], v17 offset:2048\n"
"v_mul_f32 v237, s85, v237\n"
"v_mfma_f32_16x16x32_bf16 a[168:171], v[204:207], v[104:107], a[168:171]\n"
"s_waitcnt lgkmcnt(6)\n"
"v_mul_f32 v238, s85, v238\n"
"v_mul_f32 v239, s85, v239\n"
"v_mfma_f32_16x16x32_bf16 a[172:175], v[200:203], v[92:95], 0\n"
"ds_read_b64_tr_b16 v[192:193], v24 offset:0\n"
"v_cvt_pk_bf16_f32 v236, 0, v236\n"
"v_mfma_f32_16x16x32_bf16 a[172:175], v[204:207], v[108:111], a[172:175]\n"
"buffer_load_dwordx4 v8, s[44:47], s84 offen offset:256 lds\n"
"v_mfma_f32_16x16x32_bf16 a[176:179], v[208:211], v[80:83], 0\n"
"v_cvt_pk_bf16_f32 v237, 0, v237\n"
"v_cvt_pk_bf16_f32 v238, 0, v238\n"
"v_cvt_pk_bf16_f32 v239, 0, v239\n"
"v_mfma_f32_16x16x32_bf16 a[176:179], v[212:215], v[96:99], a[176:179]\n"
"v_fma_f32 v236, v236, s86, -v29\n"
"v_fma_f32 v237, v237, s86, -v29\n"
"v_fma_f32 v238, v238, s86, -v29\n"
"v_fma_f32 v239, v239, s86, -v29\n"
"v_mfma_f32_16x16x32_bf16 v[220:223], v[168:171], a[232:235], 0\n"
"v_exp_f32 v236, v236\n"
"v_exp_f32 v237, v237\n"
"v_exp_f32 v238, v238\n"
"v_exp_f32 v239, v239\n"
"v_mfma_f32_16x16x32_bf16 v[220:223], v[172:175], a[236:239], v[220:223]\n"
"v_cvt_pk_bf16_f32 v134, v236, v237\n"
"v_cvt_pk_bf16_f32 v135, v238, v239\n"
"v_mfma_f32_16x16x32_bf16 v[220:223], v[176:179], a[240:243], v[220:223]\n"
"buffer_load_dwordx4 v9, s[44:47], s84 offen offset:3328 lds\n"
"v_mfma_f32_16x16x32_bf16 v[220:223], v[180:183], a[244:247], v[220:223]\n"
"ds_read_b64_tr_b16 v[194:195], v24 offset:6144\n"
"v_cvt_pk_bf16_f32 v216, 0, v216\n"
"v_mfma_f32_16x16x32_bf16 v[220:223], v[184:187], a[248:251], v[220:223]\n"
"ds_read_b64_tr_b16 v[196:197], v24 offset:12288\n"
"v_cvt_pk_bf16_f32 v217, 0, v217\n"
"v_mfma_f32_16x16x32_bf16 v[220:223], v[188:191], a[252:255], v[220:223]\n"
"ds_read_b64_tr_b16 v[198:199], v24 offset:18432\n"
"v_cvt_pk_bf16_f32 v218, 0, v218\n"
"v_mfma_f32_16x16x32_bf16 a[180:183], v[208:211], v[84:87], 0\n"
"ds_read_b64_tr_b16 v[200:201], v25 offset:0\n"
"v_cvt_pk_bf16_f32 v219, 0, v219\n"
"v_mfma_f32_16x16x32_bf16 a[180:183], v[212:215], v[100:103], a[180:183]\n"
"ds_read_b64_tr_b16 v[202:203], v25 offset:6144\n"
"v_mul_f32 v216, s85, v216\n"
"v_mfma_f32_16x16x32_bf16 a[184:187], v[208:211], v[88:91], 0\n"
"ds_read_b64_tr_b16 v[204:205], v25 offset:12288\n"
"v_mul_f32 v217, s85, v217\n"
"v_mfma_f32_16x16x32_bf16 a[184:187], v[212:215], v[104:107], a[184:187]\n"
"s_waitcnt lgkmcnt(7)\n"
"v_mul_f32 v218, s85, v218\n"
"v_mul_f32 v219, s85, v219\n"
"v_mfma_f32_16x16x32_bf16 a[188:191], v[208:211], v[92:95], 0\n"
"v_cvt_pk_bf16_f32 v216, 0, v216\n"
"v_cvt_pk_bf16_f32 v217, 0, v217\n"
"v_cvt_pk_bf16_f32 v218, 0, v218\n"
"v_cvt_pk_bf16_f32 v219, 0, v219\n"
"v_mfma_f32_16x16x32_bf16 a[188:191], v[212:215], v[108:111], a[188:191]\n"
"v_fma_f32 v216, v216, s86, -v30\n"
"v_fma_f32 v217, v217, s86, -v30\n"
"v_fma_f32 v218, v218, s86, -v30\n"
"v_fma_f32 v219, v219, s86, -v30\n"
"v_mfma_f32_16x16x32_bf16 v[224:227], v[144:147], v[32:35], 0\n"
"v_exp_f32 v216, v216\n"
"v_exp_f32 v217, v217\n"
"v_exp_f32 v218, v218\n"
"v_exp_f32 v219, v219\n"
"v_mfma_f32_16x16x32_bf16 v[224:227], v[148:151], v[36:39], v[224:227]\n"
"v_cvt_pk_bf16_f32 v138, v216, v217\n"
"v_cvt_pk_bf16_f32 v139, v218, v219\n"
"v_mfma_f32_16x16x32_bf16 v[224:227], v[152:155], v[40:43], v[224:227]\n"
"ds_read_b64_tr_b16 v[206:207], v25 offset:18432\n"
"v_cvt_pk_bf16_f32 v220, 0, v220\n"
"v_mfma_f32_16x16x32_bf16 v[224:227], v[156:159], v[44:47], v[224:227]\n"
"ds_read_b64_tr_b16 v[208:209], v26 offset:0\n"
"v_cvt_pk_bf16_f32 v221, 0, v221\n"
"v_mfma_f32_16x16x32_bf16 v[224:227], v[160:163], v[48:51], v[224:227]\n"
"ds_read_b64_tr_b16 v[210:211], v26 offset:6144\n"
"v_cvt_pk_bf16_f32 v222, 0, v222\n"
"v_mfma_f32_16x16x32_bf16 v[224:227], v[164:167], v[52:55], v[224:227]\n"
"ds_read_b64_tr_b16 v[212:213], v26 offset:12288\n"
"v_cvt_pk_bf16_f32 v223, 0, v223\n"
"v_mfma_f32_16x16x32_bf16 a[192:195], v[240:243], v[80:83], 0\n"
"ds_read_b64_tr_b16 v[214:215], v26 offset:18432\n"
"v_mul_f32 v220, s85, v220\n"
"v_mfma_f32_16x16x32_bf16 a[192:195], v[240:243], v[96:99], a[192:195]\n"
"ds_read_b128 v[168:171], v16 offset:6144\n"
"v_mul_f32 v221, s85, v221\n"
"v_mfma_f32_16x16x32_bf16 a[196:199], v[240:243], v[84:87], 0\n"
"ds_read_b128 v[172:175], v17 offset:6144\n"
"v_mul_f32 v222, s85, v222\n"
"v_mfma_f32_16x16x32_bf16 a[196:199], v[240:243], v[100:103], a[196:199]\n"
"v_mul_f32 v223, s85, v223\n"
"v_cvt_pk_bf16_f32 v220, 0, v220\n"
"v_mfma_f32_16x16x32_bf16 a[200:203], v[240:243], v[88:91], 0\n"
"v_cvt_pk_bf16_f32 v221, 0, v221\n"
"v_cvt_pk_bf16_f32 v222, 0, v222\n"
"v_cvt_pk_bf16_f32 v223, 0, v223\n"
"v_mfma_f32_16x16x32_bf16 a[200:203], v[240:243], v[104:107], a[200:203]\n"
"v_fma_f32 v220, v220, s86, -v31\n"
"v_fma_f32 v221, v221, s86, -v31\n"
"v_fma_f32 v222, v222, s86, -v31\n"
"v_fma_f32 v223, v223, s86, -v31\n"
"v_mfma_f32_16x16x32_bf16 a[204:207], v[240:243], v[92:95], 0\n"
"v_exp_f32 v220, v220\n"
"v_exp_f32 v221, v221\n"
"v_exp_f32 v222, v222\n"
"v_exp_f32 v223, v223\n"
"v_mfma_f32_16x16x32_bf16 a[204:207], v[240:243], v[108:111], a[204:207]\n"
"v_cvt_pk_bf16_f32 v142, v220, v221\n"
"v_cvt_pk_bf16_f32 v143, v222, v223\n"
"v_mfma_f32_16x16x32_bf16 v[228:231], v[144:147], v[56:59], 0\n"
"ds_read_b128 v[176:179], v16 offset:7168\n"
"v_cvt_pk_bf16_f32 v224, 0, v224\n"
"v_mfma_f32_16x16x32_bf16 v[228:231], v[148:151], v[60:63], v[228:231]\n"
"ds_read_b128 v[180:183], v17 offset:7168\n"
"v_cvt_pk_bf16_f32 v225, 0, v225\n"
"v_mfma_f32_16x16x32_bf16 v[228:231], v[152:155], v[64:67], v[228:231]\n"
"ds_read_b128 v[184:187], v16 offset:8192\n"
"v_cvt_pk_bf16_f32 v226, 0, v226\n"
"v_mfma_f32_16x16x32_bf16 v[228:231], v[156:159], v[68:71], v[228:231]\n"
"s_waitcnt lgkmcnt(13)\n"
"v_cvt_pk_bf16_f32 v227, 0, v227\n"
"v_mul_f32 v224, s85, v224\n"
"v_mfma_f32_16x16x32_bf16 v[228:231], v[160:163], v[72:75], v[228:231]\n"
"ds_read_b128 v[188:191], v17 offset:8192\n"
"v_mul_f32 v225, s85, v225\n"
"v_mfma_f32_16x16x32_bf16 v[228:231], v[164:167], v[76:79], v[228:231]\n"
"v_mul_f32 v226, s85, v226\n"
"v_mul_f32 v227, s85, v227\n"
"v_mfma_f32_16x16x32_bf16 a[0:3], v[192:195], v[112:115], a[0:3]\n"
"v_cvt_pk_bf16_f32 v224, 0, v224\n"
"v_cvt_pk_bf16_f32 v225, 0, v225\n"
"v_mfma_f32_16x16x32_bf16 a[0:3], v[196:199], v[128:131], a[0:3]\n"
"v_cvt_pk_bf16_f32 v226, 0, v226\n"
"v_cvt_pk_bf16_f32 v227, 0, v227\n"
"v_mfma_f32_16x16x32_bf16 a[4:7], v[192:195], v[116:119], a[4:7]\n"
"s_barrier\n"
"s_add_u32 s84, s83, 73728\n"
"v_mfma_f32_16x16x32_bf16 a[4:7], v[196:199], v[132:135], a[4:7]\n"
"s_add_u32 m0, s83, 24576\n"
"v_fma_f32 v224, v224, s86, -v28\n"
"v_fma_f32 v225, v225, s86, -v28\n"
"v_fma_f32 v226, v226, s86, -v28\n"
"v_fma_f32 v227, v227, s86, -v28\n"
"v_mfma_f32_16x16x32_bf16 a[8:11], v[192:195], v[120:123], a[8:11]\n"
"v_exp_f32 v224, v224\n"
"v_exp_f32 v225, v225\n"
"v_exp_f32 v226, v226\n"
"v_exp_f32 v227, v227\n"
"v_mfma_f32_16x16x32_bf16 a[8:11], v[196:199], v[136:139], a[8:11]\n"
"v_cvt_pk_bf16_f32 v80, v224, v225\n"
"v_cvt_pk_bf16_f32 v81, v226, v227\n"
"v_mfma_f32_16x16x32_bf16 v[232:235], v[144:147], a[208:211], 0\n"
"buffer_load_dwordx4 v7, s[48:51], s84 offen offset:0 lds\n"
"v_mfma_f32_16x16x32_bf16 v[232:235], v[148:151], a[212:215], v[232:235]\n"
"v_cvt_pk_bf16_f32 v228, 0, v228\n"
"v_cvt_pk_bf16_f32 v229, 0, v229\n"
"v_mfma_f32_16x16x32_bf16 v[232:235], v[152:155], a[216:219], v[232:235]\n"
"v_cvt_pk_bf16_f32 v230, 0, v230\n"
"v_cvt_pk_bf16_f32 v231, 0, v231\n"
"v_mfma_f32_16x16x32_bf16 v[232:235], v[156:159], a[220:223], v[232:235]\n"
"v_mul_f32 v228, s85, v228\n"
"v_mul_f32 v229, s85, v229\n"
"v_mfma_f32_16x16x32_bf16 v[232:235], v[160:163], a[224:227], v[232:235]\n"
"buffer_load_dwordx4 v7, s[48:51], s84 offen offset:3072 lds\n"
"v_mfma_f32_16x16x32_bf16 v[232:235], v[164:167], a[228:231], v[232:235]\n"
"s_add_u32 m0, s83, 25472\n"
"s_waitcnt lgkmcnt(10)\n"
"v_mul_f32 v230, s85, v230\n"
"v_mfma_f32_16x16x32_bf16 a[12:15], v[192:195], v[124:127], a[12:15]\n"
"v_mul_f32 v231, s85, v231\n"
"v_cvt_pk_bf16_f32 v228, 0, v228\n"
"v_mfma_f32_16x16x32_bf16 a[12:15], v[196:199], v[140:143], a[12:15]\n"
"s_add_u32 vcc_hi, vcc_lo, 57344\n"
"buffer_load_dword v254, v0, s[56:59], vcc_hi idxen\n"
"ds_read_b64_tr_b16 v[192:193], v27 offset:0\n"
"v_cvt_pk_bf16_f32 v229, 0, v229\n"
"v_mfma_f32_16x16x32_bf16 a[16:19], v[200:203], v[112:115], a[16:19]\n"
"ds_read_b64_tr_b16 v[194:195], v27 offset:6144\n"
"v_cvt_pk_bf16_f32 v230, 0, v230\n"
"v_mfma_f32_16x16x32_bf16 a[16:19], v[204:207], v[128:131], a[16:19]\n"
"buffer_load_dwordx4 v7, s[48:51], s84 offen offset:128 lds\n"
"v_mfma_f32_16x16x32_bf16 a[20:23], v[200:203], v[116:119], a[20:23]\n"
"v_cvt_pk_bf16_f32 v231, 0, v231\n"
"v_fma_f32 v228, v228, s86, -v29\n"
"v_mfma_f32_16x16x32_bf16 a[20:23], v[204:207], v[132:135], a[20:23]\n"
"v_fma_f32 v229, v229, s86, -v29\n"
"v_fma_f32 v230, v230, s86, -v29\n"
"v_fma_f32 v231, v231, s86, -v29\n"
"v_mfma_f32_16x16x32_bf16 v[236:239], v[144:147], a[232:235], 0\n"
"v_exp_f32 v228, v228\n"
"v_exp_f32 v229, v229\n"
"v_exp_f32 v230, v230\n"
"v_exp_f32 v231, v231\n"
"v_mfma_f32_16x16x32_bf16 v[236:239], v[148:151], a[236:239], v[236:239]\n"
"v_cvt_pk_bf16_f32 v84, v228, v229\n"
"v_cvt_pk_bf16_f32 v85, v230, v231\n"
"v_mfma_f32_16x16x32_bf16 v[236:239], v[152:155], a[240:243], v[236:239]\n"
"buffer_load_dwordx4 v7, s[48:51], s84 offen offset:3200 lds\n"
"v_mfma_f32_16x16x32_bf16 v[236:239], v[156:159], a[244:247], v[236:239]\n"
"s_add_u32 m0, s83, 26368\n"
"v_cvt_pk_bf16_f32 v232, 0, v232\n"
"v_cvt_pk_bf16_f32 v233, 0, v233\n"
"v_mfma_f32_16x16x32_bf16 v[236:239], v[160:163], a[248:251], v[236:239]\n"
"ds_read_b64_tr_b16 v[196:197], v27 offset:12288\n"
"v_cvt_pk_bf16_f32 v234, 0, v234\n"
"v_mfma_f32_16x16x32_bf16 v[236:239], v[164:167], a[252:255], v[236:239]\n"
"ds_read_b64_tr_b16 v[198:199], v27 offset:18432\n"
"v_cvt_pk_bf16_f32 v235, 0, v235\n"
"v_mfma_f32_16x16x32_bf16 a[24:27], v[200:203], v[120:123], a[24:27]\n"
"ds_read_b128 v[144:147], v16 offset:12288\n"
"v_mul_f32 v232, s85, v232\n"
"v_mfma_f32_16x16x32_bf16 a[24:27], v[204:207], v[136:139], a[24:27]\n"
"buffer_load_dwordx4 v7, s[48:51], s84 offen offset:256 lds\n"
"v_mfma_f32_16x16x32_bf16 a[28:31], v[200:203], v[124:127], a[28:31]\n"
"v_mul_f32 v233, s85, v233\n"
"v_mul_f32 v234, s85, v234\n"
"v_mfma_f32_16x16x32_bf16 a[28:31], v[204:207], v[140:143], a[28:31]\n"
"s_waitcnt lgkmcnt(5)\n"
"v_mul_f32 v235, s85, v235\n"
"v_cvt_pk_bf16_f32 v232, 0, v232\n"
"v_mfma_f32_16x16x32_bf16 a[32:35], v[208:211], v[112:115], a[32:35]\n"
"v_cvt_pk_bf16_f32 v233, 0, v233\n"
"v_cvt_pk_bf16_f32 v234, 0, v234\n"
"v_cvt_pk_bf16_f32 v235, 0, v235\n"
"v_mfma_f32_16x16x32_bf16 a[32:35], v[212:215], v[128:131], a[32:35]\n"
"v_fma_f32 v232, v232, s86, -v30\n"
"v_fma_f32 v233, v233, s86, -v30\n"
"v_fma_f32 v234, v234, s86, -v30\n"
"v_fma_f32 v235, v235, s86, -v30\n"
"v_mfma_f32_16x16x32_bf16 v[216:219], v[168:171], v[32:35], 0\n"
"v_exp_f32 v232, v232\n"
"v_exp_f32 v233, v233\n"
"v_exp_f32 v234, v234\n"
"v_exp_f32 v235, v235\n"
"v_mfma_f32_16x16x32_bf16 v[216:219], v[172:175], v[36:39], v[216:219]\n"
"v_cvt_pk_bf16_f32 v88, v232, v233\n"
"v_cvt_pk_bf16_f32 v89, v234, v235\n"
"v_mfma_f32_16x16x32_bf16 v[216:219], v[176:179], v[40:43], v[216:219]\n"
"buffer_load_dwordx4 v7, s[48:51], s84 offen offset:3328 lds\n"
"v_mfma_f32_16x16x32_bf16 v[216:219], v[180:183], v[44:47], v[216:219]\n"
"ds_read_b64_tr_b16 v[200:201], v24 offset:1024\n"
"v_cvt_pk_bf16_f32 v236, 0, v236\n"
"v_mfma_f32_16x16x32_bf16 v[216:219], v[184:187], v[48:51], v[216:219]\n"
"ds_read_b64_tr_b16 v[202:203], v24 offset:7168\n"
"v_cvt_pk_bf16_f32 v237, 0, v237\n"
"v_mfma_f32_16x16x32_bf16 v[216:219], v[188:191], v[52:55], v[216:219]\n"
"ds_read_b64_tr_b16 v[204:205], v24 offset:13312\n"
"v_cvt_pk_bf16_f32 v238, 0, v238\n"
"v_mfma_f32_16x16x32_bf16 a[36:39], v[208:211], v[116:119], a[36:39]\n"
"ds_read_b64_tr_b16 v[206:207], v24 offset:19456\n"
"v_cvt_pk_bf16_f32 v239, 0, v239\n"
"v_mfma_f32_16x16x32_bf16 a[36:39], v[212:215], v[132:135], a[36:39]\n"
"ds_read_b128 v[148:151], v17 offset:12288\n"
"v_mul_f32 v236, s85, v236\n"
"v_mfma_f32_16x16x32_bf16 a[40:43], v[208:211], v[120:123], a[40:43]\n"
"ds_read_b128 v[152:155], v16 offset:13312\n"
"v_mul_f32 v237, s85, v237\n"
"v_mfma_f32_16x16x32_bf16 a[40:43], v[212:215], v[136:139], a[40:43]\n"
"s_waitcnt lgkmcnt(7)\n"
"v_mul_f32 v238, s85, v238\n"
"v_mul_f32 v239, s85, v239\n"
"v_mfma_f32_16x16x32_bf16 a[44:47], v[208:211], v[124:127], a[44:47]\n"
"v_cvt_pk_bf16_f32 v236, 0, v236\n"
"v_cvt_pk_bf16_f32 v237, 0, v237\n"
"v_cvt_pk_bf16_f32 v238, 0, v238\n"
"v_cvt_pk_bf16_f32 v239, 0, v239\n"
"v_mfma_f32_16x16x32_bf16 a[44:47], v[212:215], v[140:143], a[44:47]\n"
"v_fma_f32 v236, v236, s86, -v31\n"
"v_fma_f32 v237, v237, s86, -v31\n"
"v_fma_f32 v238, v238, s86, -v31\n"
"v_fma_f32 v239, v239, s86, -v31\n"
"v_mfma_f32_16x16x32_bf16 a[48:51], v[192:195], v[112:115], a[48:51]\n"
"v_exp_f32 v236, v236\n"
"v_exp_f32 v237, v237\n"
"v_exp_f32 v238, v238\n"
"v_exp_f32 v239, v239\n"
"v_mfma_f32_16x16x32_bf16 a[48:51], v[196:199], v[128:131], a[48:51]\n"
"v_cvt_pk_bf16_f32 v92, v236, v237\n"
"v_cvt_pk_bf16_f32 v93, v238, v239\n"
"v_mfma_f32_16x16x32_bf16 v[220:223], v[168:171], v[56:59], 0\n"
"ds_read_b64_tr_b16 v[208:209], v25 offset:1024\n"
"v_cvt_pk_bf16_f32 v216, 0, v216\n"
"v_mfma_f32_16x16x32_bf16 v[220:223], v[172:175], v[60:63], v[220:223]\n"
"ds_read_b64_tr_b16 v[210:211], v25 offset:7168\n"
"v_cvt_pk_bf16_f32 v217, 0, v217\n"
"v_mfma_f32_16x16x32_bf16 v[220:223], v[176:179], v[64:67], v[220:223]\n"
"ds_read_b64_tr_b16 v[212:213], v25 offset:13312\n"
"v_cvt_pk_bf16_f32 v218, 0, v218\n"
"v_mfma_f32_16x16x32_bf16 v[220:223], v[180:183], v[68:71], v[220:223]\n"
"ds_read_b64_tr_b16 v[214:215], v25 offset:19456\n"
"v_cvt_pk_bf16_f32 v219, 0, v219\n"
"v_mfma_f32_16x16x32_bf16 v[220:223], v[184:187], v[72:75], v[220:223]\n"
"ds_read_b128 v[156:159], v17 offset:13312\n"
"v_mul_f32 v216, s85, v216\n"
"v_mfma_f32_16x16x32_bf16 v[220:223], v[188:191], v[76:79], v[220:223]\n"
"ds_read_b128 v[160:163], v16 offset:14336\n"
"v_mul_f32 v217, s85, v217\n"
"v_mfma_f32_16x16x32_bf16 a[52:55], v[192:195], v[116:119], a[52:55]\n"
"ds_read_b128 v[164:167], v17 offset:14336\n"
"v_mul_f32 v218, s85, v218\n"
"v_mfma_f32_16x16x32_bf16 a[52:55], v[196:199], v[132:135], a[52:55]\n"
"v_mul_f32 v219, s85, v219\n"
"v_cvt_pk_bf16_f32 v216, 0, v216\n"
"v_mfma_f32_16x16x32_bf16 a[56:59], v[192:195], v[120:123], a[56:59]\n"
"v_cvt_pk_bf16_f32 v217, 0, v217\n"
"v_cvt_pk_bf16_f32 v218, 0, v218\n"
"v_cvt_pk_bf16_f32 v219, 0, v219\n"
"v_mfma_f32_16x16x32_bf16 a[56:59], v[196:199], v[136:139], a[56:59]\n"
"v_fma_f32 v216, v216, s86, -v28\n"
"v_fma_f32 v217, v217, s86, -v28\n"
"v_fma_f32 v218, v218, s86, -v28\n"
"v_fma_f32 v219, v219, s86, -v28\n"
"v_mfma_f32_16x16x32_bf16 a[60:63], v[192:195], v[124:127], a[60:63]\n"
"v_exp_f32 v216, v216\n"
"v_exp_f32 v217, v217\n"
"v_exp_f32 v218, v218\n"
"v_exp_f32 v219, v219\n"
"v_mfma_f32_16x16x32_bf16 a[60:63], v[196:199], v[140:143], a[60:63]\n"
"v_cvt_pk_bf16_f32 v82, v216, v217\n"
"v_cvt_pk_bf16_f32 v83, v218, v219\n"
"v_mfma_f32_16x16x32_bf16 v[224:227], v[168:171], a[208:211], 0\n"
"ds_read_b64_tr_b16 v[192:193], v26 offset:1024\n"
"v_cvt_pk_bf16_f32 v220, 0, v220\n"
"v_mfma_f32_16x16x32_bf16 v[224:227], v[172:175], a[212:215], v[224:227]\n"
"ds_read_b64_tr_b16 v[194:195], v26 offset:7168\n"
"v_cvt_pk_bf16_f32 v221, 0, v221\n"
"v_mfma_f32_16x16x32_bf16 v[224:227], v[176:179], a[216:219], v[224:227]\n"
"ds_read_b64_tr_b16 v[196:197], v26 offset:13312\n"
"v_cvt_pk_bf16_f32 v222, 0, v222\n"
"v_mfma_f32_16x16x32_bf16 v[224:227], v[180:183], a[220:223], v[224:227]\n"
"s_waitcnt lgkmcnt(12)\n"
"v_cvt_pk_bf16_f32 v223, 0, v223\n"
"v_mul_f32 v220, s85, v220\n"
"v_mfma_f32_16x16x32_bf16 v[224:227], v[184:187], a[224:227], v[224:227]\n"
"ds_read_b64_tr_b16 v[198:199], v26 offset:19456\n"
"v_mul_f32 v221, s85, v221\n"
"v_mfma_f32_16x16x32_bf16 v[224:227], v[188:191], a[228:231], v[224:227]\n"
"v_mul_f32 v222, s85, v222\n"
"v_mul_f32 v223, s85, v223\n"
"v_mfma_f32_16x16x32_bf16 a[64:67], v[200:203], v[112:115], a[64:67]\n"
"v_cvt_pk_bf16_f32 v220, 0, v220\n"
"v_cvt_pk_bf16_f32 v221, 0, v221\n"
"v_mfma_f32_16x16x32_bf16 a[64:67], v[204:207], v[128:131], a[64:67]\n"
"v_cvt_pk_bf16_f32 v222, 0, v222\n"
"v_cvt_pk_bf16_f32 v223, 0, v223\n"
"v_mfma_f32_16x16x32_bf16 a[68:71], v[200:203], v[116:119], a[68:71]\n"
"v_fma_f32 v220, v220, s86, -v29\n"
"v_fma_f32 v221, v221, s86, -v29\n"
"v_mfma_f32_16x16x32_bf16 a[68:71], v[204:207], v[132:135], a[68:71]\n"
"v_fma_f32 v222, v222, s86, -v29\n"
"v_fma_f32 v223, v223, s86, -v29\n"
"v_mfma_f32_16x16x32_bf16 a[72:75], v[200:203], v[120:123], a[72:75]\n"
"v_exp_f32 v220, v220\n"
"v_mfma_f32_16x16x32_bf16 a[72:75], v[204:207], v[136:139], a[72:75]\n"
"v_exp_f32 v221, v221\n"
"v_mfma_f32_16x16x32_bf16 v[228:231], v[168:171], a[232:235], 0\n"
"v_exp_f32 v222, v222\n"
"v_exp_f32 v223, v223\n"
"v_mfma_f32_16x16x32_bf16 v[228:231], v[172:175], a[236:239], v[228:231]\n"
"v_cvt_pk_bf16_f32 v86, v220, v221\n"
"v_cvt_pk_bf16_f32 v87, v222, v223\n"
"v_mfma_f32_16x16x32_bf16 v[228:231], v[176:179], a[240:243], v[228:231]\n"
"ds_read_b128 v[168:171], v16 offset:18432\n"
"v_cvt_pk_bf16_f32 v224, 0, v224\n"
"v_mfma_f32_16x16x32_bf16 v[228:231], v[180:183], a[244:247], v[228:231]\n"
"ds_read_b128 v[172:175], v17 offset:18432\n"
"v_cvt_pk_bf16_f32 v225, 0, v225\n"
"v_mfma_f32_16x16x32_bf16 v[228:231], v[184:187], a[248:251], v[228:231]\n"
"ds_read_b128 v[176:179], v16 offset:19456\n"
"v_cvt_pk_bf16_f32 v226, 0, v226\n"
"v_mfma_f32_16x16x32_bf16 v[228:231], v[188:191], a[252:255], v[228:231]\n"
"s_waitcnt lgkmcnt(10)\n"
"v_cvt_pk_bf16_f32 v227, 0, v227\n"
"v_mul_f32 v224, s85, v224\n"
"v_mfma_f32_16x16x32_bf16 a[76:79], v[200:203], v[124:127], a[76:79]\n"
"ds_read_b128 v[180:183], v17 offset:19456\n"
"v_mul_f32 v225, s85, v225\n"
"v_mfma_f32_16x16x32_bf16 a[76:79], v[204:207], v[140:143], a[76:79]\n"
"ds_read_b64_tr_b16 v[200:201], v27 offset:1024\n"
"v_mul_f32 v226, s85, v226\n"
"v_mfma_f32_16x16x32_bf16 a[80:83], v[208:211], v[112:115], a[80:83]\n"
"ds_read_b64_tr_b16 v[202:203], v27 offset:7168\n"
"v_mul_f32 v227, s85, v227\n"
"v_mfma_f32_16x16x32_bf16 a[80:83], v[212:215], v[128:131], a[80:83]\n"
"s_waitcnt lgkmcnt(10)\n"
"v_cvt_pk_bf16_f32 v224, 0, v224\n"
"v_cvt_pk_bf16_f32 v225, 0, v225\n"
"v_mfma_f32_16x16x32_bf16 a[84:87], v[208:211], v[116:119], a[84:87]\n"
"v_cvt_pk_bf16_f32 v226, 0, v226\n"
"v_cvt_pk_bf16_f32 v227, 0, v227\n"
"v_mfma_f32_16x16x32_bf16 a[84:87], v[212:215], v[132:135], a[84:87]\n"
"v_fma_f32 v224, v224, s86, -v30\n"
"v_fma_f32 v225, v225, s86, -v30\n"
"v_fma_f32 v226, v226, s86, -v30\n"
"v_fma_f32 v227, v227, s86, -v30\n"
"v_mfma_f32_16x16x32_bf16 v[232:235], v[144:147], v[32:35], 0\n"
"v_exp_f32 v224, v224\n"
"v_exp_f32 v225, v225\n"
"v_exp_f32 v226, v226\n"
"v_exp_f32 v227, v227\n"
"v_mfma_f32_16x16x32_bf16 v[232:235], v[148:151], v[36:39], v[232:235]\n"
"v_cvt_pk_bf16_f32 v90, v224, v225\n"
"v_cvt_pk_bf16_f32 v91, v226, v227\n"
"v_mfma_f32_16x16x32_bf16 v[232:235], v[152:155], v[40:43], v[232:235]\n"
"ds_read_b64_tr_b16 v[204:205], v27 offset:13312\n"
"v_cvt_pk_bf16_f32 v228, 0, v228\n"
"v_mfma_f32_16x16x32_bf16 v[232:235], v[156:159], v[44:47], v[232:235]\n"
"ds_read_b64_tr_b16 v[206:207], v27 offset:19456\n"
"v_cvt_pk_bf16_f32 v229, 0, v229\n"
"v_mfma_f32_16x16x32_bf16 v[232:235], v[160:163], v[48:51], v[232:235]\n"
"ds_read_b128 v[184:187], v16 offset:20480\n"
"v_cvt_pk_bf16_f32 v230, 0, v230\n"
"v_mfma_f32_16x16x32_bf16 v[232:235], v[164:167], v[52:55], v[232:235]\n"
"ds_read_b128 v[188:191], v17 offset:20480\n"
"v_cvt_pk_bf16_f32 v231, 0, v231\n"
"v_mfma_f32_16x16x32_bf16 a[88:91], v[208:211], v[120:123], a[88:91]\n"
"v_mul_f32 v228, s85, v228\n"
"v_mul_f32 v229, s85, v229\n"
"v_mfma_f32_16x16x32_bf16 a[88:91], v[212:215], v[136:139], a[88:91]\n"
"s_waitcnt lgkmcnt(10)\n"
"v_mul_f32 v230, s85, v230\n"
"v_mul_f32 v231, s85, v231\n"
"v_mfma_f32_16x16x32_bf16 a[92:95], v[208:211], v[124:127], a[92:95]\n"
"v_cvt_pk_bf16_f32 v228, 0, v228\n"
"v_cvt_pk_bf16_f32 v229, 0, v229\n"
"v_mfma_f32_16x16x32_bf16 a[92:95], v[212:215], v[140:143], a[92:95]\n"
"ds_read_b64_tr_b16 v[208:209], v24 offset:2048\n"
"v_cvt_pk_bf16_f32 v230, 0, v230\n"
"v_mfma_f32_16x16x32_bf16 a[96:99], v[192:195], v[112:115], a[96:99]\n"
"v_cvt_pk_bf16_f32 v231, 0, v231\n"
"v_fma_f32 v228, v228, s86, -v31\n"
"v_mfma_f32_16x16x32_bf16 a[96:99], v[196:199], v[128:131], a[96:99]\n"
"v_fma_f32 v229, v229, s86, -v31\n"
"v_fma_f32 v230, v230, s86, -v31\n"
"v_fma_f32 v231, v231, s86, -v31\n"
"v_mfma_f32_16x16x32_bf16 a[100:103], v[192:195], v[116:119], a[100:103]\n"
"v_exp_f32 v228, v228\n"
"v_exp_f32 v229, v229\n"
"v_exp_f32 v230, v230\n"
"v_exp_f32 v231, v231\n"
"v_mfma_f32_16x16x32_bf16 a[100:103], v[196:199], v[132:135], a[100:103]\n"
"v_cvt_pk_bf16_f32 v94, v228, v229\n"
"v_cvt_pk_bf16_f32 v95, v230, v231\n"
"v_mfma_f32_16x16x32_bf16 v[236:239], v[144:147], v[56:59], 0\n"
"ds_read_b64_tr_b16 v[210:211], v24 offset:8192\n"
"v_cvt_pk_bf16_f32 v232, 0, v232\n"
"v_mfma_f32_16x16x32_bf16 v[236:239], v[148:151], v[60:63], v[236:239]\n"
"ds_read_b64_tr_b16 v[212:213], v24 offset:14336\n"
"v_cvt_pk_bf16_f32 v233, 0, v233\n"
"v_mfma_f32_16x16x32_bf16 v[236:239], v[152:155], v[64:67], v[236:239]\n"
"ds_read_b64_tr_b16 v[214:215], v24 offset:20480\n"
"v_cvt_pk_bf16_f32 v234, 0, v234\n"
"v_mfma_f32_16x16x32_bf16 v[236:239], v[156:159], v[68:71], v[236:239]\n"
"v_cvt_pk_bf16_f32 v235, 0, v235\n"
"v_mul_f32 v232, s85, v232\n"
"v_mfma_f32_16x16x32_bf16 v[236:239], v[160:163], v[72:75], v[236:239]\n"
"v_mul_f32 v233, s85, v233\n"
"v_mul_f32 v234, s85, v234\n"
"v_mfma_f32_16x16x32_bf16 v[236:239], v[164:167], v[76:79], v[236:239]\n"
"v_mul_f32 v235, s85, v235\n"
"v_cvt_pk_bf16_f32 v232, 0, v232\n"
"v_mfma_f32_16x16x32_bf16 a[104:107], v[192:195], v[120:123], a[104:107]\n"
"v_cvt_pk_bf16_f32 v233, 0, v233\n"
"v_cvt_pk_bf16_f32 v234, 0, v234\n"
"v_mfma_f32_16x16x32_bf16 a[104:107], v[196:199], v[136:139], a[104:107]\n"
"s_waitcnt lgkmcnt(6)\n"
"v_cvt_pk_bf16_f32 v235, 0, v235\n"
"v_fma_f32 v232, v232, s86, -v28\n"
"v_mfma_f32_16x16x32_bf16 a[108:111], v[192:195], v[124:127], a[108:111]\n"
"v_fma_f32 v233, v233, s86, -v28\n"
"v_fma_f32 v234, v234, s86, -v28\n"
"v_mfma_f32_16x16x32_bf16 a[108:111], v[196:199], v[140:143], a[108:111]\n"
"v_fma_f32 v235, v235, s86, -v28\n"
"v_cvt_pk_bf16_f32 v236, 0, v236\n"
"v_mfma_f32_16x16x32_bf16 a[112:115], v[200:203], v[112:115], a[112:115]\n"
"v_exp_f32 v232, v232\n"
"v_exp_f32 v233, v233\n"
"v_exp_f32 v234, v234\n"
"v_exp_f32 v235, v235\n"
"v_mfma_f32_16x16x32_bf16 a[112:115], v[204:207], v[128:131], a[112:115]\n"
"v_cvt_pk_bf16_f32 v96, v232, v233\n"
"v_cvt_pk_bf16_f32 v97, v234, v235\n"
"v_mfma_f32_16x16x32_bf16 v[216:219], v[144:147], a[208:211], 0\n"
"ds_read_b64_tr_b16 v[192:193], v25 offset:2048\n"
"v_cvt_pk_bf16_f32 v237, 0, v237\n"
"v_mfma_f32_16x16x32_bf16 v[216:219], v[148:151], a[212:215], v[216:219]\n"
"ds_read_b64_tr_b16 v[194:195], v25 offset:8192\n"
"v_cvt_pk_bf16_f32 v238, 0, v238\n"
"v_mfma_f32_16x16x32_bf16 v[216:219], v[152:155], a[216:219], v[216:219]\n"
"ds_read_b64_tr_b16 v[196:197], v25 offset:14336\n"
"v_cvt_pk_bf16_f32 v239, 0, v239\n"
"v_mfma_f32_16x16x32_bf16 v[216:219], v[156:159], a[220:223], v[216:219]\n"
"ds_read_b64_tr_b16 v[198:199], v25 offset:20480\n"
"v_mul_f32 v236, s85, v236\n"
"v_mfma_f32_16x16x32_bf16 v[216:219], v[160:163], a[224:227], v[216:219]\n"
"v_mul_f32 v237, s85, v237\n"
"v_mul_f32 v238, s85, v238\n"
"v_mfma_f32_16x16x32_bf16 v[216:219], v[164:167], a[228:231], v[216:219]\n"
"v_mul_f32 v239, s85, v239\n"
"v_cvt_pk_bf16_f32 v236, 0, v236\n"
"v_mfma_f32_16x16x32_bf16 a[116:119], v[200:203], v[116:119], a[116:119]\n"
"v_cvt_pk_bf16_f32 v237, 0, v237\n"
"v_cvt_pk_bf16_f32 v238, 0, v238\n"
"v_mfma_f32_16x16x32_bf16 a[116:119], v[204:207], v[132:135], a[116:119]\n"
"v_cvt_pk_bf16_f32 v239, 0, v239\n"
"v_fma_f32 v236, v236, s86, -v29\n"
"v_mfma_f32_16x16x32_bf16 a[120:123], v[200:203], v[120:123], a[120:123]\n"
"v_fma_f32 v237, v237, s86, -v29\n"
"v_fma_f32 v238, v238, s86, -v29\n"
"v_mfma_f32_16x16x32_bf16 a[120:123], v[204:207], v[136:139], a[120:123]\n"
"v_fma_f32 v239, v239, s86, -v29\n"
"v_cvt_pk_bf16_f32 v216, 0, v216\n"
"v_mfma_f32_16x16x32_bf16 a[124:127], v[200:203], v[124:127], a[124:127]\n"
"v_exp_f32 v236, v236\n"
"v_mfma_f32_16x16x32_bf16 a[124:127], v[204:207], v[140:143], a[124:127]\n"
"v_exp_f32 v237, v237\n"
"v_mfma_f32_16x16x32_bf16 v[220:223], v[144:147], a[232:235], 0\n"
"v_exp_f32 v238, v238\n"
"v_exp_f32 v239, v239\n"
"v_mfma_f32_16x16x32_bf16 v[220:223], v[148:151], a[236:239], v[220:223]\n"
"v_cvt_pk_bf16_f32 v100, v236, v237\n"
"v_cvt_pk_bf16_f32 v101, v238, v239\n"
"v_mfma_f32_16x16x32_bf16 v[220:223], v[152:155], a[240:243], v[220:223]\n"
"ds_read_b64_tr_b16 v[200:201], v26 offset:2048\n"
"v_cvt_pk_bf16_f32 v217, 0, v217\n"
"v_mfma_f32_16x16x32_bf16 v[220:223], v[156:159], a[244:247], v[220:223]\n"
"s_waitcnt lgkmcnt(5)\n"
"v_cvt_pk_bf16_f32 v218, 0, v218\n"
"v_cvt_pk_bf16_f32 v219, 0, v219\n"
"v_mfma_f32_16x16x32_bf16 v[220:223], v[160:163], a[248:251], v[220:223]\n"
"ds_read_b64_tr_b16 v[202:203], v26 offset:8192\n"
"v_mul_f32 v216, s85, v216\n"
"v_mfma_f32_16x16x32_bf16 v[220:223], v[164:167], a[252:255], v[220:223]\n"
"ds_read_b64_tr_b16 v[204:205], v26 offset:14336\n"
"v_mul_f32 v217, s85, v217\n"
"v_mfma_f32_16x16x32_bf16 a[128:131], v[208:211], v[112:115], a[128:131]\n"
"ds_read_b64_tr_b16 v[206:207], v26 offset:20480\n"
"v_mul_f32 v218, s85, v218\n"
"v_mfma_f32_16x16x32_bf16 a[128:131], v[212:215], v[128:131], a[128:131]\n"
"v_mul_f32 v219, s85, v219\n"
"v_cvt_pk_bf16_f32 v216, 0, v216\n"
"v_mfma_f32_16x16x32_bf16 a[132:135], v[208:211], v[116:119], a[132:135]\n"
"v_cvt_pk_bf16_f32 v217, 0, v217\n"
"v_cvt_pk_bf16_f32 v218, 0, v218\n"
"v_mfma_f32_16x16x32_bf16 a[132:135], v[212:215], v[132:135], a[132:135]\n"
"v_cvt_pk_bf16_f32 v219, 0, v219\n"
"v_fma_f32 v216, v216, s86, -v30\n"
"v_mfma_f32_16x16x32_bf16 a[136:139], v[208:211], v[120:123], a[136:139]\n"
"v_fma_f32 v217, v217, s86, -v30\n"
"v_fma_f32 v218, v218, s86, -v30\n"
"v_mfma_f32_16x16x32_bf16 a[136:139], v[212:215], v[136:139], a[136:139]\n"
"v_fma_f32 v219, v219, s86, -v30\n"
"v_cvt_pk_bf16_f32 v220, 0, v220\n"
"v_mfma_f32_16x16x32_bf16 v[224:227], v[168:171], v[32:35], 0\n"
"v_exp_f32 v216, v216\n"
"v_exp_f32 v217, v217\n"
"v_exp_f32 v218, v218\n"
"v_exp_f32 v219, v219\n"
"v_mfma_f32_16x16x32_bf16 v[224:227], v[172:175], v[36:39], v[224:227]\n"
"v_cvt_pk_bf16_f32 v104, v216, v217\n"
"v_cvt_pk_bf16_f32 v105, v218, v219\n"
"v_mfma_f32_16x16x32_bf16 v[224:227], v[176:179], v[40:43], v[224:227]\n"
"v_cvt_pk_bf16_f32 v221, 0, v221\n"
"v_cvt_pk_bf16_f32 v222, 0, v222\n"
"v_mfma_f32_16x16x32_bf16 v[224:227], v[180:183], v[44:47], v[224:227]\n"
"v_cvt_pk_bf16_f32 v223, 0, v223\n"
"v_mul_f32 v220, s85, v220\n"
"v_mfma_f32_16x16x32_bf16 v[224:227], v[184:187], v[48:51], v[224:227]\n"
"buffer_load_dword v252, off, s[36:39], s15 offset:0 sc1\n"
"v_mfma_f32_16x16x32_bf16 v[224:227], v[188:191], v[52:55], v[224:227]\n"
"s_waitcnt lgkmcnt(4)\n"
"v_mul_f32 v221, s85, v221\n"
"v_mul_f32 v222, s85, v222\n"
"v_mfma_f32_16x16x32_bf16 a[140:143], v[208:211], v[124:127], a[140:143]\n"
"v_mul_f32 v223, s85, v223\n"
"v_cvt_pk_bf16_f32 v220, 0, v220\n"
"v_mfma_f32_16x16x32_bf16 a[140:143], v[212:215], v[140:143], a[140:143]\n"
"ds_read_b64_tr_b16 v[208:209], v27 offset:2048\n"
"v_cvt_pk_bf16_f32 v221, 0, v221\n"
"v_mfma_f32_16x16x32_bf16 a[144:147], v[192:195], v[112:115], a[144:147]\n"
"ds_read_b64_tr_b16 v[210:211], v27 offset:8192\n"
"v_cvt_pk_bf16_f32 v222, 0, v222\n"
"v_mfma_f32_16x16x32_bf16 a[144:147], v[196:199], v[128:131], a[144:147]\n"
"ds_read_b64_tr_b16 v[212:213], v27 offset:14336\n"
"v_cvt_pk_bf16_f32 v223, 0, v223\n"
"v_mfma_f32_16x16x32_bf16 a[148:151], v[192:195], v[116:119], a[148:151]\n"
"s_waitcnt vmcnt(14)\n"
"s_barrier\n"
"s_add_u32 s84, s83, 122880\n"
"v_mfma_f32_16x16x32_bf16 a[148:151], v[196:199], v[132:135], a[148:151]\n"
"s_add_u32 m0, s83, 73728\n"
"v_fma_f32 v220, v220, s86, -v31\n"
"v_fma_f32 v221, v221, s86, -v31\n"
"v_fma_f32 v222, v222, s86, -v31\n"
"v_fma_f32 v223, v223, s86, -v31\n"
"v_mfma_f32_16x16x32_bf16 a[152:155], v[192:195], v[120:123], a[152:155]\n"
"v_exp_f32 v220, v220\n"
"v_exp_f32 v221, v221\n"
"v_exp_f32 v222, v222\n"
"v_exp_f32 v223, v223\n"
"v_mfma_f32_16x16x32_bf16 a[152:155], v[196:199], v[136:139], a[152:155]\n"
"v_cvt_pk_bf16_f32 v108, v220, v221\n"
"v_cvt_pk_bf16_f32 v109, v222, v223\n"
"v_mfma_f32_16x16x32_bf16 v[228:231], v[168:171], v[56:59], 0\n"
"buffer_load_dwordx4 v8, s[44:47], s84 offen offset:0 lds\n"
"v_mfma_f32_16x16x32_bf16 v[228:231], v[172:175], v[60:63], v[228:231]\n"
"ds_read_b64_tr_b16 v[214:215], v27 offset:20480\n"
"v_cvt_pk_bf16_f32 v224, 0, v224\n"
"v_mfma_f32_16x16x32_bf16 v[228:231], v[176:179], v[64:67], v[228:231]\n"
"ds_read_b128 v[144:147], v16 offset:24576\n"
"v_cvt_pk_bf16_f32 v225, 0, v225\n"
"v_mfma_f32_16x16x32_bf16 v[228:231], v[180:183], v[68:71], v[228:231]\n"
"ds_read_b128 v[148:151], v17 offset:24576\n"
"v_cvt_pk_bf16_f32 v226, 0, v226\n"
"v_mfma_f32_16x16x32_bf16 v[228:231], v[184:187], v[72:75], v[228:231]\n"
"buffer_load_dwordx4 v9, s[44:47], s84 offen offset:3072 lds\n"
"v_mfma_f32_16x16x32_bf16 v[228:231], v[188:191], v[76:79], v[228:231]\n"
"s_add_u32 m0, s83, 74624\n"
"v_cvt_pk_bf16_f32 v227, 0, v227\n"
"v_mul_f32 v224, s85, v224\n"
"v_mfma_f32_16x16x32_bf16 a[156:159], v[192:195], v[124:127], a[156:159]\n"
"s_waitcnt lgkmcnt(6)\n"
"v_mul_f32 v225, s85, v225\n"
"v_mul_f32 v226, s85, v226\n"
"v_mfma_f32_16x16x32_bf16 a[156:159], v[196:199], v[140:143], a[156:159]\n"
"v_mul_f32 v227, s85, v227\n"
"v_cvt_pk_bf16_f32 v224, 0, v224\n"
"v_mfma_f32_16x16x32_bf16 a[160:163], v[200:203], v[112:115], a[160:163]\n"
"v_cvt_pk_bf16_f32 v225, 0, v225\n"
"v_cvt_pk_bf16_f32 v226, 0, v226\n"
"v_cvt_pk_bf16_f32 v227, 0, v227\n"
"v_mfma_f32_16x16x32_bf16 a[160:163], v[204:207], v[128:131], a[160:163]\n"
"v_fma_f32 v224, v224, s86, -v28\n"
"v_fma_f32 v225, v225, s86, -v28\n"
"v_fma_f32 v226, v226, s86, -v28\n"
"v_fma_f32 v227, v227, s86, -v28\n"
"v_mfma_f32_16x16x32_bf16 a[164:167], v[200:203], v[116:119], a[164:167]\n"
"v_exp_f32 v224, v224\n"
"v_exp_f32 v225, v225\n"
"v_exp_f32 v226, v226\n"
"v_exp_f32 v227, v227\n"
"v_mfma_f32_16x16x32_bf16 a[164:167], v[204:207], v[132:135], a[164:167]\n"
"v_cvt_pk_bf16_f32 v98, v224, v225\n"
"v_cvt_pk_bf16_f32 v99, v226, v227\n"
"v_mfma_f32_16x16x32_bf16 v[232:235], v[168:171], a[208:211], 0\n"
"buffer_load_dwordx4 v8, s[44:47], s84 offen offset:128 lds\n"
"v_mfma_f32_16x16x32_bf16 v[232:235], v[172:175], a[212:215], v[232:235]\n"
"ds_read_b128 v[152:155], v16 offset:25600\n"
"v_cvt_pk_bf16_f32 v228, 0, v228\n"
"v_mfma_f32_16x16x32_bf16 v[232:235], v[176:179], a[216:219], v[232:235]\n"
"ds_read_b128 v[156:159], v17 offset:25600\n"
"v_cvt_pk_bf16_f32 v229, 0, v229\n"
"v_mfma_f32_16x16x32_bf16 v[232:235], v[180:183], a[220:223], v[232:235]\n"
"ds_read_b128 v[160:163], v16 offset:26624\n"
"v_cvt_pk_bf16_f32 v230, 0, v230\n"
"v_mfma_f32_16x16x32_bf16 v[232:235], v[184:187], a[224:227], v[232:235]\n"
"buffer_load_dwordx4 v9, s[44:47], s84 offen offset:3200 lds\n"
"v_mfma_f32_16x16x32_bf16 v[232:235], v[188:191], a[228:231], v[232:235]\n"
"s_add_u32 m0, s83, 75520\n"
"v_cvt_pk_bf16_f32 v231, 0, v231\n"
"v_mul_f32 v228, s85, v228\n"
"v_mfma_f32_16x16x32_bf16 a[168:171], v[200:203], v[120:123], a[168:171]\n"
"ds_read_b128 v[164:167], v17 offset:26624\n"
"v_mul_f32 v229, s85, v229\n"
"v_mfma_f32_16x16x32_bf16 a[168:171], v[204:207], v[136:139], a[168:171]\n"
"s_waitcnt lgkmcnt(6)\n"
"v_mul_f32 v230, s85, v230\n"
"v_mul_f32 v231, s85, v231\n"
"v_mfma_f32_16x16x32_bf16 a[172:175], v[200:203], v[124:127], a[172:175]\n"
"ds_read_b64_tr_b16 v[192:193], v20 offset:0\n"
"v_cvt_pk_bf16_f32 v228, 0, v228\n"
"v_mfma_f32_16x16x32_bf16 a[172:175], v[204:207], v[140:143], a[172:175]\n"
"buffer_load_dwordx4 v8, s[44:47], s84 offen offset:256 lds\n"
"v_mfma_f32_16x16x32_bf16 a[176:179], v[208:211], v[112:115], a[176:179]\n"
"v_cvt_pk_bf16_f32 v229, 0, v229\n"
"v_cvt_pk_bf16_f32 v230, 0, v230\n"
"v_cvt_pk_bf16_f32 v231, 0, v231\n"
"v_mfma_f32_16x16x32_bf16 a[176:179], v[212:215], v[128:131], a[176:179]\n"
"v_fma_f32 v228, v228, s86, -v29\n"
"v_fma_f32 v229, v229, s86, -v29\n"
"v_fma_f32 v230, v230, s86, -v29\n"
"v_fma_f32 v231, v231, s86, -v29\n"
"v_mfma_f32_16x16x32_bf16 v[236:239], v[168:171], a[232:235], 0\n"
"v_exp_f32 v228, v228\n"
"v_exp_f32 v229, v229\n"
"v_exp_f32 v230, v230\n"
"v_exp_f32 v231, v231\n"
"v_mfma_f32_16x16x32_bf16 v[236:239], v[172:175], a[236:239], v[236:239]\n"
"v_cvt_pk_bf16_f32 v102, v228, v229\n"
"v_cvt_pk_bf16_f32 v103, v230, v231\n"
"v_mfma_f32_16x16x32_bf16 v[236:239], v[176:179], a[240:243], v[236:239]\n"
"buffer_load_dwordx4 v9, s[44:47], s84 offen offset:3328 lds\n"
"v_mfma_f32_16x16x32_bf16 v[236:239], v[180:183], a[244:247], v[236:239]\n"
"ds_read_b64_tr_b16 v[194:195], v20 offset:6144\n"
"v_cvt_pk_bf16_f32 v232, 0, v232\n"
"v_mfma_f32_16x16x32_bf16 v[236:239], v[184:187], a[248:251], v[236:239]\n"
"ds_read_b64_tr_b16 v[196:197], v20 offset:12288\n"
"v_cvt_pk_bf16_f32 v233, 0, v233\n"
"v_mfma_f32_16x16x32_bf16 v[236:239], v[188:191], a[252:255], v[236:239]\n"
"ds_read_b64_tr_b16 v[198:199], v20 offset:18432\n"
"v_cvt_pk_bf16_f32 v234, 0, v234\n"
"v_mfma_f32_16x16x32_bf16 a[180:183], v[208:211], v[116:119], a[180:183]\n"
"ds_read_b64_tr_b16 v[200:201], v21 offset:0\n"
"v_cvt_pk_bf16_f32 v235, 0, v235\n"
"v_mfma_f32_16x16x32_bf16 a[180:183], v[212:215], v[132:135], a[180:183]\n"
"ds_read_b64_tr_b16 v[202:203], v21 offset:6144\n"
"v_mul_f32 v232, s85, v232\n"
"v_mfma_f32_16x16x32_bf16 a[184:187], v[208:211], v[120:123], a[184:187]\n"
"ds_read_b64_tr_b16 v[204:205], v21 offset:12288\n"
"v_mul_f32 v233, s85, v233\n"
"v_mfma_f32_16x16x32_bf16 a[184:187], v[212:215], v[136:139], a[184:187]\n"
"s_waitcnt lgkmcnt(7)\n"
"v_mul_f32 v234, s85, v234\n"
"v_mul_f32 v235, s85, v235\n"
"v_mfma_f32_16x16x32_bf16 a[188:191], v[208:211], v[124:127], a[188:191]\n"
"v_cvt_pk_bf16_f32 v232, 0, v232\n"
"v_cvt_pk_bf16_f32 v233, 0, v233\n"
"v_cvt_pk_bf16_f32 v234, 0, v234\n"
"v_cvt_pk_bf16_f32 v235, 0, v235\n"
"v_mfma_f32_16x16x32_bf16 a[188:191], v[212:215], v[140:143], a[188:191]\n"
"v_fma_f32 v232, v232, s86, -v30\n"
"v_fma_f32 v233, v233, s86, -v30\n"
"v_fma_f32 v234, v234, s86, -v30\n"
"v_fma_f32 v235, v235, s86, -v30\n"
"v_mfma_f32_16x16x32_bf16 v[216:219], v[144:147], v[32:35], 0\n"
"v_exp_f32 v232, v232\n"
"v_exp_f32 v233, v233\n"
"v_exp_f32 v234, v234\n"
"v_exp_f32 v235, v235\n"
"v_mfma_f32_16x16x32_bf16 v[216:219], v[148:151], v[36:39], v[216:219]\n"
"v_cvt_pk_bf16_f32 v106, v232, v233\n"
"v_cvt_pk_bf16_f32 v107, v234, v235\n"
"v_mfma_f32_16x16x32_bf16 v[216:219], v[152:155], v[40:43], v[216:219]\n"
"ds_read_b64_tr_b16 v[206:207], v21 offset:18432\n"
"v_add_u32 v5, 147456, v1\n"
"v_mfma_f32_16x16x32_bf16 v[216:219], v[156:159], v[44:47], v[216:219]\n"
"ds_read_b128 v[10:13], v5 offset:3072\n"
"v_cvt_pk_bf16_f32 v236, 0, v236\n"
"v_mfma_f32_16x16x32_bf16 v[216:219], v[160:163], v[48:51], v[216:219]\n"
"ds_read_b128 v[244:247], v5 offset:4096\n"
"v_cvt_pk_bf16_f32 v237, 0, v237\n"
"v_mfma_f32_16x16x32_bf16 v[216:219], v[164:167], v[52:55], v[216:219]\n"
"ds_read_b64_tr_b16 v[208:209], v22 offset:0\n"
"v_cvt_pk_bf16_f32 v238, 0, v238\n"
"v_mfma_f32_16x16x32_bf16 a[192:195], v[240:243], v[112:115], a[192:195]\n"
"ds_read_b64_tr_b16 v[210:211], v22 offset:6144\n"
"v_cvt_pk_bf16_f32 v239, 0, v239\n"
"v_mfma_f32_16x16x32_bf16 a[192:195], v[240:243], v[128:131], a[192:195]\n"
"ds_read_b64_tr_b16 v[212:213], v22 offset:12288\n"
"v_mul_f32 v236, s85, v236\n"
"v_mfma_f32_16x16x32_bf16 a[196:199], v[240:243], v[116:119], a[196:199]\n"
"ds_read_b64_tr_b16 v[214:215], v22 offset:18432\n"
"v_mul_f32 v237, s85, v237\n"
"v_mfma_f32_16x16x32_bf16 a[196:199], v[240:243], v[132:135], a[196:199]\n"
"v_mul_f32 v238, s85, v238\n"
"v_mul_f32 v239, s85, v239\n"
"v_mfma_f32_16x16x32_bf16 a[200:203], v[240:243], v[120:123], a[200:203]\n"
"s_waitcnt lgkmcnt(0)\n"
"v_cvt_pk_bf16_f32 v236, 0, v236\n"
"v_cvt_pk_bf16_f32 v237, 0, v237\n"
"v_cvt_pk_bf16_f32 v238, 0, v238\n"
"v_cvt_pk_bf16_f32 v239, 0, v239\n"
"v_mov_b32 v14, v244\n"
"v_mov_b32 v15, v245\n"
"v_mov_b32 v6, v246\n"
"v_mov_b32 v5, v247\n"
"v_add_u32 v2, 61440, v1\n"
"v_add_u32 v3, 122880, v1\n"
"v_add_u32 v4, 4096, v1\n"
"v_mfma_f32_16x16x32_bf16 a[200:203], v[240:243], v[136:139], a[200:203]\n"
"v_fma_f32 v236, v236, s86, -v31\n"
"v_fma_f32 v237, v237, s86, -v31\n"
"v_fma_f32 v238, v238, s86, -v31\n"
"v_fma_f32 v239, v239, s86, -v31\n"
"v_mfma_f32_16x16x32_bf16 a[204:207], v[240:243], v[124:127], a[204:207]\n"
"v_exp_f32 v236, v236\n"
"v_exp_f32 v237, v237\n"
"v_exp_f32 v238, v238\n"
"v_exp_f32 v239, v239\n"
"v_mfma_f32_16x16x32_bf16 a[204:207], v[240:243], v[140:143], a[204:207]\n"
"v_cvt_pk_bf16_f32 v110, v236, v237\n"
"v_cvt_pk_bf16_f32 v111, v238, v239\n"
"v_mfma_f32_16x16x32_bf16 v[220:223], v[144:147], v[56:59], 0\n"
"ds_read_b128 v[168:171], v16 offset:30720\n"
"v_cvt_pk_bf16_f32 v216, 0, v216\n"
"v_mfma_f32_16x16x32_bf16 v[220:223], v[148:151], v[60:63], v[220:223]\n"
"ds_read_b128 v[172:175], v17 offset:30720\n"
"v_cvt_pk_bf16_f32 v217, 0, v217\n"
"v_mfma_f32_16x16x32_bf16 v[220:223], v[152:155], v[64:67], v[220:223]\n"
"ds_read_b128 v[176:179], v16 offset:31744\n"
"v_cvt_pk_bf16_f32 v218, 0, v218\n"
"v_mfma_f32_16x16x32_bf16 v[220:223], v[156:159], v[68:71], v[220:223]\n"
"v_cvt_pk_bf16_f32 v219, 0, v219\n"
"v_mul_f32 v216, s85, v216\n"
"v_mfma_f32_16x16x32_bf16 v[220:223], v[160:163], v[72:75], v[220:223]\n"
"ds_read_b128 v[180:183], v17 offset:31744\n"
"v_mul_f32 v217, s85, v217\n"
"v_mfma_f32_16x16x32_bf16 v[220:223], v[164:167], v[76:79], v[220:223]\n"
"ds_read_b128 v[184:187], v16 offset:32768\n"
"v_mul_f32 v218, s85, v218\n"
"v_mfma_f32_16x16x32_bf16 a[0:3], v[192:195], v[80:83], a[0:3]\n"
"ds_read_b128 v[188:191], v17 offset:32768\n"
"v_mul_f32 v219, s85, v219\n"
"v_mfma_f32_16x16x32_bf16 a[0:3], v[196:199], v[96:99], a[0:3]\n"
"v_cvt_pk_bf16_f32 v216, 0, v216\n"
"v_cvt_pk_bf16_f32 v217, 0, v217\n"
"v_mfma_f32_16x16x32_bf16 a[4:7], v[192:195], v[84:87], a[4:7]\n"
"s_barrier\n"
"v_cvt_pk_bf16_f32 v218, 0, v218\n"
"v_cvt_pk_bf16_f32 v219, 0, v219\n"
"v_mfma_f32_16x16x32_bf16 a[4:7], v[196:199], v[100:103], a[4:7]\n"
"s_add_u32 s84, s83, 98304\n"
"v_fma_f32 v216, v216, s86, -v28\n"
"v_fma_f32 v217, v217, s86, -v28\n"
"v_fma_f32 v218, v218, s86, -v28\n"
"v_fma_f32 v219, v219, s86, -v28\n"
"v_mfma_f32_16x16x32_bf16 a[8:11], v[192:195], v[88:91], a[8:11]\n"
"s_add_u32 m0, s83, 49152\n"
"v_exp_f32 v216, v216\n"
"v_exp_f32 v217, v217\n"
"v_exp_f32 v218, v218\n"
"v_exp_f32 v219, v219\n"
"v_mfma_f32_16x16x32_bf16 a[8:11], v[196:199], v[104:107], a[8:11]\n"
"v_cvt_pk_bf16_f32 v112, v216, v217\n"
"v_cvt_pk_bf16_f32 v113, v218, v219\n"
"v_mfma_f32_16x16x32_bf16 v[224:227], v[144:147], a[208:211], 0\n"
"buffer_load_dwordx4 v7, s[48:51], s84 offen offset:0 lds\n"
"v_mfma_f32_16x16x32_bf16 v[224:227], v[148:151], a[212:215], v[224:227]\n"
"v_cvt_pk_bf16_f32 v220, 0, v220\n"
"v_cvt_pk_bf16_f32 v221, 0, v221\n"
"v_mfma_f32_16x16x32_bf16 v[224:227], v[152:155], a[216:219], v[224:227]\n"
"v_cvt_pk_bf16_f32 v222, 0, v222\n"
"v_cvt_pk_bf16_f32 v223, 0, v223\n"
"v_mfma_f32_16x16x32_bf16 v[224:227], v[156:159], a[220:223], v[224:227]\n"
"v_mul_f32 v220, s85, v220\n"
"v_mul_f32 v221, s85, v221\n"
"v_mfma_f32_16x16x32_bf16 v[224:227], v[160:163], a[224:227], v[224:227]\n"
"buffer_load_dwordx4 v7, s[48:51], s84 offen offset:3072 lds\n"
"v_mfma_f32_16x16x32_bf16 v[224:227], v[164:167], a[228:231], v[224:227]\n"
"s_add_u32 m0, s83, 50048\n"
"v_mul_f32 v222, s85, v222\n"
"v_mfma_f32_16x16x32_bf16 a[12:15], v[192:195], v[92:95], a[12:15]\n"
"v_mul_f32 v223, s85, v223\n"
"v_cvt_pk_bf16_f32 v220, 0, v220\n"
"v_mfma_f32_16x16x32_bf16 a[12:15], v[196:199], v[108:111], a[12:15]\n"
"ds_read_b64_tr_b16 v[192:193], v23 offset:0\n"
"v_cvt_pk_bf16_f32 v221, 0, v221\n"
"v_mfma_f32_16x16x32_bf16 a[16:19], v[200:203], v[80:83], a[16:19]\n"
"ds_read_b64_tr_b16 v[194:195], v23 offset:6144\n"
"v_cvt_pk_bf16_f32 v222, 0, v222\n"
"v_mfma_f32_16x16x32_bf16 a[16:19], v[204:207], v[96:99], a[16:19]\n"
"buffer_load_dwordx4 v7, s[48:51], s84 offen offset:128 lds\n"
"v_mfma_f32_16x16x32_bf16 a[20:23], v[200:203], v[84:87], a[20:23]\n"
"v_cvt_pk_bf16_f32 v223, 0, v223\n"
"v_fma_f32 v220, v220, s86, -v29\n"
"v_mfma_f32_16x16x32_bf16 a[20:23], v[204:207], v[100:103], a[20:23]\n"
"v_fma_f32 v221, v221, s86, -v29\n"
"v_fma_f32 v222, v222, s86, -v29\n"
"v_fma_f32 v223, v223, s86, -v29\n"
"v_mfma_f32_16x16x32_bf16 v[228:231], v[144:147], a[232:235], 0\n"
"v_exp_f32 v220, v220\n"
"v_exp_f32 v221, v221\n"
"v_exp_f32 v222, v222\n"
"v_exp_f32 v223, v223\n"
"v_mfma_f32_16x16x32_bf16 v[228:231], v[148:151], a[236:239], v[228:231]\n"
"v_cvt_pk_bf16_f32 v116, v220, v221\n"
"v_cvt_pk_bf16_f32 v117, v222, v223\n"
"v_mfma_f32_16x16x32_bf16 v[228:231], v[152:155], a[240:243], v[228:231]\n"
"buffer_load_dwordx4 v7, s[48:51], s84 offen offset:3200 lds\n"
"v_mfma_f32_16x16x32_bf16 v[228:231], v[156:159], a[244:247], v[228:231]\n"
"s_add_u32 m0, s83, 50944\n"
"v_cvt_pk_bf16_f32 v224, 0, v224\n"
"v_cvt_pk_bf16_f32 v225, 0, v225\n"
"v_mfma_f32_16x16x32_bf16 v[228:231], v[160:163], a[248:251], v[228:231]\n"
"ds_read_b64_tr_b16 v[196:197], v23 offset:12288\n"
"v_cvt_pk_bf16_f32 v226, 0, v226\n"
"v_mfma_f32_16x16x32_bf16 v[228:231], v[164:167], a[252:255], v[228:231]\n"
"ds_read_b64_tr_b16 v[198:199], v23 offset:18432\n"
"v_cvt_pk_bf16_f32 v227, 0, v227\n"
"v_mfma_f32_16x16x32_bf16 a[24:27], v[200:203], v[88:91], a[24:27]\n"
"ds_read_b128 v[144:147], v16 offset:36864\n"
"v_mul_f32 v224, s85, v224\n"
"v_mfma_f32_16x16x32_bf16 a[24:27], v[204:207], v[104:107], a[24:27]\n"
"buffer_load_dwordx4 v7, s[48:51], s84 offen offset:256 lds\n"
"v_mfma_f32_16x16x32_bf16 a[28:31], v[200:203], v[92:95], a[28:31]\n"
"v_mul_f32 v225, s85, v225\n"
"v_mul_f32 v226, s85, v226\n"
"v_mfma_f32_16x16x32_bf16 a[28:31], v[204:207], v[108:111], a[28:31]\n"
"s_waitcnt lgkmcnt(5)\n"
"v_mul_f32 v227, s85, v227\n"
"v_cvt_pk_bf16_f32 v224, 0, v224\n"
"v_mfma_f32_16x16x32_bf16 a[32:35], v[208:211], v[80:83], a[32:35]\n"
"v_cvt_pk_bf16_f32 v225, 0, v225\n"
"v_cvt_pk_bf16_f32 v226, 0, v226\n"
"v_cvt_pk_bf16_f32 v227, 0, v227\n"
"v_mfma_f32_16x16x32_bf16 a[32:35], v[212:215], v[96:99], a[32:35]\n"
"v_fma_f32 v224, v224, s86, -v30\n"
"v_fma_f32 v225, v225, s86, -v30\n"
"v_fma_f32 v226, v226, s86, -v30\n"
"v_fma_f32 v227, v227, s86, -v30\n"
"v_mfma_f32_16x16x32_bf16 v[232:235], v[168:171], v[32:35], 0\n"
"v_exp_f32 v224, v224\n"
"v_exp_f32 v225, v225\n"
"v_exp_f32 v226, v226\n"
"v_exp_f32 v227, v227\n"
"v_mfma_f32_16x16x32_bf16 v[232:235], v[172:175], v[36:39], v[232:235]\n"
"v_cvt_pk_bf16_f32 v120, v224, v225\n"
"v_cvt_pk_bf16_f32 v121, v226, v227\n"
"v_mfma_f32_16x16x32_bf16 v[232:235], v[176:179], v[40:43], v[232:235]\n"
"buffer_load_dwordx4 v7, s[48:51], s84 offen offset:3328 lds\n"
"v_mfma_f32_16x16x32_bf16 v[232:235], v[180:183], v[44:47], v[232:235]\n"
"ds_read_b64_tr_b16 v[200:201], v20 offset:1024\n"
"v_cvt_pk_bf16_f32 v228, 0, v228\n"
"v_mfma_f32_16x16x32_bf16 v[232:235], v[184:187], v[48:51], v[232:235]\n"
"ds_read_b64_tr_b16 v[202:203], v20 offset:7168\n"
"v_cvt_pk_bf16_f32 v229, 0, v229\n"
"v_mfma_f32_16x16x32_bf16 v[232:235], v[188:191], v[52:55], v[232:235]\n"
"ds_read_b64_tr_b16 v[204:205], v20 offset:13312\n"
"v_cvt_pk_bf16_f32 v230, 0, v230\n"
"v_mfma_f32_16x16x32_bf16 a[36:39], v[208:211], v[84:87], a[36:39]\n"
"ds_read_b64_tr_b16 v[206:207], v20 offset:19456\n"
"v_cvt_pk_bf16_f32 v231, 0, v231\n"
"v_mfma_f32_16x16x32_bf16 a[36:39], v[212:215], v[100:103], a[36:39]\n"
"ds_read_b128 v[148:151], v17 offset:36864\n"
"v_mul_f32 v228, s85, v228\n"
"v_mfma_f32_16x16x32_bf16 a[40:43], v[208:211], v[88:91], a[40:43]\n"
"ds_read_b128 v[152:155], v16 offset:37888\n"
"v_mul_f32 v229, s85, v229\n"
"v_mfma_f32_16x16x32_bf16 a[40:43], v[212:215], v[104:107], a[40:43]\n"
"s_waitcnt lgkmcnt(7)\n"
"v_mul_f32 v230, s85, v230\n"
"v_mul_f32 v231, s85, v231\n"
"v_mfma_f32_16x16x32_bf16 a[44:47], v[208:211], v[92:95], a[44:47]\n"
"v_cvt_pk_bf16_f32 v228, 0, v228\n"
"v_cvt_pk_bf16_f32 v229, 0, v229\n"
"v_cvt_pk_bf16_f32 v230, 0, v230\n"
"v_cvt_pk_bf16_f32 v231, 0, v231\n"
"v_mfma_f32_16x16x32_bf16 a[44:47], v[212:215], v[108:111], a[44:47]\n"
"v_fma_f32 v228, v228, s86, -v31\n"
"v_fma_f32 v229, v229, s86, -v31\n"
"v_fma_f32 v230, v230, s86, -v31\n"
"v_fma_f32 v231, v231, s86, -v31\n"
"v_mfma_f32_16x16x32_bf16 a[48:51], v[192:195], v[80:83], a[48:51]\n"
"v_exp_f32 v228, v228\n"
"v_exp_f32 v229, v229\n"
"v_exp_f32 v230, v230\n"
"v_exp_f32 v231, v231\n"
"v_mfma_f32_16x16x32_bf16 a[48:51], v[196:199], v[96:99], a[48:51]\n"
"v_cvt_pk_bf16_f32 v124, v228, v229\n"
"v_cvt_pk_bf16_f32 v125, v230, v231\n"
"v_mfma_f32_16x16x32_bf16 v[236:239], v[168:171], v[56:59], 0\n"
"ds_read_b64_tr_b16 v[208:209], v21 offset:1024\n"
"v_cvt_pk_bf16_f32 v232, 0, v232\n"
"v_mfma_f32_16x16x32_bf16 v[236:239], v[172:175], v[60:63], v[236:239]\n"
"ds_read_b64_tr_b16 v[210:211], v21 offset:7168\n"
"v_cvt_pk_bf16_f32 v233, 0, v233\n"
"v_mfma_f32_16x16x32_bf16 v[236:239], v[176:179], v[64:67], v[236:239]\n"
"ds_read_b64_tr_b16 v[212:213], v21 offset:13312\n"
"v_cvt_pk_bf16_f32 v234, 0, v234\n"
"v_mfma_f32_16x16x32_bf16 v[236:239], v[180:183], v[68:71], v[236:239]\n"
"ds_read_b64_tr_b16 v[214:215], v21 offset:19456\n"
"v_cvt_pk_bf16_f32 v235, 0, v235\n"
"v_mfma_f32_16x16x32_bf16 v[236:239], v[184:187], v[72:75], v[236:239]\n"
"ds_read_b128 v[156:159], v17 offset:37888\n"
"v_mul_f32 v232, s85, v232\n"
"v_mfma_f32_16x16x32_bf16 v[236:239], v[188:191], v[76:79], v[236:239]\n"
"ds_read_b128 v[160:163], v16 offset:38912\n"
"v_mul_f32 v233, s85, v233\n"
"v_mfma_f32_16x16x32_bf16 a[52:55], v[192:195], v[84:87], a[52:55]\n"
"ds_read_b128 v[164:167], v17 offset:38912\n"
"v_mul_f32 v234, s85, v234\n"
"v_mfma_f32_16x16x32_bf16 a[52:55], v[196:199], v[100:103], a[52:55]\n"
"v_mul_f32 v235, s85, v235\n"
"v_cvt_pk_bf16_f32 v232, 0, v232\n"
"v_mfma_f32_16x16x32_bf16 a[56:59], v[192:195], v[88:91], a[56:59]\n"
"v_cvt_pk_bf16_f32 v233, 0, v233\n"
"v_cvt_pk_bf16_f32 v234, 0, v234\n"
"v_cvt_pk_bf16_f32 v235, 0, v235\n"
"v_mfma_f32_16x16x32_bf16 a[56:59], v[196:199], v[104:107], a[56:59]\n"
"v_fma_f32 v232, v232, s86, -v28\n"
"v_fma_f32 v233, v233, s86, -v28\n"
"v_fma_f32 v234, v234, s86, -v28\n"
"v_fma_f32 v235, v235, s86, -v28\n"
"v_mfma_f32_16x16x32_bf16 a[60:63], v[192:195], v[92:95], a[60:63]\n"
"v_exp_f32 v232, v232\n"
"v_exp_f32 v233, v233\n"
"v_exp_f32 v234, v234\n"
"v_exp_f32 v235, v235\n"
"v_mfma_f32_16x16x32_bf16 a[60:63], v[196:199], v[108:111], a[60:63]\n"
"v_cvt_pk_bf16_f32 v114, v232, v233\n"
"v_cvt_pk_bf16_f32 v115, v234, v235\n"
"v_mfma_f32_16x16x32_bf16 v[216:219], v[168:171], a[208:211], 0\n"
"ds_read_b64_tr_b16 v[192:193], v22 offset:1024\n"
"v_cvt_pk_bf16_f32 v236, 0, v236\n"
"v_mfma_f32_16x16x32_bf16 v[216:219], v[172:175], a[212:215], v[216:219]\n"
"ds_read_b64_tr_b16 v[194:195], v22 offset:7168\n"
"v_cvt_pk_bf16_f32 v237, 0, v237\n"
"v_mfma_f32_16x16x32_bf16 v[216:219], v[176:179], a[216:219], v[216:219]\n"
"ds_read_b64_tr_b16 v[196:197], v22 offset:13312\n"
"v_cvt_pk_bf16_f32 v238, 0, v238\n"
"v_mfma_f32_16x16x32_bf16 v[216:219], v[180:183], a[220:223], v[216:219]\n"
"s_waitcnt lgkmcnt(12)\n"
"v_cvt_pk_bf16_f32 v239, 0, v239\n"
"v_mul_f32 v236, s85, v236\n"
"v_mfma_f32_16x16x32_bf16 v[216:219], v[184:187], a[224:227], v[216:219]\n"
"ds_read_b64_tr_b16 v[198:199], v22 offset:19456\n"
"v_mul_f32 v237, s85, v237\n"
"v_mfma_f32_16x16x32_bf16 v[216:219], v[188:191], a[228:231], v[216:219]\n"
"v_mul_f32 v238, s85, v238\n"
"v_mul_f32 v239, s85, v239\n"
"v_mfma_f32_16x16x32_bf16 a[64:67], v[200:203], v[80:83], a[64:67]\n"
"v_cvt_pk_bf16_f32 v236, 0, v236\n"
"v_cvt_pk_bf16_f32 v237, 0, v237\n"
"v_mfma_f32_16x16x32_bf16 a[64:67], v[204:207], v[96:99], a[64:67]\n"
"v_cvt_pk_bf16_f32 v238, 0, v238\n"
"v_cvt_pk_bf16_f32 v239, 0, v239\n"
"v_mfma_f32_16x16x32_bf16 a[68:71], v[200:203], v[84:87], a[68:71]\n"
"v_fma_f32 v236, v236, s86, -v29\n"
"v_fma_f32 v237, v237, s86, -v29\n"
"v_mfma_f32_16x16x32_bf16 a[68:71], v[204:207], v[100:103], a[68:71]\n"
"v_fma_f32 v238, v238, s86, -v29\n"
"v_fma_f32 v239, v239, s86, -v29\n"
"v_mfma_f32_16x16x32_bf16 a[72:75], v[200:203], v[88:91], a[72:75]\n"
"v_exp_f32 v236, v236\n"
"v_mfma_f32_16x16x32_bf16 a[72:75], v[204:207], v[104:107], a[72:75]\n"
"v_exp_f32 v237, v237\n"
"v_mfma_f32_16x16x32_bf16 v[220:223], v[168:171], a[232:235], 0\n"
"v_exp_f32 v238, v238\n"
"v_exp_f32 v239, v239\n"
"v_mfma_f32_16x16x32_bf16 v[220:223], v[172:175], a[236:239], v[220:223]\n"
"v_cvt_pk_bf16_f32 v118, v236, v237\n"
"v_cvt_pk_bf16_f32 v119, v238, v239\n"
"v_mfma_f32_16x16x32_bf16 v[220:223], v[176:179], a[240:243], v[220:223]\n"
"ds_read_b128 v[168:171], v16 offset:43008\n"
"v_cvt_pk_bf16_f32 v216, 0, v216\n"
"v_mfma_f32_16x16x32_bf16 v[220:223], v[180:183], a[244:247], v[220:223]\n"
"ds_read_b128 v[172:175], v17 offset:43008\n"
"v_cvt_pk_bf16_f32 v217, 0, v217\n"
"v_mfma_f32_16x16x32_bf16 v[220:223], v[184:187], a[248:251], v[220:223]\n"
"ds_read_b128 v[176:179], v16 offset:44032\n"
"v_cvt_pk_bf16_f32 v218, 0, v218\n"
"v_mfma_f32_16x16x32_bf16 v[220:223], v[188:191], a[252:255], v[220:223]\n"
"s_waitcnt lgkmcnt(10)\n"
"v_cvt_pk_bf16_f32 v219, 0, v219\n"
"v_mul_f32 v216, s85, v216\n"
"v_mfma_f32_16x16x32_bf16 a[76:79], v[200:203], v[92:95], a[76:79]\n"
"ds_read_b128 v[180:183], v17 offset:44032\n"
"v_mul_f32 v217, s85, v217\n"
"v_mfma_f32_16x16x32_bf16 a[76:79], v[204:207], v[108:111], a[76:79]\n"
"ds_read_b64_tr_b16 v[200:201], v23 offset:1024\n"
"v_mul_f32 v218, s85, v218\n"
"v_mfma_f32_16x16x32_bf16 a[80:83], v[208:211], v[80:83], a[80:83]\n"
"ds_read_b64_tr_b16 v[202:203], v23 offset:7168\n"
"v_mul_f32 v219, s85, v219\n"
"v_mfma_f32_16x16x32_bf16 a[80:83], v[212:215], v[96:99], a[80:83]\n"
"s_waitcnt lgkmcnt(10)\n"
"v_cvt_pk_bf16_f32 v216, 0, v216\n"
"v_cvt_pk_bf16_f32 v217, 0, v217\n"
"v_mfma_f32_16x16x32_bf16 a[84:87], v[208:211], v[84:87], a[84:87]\n"
"v_cvt_pk_bf16_f32 v218, 0, v218\n"
"v_cvt_pk_bf16_f32 v219, 0, v219\n"
"v_mfma_f32_16x16x32_bf16 a[84:87], v[212:215], v[100:103], a[84:87]\n"
"v_fma_f32 v216, v216, s86, -v30\n"
"v_fma_f32 v217, v217, s86, -v30\n"
"v_fma_f32 v218, v218, s86, -v30\n"
"v_fma_f32 v219, v219, s86, -v30\n"
"v_mfma_f32_16x16x32_bf16 v[224:227], v[144:147], v[32:35], 0\n"
"v_exp_f32 v216, v216\n"
"v_exp_f32 v217, v217\n"
"v_exp_f32 v218, v218\n"
"v_exp_f32 v219, v219\n"
"v_mfma_f32_16x16x32_bf16 v[224:227], v[148:151], v[36:39], v[224:227]\n"
"v_cvt_pk_bf16_f32 v122, v216, v217\n"
"v_cvt_pk_bf16_f32 v123, v218, v219\n"
"v_mfma_f32_16x16x32_bf16 v[224:227], v[152:155], v[40:43], v[224:227]\n"
"ds_read_b64_tr_b16 v[204:205], v23 offset:13312\n"
"v_cvt_pk_bf16_f32 v220, 0, v220\n"
"v_mfma_f32_16x16x32_bf16 v[224:227], v[156:159], v[44:47], v[224:227]\n"
"ds_read_b64_tr_b16 v[206:207], v23 offset:19456\n"
"v_cvt_pk_bf16_f32 v221, 0, v221\n"
"v_mfma_f32_16x16x32_bf16 v[224:227], v[160:163], v[48:51], v[224:227]\n"
"ds_read_b128 v[184:187], v16 offset:45056\n"
"v_cvt_pk_bf16_f32 v222, 0, v222\n"
"v_mfma_f32_16x16x32_bf16 v[224:227], v[164:167], v[52:55], v[224:227]\n"
"ds_read_b128 v[188:191], v17 offset:45056\n"
"v_cvt_pk_bf16_f32 v223, 0, v223\n"
"v_mfma_f32_16x16x32_bf16 a[88:91], v[208:211], v[88:91], a[88:91]\n"
"v_mul_f32 v220, s85, v220\n"
"v_mul_f32 v221, s85, v221\n"
"v_mfma_f32_16x16x32_bf16 a[88:91], v[212:215], v[104:107], a[88:91]\n"
"s_waitcnt lgkmcnt(10)\n"
"v_mul_f32 v222, s85, v222\n"
"v_mul_f32 v223, s85, v223\n"
"v_mfma_f32_16x16x32_bf16 a[92:95], v[208:211], v[92:95], a[92:95]\n"
"v_cvt_pk_bf16_f32 v220, 0, v220\n"
"v_cvt_pk_bf16_f32 v221, 0, v221\n"
"v_mfma_f32_16x16x32_bf16 a[92:95], v[212:215], v[108:111], a[92:95]\n"
"ds_read_b64_tr_b16 v[208:209], v20 offset:2048\n"
"v_cvt_pk_bf16_f32 v222, 0, v222\n"
"v_mfma_f32_16x16x32_bf16 a[96:99], v[192:195], v[80:83], a[96:99]\n"
"v_cvt_pk_bf16_f32 v223, 0, v223\n"
"v_fma_f32 v220, v220, s86, -v31\n"
"v_mfma_f32_16x16x32_bf16 a[96:99], v[196:199], v[96:99], a[96:99]\n"
"v_fma_f32 v221, v221, s86, -v31\n"
"v_fma_f32 v222, v222, s86, -v31\n"
"v_fma_f32 v223, v223, s86, -v31\n"
"v_mfma_f32_16x16x32_bf16 a[100:103], v[192:195], v[84:87], a[100:103]\n"
"v_exp_f32 v220, v220\n"
"v_exp_f32 v221, v221\n"
"v_exp_f32 v222, v222\n"
"v_exp_f32 v223, v223\n"
"v_mfma_f32_16x16x32_bf16 a[100:103], v[196:199], v[100:103], a[100:103]\n"
"v_cvt_pk_bf16_f32 v126, v220, v221\n"
"v_cvt_pk_bf16_f32 v127, v222, v223\n"
"v_mfma_f32_16x16x32_bf16 v[228:231], v[144:147], v[56:59], 0\n"
"ds_read_b64_tr_b16 v[210:211], v20 offset:8192\n"
"v_cvt_pk_bf16_f32 v224, 0, v224\n"
"v_mfma_f32_16x16x32_bf16 v[228:231], v[148:151], v[60:63], v[228:231]\n"
"ds_read_b64_tr_b16 v[212:213], v20 offset:14336\n"
"v_cvt_pk_bf16_f32 v225, 0, v225\n"
"v_mfma_f32_16x16x32_bf16 v[228:231], v[152:155], v[64:67], v[228:231]\n"
"ds_read_b64_tr_b16 v[214:215], v20 offset:20480\n"
"v_cvt_pk_bf16_f32 v226, 0, v226\n"
"v_mfma_f32_16x16x32_bf16 v[228:231], v[156:159], v[68:71], v[228:231]\n"
"v_cvt_pk_bf16_f32 v227, 0, v227\n"
"v_mul_f32 v224, s85, v224\n"
"v_mfma_f32_16x16x32_bf16 v[228:231], v[160:163], v[72:75], v[228:231]\n"
"v_mul_f32 v225, s85, v225\n"
"v_mul_f32 v226, s85, v226\n"
"v_mfma_f32_16x16x32_bf16 v[228:231], v[164:167], v[76:79], v[228:231]\n"
"v_mul_f32 v227, s85, v227\n"
"v_cvt_pk_bf16_f32 v224, 0, v224\n"
"v_mfma_f32_16x16x32_bf16 a[104:107], v[192:195], v[88:91], a[104:107]\n"
"v_cvt_pk_bf16_f32 v225, 0, v225\n"
"v_cvt_pk_bf16_f32 v226, 0, v226\n"
"v_mfma_f32_16x16x32_bf16 a[104:107], v[196:199], v[104:107], a[104:107]\n"
"s_waitcnt lgkmcnt(6)\n"
"v_cvt_pk_bf16_f32 v227, 0, v227\n"
"v_fma_f32 v224, v224, s86, -v28\n"
"v_mfma_f32_16x16x32_bf16 a[108:111], v[192:195], v[92:95], a[108:111]\n"
"v_fma_f32 v225, v225, s86, -v28\n"
"v_fma_f32 v226, v226, s86, -v28\n"
"v_mfma_f32_16x16x32_bf16 a[108:111], v[196:199], v[108:111], a[108:111]\n"
"v_fma_f32 v227, v227, s86, -v28\n"
"v_cvt_pk_bf16_f32 v228, 0, v228\n"
"v_mfma_f32_16x16x32_bf16 a[112:115], v[200:203], v[80:83], a[112:115]\n"
"v_exp_f32 v224, v224\n"
"v_exp_f32 v225, v225\n"
"v_exp_f32 v226, v226\n"
"v_exp_f32 v227, v227\n"
"v_mfma_f32_16x16x32_bf16 a[112:115], v[204:207], v[96:99], a[112:115]\n"
"v_cvt_pk_bf16_f32 v128, v224, v225\n"
"v_cvt_pk_bf16_f32 v129, v226, v227\n"
"v_mfma_f32_16x16x32_bf16 v[232:235], v[144:147], a[208:211], 0\n"
"ds_read_b64_tr_b16 v[192:193], v21 offset:2048\n"
"v_cvt_pk_bf16_f32 v229, 0, v229\n"
"v_mfma_f32_16x16x32_bf16 v[232:235], v[148:151], a[212:215], v[232:235]\n"
"ds_read_b64_tr_b16 v[194:195], v21 offset:8192\n"
"v_cvt_pk_bf16_f32 v230, 0, v230\n"
"v_mfma_f32_16x16x32_bf16 v[232:235], v[152:155], a[216:219], v[232:235]\n"
"ds_read_b64_tr_b16 v[196:197], v21 offset:14336\n"
"v_cvt_pk_bf16_f32 v231, 0, v231\n"
"v_mfma_f32_16x16x32_bf16 v[232:235], v[156:159], a[220:223], v[232:235]\n"
"ds_read_b64_tr_b16 v[198:199], v21 offset:20480\n"
"v_mul_f32 v228, s85, v228\n"
"v_mfma_f32_16x16x32_bf16 v[232:235], v[160:163], a[224:227], v[232:235]\n"
"v_mul_f32 v229, s85, v229\n"
"v_mul_f32 v230, s85, v230\n"
"v_mfma_f32_16x16x32_bf16 v[232:235], v[164:167], a[228:231], v[232:235]\n"
"v_mul_f32 v231, s85, v231\n"
"v_cvt_pk_bf16_f32 v228, 0, v228\n"
"v_mfma_f32_16x16x32_bf16 a[116:119], v[200:203], v[84:87], a[116:119]\n"
"v_cvt_pk_bf16_f32 v229, 0, v229\n"
"v_cvt_pk_bf16_f32 v230, 0, v230\n"
"v_mfma_f32_16x16x32_bf16 a[116:119], v[204:207], v[100:103], a[116:119]\n"
"v_cvt_pk_bf16_f32 v231, 0, v231\n"
"v_fma_f32 v228, v228, s86, -v29\n"
"v_mfma_f32_16x16x32_bf16 a[120:123], v[200:203], v[88:91], a[120:123]\n"
"v_fma_f32 v229, v229, s86, -v29\n"
"v_fma_f32 v230, v230, s86, -v29\n"
"v_mfma_f32_16x16x32_bf16 a[120:123], v[204:207], v[104:107], a[120:123]\n"
"v_fma_f32 v231, v231, s86, -v29\n"
"v_cvt_pk_bf16_f32 v232, 0, v232\n"
"v_mfma_f32_16x16x32_bf16 a[124:127], v[200:203], v[92:95], a[124:127]\n"
"v_exp_f32 v228, v228\n"
"v_mfma_f32_16x16x32_bf16 a[124:127], v[204:207], v[108:111], a[124:127]\n"
"v_exp_f32 v229, v229\n"
"v_mfma_f32_16x16x32_bf16 v[236:239], v[144:147], a[232:235], 0\n"
"v_exp_f32 v230, v230\n"
"v_exp_f32 v231, v231\n"
"v_mfma_f32_16x16x32_bf16 v[236:239], v[148:151], a[236:239], v[236:239]\n"
"v_cvt_pk_bf16_f32 v132, v228, v229\n"
"v_cvt_pk_bf16_f32 v133, v230, v231\n"
"v_mfma_f32_16x16x32_bf16 v[236:239], v[152:155], a[240:243], v[236:239]\n"
"ds_read_b64_tr_b16 v[200:201], v22 offset:2048\n"
"v_cvt_pk_bf16_f32 v233, 0, v233\n"
"v_mfma_f32_16x16x32_bf16 v[236:239], v[156:159], a[244:247], v[236:239]\n"
"s_waitcnt lgkmcnt(5)\n"
"v_cvt_pk_bf16_f32 v234, 0, v234\n"
"v_cvt_pk_bf16_f32 v235, 0, v235\n"
"v_mfma_f32_16x16x32_bf16 v[236:239], v[160:163], a[248:251], v[236:239]\n"
"ds_read_b64_tr_b16 v[202:203], v22 offset:8192\n"
"v_mul_f32 v232, s85, v232\n"
"v_mfma_f32_16x16x32_bf16 v[236:239], v[164:167], a[252:255], v[236:239]\n"
"ds_read_b64_tr_b16 v[204:205], v22 offset:14336\n"
"v_mul_f32 v233, s85, v233\n"
"v_mfma_f32_16x16x32_bf16 a[128:131], v[208:211], v[80:83], a[128:131]\n"
"ds_read_b64_tr_b16 v[206:207], v22 offset:20480\n"
"v_mul_f32 v234, s85, v234\n"
"v_mfma_f32_16x16x32_bf16 a[128:131], v[212:215], v[96:99], a[128:131]\n"
"v_mul_f32 v235, s85, v235\n"
"v_cvt_pk_bf16_f32 v232, 0, v232\n"
"v_mfma_f32_16x16x32_bf16 a[132:135], v[208:211], v[84:87], a[132:135]\n"
"v_cvt_pk_bf16_f32 v233, 0, v233\n"
"v_cvt_pk_bf16_f32 v234, 0, v234\n"
"v_mfma_f32_16x16x32_bf16 a[132:135], v[212:215], v[100:103], a[132:135]\n"
"v_cvt_pk_bf16_f32 v235, 0, v235\n"
"v_fma_f32 v232, v232, s86, -v30\n"
"v_mfma_f32_16x16x32_bf16 a[136:139], v[208:211], v[88:91], a[136:139]\n"
"v_fma_f32 v233, v233, s86, -v30\n"
"v_fma_f32 v234, v234, s86, -v30\n"
"v_mfma_f32_16x16x32_bf16 a[136:139], v[212:215], v[104:107], a[136:139]\n"
"v_fma_f32 v235, v235, s86, -v30\n"
"v_cvt_pk_bf16_f32 v236, 0, v236\n"
"v_mfma_f32_16x16x32_bf16 v[216:219], v[168:171], v[32:35], 0\n"
"v_exp_f32 v232, v232\n"
"v_exp_f32 v233, v233\n"
"v_exp_f32 v234, v234\n"
"v_exp_f32 v235, v235\n"
"v_mfma_f32_16x16x32_bf16 v[216:219], v[172:175], v[36:39], v[216:219]\n"
"v_cvt_pk_bf16_f32 v136, v232, v233\n"
"v_cvt_pk_bf16_f32 v137, v234, v235\n"
"v_mfma_f32_16x16x32_bf16 v[216:219], v[176:179], v[40:43], v[216:219]\n"
"v_cvt_pk_bf16_f32 v237, 0, v237\n"
"v_cvt_pk_bf16_f32 v238, 0, v238\n"
"v_mfma_f32_16x16x32_bf16 v[216:219], v[180:183], v[44:47], v[216:219]\n"
"v_cvt_pk_bf16_f32 v239, 0, v239\n"
"v_mul_f32 v236, s85, v236\n"
"v_mfma_f32_16x16x32_bf16 v[216:219], v[184:187], v[48:51], v[216:219]\n"
"v_mul_f32 v237, s85, v237\n"
"v_mul_f32 v238, s85, v238\n"
"v_mfma_f32_16x16x32_bf16 v[216:219], v[188:191], v[52:55], v[216:219]\n"
"s_waitcnt lgkmcnt(4)\n"
"v_mul_f32 v239, s85, v239\n"
"v_cvt_pk_bf16_f32 v236, 0, v236\n"
"v_mfma_f32_16x16x32_bf16 a[140:143], v[208:211], v[92:95], a[140:143]\n"
"v_cvt_pk_bf16_f32 v237, 0, v237\n"
"v_cvt_pk_bf16_f32 v238, 0, v238\n"
"v_mfma_f32_16x16x32_bf16 a[140:143], v[212:215], v[108:111], a[140:143]\n"
"ds_read_b64_tr_b16 v[208:209], v23 offset:2048\n"
"v_cvt_pk_bf16_f32 v239, 0, v239\n"
"v_mfma_f32_16x16x32_bf16 a[144:147], v[192:195], v[80:83], a[144:147]\n"
"ds_read_b64_tr_b16 v[210:211], v23 offset:8192\n"
"v_fma_f32 v236, v236, s86, -v31\n"
"v_mfma_f32_16x16x32_bf16 a[144:147], v[196:199], v[96:99], a[144:147]\n"
"ds_read_b64_tr_b16 v[212:213], v23 offset:14336\n"
"v_fma_f32 v237, v237, s86, -v31\n"
"v_mfma_f32_16x16x32_bf16 a[148:151], v[192:195], v[84:87], a[148:151]\n"
"s_waitcnt vmcnt(13)\n"
"s_barrier\n"
"s_add_u32 s84, s83, 147456\n"
"v_mfma_f32_16x16x32_bf16 a[148:151], v[196:199], v[100:103], a[148:151]\n"
"s_add_u32 m0, s83, 98304\n"
"v_fma_f32 v238, v238, s86, -v31\n"
"v_fma_f32 v239, v239, s86, -v31\n"
"v_mfma_f32_16x16x32_bf16 a[152:155], v[192:195], v[88:91], a[152:155]\n"
"v_exp_f32 v236, v236\n"
"v_exp_f32 v237, v237\n"
"v_exp_f32 v238, v238\n"
"v_exp_f32 v239, v239\n"
"v_mfma_f32_16x16x32_bf16 a[152:155], v[196:199], v[104:107], a[152:155]\n"
"s_waitcnt vmcnt(12)\n"
"v_cmp_gt_u32 s[92:93], 32, v252\n"
"s_nop 0\n"
"s_cmp_eq_u64 s[92:93], 0\n"
"s_cbranch_scc0 .Lspin0\n"
".Lback0:\n"
"v_cvt_pk_bf16_f32 v140, v236, v237\n"
"v_cvt_pk_bf16_f32 v141, v238, v239\n"
"v_mfma_f32_16x16x32_bf16 v[220:223], v[168:171], v[56:59], 0\n"
"buffer_load_dwordx4 v8, s[44:47], s84 offen offset:0 lds\n"
"v_mfma_f32_16x16x32_bf16 v[220:223], v[172:175], v[60:63], v[220:223]\n"
"v_cvt_pk_bf16_f32 v216, 0, v216\n"
"v_cvt_pk_bf16_f32 v217, 0, v217\n"
"v_mfma_f32_16x16x32_bf16 v[220:223], v[176:179], v[64:67], v[220:223]\n"
"s_cmp_lg_u32 s76, 0\n"
"s_cbranch_scc1 .Lsig1\n"
"s_mov_b64 exec, 0xff\n"
"v_mov_b32 v252, 1\n"
"v_lshlrev_b32 v253, 15, v0\n"
"buffer_store_dword v252, v253, s[36:39], s35 offen sc1\n"
"s_mov_b64 exec, -1\n"
".Lsig1:\n"
"v_mfma_f32_16x16x32_bf16 v[220:223], v[180:183], v[68:71], v[220:223]\n"
"ds_read_b64_tr_b16 v[214:215], v23 offset:20480\n"
"v_cvt_pk_bf16_f32 v218, 0, v218\n"
"v_mfma_f32_16x16x32_bf16 v[220:223], v[184:187], v[72:75], v[220:223]\n"
"buffer_load_dwordx4 v9, s[44:47], s84 offen offset:3072 lds\n"
"v_mfma_f32_16x16x32_bf16 v[220:223], v[188:191], v[76:79], v[220:223]\n"
"s_add_u32 m0, s83, 99200\n"
"v_cvt_pk_bf16_f32 v219, 0, v219\n"
"v_mul_f32 v216, s85, v216\n"
"v_mfma_f32_16x16x32_bf16 a[156:159], v[192:195], v[92:95], a[156:159]\n"
"s_waitcnt lgkmcnt(4)\n"
"v_mul_f32 v217, s85, v217\n"
"v_mul_f32 v218, s85, v218\n"
"v_mfma_f32_16x16x32_bf16 a[156:159], v[196:199], v[108:111], a[156:159]\n"
"v_mul_f32 v219, s85, v219\n"
"v_cvt_pk_bf16_f32 v216, 0, v216\n"
"v_mfma_f32_16x16x32_bf16 a[160:163], v[200:203], v[80:83], a[160:163]\n"
"v_cvt_pk_bf16_f32 v217, 0, v217\n"
"v_cvt_pk_bf16_f32 v218, 0, v218\n"
"v_cvt_pk_bf16_f32 v219, 0, v219\n"
"v_mfma_f32_16x16x32_bf16 a[160:163], v[204:207], v[96:99], a[160:163]\n"
"v_fma_f32 v216, v216, s86, -v28\n"
"v_fma_f32 v217, v217, s86, -v28\n"
"v_fma_f32 v218, v218, s86, -v28\n"
"v_fma_f32 v219, v219, s86, -v28\n"
"v_mfma_f32_16x16x32_bf16 a[164:167], v[200:203], v[84:87], a[164:167]\n"
"v_exp_f32 v216, v216\n"
"v_exp_f32 v217, v217\n"
"v_exp_f32 v218, v218\n"
"v_exp_f32 v219, v219\n"
"v_mfma_f32_16x16x32_bf16 a[164:167], v[204:207], v[100:103], a[164:167]\n"
"v_cvt_pk_bf16_f32 v130, v216, v217\n"
"v_cvt_pk_bf16_f32 v131, v218, v219\n"
"v_mfma_f32_16x16x32_bf16 v[224:227], v[168:171], a[208:211], 0\n"
"s_add_u32 vcc_hi, vcc_lo, 65536\n"
"buffer_load_dword v254, v0, s[56:59], vcc_hi idxen\n"
"buffer_load_dwordx4 v8, s[44:47], s84 offen offset:128 lds\n"
"v_mfma_f32_16x16x32_bf16 v[224:227], v[172:175], a[212:215], v[224:227]\n"
"ds_read_b128 v[144:147], v18 offset:0\n"
"v_cvt_pk_bf16_f32 v220, 0, v220\n"
"v_mfma_f32_16x16x32_bf16 v[224:227], v[176:179], a[216:219], v[224:227]\n"
"ds_read_b128 v[148:151], v19 offset:0\n"
"v_cvt_pk_bf16_f32 v221, 0, v221\n"
"v_mfma_f32_16x16x32_bf16 v[224:227], v[180:183], a[220:223], v[224:227]\n"
"ds_read_b128 v[152:155], v18 offset:1024\n"
"v_cvt_pk_bf16_f32 v222, 0, v222\n"
"v_mfma_f32_16x16x32_bf16 v[224:227], v[184:187], a[224:227], v[224:227]\n"
"buffer_load_dwordx4 v9, s[44:47], s84 offen offset:3200 lds\n"
"v_mfma_f32_16x16x32_bf16 v[224:227], v[188:191], a[228:231], v[224:227]\n"
"s_add_u32 m0, s83, 100096\n"
"v_cvt_pk_bf16_f32 v223, 0, v223\n"
"v_mul_f32 v220, s85, v220\n"
"v_mfma_f32_16x16x32_bf16 a[168:171], v[200:203], v[88:91], a[168:171]\n"
"ds_read_b128 v[156:159], v19 offset:1024\n"
"v_mul_f32 v221, s85, v221\n"
"v_mfma_f32_16x16x32_bf16 a[168:171], v[204:207], v[104:107], a[168:171]\n"
"s_waitcnt lgkmcnt(4)\n"
"v_mul_f32 v222, s85, v222\n"
"v_mul_f32 v223, s85, v223\n"
"v_mfma_f32_16x16x32_bf16 a[172:175], v[200:203], v[92:95], a[172:175]\n"
"ds_read_b128 v[160:163], v18 offset:2048\n"
"v_cvt_pk_bf16_f32 v220, 0, v220\n"
"v_mfma_f32_16x16x32_bf16 a[172:175], v[204:207], v[108:111], a[172:175]\n"
"buffer_load_dwordx4 v8, s[44:47], s84 offen offset:256 lds\n"
"v_mfma_f32_16x16x32_bf16 a[176:179], v[208:211], v[80:83], a[176:179]\n"
"v_cvt_pk_bf16_f32 v221, 0, v221\n"
"v_cvt_pk_bf16_f32 v222, 0, v222\n"
"v_cvt_pk_bf16_f32 v223, 0, v223\n"
"v_mfma_f32_16x16x32_bf16 a[176:179], v[212:215], v[96:99], a[176:179]\n"
"v_fma_f32 v220, v220, s86, -v29\n"
"v_fma_f32 v221, v221, s86, -v29\n"
"v_fma_f32 v222, v222, s86, -v29\n"
"v_fma_f32 v223, v223, s86, -v29\n"
"v_mfma_f32_16x16x32_bf16 v[228:231], v[168:171], a[232:235], 0\n"
"v_exp_f32 v220, v220\n"
"v_exp_f32 v221, v221\n"
"v_exp_f32 v222, v222\n"
"v_exp_f32 v223, v223\n"
"v_mfma_f32_16x16x32_bf16 v[228:231], v[172:175], a[236:239], v[228:231]\n"
"v_cvt_pk_bf16_f32 v134, v220, v221\n"
"v_cvt_pk_bf16_f32 v135, v222, v223\n"
"v_mfma_f32_16x16x32_bf16 v[228:231], v[176:179], a[240:243], v[228:231]\n"
"buffer_load_dwordx4 v9, s[44:47], s84 offen offset:3328 lds\n"
"v_mfma_f32_16x16x32_bf16 v[228:231], v[180:183], a[244:247], v[228:231]\n"
"ds_read_b128 v[164:167], v19 offset:2048\n"
"v_cvt_pk_bf16_f32 v224, 0, v224\n"
"v_mfma_f32_16x16x32_bf16 v[228:231], v[184:187], a[248:251], v[228:231]\n"
"ds_read_b64_tr_b16 v[192:193], v20 offset:24576\n"
"v_cvt_pk_bf16_f32 v225, 0, v225\n"
"v_mfma_f32_16x16x32_bf16 v[228:231], v[188:191], a[252:255], v[228:231]\n"
"ds_read_b64_tr_b16 v[194:195], v20 offset:30720\n"
"v_cvt_pk_bf16_f32 v226, 0, v226\n"
"v_mfma_f32_16x16x32_bf16 a[180:183], v[208:211], v[84:87], a[180:183]\n"
"ds_read_b64_tr_b16 v[196:197], v20 offset:36864\n"
"v_cvt_pk_bf16_f32 v227, 0, v227\n"
"v_mfma_f32_16x16x32_bf16 a[180:183], v[212:215], v[100:103], a[180:183]\n"
"ds_read_b64_tr_b16 v[198:199], v20 offset:43008\n"
"v_mul_f32 v224, s85, v224\n"
"v_mfma_f32_16x16x32_bf16 a[184:187], v[208:211], v[88:91], a[184:187]\n"
"ds_read_b64_tr_b16 v[200:201], v21 offset:24576\n"
"v_mul_f32 v225, s85, v225\n"
"v_mfma_f32_16x16x32_bf16 a[184:187], v[212:215], v[104:107], a[184:187]\n"
"s_waitcnt lgkmcnt(5)\n"
"v_mul_f32 v226, s85, v226\n"
"v_mul_f32 v227, s85, v227\n"
"v_mfma_f32_16x16x32_bf16 a[188:191], v[208:211], v[92:95], a[188:191]\n"
"v_cvt_pk_bf16_f32 v224, 0, v224\n"
"v_cvt_pk_bf16_f32 v225, 0, v225\n"
"v_cvt_pk_bf16_f32 v226, 0, v226\n"
"v_cvt_pk_bf16_f32 v227, 0, v227\n"
"v_mfma_f32_16x16x32_bf16 a[188:191], v[212:215], v[108:111], a[188:191]\n"
"v_fma_f32 v224, v224, s86, -v30\n"
"v_fma_f32 v225, v225, s86, -v30\n"
"v_fma_f32 v226, v226, s86, -v30\n"
"v_fma_f32 v227, v227, s86, -v30\n"
"v_mfma_f32_16x16x32_bf16 v[232:235], v[144:147], v[32:35], 0\n"
"v_exp_f32 v224, v224\n"
"v_exp_f32 v225, v225\n"
"v_exp_f32 v226, v226\n"
"v_exp_f32 v227, v227\n"
"v_mfma_f32_16x16x32_bf16 v[232:235], v[148:151], v[36:39], v[232:235]\n"
"v_cvt_pk_bf16_f32 v138, v224, v225\n"
"v_cvt_pk_bf16_f32 v139, v226, v227\n"
"v_mfma_f32_16x16x32_bf16 v[232:235], v[152:155], v[40:43], v[232:235]\n"
"ds_read_b64_tr_b16 v[202:203], v21 offset:30720\n"
"v_cvt_pk_bf16_f32 v228, 0, v228\n"
"v_mfma_f32_16x16x32_bf16 v[232:235], v[156:159], v[44:47], v[232:235]\n"
"ds_read_b64_tr_b16 v[204:205], v21 offset:36864\n"
"v_cvt_pk_bf16_f32 v229, 0, v229\n"
"v_mfma_f32_16x16x32_bf16 v[232:235], v[160:163], v[48:51], v[232:235]\n"
"ds_read_b64_tr_b16 v[206:207], v21 offset:43008\n"
"v_cvt_pk_bf16_f32 v230, 0, v230\n"
"v_mfma_f32_16x16x32_bf16 v[232:235], v[164:167], v[52:55], v[232:235]\n"
"ds_read_b64_tr_b16 v[208:209], v22 offset:24576\n"
"v_cvt_pk_bf16_f32 v231, 0, v231\n"
"v_mfma_f32_16x16x32_bf16 a[192:195], v[240:243], v[80:83], a[192:195]\n"
"ds_read_b64_tr_b16 v[210:211], v22 offset:30720\n"
"v_mul_f32 v228, s85, v228\n"
"v_mfma_f32_16x16x32_bf16 a[192:195], v[240:243], v[96:99], a[192:195]\n"
"ds_read_b64_tr_b16 v[212:213], v22 offset:36864\n"
"v_mul_f32 v229, s85, v229\n"
"v_mfma_f32_16x16x32_bf16 a[196:199], v[240:243], v[84:87], a[196:199]\n"
"ds_read_b64_tr_b16 v[214:215], v22 offset:43008\n"
"v_mul_f32 v230, s85, v230\n"
"v_mfma_f32_16x16x32_bf16 a[196:199], v[240:243], v[100:103], a[196:199]\n"
"v_mul_f32 v231, s85, v231\n"
"v_cvt_pk_bf16_f32 v228, 0, v228\n"
"v_mfma_f32_16x16x32_bf16 a[200:203], v[240:243], v[88:91], a[200:203]\n"
"v_cvt_pk_bf16_f32 v229, 0, v229\n"
"v_cvt_pk_bf16_f32 v230, 0, v230\n"
"v_cvt_pk_bf16_f32 v231, 0, v231\n"
"v_mfma_f32_16x16x32_bf16 a[200:203], v[240:243], v[104:107], a[200:203]\n"
"v_fma_f32 v228, v228, s86, -v31\n"
"v_fma_f32 v229, v229, s86, -v31\n"
"v_fma_f32 v230, v230, s86, -v31\n"
"v_fma_f32 v231, v231, s86, -v31\n"
"v_mfma_f32_16x16x32_bf16 a[204:207], v[240:243], v[92:95], a[204:207]\n"
"v_exp_f32 v228, v228\n"
"v_exp_f32 v229, v229\n"
"v_exp_f32 v230, v230\n"
"v_exp_f32 v231, v231\n"
"v_mfma_f32_16x16x32_bf16 a[204:207], v[240:243], v[108:111], a[204:207]\n"
"v_cvt_pk_bf16_f32 v142, v228, v229\n"
"v_cvt_pk_bf16_f32 v143, v230, v231\n"
"v_mfma_f32_16x16x32_bf16 v[236:239], v[144:147], v[56:59], 0\n"
"ds_read_b128 v[168:171], v18 offset:6144\n"
"v_cvt_pk_bf16_f32 v232, 0, v232\n"
"v_mfma_f32_16x16x32_bf16 v[236:239], v[148:151], v[60:63], v[236:239]\n"
"ds_read_b128 v[172:175], v19 offset:6144\n"
"v_cvt_pk_bf16_f32 v233, 0, v233\n"
"v_mfma_f32_16x16x32_bf16 v[236:239], v[152:155], v[64:67], v[236:239]\n"
"ds_read_b128 v[176:179], v18 offset:7168\n"
"v_cvt_pk_bf16_f32 v234, 0, v234\n"
"v_mfma_f32_16x16x32_bf16 v[236:239], v[156:159], v[68:71], v[236:239]\n"
"s_waitcnt lgkmcnt(11)\n"
"v_cvt_pk_bf16_f32 v235, 0, v235\n"
"v_mul_f32 v232, s85, v232\n"
"v_mfma_f32_16x16x32_bf16 v[236:239], v[160:163], v[72:75], v[236:239]\n"
"ds_read_b128 v[180:183], v19 offset:7168\n"
"v_mul_f32 v233, s85, v233\n"
"v_mfma_f32_16x16x32_bf16 v[236:239], v[164:167], v[76:79], v[236:239]\n"
"ds_read_b128 v[184:187], v18 offset:8192\n"
"v_mul_f32 v234, s85, v234\n"
"v_mfma_f32_16x16x32_bf16 a[0:3], v[192:195], v[112:115], a[0:3]\n"
"ds_read_b128 v[188:191], v19 offset:8192\n"
"v_mul_f32 v235, s85, v235\n"
"v_mfma_f32_16x16x32_bf16 a[0:3], v[196:199], v[128:131], a[0:3]\n"
"v_cvt_pk_bf16_f32 v232, 0, v232\n"
"v_cvt_pk_bf16_f32 v233, 0, v233\n"
"v_mfma_f32_16x16x32_bf16 a[4:7], v[192:195], v[116:119], a[4:7]\n"
"s_barrier\n"
"v_cvt_pk_bf16_f32 v234, 0, v234\n"
"v_cvt_pk_bf16_f32 v235, 0, v235\n"
"v_mfma_f32_16x16x32_bf16 a[4:7], v[196:199], v[132:135], a[4:7]\n"
"s_add_u32 s84, s83, 122880\n"
"v_fma_f32 v232, v232, s86, -v28\n"
"v_fma_f32 v233, v233, s86, -v28\n"
"v_fma_f32 v234, v234, s86, -v28\n"
"v_fma_f32 v235, v235, s86, -v28\n"
"v_mfma_f32_16x16x32_bf16 a[8:11], v[192:195], v[120:123], a[8:11]\n"
"s_add_u32 m0, s83, 0\n"
"v_exp_f32 v232, v232\n"
"v_exp_f32 v233, v233\n"
"v_exp_f32 v234, v234\n"
"v_exp_f32 v235, v235\n"
"v_mfma_f32_16x16x32_bf16 a[8:11], v[196:199], v[136:139], a[8:11]\n"
"v_cvt_pk_bf16_f32 v80, v232, v233\n"
"v_cvt_pk_bf16_f32 v81, v234, v235\n"
"v_mfma_f32_16x16x32_bf16 v[216:219], v[144:147], a[208:211], 0\n"
"buffer_load_dwordx4 v7, s[48:51], s84 offen offset:0 lds\n"
"v_mfma_f32_16x16x32_bf16 v[216:219], v[148:151], a[212:215], v[216:219]\n"
"v_cvt_pk_bf16_f32 v236, 0, v236\n"
"v_cvt_pk_bf16_f32 v237, 0, v237\n"
"v_mfma_f32_16x16x32_bf16 v[216:219], v[152:155], a[216:219], v[216:219]\n"
"v_cvt_pk_bf16_f32 v238, 0, v238\n"
"v_cvt_pk_bf16_f32 v239, 0, v239\n"
"v_mfma_f32_16x16x32_bf16 v[216:219], v[156:159], a[220:223], v[216:219]\n"
"v_mul_f32 v236, s85, v236\n"
"v_mul_f32 v237, s85, v237\n"
"v_mfma_f32_16x16x32_bf16 v[216:219], v[160:163], a[224:227], v[216:219]\n"
"buffer_load_dwordx4 v7, s[48:51], s84 offen offset:3072 lds\n"
"v_mfma_f32_16x16x32_bf16 v[216:219], v[164:167], a[228:231], v[216:219]\n"
"s_add_u32 m0, s83, 896\n"
"s_waitcnt lgkmcnt(10)\n"
"v_mul_f32 v238, s85, v238\n"
"v_mfma_f32_16x16x32_bf16 a[12:15], v[192:195], v[124:127], a[12:15]\n"
"v_mul_f32 v239, s85, v239\n"
"v_cvt_pk_bf16_f32 v236, 0, v236\n"
"v_mfma_f32_16x16x32_bf16 a[12:15], v[196:199], v[140:143], a[12:15]\n"
"ds_read_b64_tr_b16 v[192:193], v23 offset:24576\n"
"v_cvt_pk_bf16_f32 v237, 0, v237\n"
"v_mfma_f32_16x16x32_bf16 a[16:19], v[200:203], v[112:115], a[16:19]\n"
"ds_read_b64_tr_b16 v[194:195], v23 offset:30720\n"
"v_cvt_pk_bf16_f32 v238, 0, v238\n"
"v_mfma_f32_16x16x32_bf16 a[16:19], v[204:207], v[128:131], a[16:19]\n"
"buffer_load_dwordx4 v7, s[48:51], s84 offen offset:128 lds\n"
"v_mfma_f32_16x16x32_bf16 a[20:23], v[200:203], v[116:119], a[20:23]\n"
"v_cvt_pk_bf16_f32 v239, 0, v239\n"
"v_fma_f32 v236, v236, s86, -v29\n"
"v_mfma_f32_16x16x32_bf16 a[20:23], v[204:207], v[132:135], a[20:23]\n"
"v_fma_f32 v237, v237, s86, -v29\n"
"v_fma_f32 v238, v238, s86, -v29\n"
"v_fma_f32 v239, v239, s86, -v29\n"
"v_mfma_f32_16x16x32_bf16 v[220:223], v[144:147], a[232:235], 0\n"
"v_exp_f32 v236, v236\n"
"v_exp_f32 v237, v237\n"
"v_exp_f32 v238, v238\n"
"v_exp_f32 v239, v239\n"
"v_mfma_f32_16x16x32_bf16 v[220:223], v[148:151], a[236:239], v[220:223]\n"
"v_cvt_pk_bf16_f32 v84, v236, v237\n"
"v_cvt_pk_bf16_f32 v85, v238, v239\n"
"v_mfma_f32_16x16x32_bf16 v[220:223], v[152:155], a[240:243], v[220:223]\n"
"v_mov_b32 v248, -1\n"
"v_mov_b32 v249, -1\n"
"v_mov_b32 v250, -1\n"
"v_mov_b32 v251, -1\n"
"s_add_u32 s87, s17, 20971520\n"
"s_add_u32 s87, s87, 5120\n"
"buffer_store_dwordx4 v[248:251], v1, s[36:39], s87 offen offset:0 sc1\n"
"v_mfma_f32_16x16x32_bf16 v[220:223], v[156:159], a[244:247], v[220:223]\n"
"ds_read_b64_tr_b16 v[196:197], v23 offset:36864\n"
"v_cvt_pk_bf16_f32 v216, 0, v216\n"
"v_mfma_f32_16x16x32_bf16 v[220:223], v[160:163], a[248:251], v[220:223]\n"
"ds_read_b64_tr_b16 v[198:199], v23 offset:43008\n"
"v_cvt_pk_bf16_f32 v217, 0, v217\n"
"v_mfma_f32_16x16x32_bf16 v[220:223], v[164:167], a[252:255], v[220:223]\n"
"buffer_load_dwordx4 v7, s[48:51], s84 offen offset:3200 lds\n"
"v_mfma_f32_16x16x32_bf16 a[24:27], v[200:203], v[120:123], a[24:27]\n"
"s_add_u32 m0, s83, 1792\n"
"v_cvt_pk_bf16_f32 v218, 0, v218\n"
"v_cvt_pk_bf16_f32 v219, 0, v219\n"
"v_mfma_f32_16x16x32_bf16 a[24:27], v[204:207], v[136:139], a[24:27]\n"
"v_mul_f32 v216, s85, v216\n"
"v_mul_f32 v217, s85, v217\n"
"v_mfma_f32_16x16x32_bf16 a[28:31], v[200:203], v[124:127], a[28:31]\n"
"ds_read_b128 v[144:147], v18 offset:12288\n"
"v_mul_f32 v218, s85, v218\n"
"v_mfma_f32_16x16x32_bf16 a[28:31], v[204:207], v[140:143], a[28:31]\n"
"s_waitcnt lgkmcnt(5)\n"
"v_mul_f32 v219, s85, v219\n"
"v_cvt_pk_bf16_f32 v216, 0, v216\n"
"v_mfma_f32_16x16x32_bf16 a[32:35], v[208:211], v[112:115], a[32:35]\n"
"v_cvt_pk_bf16_f32 v217, 0, v217\n"
"v_cvt_pk_bf16_f32 v218, 0, v218\n"
"v_cvt_pk_bf16_f32 v219, 0, v219\n"
"v_mfma_f32_16x16x32_bf16 a[32:35], v[212:215], v[128:131], a[32:35]\n"
"v_fma_f32 v216, v216, s86, -v30\n"
"v_fma_f32 v217, v217, s86, -v30\n"
"v_fma_f32 v218, v218, s86, -v30\n"
"v_fma_f32 v219, v219, s86, -v30\n"
"v_mfma_f32_16x16x32_bf16 v[224:227], v[168:171], v[32:35], 0\n"
"v_exp_f32 v216, v216\n"
"v_exp_f32 v217, v217\n"
"v_exp_f32 v218, v218\n"
"v_exp_f32 v219, v219\n"
"v_mfma_f32_16x16x32_bf16 v[224:227], v[172:175], v[36:39], v[224:227]\n"
"v_cvt_pk_bf16_f32 v88, v216, v217\n"
"v_cvt_pk_bf16_f32 v89, v218, v219\n"
"v_mfma_f32_16x16x32_bf16 v[224:227], v[176:179], v[40:43], v[224:227]\n"
"buffer_load_dwordx4 v7, s[48:51], s84 offen offset:256 lds\n"
"v_mfma_f32_16x16x32_bf16 v[224:227], v[180:183], v[44:47], v[224:227]\n"
"ds_read_b64_tr_b16 v[200:201], v20 offset:25600\n"
"v_cvt_pk_bf16_f32 v220, 0, v220\n"
"v_mfma_f32_16x16x32_bf16 v[224:227], v[184:187], v[48:51], v[224:227]\n"
"ds_read_b64_tr_b16 v[202:203], v20 offset:31744\n"
"v_cvt_pk_bf16_f32 v221, 0, v221\n"
"v_mfma_f32_16x16x32_bf16 v[224:227], v[188:191], v[52:55], v[224:227]\n"
"ds_read_b64_tr_b16 v[204:205], v20 offset:37888\n"
"v_cvt_pk_bf16_f32 v222, 0, v222\n"
"v_mfma_f32_16x16x32_bf16 a[36:39], v[208:211], v[116:119], a[36:39]\n"
"buffer_load_dwordx4 v7, s[48:51], s84 offen offset:3328 lds\n"
"v_mfma_f32_16x16x32_bf16 a[36:39], v[212:215], v[132:135], a[36:39]\n"
"ds_read_b64_tr_b16 v[206:207], v20 offset:44032\n"
"v_cvt_pk_bf16_f32 v223, 0, v223\n"
"v_mfma_f32_16x16x32_bf16 a[40:43], v[208:211], v[120:123], a[40:43]\n"
"ds_read_b128 v[148:151], v19 offset:12288\n"
"v_mul_f32 v220, s85, v220\n"
"v_mfma_f32_16x16x32_bf16 a[40:43], v[212:215], v[136:139], a[40:43]\n"
"s_waitcnt lgkmcnt(6)\n"
"v_mul_f32 v221, s85, v221\n"
"v_mul_f32 v222, s85, v222\n"
"v_mul_f32 v223, s85, v223\n"
"v_mfma_f32_16x16x32_bf16 a[44:47], v[208:211], v[124:127], a[44:47]\n"
"v_cvt_pk_bf16_f32 v220, 0, v220\n"
"v_cvt_pk_bf16_f32 v221, 0, v221\n"
"v_cvt_pk_bf16_f32 v222, 0, v222\n"
"v_cvt_pk_bf16_f32 v223, 0, v223\n"
"v_mfma_f32_16x16x32_bf16 a[44:47], v[212:215], v[140:143], a[44:47]\n"
"v_fma_f32 v220, v220, s86, -v31\n"
"v_fma_f32 v221, v221, s86, -v31\n"
"v_fma_f32 v222, v222, s86, -v31\n"
"v_fma_f32 v223, v223, s86, -v31\n"
"v_mfma_f32_16x16x32_bf16 a[48:51], v[192:195], v[112:115], a[48:51]\n"
"v_exp_f32 v220, v220\n"
"v_exp_f32 v221, v221\n"
"v_exp_f32 v222, v222\n"
"v_exp_f32 v223, v223\n"
"v_mfma_f32_16x16x32_bf16 a[48:51], v[196:199], v[128:131], a[48:51]\n"
"v_cvt_pk_bf16_f32 v92, v220, v221\n"
"v_cvt_pk_bf16_f32 v93, v222, v223\n"
"v_mfma_f32_16x16x32_bf16 v[228:231], v[168:171], v[56:59], 0\n"
"ds_read_b64_tr_b16 v[208:209], v21 offset:25600\n"
"v_cvt_pk_bf16_f32 v224, 0, v224\n"
"v_mfma_f32_16x16x32_bf16 v[228:231], v[172:175], v[60:63], v[228:231]\n"
"ds_read_b64_tr_b16 v[210:211], v21 offset:31744\n"
"v_cvt_pk_bf16_f32 v225, 0, v225\n"
"v_mfma_f32_16x16x32_bf16 v[228:231], v[176:179], v[64:67], v[228:231]\n"
"ds_read_b64_tr_b16 v[212:213], v21 offset:37888\n"
"v_cvt_pk_bf16_f32 v226, 0, v226\n"
"v_mfma_f32_16x16x32_bf16 v[228:231], v[180:183], v[68:71], v[228:231]\n"
"ds_read_b64_tr_b16 v[214:215], v21 offset:44032\n"
"v_cvt_pk_bf16_f32 v227, 0, v227\n"
"v_mfma_f32_16x16x32_bf16 v[228:231], v[184:187], v[72:75], v[228:231]\n"
"ds_read_b128 v[152:155], v18 offset:13312\n"
"v_mul_f32 v224, s85, v224\n"
"v_mfma_f32_16x16x32_bf16 v[228:231], v[188:191], v[76:79], v[228:231]\n"
"ds_read_b128 v[156:159], v19 offset:13312\n"
"v_mul_f32 v225, s85, v225\n"
"v_mfma_f32_16x16x32_bf16 a[52:55], v[192:195], v[116:119], a[52:55]\n"
"ds_read_b128 v[160:163], v18 offset:14336\n"
"v_mul_f32 v226, s85, v226\n"
"v_mfma_f32_16x16x32_bf16 a[52:55], v[196:199], v[132:135], a[52:55]\n"
"v_mul_f32 v227, s85, v227\n"
"v_cvt_pk_bf16_f32 v224, 0, v224\n"
"v_mfma_f32_16x16x32_bf16 a[56:59], v[192:195], v[120:123], a[56:59]\n"
"v_cvt_pk_bf16_f32 v225, 0, v225\n"
"v_cvt_pk_bf16_f32 v226, 0, v226\n"
"v_cvt_pk_bf16_f32 v227, 0, v227\n"
"v_mfma_f32_16x16x32_bf16 a[56:59], v[196:199], v[136:139], a[56:59]\n"
"v_fma_f32 v224, v224, s86, -v28\n"
"v_fma_f32 v225, v225, s86, -v28\n"
"v_fma_f32 v226, v226, s86, -v28\n"
"v_fma_f32 v227, v227, s86, -v28\n"
"v_mfma_f32_16x16x32_bf16 a[60:63], v[192:195], v[124:127], a[60:63]\n"
"v_exp_f32 v224, v224\n"
"v_exp_f32 v225, v225\n"
"v_exp_f32 v226, v226\n"
"v_exp_f32 v227, v227\n"
"v_mfma_f32_16x16x32_bf16 a[60:63], v[196:199], v[140:143], a[60:63]\n"
"v_cvt_pk_bf16_f32 v82, v224, v225\n"
"v_cvt_pk_bf16_f32 v83, v226, v227\n"
"v_mfma_f32_16x16x32_bf16 v[232:235], v[168:171], a[208:211], 0\n"
"buffer_store_dwordx4 v[248:251], v1, s[36:39], s87 offen offset:1024 sc1\n"
"v_mfma_f32_16x16x32_bf16 v[232:235], v[172:175], a[212:215], v[232:235]\n"
"ds_read_b128 v[164:167], v19 offset:14336\n"
"v_cvt_pk_bf16_f32 v228, 0, v228\n"
"v_mfma_f32_16x16x32_bf16 v[232:235], v[176:179], a[216:219], v[232:235]\n"
"ds_read_b64_tr_b16 v[192:193], v22 offset:25600\n"
"v_cvt_pk_bf16_f32 v229, 0, v229\n"
"v_mfma_f32_16x16x32_bf16 v[232:235], v[180:183], a[220:223], v[232:235]\n"
"s_waitcnt lgkmcnt(10)\n"
"v_cvt_pk_bf16_f32 v230, 0, v230\n"
"v_cvt_pk_bf16_f32 v231, 0, v231\n"
"v_mfma_f32_16x16x32_bf16 v[232:235], v[184:187], a[224:227], v[232:235]\n"
"ds_read_b64_tr_b16 v[194:195], v22 offset:31744\n"
"v_mul_f32 v228, s85, v228\n"
"v_mfma_f32_16x16x32_bf16 v[232:235], v[188:191], a[228:231], v[232:235]\n"
"ds_read_b64_tr_b16 v[196:197], v22 offset:37888\n"
"v_mul_f32 v229, s85, v229\n"
"v_mfma_f32_16x16x32_bf16 a[64:67], v[200:203], v[112:115], a[64:67]\n"
"ds_read_b64_tr_b16 v[198:199], v22 offset:44032\n"
"v_mul_f32 v230, s85, v230\n"
"v_mfma_f32_16x16x32_bf16 a[64:67], v[204:207], v[128:131], a[64:67]\n"
"v_mul_f32 v231, s85, v231\n"
"v_cvt_pk_bf16_f32 v228, 0, v228\n"
"v_mfma_f32_16x16x32_bf16 a[68:71], v[200:203], v[116:119], a[68:71]\n"
"v_cvt_pk_bf16_f32 v229, 0, v229\n"
"v_cvt_pk_bf16_f32 v230, 0, v230\n"
"v_mfma_f32_16x16x32_bf16 a[68:71], v[204:207], v[132:135], a[68:71]\n"
"v_cvt_pk_bf16_f32 v231, 0, v231\n"
"v_fma_f32 v228, v228, s86, -v29\n"
"v_mfma_f32_16x16x32_bf16 a[72:75], v[200:203], v[120:123], a[72:75]\n"
"v_fma_f32 v229, v229, s86, -v29\n"
"v_fma_f32 v230, v230, s86, -v29\n"
"v_mfma_f32_16x16x32_bf16 a[72:75], v[204:207], v[136:139], a[72:75]\n"
"v_fma_f32 v231, v231, s86, -v29\n"
"v_cvt_pk_bf16_f32 v232, 0, v232\n"
"v_mfma_f32_16x16x32_bf16 v[236:239], v[168:171], a[232:235], 0\n"
"v_exp_f32 v228, v228\n"
"v_exp_f32 v229, v229\n"
"v_exp_f32 v230, v230\n"
"v_exp_f32 v231, v231\n"
"v_mfma_f32_16x16x32_bf16 v[236:239], v[172:175], a[236:239], v[236:239]\n"
"v_cvt_pk_bf16_f32 v86, v228, v229\n"
"v_cvt_pk_bf16_f32 v87, v230, v231\n"
"v_mfma_f32_16x16x32_bf16 v[236:239], v[176:179], a[240:243], v[236:239]\n"
"ds_read_b128 v[168:171], v18 offset:18432\n"
"v_cvt_pk_bf16_f32 v233, 0, v233\n"
"v_mfma_f32_16x16x32_bf16 v[236:239], v[180:183], a[244:247], v[236:239]\n"
"ds_read_b128 v[172:175], v19 offset:18432\n"
"v_cvt_pk_bf16_f32 v234, 0, v234\n"
"v_mfma_f32_16x16x32_bf16 v[236:239], v[184:187], a[248:251], v[236:239]\n"
"ds_read_b128 v[176:179], v18 offset:19456\n"
"v_cvt_pk_bf16_f32 v235, 0, v235\n"
"v_mfma_f32_16x16x32_bf16 v[236:239], v[188:191], a[252:255], v[236:239]\n"
"s_waitcnt lgkmcnt(11)\n"
"v_mul_f32 v232, s85, v232\n"
"v_mul_f32 v233, s85, v233\n"
"v_mfma_f32_16x16x32_bf16 a[76:79], v[200:203], v[124:127], a[76:79]\n"
"ds_read_b128 v[180:183], v19 offset:19456\n"
"v_mul_f32 v234, s85, v234\n"
"v_mfma_f32_16x16x32_bf16 a[76:79], v[204:207], v[140:143], a[76:79]\n"
"ds_read_b64_tr_b16 v[200:201], v23 offset:25600\n"
"v_mul_f32 v235, s85, v235\n"
"v_mfma_f32_16x16x32_bf16 a[80:83], v[208:211], v[112:115], a[80:83]\n"
"ds_read_b64_tr_b16 v[202:203], v23 offset:31744\n"
"v_cvt_pk_bf16_f32 v232, 0, v232\n"
"v_mfma_f32_16x16x32_bf16 a[80:83], v[212:215], v[128:131], a[80:83]\n"
"s_waitcnt lgkmcnt(10)\n"
"v_cvt_pk_bf16_f32 v233, 0, v233\n"
"v_cvt_pk_bf16_f32 v234, 0, v234\n"
"v_mfma_f32_16x16x32_bf16 a[84:87], v[208:211], v[116:119], a[84:87]\n"
"v_cvt_pk_bf16_f32 v235, 0, v235\n"
"v_fma_f32 v232, v232, s86, -v30\n"
"v_mfma_f32_16x16x32_bf16 a[84:87], v[212:215], v[132:135], a[84:87]\n"
"v_fma_f32 v233, v233, s86, -v30\n"
"v_fma_f32 v234, v234, s86, -v30\n"
"v_fma_f32 v235, v235, s86, -v30\n"
"v_mfma_f32_16x16x32_bf16 v[216:219], v[144:147], v[32:35], 0\n"
"v_exp_f32 v232, v232\n"
"v_exp_f32 v233, v233\n"
"v_exp_f32 v234, v234\n"
"v_exp_f32 v235, v235\n"
"v_mfma_f32_16x16x32_bf16 v[216:219], v[148:151], v[36:39], v[216:219]\n"
"v_cvt_pk_bf16_f32 v90, v232, v233\n"
"v_cvt_pk_bf16_f32 v91, v234, v235\n"
"v_mfma_f32_16x16x32_bf16 v[216:219], v[152:155], v[40:43], v[216:219]\n"
"ds_read_b64_tr_b16 v[204:205], v23 offset:37888\n"
"v_cvt_pk_bf16_f32 v236, 0, v236\n"
"v_mfma_f32_16x16x32_bf16 v[216:219], v[156:159], v[44:47], v[216:219]\n"
"ds_read_b64_tr_b16 v[206:207], v23 offset:44032\n"
"v_cvt_pk_bf16_f32 v237, 0, v237\n"
"v_mfma_f32_16x16x32_bf16 v[216:219], v[160:163], v[48:51], v[216:219]\n"
"ds_read_b128 v[184:187], v18 offset:20480\n"
"v_cvt_pk_bf16_f32 v238, 0, v238\n"
"v_mfma_f32_16x16x32_bf16 v[216:219], v[164:167], v[52:55], v[216:219]\n"
"ds_read_b128 v[188:191], v19 offset:20480\n"
"v_cvt_pk_bf16_f32 v239, 0, v239\n"
"v_mfma_f32_16x16x32_bf16 a[88:91], v[208:211], v[120:123], a[88:91]\n"
"v_mul_f32 v236, s85, v236\n"
"v_mul_f32 v237, s85, v237\n"
"v_mfma_f32_16x16x32_bf16 a[88:91], v[212:215], v[136:139], a[88:91]\n"
"s_waitcnt lgkmcnt(10)\n"
"v_mul_f32 v238, s85, v238\n"
"v_mul_f32 v239, s85, v239\n"
"v_mfma_f32_16x16x32_bf16 a[92:95], v[208:211], v[124:127], a[92:95]\n"
"v_cvt_pk_bf16_f32 v236, 0, v236\n"
"v_cvt_pk_bf16_f32 v237, 0, v237\n"
"v_mfma_f32_16x16x32_bf16 a[92:95], v[212:215], v[140:143], a[92:95]\n"
"ds_read_b64_tr_b16 v[208:209], v20 offset:26624\n"
"v_cvt_pk_bf16_f32 v238, 0, v238\n"
"v_mfma_f32_16x16x32_bf16 a[96:99], v[192:195], v[112:115], a[96:99]\n"
"v_cvt_pk_bf16_f32 v239, 0, v239\n"
"v_fma_f32 v236, v236, s86, -v31\n"
"v_mfma_f32_16x16x32_bf16 a[96:99], v[196:199], v[128:131], a[96:99]\n"
"v_fma_f32 v237, v237, s86, -v31\n"
"v_fma_f32 v238, v238, s86, -v31\n"
"v_fma_f32 v239, v239, s86, -v31\n"
"v_mfma_f32_16x16x32_bf16 a[100:103], v[192:195], v[116:119], a[100:103]\n"
"v_exp_f32 v236, v236\n"
"v_exp_f32 v237, v237\n"
"v_exp_f32 v238, v238\n"
"v_exp_f32 v239, v239\n"
"v_mfma_f32_16x16x32_bf16 a[100:103], v[196:199], v[132:135], a[100:103]\n"
"v_cvt_pk_bf16_f32 v94, v236, v237\n"
"v_cvt_pk_bf16_f32 v95, v238, v239\n"
"v_mfma_f32_16x16x32_bf16 v[220:223], v[144:147], v[56:59], 0\n"
"ds_read_b64_tr_b16 v[210:211], v20 offset:32768\n"
"v_cvt_pk_bf16_f32 v216, 0, v216\n"
"v_mfma_f32_16x16x32_bf16 v[220:223], v[148:151], v[60:63], v[220:223]\n"
"ds_read_b64_tr_b16 v[212:213], v20 offset:38912\n"
"v_cvt_pk_bf16_f32 v217, 0, v217\n"
"v_mfma_f32_16x16x32_bf16 v[220:223], v[152:155], v[64:67], v[220:223]\n"
"ds_read_b64_tr_b16 v[214:215], v20 offset:45056\n"
"v_cvt_pk_bf16_f32 v218, 0, v218\n"
"v_mfma_f32_16x16x32_bf16 v[220:223], v[156:159], v[68:71], v[220:223]\n"
"v_cvt_pk_bf16_f32 v219, 0, v219\n"
"v_mul_f32 v216, s85, v216\n"
"v_mfma_f32_16x16x32_bf16 v[220:223], v[160:163], v[72:75], v[220:223]\n"
"buffer_store_dwordx2 v[248:249], v5, s[36:39], s87 offen offset:0 sc1\n"
"v_mfma_f32_16x16x32_bf16 v[220:223], v[164:167], v[76:79], v[220:223]\n"
"s_add_u32 s87, s87, 5120\n"
"v_mul_f32 v217, s85, v217\n"
"v_mul_f32 v218, s85, v218\n"
"v_mfma_f32_16x16x32_bf16 a[104:107], v[192:195], v[120:123], a[104:107]\n"
"v_mul_f32 v219, s85, v219\n"
"v_cvt_pk_bf16_f32 v216, 0, v216\n"
"v_mfma_f32_16x16x32_bf16 a[104:107], v[196:199], v[136:139], a[104:107]\n"
"s_waitcnt lgkmcnt(6)\n"
"v_cvt_pk_bf16_f32 v217, 0, v217\n"
"v_cvt_pk_bf16_f32 v218, 0, v218\n"
"v_mfma_f32_16x16x32_bf16 a[108:111], v[192:195], v[124:127], a[108:111]\n"
"v_cvt_pk_bf16_f32 v219, 0, v219\n"
"v_fma_f32 v216, v216, s86, -v28\n"
"v_mfma_f32_16x16x32_bf16 a[108:111], v[196:199], v[140:143], a[108:111]\n"
"v_fma_f32 v217, v217, s86, -v28\n"
"v_fma_f32 v218, v218, s86, -v28\n"
"v_fma_f32 v219, v219, s86, -v28\n"
"v_mfma_f32_16x16x32_bf16 a[112:115], v[200:203], v[112:115], a[112:115]\n"
"v_exp_f32 v216, v216\n"
"v_exp_f32 v217, v217\n"
"v_exp_f32 v218, v218\n"
"v_exp_f32 v219, v219\n"
"v_mfma_f32_16x16x32_bf16 a[112:115], v[204:207], v[128:131], a[112:115]\n"
"v_cvt_pk_bf16_f32 v96, v216, v217\n"
"v_cvt_pk_bf16_f32 v97, v218, v219\n"
"v_mfma_f32_16x16x32_bf16 v[224:227], v[144:147], a[208:211], 0\n"
"ds_read_b64_tr_b16 v[192:193], v21 offset:26624\n"
"v_cvt_pk_bf16_f32 v220, 0, v220\n"
"v_mfma_f32_16x16x32_bf16 v[224:227], v[148:151], a[212:215], v[224:227]\n"
"ds_read_b64_tr_b16 v[194:195], v21 offset:32768\n"
"v_cvt_pk_bf16_f32 v221, 0, v221\n"
"v_mfma_f32_16x16x32_bf16 v[224:227], v[152:155], a[216:219], v[224:227]\n"
"ds_read_b64_tr_b16 v[196:197], v21 offset:38912\n"
"v_cvt_pk_bf16_f32 v222, 0, v222\n"
"v_mfma_f32_16x16x32_bf16 v[224:227], v[156:159], a[220:223], v[224:227]\n"
"ds_read_b64_tr_b16 v[198:199], v21 offset:45056\n"
"v_cvt_pk_bf16_f32 v223, 0, v223\n"
"v_mfma_f32_16x16x32_bf16 v[224:227], v[160:163], a[224:227], v[224:227]\n"
"v_mul_f32 v220, s85, v220\n"
"v_mul_f32 v221, s85, v221\n"
"v_mfma_f32_16x16x32_bf16 v[224:227], v[164:167], a[228:231], v[224:227]\n"
"v_mul_f32 v222, s85, v222\n"
"v_mul_f32 v223, s85, v223\n"
"v_mfma_f32_16x16x32_bf16 a[116:119], v[200:203], v[116:119], a[116:119]\n"
"v_cvt_pk_bf16_f32 v220, 0, v220\n"
"v_cvt_pk_bf16_f32 v221, 0, v221\n"
"v_mfma_f32_16x16x32_bf16 a[116:119], v[204:207], v[132:135], a[116:119]\n"
"v_cvt_pk_bf16_f32 v222, 0, v222\n"
"v_cvt_pk_bf16_f32 v223, 0, v223\n"
"v_mfma_f32_16x16x32_bf16 a[120:123], v[200:203], v[120:123], a[120:123]\n"
"v_fma_f32 v220, v220, s86, -v29\n"
"v_fma_f32 v221, v221, s86, -v29\n"
"v_mfma_f32_16x16x32_bf16 a[120:123], v[204:207], v[136:139], a[120:123]\n"
"v_fma_f32 v222, v222, s86, -v29\n"
"v_fma_f32 v223, v223, s86, -v29\n"
"v_mfma_f32_16x16x32_bf16 a[124:127], v[200:203], v[124:127], a[124:127]\n"
"v_exp_f32 v220, v220\n"
"v_mfma_f32_16x16x32_bf16 a[124:127], v[204:207], v[140:143], a[124:127]\n"
"v_exp_f32 v221, v221\n"
"v_mfma_f32_16x16x32_bf16 v[228:231], v[144:147], a[232:235], 0\n"
"v_exp_f32 v222, v222\n"
"v_exp_f32 v223, v223\n"
"v_mfma_f32_16x16x32_bf16 v[228:231], v[148:151], a[236:239], v[228:231]\n"
"v_cvt_pk_bf16_f32 v100, v220, v221\n"
"v_cvt_pk_bf16_f32 v101, v222, v223\n"
"v_mfma_f32_16x16x32_bf16 v[228:231], v[152:155], a[240:243], v[228:231]\n"
"ds_read_b64_tr_b16 v[200:201], v22 offset:26624\n"
"v_cvt_pk_bf16_f32 v224, 0, v224\n"
"v_mfma_f32_16x16x32_bf16 v[228:231], v[156:159], a[244:247], v[228:231]\n"
"s_waitcnt lgkmcnt(5)\n"
"v_cvt_pk_bf16_f32 v225, 0, v225\n"
"v_cvt_pk_bf16_f32 v226, 0, v226\n"
"v_mfma_f32_16x16x32_bf16 v[228:231], v[160:163], a[248:251], v[228:231]\n"
"ds_read_b64_tr_b16 v[202:203], v22 offset:32768\n"
"v_cvt_pk_bf16_f32 v227, 0, v227\n"
"v_mfma_f32_16x16x32_bf16 v[228:231], v[164:167], a[252:255], v[228:231]\n"
"ds_read_b64_tr_b16 v[204:205], v22 offset:38912\n"
"v_mul_f32 v224, s85, v224\n"
"v_mfma_f32_16x16x32_bf16 a[128:131], v[208:211], v[112:115], a[128:131]\n"
"ds_read_b64_tr_b16 v[206:207], v22 offset:45056\n"
"v_mul_f32 v225, s85, v225\n"
"v_mfma_f32_16x16x32_bf16 a[128:131], v[212:215], v[128:131], a[128:131]\n"
"v_mul_f32 v226, s85, v226\n"
"v_mul_f32 v227, s85, v227\n"
"v_mfma_f32_16x16x32_bf16 a[132:135], v[208:211], v[116:119], a[132:135]\n"
"v_cvt_pk_bf16_f32 v224, 0, v224\n"
"v_cvt_pk_bf16_f32 v225, 0, v225\n"
"v_mfma_f32_16x16x32_bf16 a[132:135], v[212:215], v[132:135], a[132:135]\n"
"v_cvt_pk_bf16_f32 v226, 0, v226\n"
"v_cvt_pk_bf16_f32 v227, 0, v227\n"
"v_mfma_f32_16x16x32_bf16 a[136:139], v[208:211], v[120:123], a[136:139]\n"
"v_fma_f32 v224, v224, s86, -v30\n"
"v_fma_f32 v225, v225, s86, -v30\n"
"v_mfma_f32_16x16x32_bf16 a[136:139], v[212:215], v[136:139], a[136:139]\n"
"v_fma_f32 v226, v226, s86, -v30\n"
"v_fma_f32 v227, v227, s86, -v30\n"
"v_mfma_f32_16x16x32_bf16 v[232:235], v[168:171], v[32:35], 0\n"
"v_exp_f32 v224, v224\n"
"v_exp_f32 v225, v225\n"
"v_exp_f32 v226, v226\n"
"v_exp_f32 v227, v227\n"
"v_mfma_f32_16x16x32_bf16 v[232:235], v[172:175], v[36:39], v[232:235]\n"
"v_cvt_pk_bf16_f32 v104, v224, v225\n"
"v_cvt_pk_bf16_f32 v105, v226, v227\n"
"v_mfma_f32_16x16x32_bf16 v[232:235], v[176:179], v[40:43], v[232:235]\n"
"v_cvt_pk_bf16_f32 v228, 0, v228\n"
"v_cvt_pk_bf16_f32 v229, 0, v229\n"
"v_mfma_f32_16x16x32_bf16 v[232:235], v[180:183], v[44:47], v[232:235]\n"
"v_cvt_pk_bf16_f32 v230, 0, v230\n"
"v_cvt_pk_bf16_f32 v231, 0, v231\n"
"v_mfma_f32_16x16x32_bf16 v[232:235], v[184:187], v[48:51], v[232:235]\n"
"v_mul_f32 v228, s85, v228\n"
"v_mul_f32 v229, s85, v229\n"
"v_mfma_f32_16x16x32_bf16 v[232:235], v[188:191], v[52:55], v[232:235]\n"
"s_waitcnt lgkmcnt(4)\n"
"v_mul_f32 v230, s85, v230\n"
"v_mul_f32 v231, s85, v231\n"
"v_mfma_f32_16x16x32_bf16 a[140:143], v[208:211], v[124:127], a[140:143]\n"
"v_cvt_pk_bf16_f32 v228, 0, v228\n"
"v_cvt_pk_bf16_f32 v229, 0, v229\n"
"v_mfma_f32_16x16x32_bf16 a[140:143], v[212:215], v[140:143], a[140:143]\n"
"ds_read_b64_tr_b16 v[208:209], v23 offset:26624\n"
"v_cvt_pk_bf16_f32 v230, 0, v230\n"
"v_mfma_f32_16x16x32_bf16 a[144:147], v[192:195], v[112:115], a[144:147]\n"
"ds_read_b64_tr_b16 v[210:211], v23 offset:32768\n"
"v_cvt_pk_bf16_f32 v231, 0, v231\n"
"v_mfma_f32_16x16x32_bf16 a[144:147], v[196:199], v[128:131], a[144:147]\n"
"buffer_store_dwordx4 v[248:251], v1, s[36:39], s87 offen offset:0 sc1\n"
"v_mfma_f32_16x16x32_bf16 a[148:151], v[192:195], v[116:119], a[148:151]\n"
"s_waitcnt vmcnt(17)\n"
"s_barrier\n"
"s_add_u32 s84, s83, 172032\n"
"v_mfma_f32_16x16x32_bf16 a[148:151], v[196:199], v[132:135], a[148:151]\n"
"s_add_u32 m0, s83, 122880\n"
"v_fma_f32 v228, v228, s86, -v31\n"
"v_fma_f32 v229, v229, s86, -v31\n"
"v_fma_f32 v230, v230, s86, -v31\n"
"v_fma_f32 v231, v231, s86, -v31\n"
"v_mfma_f32_16x16x32_bf16 a[152:155], v[192:195], v[120:123], a[152:155]\n"
"v_exp_f32 v228, v228\n"
"v_exp_f32 v229, v229\n"
"v_exp_f32 v230, v230\n"
"v_exp_f32 v231, v231\n"
"v_mfma_f32_16x16x32_bf16 a[152:155], v[196:199], v[136:139], a[152:155]\n"
"v_cvt_pk_bf16_f32 v108, v228, v229\n"
"v_cvt_pk_bf16_f32 v109, v230, v231\n"
"v_mfma_f32_16x16x32_bf16 v[236:239], v[168:171], v[56:59], 0\n"
"buffer_load_dwordx4 v8, s[44:47], s84 offen offset:0 lds\n"
"v_mfma_f32_16x16x32_bf16 v[236:239], v[172:175], v[60:63], v[236:239]\n"
"ds_read_b64_tr_b16 v[212:213], v23 offset:38912\n"
"v_cvt_pk_bf16_f32 v232, 0, v232\n"
"v_mfma_f32_16x16x32_bf16 v[236:239], v[176:179], v[64:67], v[236:239]\n"
"ds_read_b64_tr_b16 v[214:215], v23 offset:45056\n"
"v_cvt_pk_bf16_f32 v233, 0, v233\n"
"v_mfma_f32_16x16x32_bf16 v[236:239], v[180:183], v[68:71], v[236:239]\n"
"ds_read_b128 v[144:147], v16 offset:0\n"
"v_cvt_pk_bf16_f32 v234, 0, v234\n"
"v_mfma_f32_16x16x32_bf16 v[236:239], v[184:187], v[72:75], v[236:239]\n"
"buffer_load_dwordx4 v9, s[44:47], s84 offen offset:3072 lds\n"
"v_mfma_f32_16x16x32_bf16 v[236:239], v[188:191], v[76:79], v[236:239]\n"
"s_add_u32 m0, s83, 123776\n"
"v_cvt_pk_bf16_f32 v235, 0, v235\n"
"v_mul_f32 v232, s85, v232\n"
"v_mfma_f32_16x16x32_bf16 a[156:159], v[192:195], v[124:127], a[156:159]\n"
"s_waitcnt lgkmcnt(5)\n"
"v_mul_f32 v233, s85, v233\n"
"v_mul_f32 v234, s85, v234\n"
"v_mfma_f32_16x16x32_bf16 a[156:159], v[196:199], v[140:143], a[156:159]\n"
"v_mul_f32 v235, s85, v235\n"
"v_cvt_pk_bf16_f32 v232, 0, v232\n"
"v_mfma_f32_16x16x32_bf16 a[160:163], v[200:203], v[112:115], a[160:163]\n"
"v_cvt_pk_bf16_f32 v233, 0, v233\n"
"v_cvt_pk_bf16_f32 v234, 0, v234\n"
"v_cvt_pk_bf16_f32 v235, 0, v235\n"
"v_mfma_f32_16x16x32_bf16 a[160:163], v[204:207], v[128:131], a[160:163]\n"
"v_fma_f32 v232, v232, s86, -v28\n"
"v_fma_f32 v233, v233, s86, -v28\n"
"v_fma_f32 v234, v234, s86, -v28\n"
"v_fma_f32 v235, v235, s86, -v28\n"
"v_mfma_f32_16x16x32_bf16 a[164:167], v[200:203], v[116:119], a[164:167]\n"
"v_exp_f32 v232, v232\n"
"v_exp_f32 v233, v233\n"
"v_exp_f32 v234, v234\n"
"v_exp_f32 v235, v235\n"
"v_mfma_f32_16x16x32_bf16 a[164:167], v[204:207], v[132:135], a[164:167]\n"
"v_cvt_pk_bf16_f32 v98, v232, v233\n"
"v_cvt_pk_bf16_f32 v99, v234, v235\n"
"v_mfma_f32_16x16x32_bf16 v[216:219], v[168:171], a[208:211], 0\n"
"buffer_load_dwordx4 v8, s[44:47], s84 offen offset:128 lds\n"
"v_mfma_f32_16x16x32_bf16 v[216:219], v[172:175], a[212:215], v[216:219]\n"
"ds_read_b128 v[148:151], v17 offset:0\n"
"v_cvt_pk_bf16_f32 v236, 0, v236\n"
"v_mfma_f32_16x16x32_bf16 v[216:219], v[176:179], a[216:219], v[216:219]\n"
"ds_read_b128 v[152:155], v16 offset:1024\n"
"v_cvt_pk_bf16_f32 v237, 0, v237\n"
"v_mfma_f32_16x16x32_bf16 v[216:219], v[180:183], a[220:223], v[216:219]\n"
"ds_read_b128 v[156:159], v17 offset:1024\n"
"v_cvt_pk_bf16_f32 v238, 0, v238\n"
"v_mfma_f32_16x16x32_bf16 v[216:219], v[184:187], a[224:227], v[216:219]\n"
"buffer_load_dwordx4 v9, s[44:47], s84 offen offset:3200 lds\n"
"v_mfma_f32_16x16x32_bf16 v[216:219], v[188:191], a[228:231], v[216:219]\n"
"s_add_u32 m0, s83, 124672\n"
"v_cvt_pk_bf16_f32 v239, 0, v239\n"
"v_mul_f32 v236, s85, v236\n"
"v_mfma_f32_16x16x32_bf16 a[168:171], v[200:203], v[120:123], a[168:171]\n"
"ds_read_b128 v[160:163], v16 offset:2048\n"
"v_mul_f32 v237, s85, v237\n"
"v_mfma_f32_16x16x32_bf16 a[168:171], v[204:207], v[136:139], a[168:171]\n"
"s_waitcnt lgkmcnt(5)\n"
"v_mul_f32 v238, s85, v238\n"
"v_mul_f32 v239, s85, v239\n"
"v_mfma_f32_16x16x32_bf16 a[172:175], v[200:203], v[124:127], a[172:175]\n"
"ds_read_b128 v[164:167], v17 offset:2048\n"
"v_cvt_pk_bf16_f32 v236, 0, v236\n"
"v_mfma_f32_16x16x32_bf16 a[172:175], v[204:207], v[140:143], a[172:175]\n"
"buffer_load_dwordx4 v8, s[44:47], s84 offen offset:256 lds\n"
"v_mfma_f32_16x16x32_bf16 a[176:179], v[208:211], v[112:115], a[176:179]\n"
"v_cvt_pk_bf16_f32 v237, 0, v237\n"
"v_cvt_pk_bf16_f32 v238, 0, v238\n"
"v_cvt_pk_bf16_f32 v239, 0, v239\n"
"v_mfma_f32_16x16x32_bf16 a[176:179], v[212:215], v[128:131], a[176:179]\n"
"v_fma_f32 v236, v236, s86, -v29\n"
"v_fma_f32 v237, v237, s86, -v29\n"
"v_fma_f32 v238, v238, s86, -v29\n"
"v_fma_f32 v239, v239, s86, -v29\n"
"v_mfma_f32_16x16x32_bf16 v[220:223], v[168:171], a[232:235], 0\n"
"v_exp_f32 v236, v236\n"
"v_exp_f32 v237, v237\n"
"v_exp_f32 v238, v238\n"
"v_exp_f32 v239, v239\n"
"v_mfma_f32_16x16x32_bf16 v[220:223], v[172:175], a[236:239], v[220:223]\n"
"v_cvt_pk_bf16_f32 v102, v236, v237\n"
"v_cvt_pk_bf16_f32 v103, v238, v239\n"
"v_mfma_f32_16x16x32_bf16 v[220:223], v[176:179], a[240:243], v[220:223]\n"
"buffer_load_dwordx4 v9, s[44:47], s84 offen offset:3328 lds\n"
"v_mfma_f32_16x16x32_bf16 v[220:223], v[180:183], a[244:247], v[220:223]\n"
"ds_read_b64_tr_b16 v[192:193], v24 offset:0\n"
"v_cvt_pk_bf16_f32 v216, 0, v216\n"
"v_mfma_f32_16x16x32_bf16 v[220:223], v[184:187], a[248:251], v[220:223]\n"
"ds_read_b64_tr_b16 v[194:195], v24 offset:6144\n"
"v_cvt_pk_bf16_f32 v217, 0, v217\n"
"v_mfma_f32_16x16x32_bf16 v[220:223], v[188:191], a[252:255], v[220:223]\n"
"ds_read_b64_tr_b16 v[196:197], v24 offset:12288\n"
"v_cvt_pk_bf16_f32 v218, 0, v218\n"
"v_mfma_f32_16x16x32_bf16 a[180:183], v[208:211], v[116:119], a[180:183]\n"
"ds_read_b64_tr_b16 v[198:199], v24 offset:18432\n"
"v_cvt_pk_bf16_f32 v219, 0, v219\n"
"v_mfma_f32_16x16x32_bf16 a[180:183], v[212:215], v[132:135], a[180:183]\n"
"ds_read_b64_tr_b16 v[200:201], v25 offset:0\n"
"v_mul_f32 v216, s85, v216\n"
"v_mfma_f32_16x16x32_bf16 a[184:187], v[208:211], v[120:123], a[184:187]\n"
"ds_read_b64_tr_b16 v[202:203], v25 offset:6144\n"
"v_mul_f32 v217, s85, v217\n"
"v_mfma_f32_16x16x32_bf16 a[184:187], v[212:215], v[136:139], a[184:187]\n"
"s_waitcnt lgkmcnt(6)\n"
"v_mul_f32 v218, s85, v218\n"
"v_mul_f32 v219, s85, v219\n"
"v_mfma_f32_16x16x32_bf16 a[188:191], v[208:211], v[124:127], a[188:191]\n"
"v_cvt_pk_bf16_f32 v216, 0, v216\n"
"v_cvt_pk_bf16_f32 v217, 0, v217\n"
"v_cvt_pk_bf16_f32 v218, 0, v218\n"
"v_cvt_pk_bf16_f32 v219, 0, v219\n"
"v_mfma_f32_16x16x32_bf16 a[188:191], v[212:215], v[140:143], a[188:191]\n"
"v_fma_f32 v216, v216, s86, -v30\n"
"v_fma_f32 v217, v217, s86, -v30\n"
"v_fma_f32 v218, v218, s86, -v30\n"
"v_fma_f32 v219, v219, s86, -v30\n"
"v_mfma_f32_16x16x32_bf16 v[224:227], v[144:147], v[32:35], 0\n"
"buffer_store_dwordx4 v[248:251], v1, s[36:39], s87 offen offset:1024 sc1\n"
"v_exp_f32 v216, v216\n"
"v_exp_f32 v217, v217\n"
"v_exp_f32 v218, v218\n"
"v_exp_f32 v219, v219\n"
"v_mfma_f32_16x16x32_bf16 v[224:227], v[148:151], v[36:39], v[224:227]\n"
"v_cvt_pk_bf16_f32 v106, v216, v217\n"
"v_cvt_pk_bf16_f32 v107, v218, v219\n"
"v_mfma_f32_16x16x32_bf16 v[224:227], v[152:155], v[40:43], v[224:227]\n"
"ds_read_b64_tr_b16 v[204:205], v25 offset:12288\n"
"v_cvt_pk_bf16_f32 v220, 0, v220\n"
"v_mfma_f32_16x16x32_bf16 v[224:227], v[156:159], v[44:47], v[224:227]\n"
"ds_read_b64_tr_b16 v[206:207], v25 offset:18432\n"
"v_cvt_pk_bf16_f32 v221, 0, v221\n"
"v_mfma_f32_16x16x32_bf16 v[224:227], v[160:163], v[48:51], v[224:227]\n"
"ds_read_b64_tr_b16 v[208:209], v26 offset:0\n"
"v_cvt_pk_bf16_f32 v222, 0, v222\n"
"v_mfma_f32_16x16x32_bf16 v[224:227], v[164:167], v[52:55], v[224:227]\n"
"ds_read_b64_tr_b16 v[210:211], v26 offset:6144\n"
"v_cvt_pk_bf16_f32 v223, 0, v223\n"
"v_mfma_f32_16x16x32_bf16 a[192:195], v[240:243], v[112:115], a[192:195]\n"
"ds_read_b64_tr_b16 v[212:213], v26 offset:12288\n"
"v_mul_f32 v220, s85, v220\n"
"v_mfma_f32_16x16x32_bf16 a[192:195], v[240:243], v[128:131], a[192:195]\n"
"ds_read_b64_tr_b16 v[214:215], v26 offset:18432\n"
"v_mul_f32 v221, s85, v221\n"
"v_mfma_f32_16x16x32_bf16 a[196:199], v[240:243], v[116:119], a[196:199]\n"
"ds_read_b128 v[168:171], v16 offset:6144\n"
"v_mul_f32 v222, s85, v222\n"
"v_mfma_f32_16x16x32_bf16 a[196:199], v[240:243], v[132:135], a[196:199]\n"
"v_mul_f32 v223, s85, v223\n"
"v_cvt_pk_bf16_f32 v220, 0, v220\n"
"v_mfma_f32_16x16x32_bf16 a[200:203], v[240:243], v[120:123], a[200:203]\n"
"v_cvt_pk_bf16_f32 v221, 0, v221\n"
"v_cvt_pk_bf16_f32 v222, 0, v222\n"
"v_cvt_pk_bf16_f32 v223, 0, v223\n"
"v_mfma_f32_16x16x32_bf16 a[200:203], v[240:243], v[136:139], a[200:203]\n"
"v_fma_f32 v220, v220, s86, -v31\n"
"v_fma_f32 v221, v221, s86, -v31\n"
"v_fma_f32 v222, v222, s86, -v31\n"
"v_fma_f32 v223, v223, s86, -v31\n"
"v_mfma_f32_16x16x32_bf16 a[204:207], v[240:243], v[124:127], a[204:207]\n"
"v_exp_f32 v220, v220\n"
"v_exp_f32 v221, v221\n"
"v_exp_f32 v222, v222\n"
"v_exp_f32 v223, v223\n"
"v_mfma_f32_16x16x32_bf16 a[204:207], v[240:243], v[140:143], a[204:207]\n"
"v_cvt_pk_bf16_f32 v110, v220, v221\n"
"v_cvt_pk_bf16_f32 v111, v222, v223\n"
"v_mfma_f32_16x16x32_bf16 v[228:231], v[144:147], v[56:59], 0\n"
"ds_read_b128 v[172:175], v17 offset:6144\n"
"v_cvt_pk_bf16_f32 v224, 0, v224\n"
"v_mfma_f32_16x16x32_bf16 v[228:231], v[148:151], v[60:63], v[228:231]\n"
"ds_read_b128 v[176:179], v16 offset:7168\n"
"v_cvt_pk_bf16_f32 v225, 0, v225\n"
"v_mfma_f32_16x16x32_bf16 v[228:231], v[152:155], v[64:67], v[228:231]\n"
"ds_read_b128 v[180:183], v17 offset:7168\n"
"v_cvt_pk_bf16_f32 v226, 0, v226\n"
"v_mfma_f32_16x16x32_bf16 v[228:231], v[156:159], v[68:71], v[228:231]\n"
"s_waitcnt lgkmcnt(12)\n"
"v_cvt_pk_bf16_f32 v227, 0, v227\n"
"v_and_b32 v255, 7, v0\n"
"v_mfma_f32_16x16x32_bf16 v[228:231], v[160:163], v[72:75], v[228:231]\n"
"ds_read_b128 v[184:187], v16 offset:8192\n"
"v_lshlrev_b32 v255, 2, v255\n"
"v_mfma_f32_16x16x32_bf16 v[228:231], v[164:167], v[76:79], v[228:231]\n"
"s_lshl_b32 s90, s78, 10\n"
"s_add_u32 s90, s90, s15\n"
"s_sub_u32 s90, s90, 256\n"
"v_mfma_f32_16x16x32_bf16 a[0:3], v[192:195], v[80:83], a[0:3]\n"
"buffer_load_dword v252, v255, s[36:39], s90 offen offset:0 sc1\n"
"v_mfma_f32_16x16x32_bf16 a[0:3], v[196:199], v[96:99], a[0:3]\n"
"buffer_load_dword v253, off, s[36:39], s14 offset:3072 sc1\n"
"v_mul_f32 v224, s85, v224\n"
"v_mul_f32 v225, s85, v225\n"
"v_mul_f32 v226, s85, v226\n"
"v_mul_f32 v227, s85, v227\n"
"v_mfma_f32_16x16x32_bf16 a[4:7], v[192:195], v[84:87], a[4:7]\n"
"s_barrier\n"
"v_cvt_pk_bf16_f32 v224, 0, v224\n"
"v_cvt_pk_bf16_f32 v225, 0, v225\n"
"v_cvt_pk_bf16_f32 v226, 0, v226\n"
"v_cvt_pk_bf16_f32 v227, 0, v227\n"
"v_mfma_f32_16x16x32_bf16 a[4:7], v[196:199], v[100:103], a[4:7]\n"
"s_add_u32 s84, s83, 147456\n"
"v_fma_f32 v224, v224, s86, -v28\n"
"v_fma_f32 v225, v225, s86, -v28\n"
"v_fma_f32 v226, v226, s86, -v28\n"
"v_fma_f32 v227, v227, s86, -v28\n"
"v_mfma_f32_16x16x32_bf16 a[8:11], v[192:195], v[88:91], a[8:11]\n"
"s_add_u32 m0, s83, 24576\n"
"v_exp_f32 v224, v224\n"
"v_exp_f32 v225, v225\n"
"v_exp_f32 v226, v226\n"
"v_exp_f32 v227, v227\n"
"v_mfma_f32_16x16x32_bf16 a[8:11], v[196:199], v[104:107], a[8:11]\n"
"v_cvt_pk_bf16_f32 v112, v224, v225\n"
"v_cvt_pk_bf16_f32 v113, v226, v227\n"
"v_mfma_f32_16x16x32_bf16 v[232:235], v[144:147], a[208:211], 0\n"
"buffer_load_dwordx4 v7, s[48:51], s84 offen offset:0 lds\n"
"v_mfma_f32_16x16x32_bf16 v[232:235], v[148:151], a[212:215], v[232:235]\n"
"ds_read_b128 v[188:191], v17 offset:8192\n"
"v_cvt_pk_bf16_f32 v228, 0, v228\n"
"v_mfma_f32_16x16x32_bf16 v[232:235], v[152:155], a[216:219], v[232:235]\n"
"v_cvt_pk_bf16_f32 v229, 0, v229\n"
"v_cvt_pk_bf16_f32 v230, 0, v230\n"
"v_mfma_f32_16x16x32_bf16 v[232:235], v[156:159], a[220:223], v[232:235]\n"
"v_cvt_pk_bf16_f32 v231, 0, v231\n"
"v_mul_f32 v228, s85, v228\n"
"v_mfma_f32_16x16x32_bf16 v[232:235], v[160:163], a[224:227], v[232:235]\n"
"buffer_load_dwordx4 v7, s[48:51], s84 offen offset:3072 lds\n"
"v_mfma_f32_16x16x32_bf16 v[232:235], v[164:167], a[228:231], v[232:235]\n"
"s_add_u32 m0, s83, 25472\n"
"s_waitcnt lgkmcnt(10)\n"
"v_mul_f32 v229, s85, v229\n"
"v_mfma_f32_16x16x32_bf16 a[12:15], v[192:195], v[92:95], a[12:15]\n"
"v_mul_f32 v230, s85, v230\n"
"v_mul_f32 v231, s85, v231\n"
"v_mfma_f32_16x16x32_bf16 a[12:15], v[196:199], v[108:111], a[12:15]\n"
"ds_read_b64_tr_b16 v[192:193], v27 offset:0\n"
"v_cvt_pk_bf16_f32 v228, 0, v228\n"
"v_mfma_f32_16x16x32_bf16 a[16:19], v[200:203], v[80:83], a[16:19]\n"
"ds_read_b64_tr_b16 v[194:195], v27 offset:6144\n"
"v_cvt_pk_bf16_f32 v229, 0, v229\n"
"v_mfma_f32_16x16x32_bf16 a[16:19], v[204:207], v[96:99], a[16:19]\n"
"buffer_load_dwordx4 v7, s[48:51], s84 offen offset:128 lds\n"
"v_mfma_f32_16x16x32_bf16 a[20:23], v[200:203], v[84:87], a[20:23]\n"
"v_cvt_pk_bf16_f32 v230, 0, v230\n"
"v_cvt_pk_bf16_f32 v231, 0, v231\n"
"v_mfma_f32_16x16x32_bf16 a[20:23], v[204:207], v[100:103], a[20:23]\n"
"v_fma_f32 v228, v228, s86, -v29\n"
"v_fma_f32 v229, v229, s86, -v29\n"
"v_fma_f32 v230, v230, s86, -v29\n"
"v_fma_f32 v231, v231, s86, -v29\n"
"v_mfma_f32_16x16x32_bf16 v[236:239], v[144:147], a[232:235], 0\n"
"v_exp_f32 v228, v228\n"
"v_exp_f32 v229, v229\n"
"v_exp_f32 v230, v230\n"
"v_exp_f32 v231, v231\n"
"v_mfma_f32_16x16x32_bf16 v[236:239], v[148:151], a[236:239], v[236:239]\n"
"v_cvt_pk_bf16_f32 v116, v228, v229\n"
"v_cvt_pk_bf16_f32 v117, v230, v231\n"
"v_mfma_f32_16x16x32_bf16 v[236:239], v[152:155], a[240:243], v[236:239]\n"
"buffer_store_dwordx2 v[248:249], v5, s[36:39], s87 offen offset:0 sc1\n"
"v_mfma_f32_16x16x32_bf16 v[236:239], v[156:159], a[244:247], v[236:239]\n"
"ds_read_b64_tr_b16 v[196:197], v27 offset:12288\n"
"v_cvt_pk_bf16_f32 v232, 0, v232\n"
"v_mfma_f32_16x16x32_bf16 v[236:239], v[160:163], a[248:251], v[236:239]\n"
"ds_read_b64_tr_b16 v[198:199], v27 offset:18432\n"
"v_cvt_pk_bf16_f32 v233, 0, v233\n"
"v_mfma_f32_16x16x32_bf16 v[236:239], v[164:167], a[252:255], v[236:239]\n"
"buffer_load_dwordx4 v7, s[48:51], s84 offen offset:3200 lds\n"
"v_mfma_f32_16x16x32_bf16 a[24:27], v[200:203], v[88:91], a[24:27]\n"
"s_add_u32 m0, s83, 26368\n"
"v_cvt_pk_bf16_f32 v234, 0, v234\n"
"v_cvt_pk_bf16_f32 v235, 0, v235\n"
"v_mfma_f32_16x16x32_bf16 a[24:27], v[204:207], v[104:107], a[24:27]\n"
"v_mul_f32 v232, s85, v232\n"
"v_mul_f32 v233, s85, v233\n"
"v_mfma_f32_16x16x32_bf16 a[28:31], v[200:203], v[92:95], a[28:31]\n"
"s_add_u32 s87, s87, 5120\n"
"v_mul_f32 v234, s85, v234\n"
"v_mul_f32 v235, s85, v235\n"
"v_mfma_f32_16x16x32_bf16 a[28:31], v[204:207], v[108:111], a[28:31]\n"
"s_waitcnt lgkmcnt(4)\n"
"v_cvt_pk_bf16_f32 v232, 0, v232\n"
"v_cvt_pk_bf16_f32 v233, 0, v233\n"
"v_mfma_f32_16x16x32_bf16 a[32:35], v[208:211], v[80:83], a[32:35]\n"
"v_cvt_pk_bf16_f32 v234, 0, v234\n"
"v_cvt_pk_bf16_f32 v235, 0, v235\n"
"v_mfma_f32_16x16x32_bf16 a[32:35], v[212:215], v[96:99], a[32:35]\n"
"v_fma_f32 v232, v232, s86, -v30\n"
"v_fma_f32 v233, v233, s86, -v30\n"
"v_fma_f32 v234, v234, s86, -v30\n"
"v_fma_f32 v235, v235, s86, -v30\n"
"v_mfma_f32_16x16x32_bf16 v[216:219], v[168:171], v[32:35], 0\n"
"v_exp_f32 v232, v232\n"
"v_exp_f32 v233, v233\n"
"v_exp_f32 v234, v234\n"
"v_exp_f32 v235, v235\n"
"v_mfma_f32_16x16x32_bf16 v[216:219], v[172:175], v[36:39], v[216:219]\n"
"v_cvt_pk_bf16_f32 v120, v232, v233\n"
"v_cvt_pk_bf16_f32 v121, v234, v235\n"
"v_mfma_f32_16x16x32_bf16 v[216:219], v[176:179], v[40:43], v[216:219]\n"
"buffer_load_dwordx4 v7, s[48:51], s84 offen offset:256 lds\n"
"v_mfma_f32_16x16x32_bf16 v[216:219], v[180:183], v[44:47], v[216:219]\n"
"ds_read_b64_tr_b16 v[200:201], v24 offset:1024\n"
"v_cvt_pk_bf16_f32 v236, 0, v236\n"
"v_mfma_f32_16x16x32_bf16 v[216:219], v[184:187], v[48:51], v[216:219]\n"
"ds_read_b64_tr_b16 v[202:203], v24 offset:7168\n"
"v_cvt_pk_bf16_f32 v237, 0, v237\n"
"v_mfma_f32_16x16x32_bf16 v[216:219], v[188:191], v[52:55], v[216:219]\n"
"ds_read_b64_tr_b16 v[204:205], v24 offset:13312\n"
"v_cvt_pk_bf16_f32 v238, 0, v238\n"
"v_mfma_f32_16x16x32_bf16 a[36:39], v[208:211], v[84:87], a[36:39]\n"
"buffer_load_dwordx4 v7, s[48:51], s84 offen offset:3328 lds\n"
"v_mfma_f32_16x16x32_bf16 a[36:39], v[212:215], v[100:103], a[36:39]\n"
"ds_read_b64_tr_b16 v[206:207], v24 offset:19456\n"
"v_cvt_pk_bf16_f32 v239, 0, v239\n"
"v_mfma_f32_16x16x32_bf16 a[40:43], v[208:211], v[88:91], a[40:43]\n"
"ds_read_b128 v[144:147], v16 offset:12288\n"
"v_mul_f32 v236, s85, v236\n"
"v_mfma_f32_16x16x32_bf16 a[40:43], v[212:215], v[104:107], a[40:43]\n"
"s_waitcnt lgkmcnt(5)\n"
"v_mul_f32 v237, s85, v237\n"
"v_mul_f32 v238, s85, v238\n"
"v_mul_f32 v239, s85, v239\n"
"v_mfma_f32_16x16x32_bf16 a[44:47], v[208:211], v[92:95], a[44:47]\n"
"v_cvt_pk_bf16_f32 v236, 0, v236\n"
"v_cvt_pk_bf16_f32 v237, 0, v237\n"
"v_cvt_pk_bf16_f32 v238, 0, v238\n"
"v_cvt_pk_bf16_f32 v239, 0, v239\n"
"v_mfma_f32_16x16x32_bf16 a[44:47], v[212:215], v[108:111], a[44:47]\n"
"v_fma_f32 v236, v236, s86, -v31\n"
"v_fma_f32 v237, v237, s86, -v31\n"
"v_fma_f32 v238, v238, s86, -v31\n"
"v_fma_f32 v239, v239, s86, -v31\n"
"v_mfma_f32_16x16x32_bf16 a[48:51], v[192:195], v[80:83], a[48:51]\n"
"v_exp_f32 v236, v236\n"
"v_exp_f32 v237, v237\n"
"v_exp_f32 v238, v238\n"
"v_exp_f32 v239, v239\n"
"v_mfma_f32_16x16x32_bf16 a[48:51], v[196:199], v[96:99], a[48:51]\n"
"v_cvt_pk_bf16_f32 v124, v236, v237\n"
"v_cvt_pk_bf16_f32 v125, v238, v239\n"
"v_mfma_f32_16x16x32_bf16 v[220:223], v[168:171], v[56:59], 0\n"
"ds_read_b64_tr_b16 v[208:209], v25 offset:1024\n"
"v_cvt_pk_bf16_f32 v216, 0, v216\n"
"v_mfma_f32_16x16x32_bf16 v[220:223], v[172:175], v[60:63], v[220:223]\n"
"ds_read_b64_tr_b16 v[210:211], v25 offset:7168\n"
"v_cvt_pk_bf16_f32 v217, 0, v217\n"
"v_mfma_f32_16x16x32_bf16 v[220:223], v[176:179], v[64:67], v[220:223]\n"
"ds_read_b64_tr_b16 v[212:213], v25 offset:13312\n"
"v_cvt_pk_bf16_f32 v218, 0, v218\n"
"v_mfma_f32_16x16x32_bf16 v[220:223], v[180:183], v[68:71], v[220:223]\n"
"ds_read_b64_tr_b16 v[214:215], v25 offset:19456\n"
"v_cvt_pk_bf16_f32 v219, 0, v219\n"
"v_mfma_f32_16x16x32_bf16 v[220:223], v[184:187], v[72:75], v[220:223]\n"
"ds_read_b128 v[148:151], v17 offset:12288\n"
"v_mul_f32 v216, s85, v216\n"
"v_mfma_f32_16x16x32_bf16 v[220:223], v[188:191], v[76:79], v[220:223]\n"
"ds_read_b128 v[152:155], v16 offset:13312\n"
"v_mul_f32 v217, s85, v217\n"
"v_mfma_f32_16x16x32_bf16 a[52:55], v[192:195], v[84:87], a[52:55]\n"
"ds_read_b128 v[156:159], v17 offset:13312\n"
"v_mul_f32 v218, s85, v218\n"
"v_mfma_f32_16x16x32_bf16 a[52:55], v[196:199], v[100:103], a[52:55]\n"
"v_mul_f32 v219, s85, v219\n"
"v_cvt_pk_bf16_f32 v216, 0, v216\n"
"v_mfma_f32_16x16x32_bf16 a[56:59], v[192:195], v[88:91], a[56:59]\n"
"v_cvt_pk_bf16_f32 v217, 0, v217\n"
"v_cvt_pk_bf16_f32 v218, 0, v218\n"
"v_cvt_pk_bf16_f32 v219, 0, v219\n"
"v_mfma_f32_16x16x32_bf16 a[56:59], v[196:199], v[104:107], a[56:59]\n"
"v_fma_f32 v216, v216, s86, -v28\n"
"v_fma_f32 v217, v217, s86, -v28\n"
"v_fma_f32 v218, v218, s86, -v28\n"
"v_fma_f32 v219, v219, s86, -v28\n"
"v_mfma_f32_16x16x32_bf16 a[60:63], v[192:195], v[92:95], a[60:63]\n"
"v_exp_f32 v216, v216\n"
"v_exp_f32 v217, v217\n"
"v_exp_f32 v218, v218\n"
"v_exp_f32 v219, v219\n"
"v_mfma_f32_16x16x32_bf16 a[60:63], v[196:199], v[108:111], a[60:63]\n"
"v_cvt_pk_bf16_f32 v114, v216, v217\n"
"v_cvt_pk_bf16_f32 v115, v218, v219\n"
"v_mfma_f32_16x16x32_bf16 v[224:227], v[168:171], a[208:211], 0\n"
"buffer_store_dwordx4 v[248:251], v1, s[36:39], s87 offen offset:0 sc1\n"
"v_mfma_f32_16x16x32_bf16 v[224:227], v[172:175], a[212:215], v[224:227]\n"
"ds_read_b128 v[160:163], v16 offset:14336\n"
"v_cvt_pk_bf16_f32 v220, 0, v220\n"
"v_mfma_f32_16x16x32_bf16 v[224:227], v[176:179], a[216:219], v[224:227]\n"
"ds_read_b128 v[164:167], v17 offset:14336\n"
"v_cvt_pk_bf16_f32 v221, 0, v221\n"
"v_mfma_f32_16x16x32_bf16 v[224:227], v[180:183], a[220:223], v[224:227]\n"
"s_waitcnt lgkmcnt(10)\n"
"v_cvt_pk_bf16_f32 v222, 0, v222\n"
"v_cvt_pk_bf16_f32 v223, 0, v223\n"
"v_mfma_f32_16x16x32_bf16 v[224:227], v[184:187], a[224:227], v[224:227]\n"
"ds_read_b64_tr_b16 v[192:193], v26 offset:1024\n"
"v_mul_f32 v220, s85, v220\n"
"v_mfma_f32_16x16x32_bf16 v[224:227], v[188:191], a[228:231], v[224:227]\n"
"ds_read_b64_tr_b16 v[194:195], v26 offset:7168\n"
"v_mul_f32 v221, s85, v221\n"
"v_mfma_f32_16x16x32_bf16 a[64:67], v[200:203], v[80:83], a[64:67]\n"
"ds_read_b64_tr_b16 v[196:197], v26 offset:13312\n"
"v_mul_f32 v222, s85, v222\n"
"v_mfma_f32_16x16x32_bf16 a[64:67], v[204:207], v[96:99], a[64:67]\n"
"ds_read_b64_tr_b16 v[198:199], v26 offset:19456\n"
"v_mul_f32 v223, s85, v223\n"
"v_mfma_f32_16x16x32_bf16 a[68:71], v[200:203], v[84:87], a[68:71]\n"
"v_cvt_pk_bf16_f32 v220, 0, v220\n"
"v_cvt_pk_bf16_f32 v221, 0, v221\n"
"v_mfma_f32_16x16x32_bf16 a[68:71], v[204:207], v[100:103], a[68:71]\n"
"v_cvt_pk_bf16_f32 v222, 0, v222\n"
"v_cvt_pk_bf16_f32 v223, 0, v223\n"
"v_mfma_f32_16x16x32_bf16 a[72:75], v[200:203], v[88:91], a[72:75]\n"
"v_fma_f32 v220, v220, s86, -v29\n"
"v_fma_f32 v221, v221, s86, -v29\n"
"v_mfma_f32_16x16x32_bf16 a[72:75], v[204:207], v[104:107], a[72:75]\n"
"v_fma_f32 v222, v222, s86, -v29\n"
"v_fma_f32 v223, v223, s86, -v29\n"
"v_mfma_f32_16x16x32_bf16 v[228:231], v[168:171], a[232:235], 0\n"
"v_exp_f32 v220, v220\n"
"v_exp_f32 v221, v221\n"
"v_exp_f32 v222, v222\n"
"v_exp_f32 v223, v223\n"
"v_mfma_f32_16x16x32_bf16 v[228:231], v[172:175], a[236:239], v[228:231]\n"
"v_cvt_pk_bf16_f32 v118, v220, v221\n"
"v_cvt_pk_bf16_f32 v119, v222, v223\n"
"v_mfma_f32_16x16x32_bf16 v[228:231], v[176:179], a[240:243], v[228:231]\n"
"ds_read_b128 v[168:171], v16 offset:18432\n"
"v_cvt_pk_bf16_f32 v224, 0, v224\n"
"v_mfma_f32_16x16x32_bf16 v[228:231], v[180:183], a[244:247], v[228:231]\n"
"ds_read_b128 v[172:175], v17 offset:18432\n"
"v_cvt_pk_bf16_f32 v225, 0, v225\n"
"v_mfma_f32_16x16x32_bf16 v[228:231], v[184:187], a[248:251], v[228:231]\n"
"ds_read_b128 v[176:179], v16 offset:19456\n"
"v_cvt_pk_bf16_f32 v226, 0, v226\n"
"v_mfma_f32_16x16x32_bf16 v[228:231], v[188:191], a[252:255], v[228:231]\n"
"s_waitcnt lgkmcnt(12)\n"
"v_cvt_pk_bf16_f32 v227, 0, v227\n"
"v_mul_f32 v224, s85, v224\n"
"v_mfma_f32_16x16x32_bf16 a[76:79], v[200:203], v[92:95], a[76:79]\n"
"ds_read_b128 v[180:183], v17 offset:19456\n"
"v_mul_f32 v225, s85, v225\n"
"v_mfma_f32_16x16x32_bf16 a[76:79], v[204:207], v[108:111], a[76:79]\n"
"ds_read_b64_tr_b16 v[200:201], v27 offset:1024\n"
"v_mul_f32 v226, s85, v226\n"
"v_mfma_f32_16x16x32_bf16 a[80:83], v[208:211], v[80:83], a[80:83]\n"
"ds_read_b64_tr_b16 v[202:203], v27 offset:7168\n"
"v_mul_f32 v227, s85, v227\n"
"v_mfma_f32_16x16x32_bf16 a[80:83], v[212:215], v[96:99], a[80:83]\n"
"s_waitcnt lgkmcnt(10)\n"
"v_cvt_pk_bf16_f32 v224, 0, v224\n"
"v_cvt_pk_bf16_f32 v225, 0, v225\n"
"v_mfma_f32_16x16x32_bf16 a[84:87], v[208:211], v[84:87], a[84:87]\n"
"v_cvt_pk_bf16_f32 v226, 0, v226\n"
"v_cvt_pk_bf16_f32 v227, 0, v227\n"
"v_mfma_f32_16x16x32_bf16 a[84:87], v[212:215], v[100:103], a[84:87]\n"
"v_fma_f32 v224, v224, s86, -v30\n"
"v_fma_f32 v225, v225, s86, -v30\n"
"v_fma_f32 v226, v226, s86, -v30\n"
"v_fma_f32 v227, v227, s86, -v30\n"
"v_mfma_f32_16x16x32_bf16 v[232:235], v[144:147], v[32:35], 0\n"
"v_exp_f32 v224, v224\n"
"v_exp_f32 v225, v225\n"
"v_exp_f32 v226, v226\n"
"v_exp_f32 v227, v227\n"
"v_mfma_f32_16x16x32_bf16 v[232:235], v[148:151], v[36:39], v[232:235]\n"
"v_cvt_pk_bf16_f32 v122, v224, v225\n"
"v_cvt_pk_bf16_f32 v123, v226, v227\n"
"v_mfma_f32_16x16x32_bf16 v[232:235], v[152:155], v[40:43], v[232:235]\n"
"ds_read_b64_tr_b16 v[204:205], v27 offset:13312\n"
"v_cvt_pk_bf16_f32 v228, 0, v228\n"
"v_mfma_f32_16x16x32_bf16 v[232:235], v[156:159], v[44:47], v[232:235]\n"
"ds_read_b64_tr_b16 v[206:207], v27 offset:19456\n"
"v_cvt_pk_bf16_f32 v229, 0, v229\n"
"v_mfma_f32_16x16x32_bf16 v[232:235], v[160:163], v[48:51], v[232:235]\n"
"ds_read_b128 v[184:187], v16 offset:20480\n"
"v_cvt_pk_bf16_f32 v230, 0, v230\n"
"v_mfma_f32_16x16x32_bf16 v[232:235], v[164:167], v[52:55], v[232:235]\n"
"ds_read_b128 v[188:191], v17 offset:20480\n"
"v_cvt_pk_bf16_f32 v231, 0, v231\n"
"v_mfma_f32_16x16x32_bf16 a[88:91], v[208:211], v[88:91], a[88:91]\n"
"v_mul_f32 v228, s85, v228\n"
"v_mul_f32 v229, s85, v229\n"
"v_mfma_f32_16x16x32_bf16 a[88:91], v[212:215], v[104:107], a[88:91]\n"
"s_waitcnt lgkmcnt(10)\n"
"v_mul_f32 v230, s85, v230\n"
"v_mul_f32 v231, s85, v231\n"
"v_mfma_f32_16x16x32_bf16 a[92:95], v[208:211], v[92:95], a[92:95]\n"
"v_cvt_pk_bf16_f32 v228, 0, v228\n"
"v_cvt_pk_bf16_f32 v229, 0, v229\n"
"v_mfma_f32_16x16x32_bf16 a[92:95], v[212:215], v[108:111], a[92:95]\n"
"ds_read_b64_tr_b16 v[208:209], v24 offset:2048\n"
"v_cvt_pk_bf16_f32 v230, 0, v230\n"
"v_mfma_f32_16x16x32_bf16 a[96:99], v[192:195], v[80:83], a[96:99]\n"
"v_cvt_pk_bf16_f32 v231, 0, v231\n"
"v_fma_f32 v228, v228, s86, -v31\n"
"v_mfma_f32_16x16x32_bf16 a[96:99], v[196:199], v[96:99], a[96:99]\n"
"v_fma_f32 v229, v229, s86, -v31\n"
"v_fma_f32 v230, v230, s86, -v31\n"
"v_fma_f32 v231, v231, s86, -v31\n"
"v_mfma_f32_16x16x32_bf16 a[100:103], v[192:195], v[84:87], a[100:103]\n"
"v_exp_f32 v228, v228\n"
"v_exp_f32 v229, v229\n"
"v_exp_f32 v230, v230\n"
"v_exp_f32 v231, v231\n"
"v_mfma_f32_16x16x32_bf16 a[100:103], v[196:199], v[100:103], a[100:103]\n"
"v_cvt_pk_bf16_f32 v126, v228, v229\n"
"v_cvt_pk_bf16_f32 v127, v230, v231\n"
"v_mfma_f32_16x16x32_bf16 v[236:239], v[144:147], v[56:59], 0\n"
"ds_read_b64_tr_b16 v[210:211], v24 offset:8192\n"
"v_cvt_pk_bf16_f32 v232, 0, v232\n"
"v_mfma_f32_16x16x32_bf16 v[236:239], v[148:151], v[60:63], v[236:239]\n"
"s_add_u32 vcc_hi, vcc_lo, 73728\n"
"buffer_load_dword v254, v0, s[56:59], vcc_hi idxen\n"
"ds_read_b64_tr_b16 v[212:213], v24 offset:14336\n"
"v_cvt_pk_bf16_f32 v233, 0, v233\n"
"v_mfma_f32_16x16x32_bf16 v[236:239], v[152:155], v[64:67], v[236:239]\n"
"ds_read_b64_tr_b16 v[214:215], v24 offset:20480\n"
"v_cvt_pk_bf16_f32 v234, 0, v234\n"
"v_mfma_f32_16x16x32_bf16 v[236:239], v[156:159], v[68:71], v[236:239]\n"
"v_cvt_pk_bf16_f32 v235, 0, v235\n"
"v_mul_f32 v232, s85, v232\n"
"v_mfma_f32_16x16x32_bf16 v[236:239], v[160:163], v[72:75], v[236:239]\n"
"buffer_store_dwordx4 v[248:251], v1, s[36:39], s87 offen offset:1024 sc1\n"
"v_mfma_f32_16x16x32_bf16 v[236:239], v[164:167], v[76:79], v[236:239]\n"
"v_mul_f32 v233, s85, v233\n"
"v_mul_f32 v234, s85, v234\n"
"v_mfma_f32_16x16x32_bf16 a[104:107], v[192:195], v[88:91], a[104:107]\n"
"v_mul_f32 v235, s85, v235\n"
"v_cvt_pk_bf16_f32 v232, 0, v232\n"
"v_mfma_f32_16x16x32_bf16 a[104:107], v[196:199], v[104:107], a[104:107]\n"
"s_waitcnt lgkmcnt(6)\n"
"v_cvt_pk_bf16_f32 v233, 0, v233\n"
"v_cvt_pk_bf16_f32 v234, 0, v234\n"
"v_mfma_f32_16x16x32_bf16 a[108:111], v[192:195], v[92:95], a[108:111]\n"
"v_cvt_pk_bf16_f32 v235, 0, v235\n"
"v_fma_f32 v232, v232, s86, -v28\n"
"v_mfma_f32_16x16x32_bf16 a[108:111], v[196:199], v[108:111], a[108:111]\n"
"v_fma_f32 v233, v233, s86, -v28\n"
"v_fma_f32 v234, v234, s86, -v28\n"
"v_fma_f32 v235, v235, s86, -v28\n"
"v_mfma_f32_16x16x32_bf16 a[112:115], v[200:203], v[80:83], a[112:115]\n"
"v_exp_f32 v232, v232\n"
"v_exp_f32 v233, v233\n"
"v_exp_f32 v234, v234\n"
"v_exp_f32 v235, v235\n"
"v_mfma_f32_16x16x32_bf16 a[112:115], v[204:207], v[96:99], a[112:115]\n"
"v_cvt_pk_bf16_f32 v128, v232, v233\n"
"v_cvt_pk_bf16_f32 v129, v234, v235\n"
"v_mfma_f32_16x16x32_bf16 v[216:219], v[144:147], a[208:211], 0\n"
"ds_read_b64_tr_b16 v[192:193], v25 offset:2048\n"
"v_cvt_pk_bf16_f32 v236, 0, v236\n"
"v_mfma_f32_16x16x32_bf16 v[216:219], v[148:151], a[212:215], v[216:219]\n"
"ds_read_b64_tr_b16 v[194:195], v25 offset:8192\n"
"v_cvt_pk_bf16_f32 v237, 0, v237\n"
"v_mfma_f32_16x16x32_bf16 v[216:219], v[152:155], a[216:219], v[216:219]\n"
"ds_read_b64_tr_b16 v[196:197], v25 offset:14336\n"
"v_cvt_pk_bf16_f32 v238, 0, v238\n"
"v_mfma_f32_16x16x32_bf16 v[216:219], v[156:159], a[220:223], v[216:219]\n"
"ds_read_b64_tr_b16 v[198:199], v25 offset:20480\n"
"v_cvt_pk_bf16_f32 v239, 0, v239\n"
"v_mfma_f32_16x16x32_bf16 v[216:219], v[160:163], a[224:227], v[216:219]\n"
"v_mul_f32 v236, s85, v236\n"
"v_mul_f32 v237, s85, v237\n"
"v_mfma_f32_16x16x32_bf16 v[216:219], v[164:167], a[228:231], v[216:219]\n"
"v_mul_f32 v238, s85, v238\n"
"v_mul_f32 v239, s85, v239\n"
"v_mfma_f32_16x16x32_bf16 a[116:119], v[200:203], v[84:87], a[116:119]\n"
"v_cvt_pk_bf16_f32 v236, 0, v236\n"
"v_cvt_pk_bf16_f32 v237, 0, v237\n"
"v_mfma_f32_16x16x32_bf16 a[116:119], v[204:207], v[100:103], a[116:119]\n"
"v_cvt_pk_bf16_f32 v238, 0, v238\n"
"v_cvt_pk_bf16_f32 v239, 0, v239\n"
"v_mfma_f32_16x16x32_bf16 a[120:123], v[200:203], v[88:91], a[120:123]\n"
"v_fma_f32 v236, v236, s86, -v29\n"
"v_fma_f32 v237, v237, s86, -v29\n"
"v_mfma_f32_16x16x32_bf16 a[120:123], v[204:207], v[104:107], a[120:123]\n"
"v_fma_f32 v238, v238, s86, -v29\n"
"v_fma_f32 v239, v239, s86, -v29\n"
"v_mfma_f32_16x16x32_bf16 a[124:127], v[200:203], v[92:95], a[124:127]\n"
"v_exp_f32 v236, v236\n"
"v_mfma_f32_16x16x32_bf16 a[124:127], v[204:207], v[108:111], a[124:127]\n"
"v_exp_f32 v237, v237\n"
"v_mfma_f32_16x16x32_bf16 v[220:223], v[144:147], a[232:235], 0\n"
"v_exp_f32 v238, v238\n"
"v_exp_f32 v239, v239\n"
"v_mfma_f32_16x16x32_bf16 v[220:223], v[148:151], a[236:239], v[220:223]\n"
"v_cvt_pk_bf16_f32 v132, v236, v237\n"
"v_cvt_pk_bf16_f32 v133, v238, v239\n"
"v_mfma_f32_16x16x32_bf16 v[220:223], v[152:155], a[240:243], v[220:223]\n"
"ds_read_b64_tr_b16 v[200:201], v26 offset:2048\n"
"v_cvt_pk_bf16_f32 v216, 0, v216\n"
"v_mfma_f32_16x16x32_bf16 v[220:223], v[156:159], a[244:247], v[220:223]\n"
"s_waitcnt lgkmcnt(5)\n"
"v_cvt_pk_bf16_f32 v217, 0, v217\n"
"v_cvt_pk_bf16_f32 v218, 0, v218\n"
"v_mfma_f32_16x16x32_bf16 v[220:223], v[160:163], a[248:251], v[220:223]\n"
"ds_read_b64_tr_b16 v[202:203], v26 offset:8192\n"
"v_cvt_pk_bf16_f32 v219, 0, v219\n"
"v_mfma_f32_16x16x32_bf16 v[220:223], v[164:167], a[252:255], v[220:223]\n"
"ds_read_b64_tr_b16 v[204:205], v26 offset:14336\n"
"v_mul_f32 v216, s85, v216\n"
"v_mfma_f32_16x16x32_bf16 a[128:131], v[208:211], v[80:83], a[128:131]\n"
"ds_read_b64_tr_b16 v[206:207], v26 offset:20480\n"
"v_mul_f32 v217, s85, v217\n"
"v_mfma_f32_16x16x32_bf16 a[128:131], v[212:215], v[96:99], a[128:131]\n"
"v_mul_f32 v218, s85, v218\n"
"v_mul_f32 v219, s85, v219\n"
"v_mfma_f32_16x16x32_bf16 a[132:135], v[208:211], v[84:87], a[132:135]\n"
"v_cvt_pk_bf16_f32 v216, 0, v216\n"
"v_cvt_pk_bf16_f32 v217, 0, v217\n"
"v_mfma_f32_16x16x32_bf16 a[132:135], v[212:215], v[100:103], a[132:135]\n"
"v_cvt_pk_bf16_f32 v218, 0, v218\n"
"v_cvt_pk_bf16_f32 v219, 0, v219\n"
"v_mfma_f32_16x16x32_bf16 a[136:139], v[208:211], v[88:91], a[136:139]\n"
"v_fma_f32 v216, v216, s86, -v30\n"
"v_fma_f32 v217, v217, s86, -v30\n"
"v_mfma_f32_16x16x32_bf16 a[136:139], v[212:215], v[104:107], a[136:139]\n"
"v_fma_f32 v218, v218, s86, -v30\n"
"v_fma_f32 v219, v219, s86, -v30\n"
"v_mfma_f32_16x16x32_bf16 v[224:227], v[168:171], v[32:35], 0\n"
"v_exp_f32 v216, v216\n"
"v_exp_f32 v217, v217\n"
"v_exp_f32 v218, v218\n"
"v_exp_f32 v219, v219\n"
"v_mfma_f32_16x16x32_bf16 v[224:227], v[172:175], v[36:39], v[224:227]\n"
"v_cvt_pk_bf16_f32 v136, v216, v217\n"
"v_cvt_pk_bf16_f32 v137, v218, v219\n"
"v_mfma_f32_16x16x32_bf16 v[224:227], v[176:179], v[40:43], v[224:227]\n"
"v_cvt_pk_bf16_f32 v220, 0, v220\n"
"v_cvt_pk_bf16_f32 v221, 0, v221\n"
"v_mfma_f32_16x16x32_bf16 v[224:227], v[180:183], v[44:47], v[224:227]\n"
"v_cvt_pk_bf16_f32 v222, 0, v222\n"
"v_cvt_pk_bf16_f32 v223, 0, v223\n"
"v_mfma_f32_16x16x32_bf16 v[224:227], v[184:187], v[48:51], v[224:227]\n"
"v_mul_f32 v220, s85, v220\n"
"v_mul_f32 v221, s85, v221\n"
"v_mfma_f32_16x16x32_bf16 v[224:227], v[188:191], v[52:55], v[224:227]\n"
"s_waitcnt lgkmcnt(4)\n"
"v_mul_f32 v222, s85, v222\n"
"v_mul_f32 v223, s85, v223\n"
"v_mfma_f32_16x16x32_bf16 a[140:143], v[208:211], v[92:95], a[140:143]\n"
"v_cvt_pk_bf16_f32 v220, 0, v220\n"
"v_cvt_pk_bf16_f32 v221, 0, v221\n"
"v_mfma_f32_16x16x32_bf16 a[140:143], v[212:215], v[108:111], a[140:143]\n"
"ds_read_b64_tr_b16 v[208:209], v27 offset:2048\n"
"v_cvt_pk_bf16_f32 v222, 0, v222\n"
"v_mfma_f32_16x16x32_bf16 a[144:147], v[192:195], v[80:83], a[144:147]\n"
"ds_read_b64_tr_b16 v[210:211], v27 offset:8192\n"
"v_cvt_pk_bf16_f32 v223, 0, v223\n"
"v_mfma_f32_16x16x32_bf16 a[144:147], v[196:199], v[96:99], a[144:147]\n"
"buffer_store_dwordx2 v[248:249], v5, s[36:39], s87 offen offset:0 sc1\n"
"v_mfma_f32_16x16x32_bf16 a[148:151], v[192:195], v[84:87], a[148:151]\n"
"s_waitcnt vmcnt(23)\n"
"s_barrier\n"
"v_fma_f32 v220, v220, s86, -v31\n"
"v_mfma_f32_16x16x32_bf16 a[148:151], v[196:199], v[100:103], a[148:151]\n"
"v_fma_f32 v221, v221, s86, -v31\n"
"v_fma_f32 v222, v222, s86, -v31\n"
"v_fma_f32 v223, v223, s86, -v31\n"
"v_mfma_f32_16x16x32_bf16 a[152:155], v[192:195], v[88:91], a[152:155]\n"
"v_exp_f32 v220, v220\n"
"v_exp_f32 v221, v221\n"
"v_exp_f32 v222, v222\n"
"v_exp_f32 v223, v223\n"
"v_mfma_f32_16x16x32_bf16 a[152:155], v[196:199], v[104:107], a[152:155]\n"
"s_waitcnt vmcnt(12)\n"
"v_cmp_gt_u32 s[92:93], 1, v252\n"
"s_nop 0\n"
"s_cmp_eq_u64 s[92:93], 0\n"
"s_cbranch_scc0 .Lspin1\n"
".Lback1:\n"
"v_cvt_pk_bf16_f32 v140, v220, v221\n"
"v_cvt_pk_bf16_f32 v141, v222, v223\n"
"v_mfma_f32_16x16x32_bf16 v[228:231], v[168:171], v[56:59], 0\n"
"v_cvt_pk_bf16_f32 v224, 0, v224\n"
"v_cvt_pk_bf16_f32 v225, 0, v225\n"
"v_mfma_f32_16x16x32_bf16 v[228:231], v[172:175], v[60:63], v[228:231]\n"
"v_cvt_pk_bf16_f32 v226, 0, v226\n"
"v_cvt_pk_bf16_f32 v227, 0, v227\n"
"v_mfma_f32_16x16x32_bf16 v[228:231], v[176:179], v[64:67], v[228:231]\n"
"v_mul_f32 v224, s85, v224\n"
"v_mul_f32 v225, s85, v225\n"
"v_mfma_f32_16x16x32_bf16 v[228:231], v[180:183], v[68:71], v[228:231]\n"
"v_mul_f32 v226, s85, v226\n"
"v_mul_f32 v227, s85, v227\n"
"v_mfma_f32_16x16x32_bf16 v[228:231], v[184:187], v[72:75], v[228:231]\n"
"s_waitcnt vmcnt(11)\n"
"v_cmp_gt_u32 s[92:93], 8, v253\n"
"s_nop 0\n"
"s_cmp_eq_u64 s[92:93], 0\n"
"s_cbranch_scc0 .Lspin2\n"
".Lback2:\n"
"v_mfma_f32_16x16x32_bf16 v[228:231], v[188:191], v[76:79], v[228:231]\n"
"s_waitcnt lgkmcnt(2)\n"
"v_cvt_pk_bf16_f32 v224, 0, v224\n"
"v_cvt_pk_bf16_f32 v225, 0, v225\n"
"v_mfma_f32_16x16x32_bf16 a[156:159], v[192:195], v[92:95], a[156:159]\n"
"ds_read_b64_tr_b16 v[212:213], v27 offset:14336\n"
"v_cvt_pk_bf16_f32 v226, 0, v226\n"
"v_mfma_f32_16x16x32_bf16 a[156:159], v[196:199], v[108:111], a[156:159]\n"
"ds_read_b64_tr_b16 v[214:215], v27 offset:20480\n"
"v_cvt_pk_bf16_f32 v227, 0, v227\n"
"v_mfma_f32_16x16x32_bf16 a[160:163], v[200:203], v[80:83], a[160:163]\n"
"ds_read_b128 v[144:147], v16 offset:24576\n"
"v_fma_f32 v224, v224, s86, -v28\n"
"v_mfma_f32_16x16x32_bf16 a[160:163], v[204:207], v[96:99], a[160:163]\n"
"v_fma_f32 v225, v225, s86, -v28\n"
"v_fma_f32 v226, v226, s86, -v28\n"
"v_fma_f32 v227, v227, s86, -v28\n"
"v_mfma_f32_16x16x32_bf16 a[164:167], v[200:203], v[84:87], a[164:167]\n"
"s_cmp_lg_u32 s76, 0\n"
"s_cbranch_scc1 .Lsig2\n"
"s_mov_b64 exec, 0xff\n"
"v_mov_b32 v252, 1\n"
"v_lshlrev_b32 v253, 15, v0\n"
"buffer_store_dword v252, v253, s[36:39], s35 offen sc1\n"
"s_mov_b64 exec, -1\n"
".Lsig2:\n"
"v_exp_f32 v224, v224\n"
"v_exp_f32 v225, v225\n"
"v_exp_f32 v226, v226\n"
"v_exp_f32 v227, v227\n"
"v_mfma_f32_16x16x32_bf16 a[164:167], v[204:207], v[100:103], a[164:167]\n"
"v_cvt_pk_bf16_f32 v130, v224, v225\n"
"v_cvt_pk_bf16_f32 v131, v226, v227\n"
"v_mfma_f32_16x16x32_bf16 v[232:235], v[168:171], a[208:211], 0\n"
"ds_read_b128 v[148:151], v17 offset:24576\n"
"v_cvt_pk_bf16_f32 v228, 0, v228\n"
"v_mfma_f32_16x16x32_bf16 v[232:235], v[172:175], a[212:215], v[232:235]\n"
"ds_read_b128 v[152:155], v16 offset:25600\n"
"v_cvt_pk_bf16_f32 v229, 0, v229\n"
"v_mfma_f32_16x16x32_bf16 v[232:235], v[176:179], a[216:219], v[232:235]\n"
"ds_read_b128 v[156:159], v17 offset:25600\n"
"v_cvt_pk_bf16_f32 v230, 0, v230\n"
"v_mfma_f32_16x16x32_bf16 v[232:235], v[180:183], a[220:223], v[232:235]\n"
"ds_read_b128 v[160:163], v16 offset:26624\n"
"v_cvt_pk_bf16_f32 v231, 0, v231\n"
"v_mfma_f32_16x16x32_bf16 v[232:235], v[184:187], a[224:227], v[232:235]\n"
"ds_read_b128 v[164:167], v17 offset:26624\n"
"v_mul_f32 v228, s85, v228\n"
"v_mfma_f32_16x16x32_bf16 v[232:235], v[188:191], a[228:231], v[232:235]\n"
"ds_read_b64_tr_b16 v[192:193], v20 offset:0\n"
"v_mul_f32 v229, s85, v229\n"
"v_mfma_f32_16x16x32_bf16 a[168:171], v[200:203], v[88:91], a[168:171]\n"
"ds_read_b64_tr_b16 v[194:195], v20 offset:6144\n"
"v_mul_f32 v230, s85, v230\n"
"v_mfma_f32_16x16x32_bf16 a[168:171], v[204:207], v[104:107], a[168:171]\n"
"s_waitcnt lgkmcnt(8)\n"
"v_mul_f32 v231, s85, v231\n"
"v_cvt_pk_bf16_f32 v228, 0, v228\n"
"v_mfma_f32_16x16x32_bf16 a[172:175], v[200:203], v[92:95], a[172:175]\n"
"ds_read_b64_tr_b16 v[196:197], v20 offset:12288\n"
"v_cvt_pk_bf16_f32 v229, 0, v229\n"
"v_mfma_f32_16x16x32_bf16 a[172:175], v[204:207], v[108:111], a[172:175]\n"
"ds_read_b64_tr_b16 v[198:199], v20 offset:18432\n"
"v_cvt_pk_bf16_f32 v230, 0, v230\n"
"v_mfma_f32_16x16x32_bf16 a[176:179], v[208:211], v[80:83], a[176:179]\n"
"v_cvt_pk_bf16_f32 v231, 0, v231\n"
"v_fma_f32 v228, v228, s86, -v29\n"
"v_mfma_f32_16x16x32_bf16 a[176:179], v[212:215], v[96:99], a[176:179]\n"
"v_fma_f32 v229, v229, s86, -v29\n"
"v_fma_f32 v230, v230, s86, -v29\n"
"v_fma_f32 v231, v231, s86, -v29\n"
"v_mfma_f32_16x16x32_bf16 v[236:239], v[168:171], a[232:235], 0\n"
"v_exp_f32 v228, v228\n"
"v_exp_f32 v229, v229\n"
"v_exp_f32 v230, v230\n"
"v_exp_f32 v231, v231\n"
"v_mfma_f32_16x16x32_bf16 v[236:239], v[172:175], a[236:239], v[236:239]\n"
"v_cvt_pk_bf16_f32 v134, v228, v229\n"
"v_cvt_pk_bf16_f32 v135, v230, v231\n"
"v_mfma_f32_16x16x32_bf16 v[236:239], v[176:179], a[240:243], v[236:239]\n"
"ds_read_b64_tr_b16 v[200:201], v21 offset:0\n"
"v_cvt_pk_bf16_f32 v232, 0, v232\n"
"v_mfma_f32_16x16x32_bf16 v[236:239], v[180:183], a[244:247], v[236:239]\n"
"ds_read_b64_tr_b16 v[202:203], v21 offset:6144\n"
"v_cvt_pk_bf16_f32 v233, 0, v233\n"
"v_mfma_f32_16x16x32_bf16 v[236:239], v[184:187], a[248:251], v[236:239]\n"
"ds_read_b64_tr_b16 v[204:205], v21 offset:12288\n"
"v_cvt_pk_bf16_f32 v234, 0, v234\n"
"v_mfma_f32_16x16x32_bf16 v[236:239], v[188:191], a[252:255], v[236:239]\n"
"ds_read_b64_tr_b16 v[206:207], v21 offset:18432\n"
"v_cvt_pk_bf16_f32 v235, 0, v235\n"
"v_mfma_f32_16x16x32_bf16 a[180:183], v[208:211], v[84:87], a[180:183]\n"
"ds_read_b128 v[168:171], v16 offset:30720\n"
"v_mul_f32 v232, s85, v232\n"
"v_mfma_f32_16x16x32_bf16 a[180:183], v[212:215], v[100:103], a[180:183]\n"
"ds_read_b128 v[172:175], v17 offset:30720\n"
"v_mul_f32 v233, s85, v233\n"
"v_mfma_f32_16x16x32_bf16 a[184:187], v[208:211], v[88:91], a[184:187]\n"
"ds_read_b128 v[176:179], v16 offset:31744\n"
"v_mul_f32 v234, s85, v234\n"
"v_mfma_f32_16x16x32_bf16 a[184:187], v[212:215], v[104:107], a[184:187]\n"
"s_waitcnt lgkmcnt(11)\n"
"v_mul_f32 v235, s85, v235\n"
"v_cvt_pk_bf16_f32 v232, 0, v232\n"
"v_mfma_f32_16x16x32_bf16 a[188:191], v[208:211], v[92:95], a[188:191]\n"
"v_cvt_pk_bf16_f32 v233, 0, v233\n"
"v_cvt_pk_bf16_f32 v234, 0, v234\n"
"v_cvt_pk_bf16_f32 v235, 0, v235\n"
"v_mfma_f32_16x16x32_bf16 a[188:191], v[212:215], v[108:111], a[188:191]\n"
"v_fma_f32 v232, v232, s86, -v30\n"
"v_fma_f32 v233, v233, s86, -v30\n"
"v_fma_f32 v234, v234, s86, -v30\n"
"v_fma_f32 v235, v235, s86, -v30\n"
"v_mfma_f32_16x16x32_bf16 v[216:219], v[144:147], v[32:35], 0\n"
"v_exp_f32 v232, v232\n"
"v_exp_f32 v233, v233\n"
"v_exp_f32 v234, v234\n"
"v_exp_f32 v235, v235\n"
"v_mfma_f32_16x16x32_bf16 v[216:219], v[148:151], v[36:39], v[216:219]\n"
"v_cvt_pk_bf16_f32 v138, v232, v233\n"
"v_cvt_pk_bf16_f32 v139, v234, v235\n"
"v_mfma_f32_16x16x32_bf16 v[216:219], v[152:155], v[40:43], v[216:219]\n"
"ds_read_b64_tr_b16 v[208:209], v22 offset:0\n"
"v_cvt_pk_bf16_f32 v236, 0, v236\n"
"v_mfma_f32_16x16x32_bf16 v[216:219], v[156:159], v[44:47], v[216:219]\n"
"ds_read_b64_tr_b16 v[210:211], v22 offset:6144\n"
"v_cvt_pk_bf16_f32 v237, 0, v237\n"
"v_mfma_f32_16x16x32_bf16 v[216:219], v[160:163], v[48:51], v[216:219]\n"
"ds_read_b64_tr_b16 v[212:213], v22 offset:12288\n"
"v_cvt_pk_bf16_f32 v238, 0, v238\n"
"v_mfma_f32_16x16x32_bf16 v[216:219], v[164:167], v[52:55], v[216:219]\n"
"ds_read_b64_tr_b16 v[214:215], v22 offset:18432\n"
"v_cvt_pk_bf16_f32 v239, 0, v239\n"
"v_mfma_f32_16x16x32_bf16 a[192:195], v[240:243], v[80:83], a[192:195]\n"
"ds_read_b128 v[180:183], v17 offset:31744\n"
"v_mul_f32 v236, s85, v236\n"
"v_mfma_f32_16x16x32_bf16 a[192:195], v[240:243], v[96:99], a[192:195]\n"
"ds_read_b128 v[184:187], v16 offset:32768\n"
"v_mul_f32 v237, s85, v237\n"
"v_mfma_f32_16x16x32_bf16 a[196:199], v[240:243], v[84:87], a[196:199]\n"
"ds_read_b128 v[188:191], v17 offset:32768\n"
"v_mul_f32 v238, s85, v238\n"
"v_mfma_f32_16x16x32_bf16 a[196:199], v[240:243], v[100:103], a[196:199]\n"
"v_mul_f32 v239, s85, v239\n"
"v_cvt_pk_bf16_f32 v236, 0, v236\n"
"v_mfma_f32_16x16x32_bf16 a[200:203], v[240:243], v[88:91], a[200:203]\n"
"v_cvt_pk_bf16_f32 v237, 0, v237\n"
"v_cvt_pk_bf16_f32 v238, 0, v238\n"
"v_cvt_pk_bf16_f32 v239, 0, v239\n"
"v_mfma_f32_16x16x32_bf16 a[200:203], v[240:243], v[104:107], a[200:203]\n"
"v_fma_f32 v236, v236, s86, -v31\n"
"v_fma_f32 v237, v237, s86, -v31\n"
"v_fma_f32 v238, v238, s86, -v31\n"
"v_fma_f32 v239, v239, s86, -v31\n"
"v_mfma_f32_16x16x32_bf16 a[204:207], v[240:243], v[92:95], a[204:207]\n"
"v_exp_f32 v236, v236\n"
"v_exp_f32 v237, v237\n"
"v_exp_f32 v238, v238\n"
"v_exp_f32 v239, v239\n"
"v_mfma_f32_16x16x32_bf16 a[204:207], v[240:243], v[108:111], a[204:207]\n"
"v_cvt_pk_bf16_f32 v142, v236, v237\n"
"v_cvt_pk_bf16_f32 v143, v238, v239\n"
"v_mfma_f32_16x16x32_bf16 v[220:223], v[144:147], v[56:59], 0\n"
"v_cvt_pk_bf16_f32 v216, 0, v216\n"
"v_cvt_pk_bf16_f32 v217, 0, v217\n"
"v_mfma_f32_16x16x32_bf16 v[220:223], v[148:151], v[60:63], v[220:223]\n"
"v_cvt_pk_bf16_f32 v218, 0, v218\n"
"v_cvt_pk_bf16_f32 v219, 0, v219\n"
"v_mfma_f32_16x16x32_bf16 v[220:223], v[152:155], v[64:67], v[220:223]\n"
"v_mul_f32 v216, s85, v216\n"
"v_mul_f32 v217, s85, v217\n"
"v_mfma_f32_16x16x32_bf16 v[220:223], v[156:159], v[68:71], v[220:223]\n"
"s_waitcnt lgkmcnt(14)\n"
"v_mul_f32 v218, s85, v218\n"
"v_mul_f32 v219, s85, v219\n"
"v_mfma_f32_16x16x32_bf16 v[220:223], v[160:163], v[72:75], v[220:223]\n"
"v_cvt_pk_bf16_f32 v216, 0, v216\n"
"v_cvt_pk_bf16_f32 v217, 0, v217\n"
"v_mfma_f32_16x16x32_bf16 v[220:223], v[164:167], v[76:79], v[220:223]\n"
"v_cvt_pk_bf16_f32 v218, 0, v218\n"
"v_cvt_pk_bf16_f32 v219, 0, v219\n"
"v_mfma_f32_16x16x32_bf16 a[0:3], v[192:195], v[112:115], a[0:3]\n"
"v_fma_f32 v216, v216, s86, -v28\n"
"v_fma_f32 v217, v217, s86, -v28\n"
"v_mfma_f32_16x16x32_bf16 a[0:3], v[196:199], v[128:131], a[0:3]\n"
"v_fma_f32 v218, v218, s86, -v28\n"
"v_fma_f32 v219, v219, s86, -v28\n"
"v_mfma_f32_16x16x32_bf16 a[4:7], v[192:195], v[116:119], a[4:7]\n"
"s_barrier\n"
"s_add_u32 s84, s83, 172032\n"
"v_mfma_f32_16x16x32_bf16 a[4:7], v[196:199], v[132:135], a[4:7]\n"
"s_add_u32 m0, s83, 49152\n"
"v_exp_f32 v216, v216\n"
"v_mfma_f32_16x16x32_bf16 a[8:11], v[192:195], v[120:123], a[8:11]\n"
"v_exp_f32 v217, v217\n"
"v_exp_f32 v218, v218\n"
"v_exp_f32 v219, v219\n"
"v_mfma_f32_16x16x32_bf16 a[8:11], v[196:199], v[136:139], a[8:11]\n"
"v_cvt_pk_bf16_f32 v80, v216, v217\n"
"v_cvt_pk_bf16_f32 v81, v218, v219\n"
"v_mfma_f32_16x16x32_bf16 v[224:227], v[144:147], a[208:211], 0\n"
"buffer_load_dwordx4 v7, s[48:51], s84 offen offset:0 lds\n"
"v_mfma_f32_16x16x32_bf16 v[224:227], v[148:151], a[212:215], v[224:227]\n"
"v_cvt_pk_bf16_f32 v220, 0, v220\n"
"v_cvt_pk_bf16_f32 v221, 0, v221\n"
"v_mfma_f32_16x16x32_bf16 v[224:227], v[152:155], a[216:219], v[224:227]\n"
"v_cvt_pk_bf16_f32 v222, 0, v222\n"
"v_cvt_pk_bf16_f32 v223, 0, v223\n"
"v_mfma_f32_16x16x32_bf16 v[224:227], v[156:159], a[220:223], v[224:227]\n"
"v_mul_f32 v220, s85, v220\n"
"v_mul_f32 v221, s85, v221\n"
"v_mfma_f32_16x16x32_bf16 v[224:227], v[160:163], a[224:227], v[224:227]\n"
"buffer_load_dwordx4 v7, s[48:51], s84 offen offset:3072 lds\n"
"v_mfma_f32_16x16x32_bf16 v[224:227], v[164:167], a[228:231], v[224:227]\n"
"s_add_u32 m0, s83, 50048\n"
"s_waitcnt lgkmcnt(10)\n"
"v_mul_f32 v222, s85, v222\n"
"v_mfma_f32_16x16x32_bf16 a[12:15], v[192:195], v[124:127], a[12:15]\n"
"v_mul_f32 v223, s85, v223\n"
"v_cvt_pk_bf16_f32 v220, 0, v220\n"
"v_mfma_f32_16x16x32_bf16 a[12:15], v[196:199], v[140:143], a[12:15]\n"
"ds_read_b64_tr_b16 v[192:193], v23 offset:0\n"
"v_cvt_pk_bf16_f32 v221, 0, v221\n"
"v_mfma_f32_16x16x32_bf16 a[16:19], v[200:203], v[112:115], a[16:19]\n"
"ds_read_b64_tr_b16 v[194:195], v23 offset:6144\n"
"v_cvt_pk_bf16_f32 v222, 0, v222\n"
"v_mfma_f32_16x16x32_bf16 a[16:19], v[204:207], v[128:131], a[16:19]\n"
"buffer_load_dwordx4 v7, s[48:51], s84 offen offset:128 lds\n"
"v_mfma_f32_16x16x32_bf16 a[20:23], v[200:203], v[116:119], a[20:23]\n"
"v_cvt_pk_bf16_f32 v223, 0, v223\n"
"v_fma_f32 v220, v220, s86, -v29\n"
"v_mfma_f32_16x16x32_bf16 a[20:23], v[204:207], v[132:135], a[20:23]\n"
"v_fma_f32 v221, v221, s86, -v29\n"
"v_fma_f32 v222, v222, s86, -v29\n"
"v_fma_f32 v223, v223, s86, -v29\n"
"v_mfma_f32_16x16x32_bf16 v[228:231], v[144:147], a[232:235], 0\n"
"v_exp_f32 v220, v220\n"
"v_exp_f32 v221, v221\n"
"v_exp_f32 v222, v222\n"
"v_exp_f32 v223, v223\n"
"v_mfma_f32_16x16x32_bf16 v[228:231], v[148:151], a[236:239], v[228:231]\n"
"v_cvt_pk_bf16_f32 v84, v220, v221\n"
"v_cvt_pk_bf16_f32 v85, v222, v223\n"
"v_mfma_f32_16x16x32_bf16 v[228:231], v[152:155], a[240:243], v[228:231]\n"
"buffer_load_dwordx4 v7, s[48:51], s84 offen offset:3200 lds\n"
"v_mfma_f32_16x16x32_bf16 v[228:231], v[156:159], a[244:247], v[228:231]\n"
"s_add_u32 m0, s83, 50944\n"
"v_cvt_pk_bf16_f32 v224, 0, v224\n"
"v_cvt_pk_bf16_f32 v225, 0, v225\n"
"v_mfma_f32_16x16x32_bf16 v[228:231], v[160:163], a[248:251], v[228:231]\n"
"ds_read_b64_tr_b16 v[196:197], v23 offset:12288\n"
"v_cvt_pk_bf16_f32 v226, 0, v226\n"
"v_mfma_f32_16x16x32_bf16 v[228:231], v[164:167], a[252:255], v[228:231]\n"
"ds_read_b64_tr_b16 v[198:199], v23 offset:18432\n"
"v_cvt_pk_bf16_f32 v227, 0, v227\n"
"v_mfma_f32_16x16x32_bf16 a[24:27], v[200:203], v[120:123], a[24:27]\n"
"ds_read_b128 v[144:147], v16 offset:36864\n"
"v_mul_f32 v224, s85, v224\n"
"v_mfma_f32_16x16x32_bf16 a[24:27], v[204:207], v[136:139], a[24:27]\n"
"buffer_load_dwordx4 v7, s[48:51], s84 offen offset:256 lds\n"
"v_mfma_f32_16x16x32_bf16 a[28:31], v[200:203], v[124:127], a[28:31]\n"
"v_mul_f32 v225, s85, v225\n"
"v_mul_f32 v226, s85, v226\n"
"v_mfma_f32_16x16x32_bf16 a[28:31], v[204:207], v[140:143], a[28:31]\n"
"s_waitcnt lgkmcnt(5)\n"
"v_mul_f32 v227, s85, v227\n"
"v_cvt_pk_bf16_f32 v224, 0, v224\n"
"v_mfma_f32_16x16x32_bf16 a[32:35], v[208:211], v[112:115], a[32:35]\n"
"v_cvt_pk_bf16_f32 v225, 0, v225\n"
"v_cvt_pk_bf16_f32 v226, 0, v226\n"
"v_cvt_pk_bf16_f32 v227, 0, v227\n"
"v_mfma_f32_16x16x32_bf16 a[32:35], v[212:215], v[128:131], a[32:35]\n"
"v_fma_f32 v224, v224, s86, -v30\n"
"v_fma_f32 v225, v225, s86, -v30\n"
"v_fma_f32 v226, v226, s86, -v30\n"
"v_fma_f32 v227, v227, s86, -v30\n"
"v_mfma_f32_16x16x32_bf16 v[232:235], v[168:171], v[32:35], 0\n"
"v_exp_f32 v224, v224\n"
"v_exp_f32 v225, v225\n"
"v_exp_f32 v226, v226\n"
"v_exp_f32 v227, v227\n"
"v_mfma_f32_16x16x32_bf16 v[232:235], v[172:175], v[36:39], v[232:235]\n"
"v_cvt_pk_bf16_f32 v88, v224, v225\n"
"v_cvt_pk_bf16_f32 v89, v226, v227\n"
"v_mfma_f32_16x16x32_bf16 v[232:235], v[176:179], v[40:43], v[232:235]\n"
"buffer_load_dwordx4 v7, s[48:51], s84 offen offset:3328 lds\n"
"v_mfma_f32_16x16x32_bf16 v[232:235], v[180:183], v[44:47], v[232:235]\n"
"ds_read_b64_tr_b16 v[200:201], v20 offset:1024\n"
"v_cvt_pk_bf16_f32 v228, 0, v228\n"
"v_mfma_f32_16x16x32_bf16 v[232:235], v[184:187], v[48:51], v[232:235]\n"
"ds_read_b64_tr_b16 v[202:203], v20 offset:7168\n"
"v_cvt_pk_bf16_f32 v229, 0, v229\n"
"v_mfma_f32_16x16x32_bf16 v[232:235], v[188:191], v[52:55], v[232:235]\n"
"ds_read_b64_tr_b16 v[204:205], v20 offset:13312\n"
"v_cvt_pk_bf16_f32 v230, 0, v230\n"
"v_mfma_f32_16x16x32_bf16 a[36:39], v[208:211], v[116:119], a[36:39]\n"
"ds_read_b64_tr_b16 v[206:207], v20 offset:19456\n"
"v_cvt_pk_bf16_f32 v231, 0, v231\n"
"v_mfma_f32_16x16x32_bf16 a[36:39], v[212:215], v[132:135], a[36:39]\n"
"ds_read_b128 v[148:151], v17 offset:36864\n"
"v_mul_f32 v228, s85, v228\n"
"v_mfma_f32_16x16x32_bf16 a[40:43], v[208:211], v[120:123], a[40:43]\n"
"ds_read_b128 v[152:155], v16 offset:37888\n"
"v_mul_f32 v229, s85, v229\n"
"v_mfma_f32_16x16x32_bf16 a[40:43], v[212:215], v[136:139], a[40:43]\n"
"s_waitcnt lgkmcnt(7)\n"
"v_mul_f32 v230, s85, v230\n"
"v_mul_f32 v231, s85, v231\n"
"v_mfma_f32_16x16x32_bf16 a[44:47], v[208:211], v[124:127], a[44:47]\n"
"v_cvt_pk_bf16_f32 v228, 0, v228\n"
"v_cvt_pk_bf16_f32 v229, 0, v229\n"
"v_cvt_pk_bf16_f32 v230, 0, v230\n"
"v_cvt_pk_bf16_f32 v231, 0, v231\n"
"v_mfma_f32_16x16x32_bf16 a[44:47], v[212:215], v[140:143], a[44:47]\n"
"v_fma_f32 v228, v228, s86, -v31\n"
"v_fma_f32 v229, v229, s86, -v31\n"
"v_fma_f32 v230, v230, s86, -v31\n"
"v_fma_f32 v231, v231, s86, -v31\n"
"v_mfma_f32_16x16x32_bf16 a[48:51], v[192:195], v[112:115], a[48:51]\n"
"v_exp_f32 v228, v228\n"
"v_exp_f32 v229, v229\n"
"v_exp_f32 v230, v230\n"
"v_exp_f32 v231, v231\n"
"v_mfma_f32_16x16x32_bf16 a[48:51], v[196:199], v[128:131], a[48:51]\n"
"v_cvt_pk_bf16_f32 v92, v228, v229\n"
"v_cvt_pk_bf16_f32 v93, v230, v231\n"
"v_mfma_f32_16x16x32_bf16 v[236:239], v[168:171], v[56:59], 0\n"
"ds_read_b64_tr_b16 v[208:209], v21 offset:1024\n"
"v_cvt_pk_bf16_f32 v232, 0, v232\n"
"v_mfma_f32_16x16x32_bf16 v[236:239], v[172:175], v[60:63], v[236:239]\n"
"ds_read_b64_tr_b16 v[210:211], v21 offset:7168\n"
"v_cvt_pk_bf16_f32 v233, 0, v233\n"
"v_mfma_f32_16x16x32_bf16 v[236:239], v[176:179], v[64:67], v[236:239]\n"
"ds_read_b64_tr_b16 v[212:213], v21 offset:13312\n"
"v_cvt_pk_bf16_f32 v234, 0, v234\n"
"v_mfma_f32_16x16x32_bf16 v[236:239], v[180:183], v[68:71], v[236:239]\n"
"ds_read_b64_tr_b16 v[214:215], v21 offset:19456\n"
"v_cvt_pk_bf16_f32 v235, 0, v235\n"
"v_mfma_f32_16x16x32_bf16 v[236:239], v[184:187], v[72:75], v[236:239]\n"
"ds_read_b128 v[156:159], v17 offset:37888\n"
"v_mul_f32 v232, s85, v232\n"
"v_mfma_f32_16x16x32_bf16 v[236:239], v[188:191], v[76:79], v[236:239]\n"
"ds_read_b128 v[160:163], v16 offset:38912\n"
"v_mul_f32 v233, s85, v233\n"
"v_mfma_f32_16x16x32_bf16 a[52:55], v[192:195], v[116:119], a[52:55]\n"
"ds_read_b128 v[164:167], v17 offset:38912\n"
"v_mul_f32 v234, s85, v234\n"
"v_mfma_f32_16x16x32_bf16 a[52:55], v[196:199], v[132:135], a[52:55]\n"
"v_mul_f32 v235, s85, v235\n"
"v_cvt_pk_bf16_f32 v232, 0, v232\n"
"v_mfma_f32_16x16x32_bf16 a[56:59], v[192:195], v[120:123], a[56:59]\n"
"v_cvt_pk_bf16_f32 v233, 0, v233\n"
"v_cvt_pk_bf16_f32 v234, 0, v234\n"
"v_cvt_pk_bf16_f32 v235, 0, v235\n"
"v_mfma_f32_16x16x32_bf16 a[56:59], v[196:199], v[136:139], a[56:59]\n"
"v_fma_f32 v232, v232, s86, -v28\n"
"v_fma_f32 v233, v233, s86, -v28\n"
"v_fma_f32 v234, v234, s86, -v28\n"
"v_fma_f32 v235, v235, s86, -v28\n"
"v_mfma_f32_16x16x32_bf16 a[60:63], v[192:195], v[124:127], a[60:63]\n"
"v_exp_f32 v232, v232\n"
"v_exp_f32 v233, v233\n"
"v_exp_f32 v234, v234\n"
"v_exp_f32 v235, v235\n"
"v_mfma_f32_16x16x32_bf16 a[60:63], v[196:199], v[140:143], a[60:63]\n"
"v_cvt_pk_bf16_f32 v82, v232, v233\n"
"v_cvt_pk_bf16_f32 v83, v234, v235\n"
"v_mfma_f32_16x16x32_bf16 v[216:219], v[168:171], a[208:211], 0\n"
"ds_read_b64_tr_b16 v[192:193], v22 offset:1024\n"
"v_cvt_pk_bf16_f32 v236, 0, v236\n"
"v_mfma_f32_16x16x32_bf16 v[216:219], v[172:175], a[212:215], v[216:219]\n"
"ds_read_b64_tr_b16 v[194:195], v22 offset:7168\n"
"v_cvt_pk_bf16_f32 v237, 0, v237\n"
"v_mfma_f32_16x16x32_bf16 v[216:219], v[176:179], a[216:219], v[216:219]\n"
"ds_read_b64_tr_b16 v[196:197], v22 offset:13312\n"
"v_cvt_pk_bf16_f32 v238, 0, v238\n"
"v_mfma_f32_16x16x32_bf16 v[216:219], v[180:183], a[220:223], v[216:219]\n"
"s_waitcnt lgkmcnt(12)\n"
"v_cvt_pk_bf16_f32 v239, 0, v239\n"
"v_mul_f32 v236, s85, v236\n"
"v_mfma_f32_16x16x32_bf16 v[216:219], v[184:187], a[224:227], v[216:219]\n"
"ds_read_b64_tr_b16 v[198:199], v22 offset:19456\n"
"v_mul_f32 v237, s85, v237\n"
"v_mfma_f32_16x16x32_bf16 v[216:219], v[188:191], a[228:231], v[216:219]\n"
"v_mul_f32 v238, s85, v238\n"
"v_mul_f32 v239, s85, v239\n"
"v_mfma_f32_16x16x32_bf16 a[64:67], v[200:203], v[112:115], a[64:67]\n"
"v_cvt_pk_bf16_f32 v236, 0, v236\n"
"v_cvt_pk_bf16_f32 v237, 0, v237\n"
"v_mfma_f32_16x16x32_bf16 a[64:67], v[204:207], v[128:131], a[64:67]\n"
"v_cvt_pk_bf16_f32 v238, 0, v238\n"
"v_cvt_pk_bf16_f32 v239, 0, v239\n"
"v_mfma_f32_16x16x32_bf16 a[68:71], v[200:203], v[116:119], a[68:71]\n"
"v_fma_f32 v236, v236, s86, -v29\n"
"v_fma_f32 v237, v237, s86, -v29\n"
"v_mfma_f32_16x16x32_bf16 a[68:71], v[204:207], v[132:135], a[68:71]\n"
"v_fma_f32 v238, v238, s86, -v29\n"
"v_fma_f32 v239, v239, s86, -v29\n"
"v_mfma_f32_16x16x32_bf16 a[72:75], v[200:203], v[120:123], a[72:75]\n"
"v_exp_f32 v236, v236\n"
"v_mfma_f32_16x16x32_bf16 a[72:75], v[204:207], v[136:139], a[72:75]\n"
"v_exp_f32 v237, v237\n"
"v_mfma_f32_16x16x32_bf16 v[220:223], v[168:171], a[232:235], 0\n"
"v_exp_f32 v238, v238\n"
"v_exp_f32 v239, v239\n"
"v_mfma_f32_16x16x32_bf16 v[220:223], v[172:175], a[236:239], v[220:223]\n"
"v_cvt_pk_bf16_f32 v86, v236, v237\n"
"v_cvt_pk_bf16_f32 v87, v238, v239\n"
"v_mfma_f32_16x16x32_bf16 v[220:223], v[176:179], a[240:243], v[220:223]\n"
"ds_read_b128 v[168:171], v16 offset:43008\n"
"v_cvt_pk_bf16_f32 v216, 0, v216\n"
"v_mfma_f32_16x16x32_bf16 v[220:223], v[180:183], a[244:247], v[220:223]\n"
"ds_read_b128 v[172:175], v17 offset:43008\n"
"v_cvt_pk_bf16_f32 v217, 0, v217\n"
"v_mfma_f32_16x16x32_bf16 v[220:223], v[184:187], a[248:251], v[220:223]\n"
"ds_read_b128 v[176:179], v16 offset:44032\n"
"v_cvt_pk_bf16_f32 v218, 0, v218\n"
"v_mfma_f32_16x16x32_bf16 v[220:223], v[188:191], a[252:255], v[220:223]\n"
"s_waitcnt lgkmcnt(10)\n"
"v_cvt_pk_bf16_f32 v219, 0, v219\n"
"v_mul_f32 v216, s85, v216\n"
"v_mfma_f32_16x16x32_bf16 a[76:79], v[200:203], v[124:127], a[76:79]\n"
"ds_read_b128 v[180:183], v17 offset:44032\n"
"v_mul_f32 v217, s85, v217\n"
"v_mfma_f32_16x16x32_bf16 a[76:79], v[204:207], v[140:143], a[76:79]\n"
"ds_read_b64_tr_b16 v[200:201], v23 offset:1024\n"
"v_mul_f32 v218, s85, v218\n"
"v_mfma_f32_16x16x32_bf16 a[80:83], v[208:211], v[112:115], a[80:83]\n"
"ds_read_b64_tr_b16 v[202:203], v23 offset:7168\n"
"v_mul_f32 v219, s85, v219\n"
"v_mfma_f32_16x16x32_bf16 a[80:83], v[212:215], v[128:131], a[80:83]\n"
"s_waitcnt lgkmcnt(10)\n"
"v_cvt_pk_bf16_f32 v216, 0, v216\n"
"v_cvt_pk_bf16_f32 v217, 0, v217\n"
"v_mfma_f32_16x16x32_bf16 a[84:87], v[208:211], v[116:119], a[84:87]\n"
"v_cvt_pk_bf16_f32 v218, 0, v218\n"
"v_cvt_pk_bf16_f32 v219, 0, v219\n"
"v_mfma_f32_16x16x32_bf16 a[84:87], v[212:215], v[132:135], a[84:87]\n"
"v_fma_f32 v216, v216, s86, -v30\n"
"v_fma_f32 v217, v217, s86, -v30\n"
"v_fma_f32 v218, v218, s86, -v30\n"
"v_fma_f32 v219, v219, s86, -v30\n"
"v_mfma_f32_16x16x32_bf16 v[224:227], v[144:147], v[32:35], 0\n"
"v_exp_f32 v216, v216\n"
"v_exp_f32 v217, v217\n"
"v_exp_f32 v218, v218\n"
"v_exp_f32 v219, v219\n"
"v_mfma_f32_16x16x32_bf16 v[224:227], v[148:151], v[36:39], v[224:227]\n"
"v_cvt_pk_bf16_f32 v90, v216, v217\n"
"v_cvt_pk_bf16_f32 v91, v218, v219\n"
"v_mfma_f32_16x16x32_bf16 v[224:227], v[152:155], v[40:43], v[224:227]\n"
"ds_read_b64_tr_b16 v[204:205], v23 offset:13312\n"
"v_cvt_pk_bf16_f32 v220, 0, v220\n"
"v_mfma_f32_16x16x32_bf16 v[224:227], v[156:159], v[44:47], v[224:227]\n"
"ds_read_b64_tr_b16 v[206:207], v23 offset:19456\n"
"v_cvt_pk_bf16_f32 v221, 0, v221\n"
"v_mfma_f32_16x16x32_bf16 v[224:227], v[160:163], v[48:51], v[224:227]\n"
"ds_read_b128 v[184:187], v16 offset:45056\n"
"v_cvt_pk_bf16_f32 v222, 0, v222\n"
"v_mfma_f32_16x16x32_bf16 v[224:227], v[164:167], v[52:55], v[224:227]\n"
"ds_read_b128 v[188:191], v17 offset:45056\n"
"v_cvt_pk_bf16_f32 v223, 0, v223\n"
"v_mfma_f32_16x16x32_bf16 a[88:91], v[208:211], v[120:123], a[88:91]\n"
"v_mul_f32 v220, s85, v220\n"
"v_mul_f32 v221, s85, v221\n"
"v_mfma_f32_16x16x32_bf16 a[88:91], v[212:215], v[136:139], a[88:91]\n"
"s_waitcnt lgkmcnt(10)\n"
"v_mul_f32 v222, s85, v222\n"
"v_mul_f32 v223, s85, v223\n"
"v_mfma_f32_16x16x32_bf16 a[92:95], v[208:211], v[124:127], a[92:95]\n"
"v_cvt_pk_bf16_f32 v220, 0, v220\n"
"v_cvt_pk_bf16_f32 v221, 0, v221\n"
"v_mfma_f32_16x16x32_bf16 a[92:95], v[212:215], v[140:143], a[92:95]\n"
"ds_read_b64_tr_b16 v[208:209], v20 offset:2048\n"
"v_cvt_pk_bf16_f32 v222, 0, v222\n"
"v_mfma_f32_16x16x32_bf16 a[96:99], v[192:195], v[112:115], a[96:99]\n"
"v_cvt_pk_bf16_f32 v223, 0, v223\n"
"v_fma_f32 v220, v220, s86, -v31\n"
"v_mfma_f32_16x16x32_bf16 a[96:99], v[196:199], v[128:131], a[96:99]\n"
"v_fma_f32 v221, v221, s86, -v31\n"
"v_fma_f32 v222, v222, s86, -v31\n"
"v_fma_f32 v223, v223, s86, -v31\n"
"v_mfma_f32_16x16x32_bf16 a[100:103], v[192:195], v[116:119], a[100:103]\n"
"v_exp_f32 v220, v220\n"
"v_exp_f32 v221, v221\n"
"v_exp_f32 v222, v222\n"
"v_exp_f32 v223, v223\n"
"v_mfma_f32_16x16x32_bf16 a[100:103], v[196:199], v[132:135], a[100:103]\n"
"v_cvt_pk_bf16_f32 v94, v220, v221\n"
"v_cvt_pk_bf16_f32 v95, v222, v223\n"
"v_mfma_f32_16x16x32_bf16 v[228:231], v[144:147], v[56:59], 0\n"
"ds_read_b64_tr_b16 v[210:211], v20 offset:8192\n"
"v_cvt_pk_bf16_f32 v224, 0, v224\n"
"v_mfma_f32_16x16x32_bf16 v[228:231], v[148:151], v[60:63], v[228:231]\n"
"ds_read_b64_tr_b16 v[212:213], v20 offset:14336\n"
"v_cvt_pk_bf16_f32 v225, 0, v225\n"
"v_mfma_f32_16x16x32_bf16 v[228:231], v[152:155], v[64:67], v[228:231]\n"
"ds_read_b64_tr_b16 v[214:215], v20 offset:20480\n"
"v_cvt_pk_bf16_f32 v226, 0, v226\n"
"v_mfma_f32_16x16x32_bf16 v[228:231], v[156:159], v[68:71], v[228:231]\n"
"v_cvt_pk_bf16_f32 v227, 0, v227\n"
"v_mul_f32 v224, s85, v224\n"
"v_mfma_f32_16x16x32_bf16 v[228:231], v[160:163], v[72:75], v[228:231]\n"
"v_mul_f32 v225, s85, v225\n"
"v_mul_f32 v226, s85, v226\n"
"v_mfma_f32_16x16x32_bf16 v[228:231], v[164:167], v[76:79], v[228:231]\n"
"v_mul_f32 v227, s85, v227\n"
"v_cvt_pk_bf16_f32 v224, 0, v224\n"
"v_mfma_f32_16x16x32_bf16 a[104:107], v[192:195], v[120:123], a[104:107]\n"
"v_cvt_pk_bf16_f32 v225, 0, v225\n"
"v_cvt_pk_bf16_f32 v226, 0, v226\n"
"v_mfma_f32_16x16x32_bf16 a[104:107], v[196:199], v[136:139], a[104:107]\n"
"s_waitcnt lgkmcnt(6)\n"
"v_cvt_pk_bf16_f32 v227, 0, v227\n"
"v_fma_f32 v224, v224, s86, -v28\n"
"v_mfma_f32_16x16x32_bf16 a[108:111], v[192:195], v[124:127], a[108:111]\n"
"v_fma_f32 v225, v225, s86, -v28\n"
"v_fma_f32 v226, v226, s86, -v28\n"
"v_mfma_f32_16x16x32_bf16 a[108:111], v[196:199], v[140:143], a[108:111]\n"
"v_fma_f32 v227, v227, s86, -v28\n"
"v_cvt_pk_bf16_f32 v228, 0, v228\n"
"v_mfma_f32_16x16x32_bf16 a[112:115], v[200:203], v[112:115], a[112:115]\n"
"v_exp_f32 v224, v224\n"
"v_exp_f32 v225, v225\n"
"v_exp_f32 v226, v226\n"
"v_exp_f32 v227, v227\n"
"v_mfma_f32_16x16x32_bf16 a[112:115], v[204:207], v[128:131], a[112:115]\n"
"v_cvt_pk_bf16_f32 v96, v224, v225\n"
"v_cvt_pk_bf16_f32 v97, v226, v227\n"
"v_mfma_f32_16x16x32_bf16 v[232:235], v[144:147], a[208:211], 0\n"
"ds_read_b64_tr_b16 v[192:193], v21 offset:2048\n"
"v_cvt_pk_bf16_f32 v229, 0, v229\n"
"v_mfma_f32_16x16x32_bf16 v[232:235], v[148:151], a[212:215], v[232:235]\n"
"ds_read_b64_tr_b16 v[194:195], v21 offset:8192\n"
"v_cvt_pk_bf16_f32 v230, 0, v230\n"
"v_mfma_f32_16x16x32_bf16 v[232:235], v[152:155], a[216:219], v[232:235]\n"
"ds_read_b64_tr_b16 v[196:197], v21 offset:14336\n"
"v_cvt_pk_bf16_f32 v231, 0, v231\n"
"v_mfma_f32_16x16x32_bf16 v[232:235], v[156:159], a[220:223], v[232:235]\n"
"ds_read_b64_tr_b16 v[198:199], v21 offset:20480\n"
"v_mul_f32 v228, s85, v228\n"
"v_mfma_f32_16x16x32_bf16 v[232:235], v[160:163], a[224:227], v[232:235]\n"
"v_mul_f32 v229, s85, v229\n"
"v_mul_f32 v230, s85, v230\n"
"v_mfma_f32_16x16x32_bf16 v[232:235], v[164:167], a[228:231], v[232:235]\n"
"v_mul_f32 v231, s85, v231\n"
"v_cvt_pk_bf16_f32 v228, 0, v228\n"
"v_mfma_f32_16x16x32_bf16 a[116:119], v[200:203], v[116:119], a[116:119]\n"
"v_cvt_pk_bf16_f32 v229, 0, v229\n"
"v_cvt_pk_bf16_f32 v230, 0, v230\n"
"v_mfma_f32_16x16x32_bf16 a[116:119], v[204:207], v[132:135], a[116:119]\n"
"v_cvt_pk_bf16_f32 v231, 0, v231\n"
"v_fma_f32 v228, v228, s86, -v29\n"
"v_mfma_f32_16x16x32_bf16 a[120:123], v[200:203], v[120:123], a[120:123]\n"
"v_fma_f32 v229, v229, s86, -v29\n"
"v_fma_f32 v230, v230, s86, -v29\n"
"v_mfma_f32_16x16x32_bf16 a[120:123], v[204:207], v[136:139], a[120:123]\n"
"v_fma_f32 v231, v231, s86, -v29\n"
"v_cvt_pk_bf16_f32 v232, 0, v232\n"
"v_mfma_f32_16x16x32_bf16 a[124:127], v[200:203], v[124:127], a[124:127]\n"
"v_exp_f32 v228, v228\n"
"v_mfma_f32_16x16x32_bf16 a[124:127], v[204:207], v[140:143], a[124:127]\n"
"v_exp_f32 v229, v229\n"
"v_mfma_f32_16x16x32_bf16 v[236:239], v[144:147], a[232:235], 0\n"
"v_exp_f32 v230, v230\n"
"v_exp_f32 v231, v231\n"
"v_mfma_f32_16x16x32_bf16 v[236:239], v[148:151], a[236:239], v[236:239]\n"
"v_cvt_pk_bf16_f32 v100, v228, v229\n"
"v_cvt_pk_bf16_f32 v101, v230, v231\n"
"v_mfma_f32_16x16x32_bf16 v[236:239], v[152:155], a[240:243], v[236:239]\n"
"ds_read_b64_tr_b16 v[200:201], v22 offset:2048\n"
"v_cvt_pk_bf16_f32 v233, 0, v233\n"
"v_mfma_f32_16x16x32_bf16 v[236:239], v[156:159], a[244:247], v[236:239]\n"
"s_waitcnt lgkmcnt(5)\n"
"v_cvt_pk_bf16_f32 v234, 0, v234\n"
"v_cvt_pk_bf16_f32 v235, 0, v235\n"
"v_mfma_f32_16x16x32_bf16 v[236:239], v[160:163], a[248:251], v[236:239]\n"
"ds_read_b64_tr_b16 v[202:203], v22 offset:8192\n"
"v_mul_f32 v232, s85, v232\n"
"v_mfma_f32_16x16x32_bf16 v[236:239], v[164:167], a[252:255], v[236:239]\n"
"ds_read_b64_tr_b16 v[204:205], v22 offset:14336\n"
"v_mul_f32 v233, s85, v233\n"
"v_mfma_f32_16x16x32_bf16 a[128:131], v[208:211], v[112:115], a[128:131]\n"
"ds_read_b64_tr_b16 v[206:207], v22 offset:20480\n"
"v_mul_f32 v234, s85, v234\n"
"v_mfma_f32_16x16x32_bf16 a[128:131], v[212:215], v[128:131], a[128:131]\n"
"v_mul_f32 v235, s85, v235\n"
"v_cvt_pk_bf16_f32 v232, 0, v232\n"
"v_mfma_f32_16x16x32_bf16 a[132:135], v[208:211], v[116:119], a[132:135]\n"
"v_cvt_pk_bf16_f32 v233, 0, v233\n"
"v_cvt_pk_bf16_f32 v234, 0, v234\n"
"v_mfma_f32_16x16x32_bf16 a[132:135], v[212:215], v[132:135], a[132:135]\n"
"v_cvt_pk_bf16_f32 v235, 0, v235\n"
"v_fma_f32 v232, v232, s86, -v30\n"
"v_mfma_f32_16x16x32_bf16 a[136:139], v[208:211], v[120:123], a[136:139]\n"
"v_fma_f32 v233, v233, s86, -v30\n"
"v_fma_f32 v234, v234, s86, -v30\n"
"v_mfma_f32_16x16x32_bf16 a[136:139], v[212:215], v[136:139], a[136:139]\n"
"v_fma_f32 v235, v235, s86, -v30\n"
"v_cvt_pk_bf16_f32 v236, 0, v236\n"
"v_mfma_f32_16x16x32_bf16 v[216:219], v[168:171], v[32:35], 0\n"
"v_exp_f32 v232, v232\n"
"v_exp_f32 v233, v233\n"
"v_exp_f32 v234, v234\n"
"v_exp_f32 v235, v235\n"
"v_mfma_f32_16x16x32_bf16 v[216:219], v[172:175], v[36:39], v[216:219]\n"
"v_cvt_pk_bf16_f32 v104, v232, v233\n"
"v_cvt_pk_bf16_f32 v105, v234, v235\n"
"v_mfma_f32_16x16x32_bf16 v[216:219], v[176:179], v[40:43], v[216:219]\n"
"v_cvt_pk_bf16_f32 v237, 0, v237\n"
"v_cvt_pk_bf16_f32 v238, 0, v238\n"
"v_mfma_f32_16x16x32_bf16 v[216:219], v[180:183], v[44:47], v[216:219]\n"
"v_cvt_pk_bf16_f32 v239, 0, v239\n"
"v_mul_f32 v236, s85, v236\n"
"v_mfma_f32_16x16x32_bf16 v[216:219], v[184:187], v[48:51], v[216:219]\n"
"v_mul_f32 v237, s85, v237\n"
"v_mul_f32 v238, s85, v238\n"
"v_mfma_f32_16x16x32_bf16 v[216:219], v[188:191], v[52:55], v[216:219]\n"
"s_waitcnt lgkmcnt(4)\n"
"v_mul_f32 v239, s85, v239\n"
"v_cvt_pk_bf16_f32 v236, 0, v236\n"
"v_mfma_f32_16x16x32_bf16 a[140:143], v[208:211], v[124:127], a[140:143]\n"
"v_cvt_pk_bf16_f32 v237, 0, v237\n"
"v_cvt_pk_bf16_f32 v238, 0, v238\n"
"v_mfma_f32_16x16x32_bf16 a[140:143], v[212:215], v[140:143], a[140:143]\n"
"ds_read_b64_tr_b16 v[208:209], v23 offset:2048\n"
"v_cvt_pk_bf16_f32 v239, 0, v239\n"
"v_mfma_f32_16x16x32_bf16 a[144:147], v[192:195], v[112:115], a[144:147]\n"
"ds_read_b64_tr_b16 v[210:211], v23 offset:8192\n"
"v_fma_f32 v236, v236, s86, -v31\n"
"v_mfma_f32_16x16x32_bf16 a[144:147], v[196:199], v[128:131], a[144:147]\n"
"ds_read_b64_tr_b16 v[212:213], v23 offset:14336\n"
"v_fma_f32 v237, v237, s86, -v31\n"
"v_mfma_f32_16x16x32_bf16 a[148:151], v[192:195], v[116:119], a[148:151]\n"
"s_waitcnt vmcnt(10)\n"
"s_barrier\n"
"s_add_u32 m0, s7, 81920\n"
"v_mfma_f32_16x16x32_bf16 a[148:151], v[196:199], v[132:135], a[148:151]\n"
"v_fma_f32 v238, v238, s86, -v31\n"
"v_fma_f32 v239, v239, s86, -v31\n"
"v_mfma_f32_16x16x32_bf16 a[152:155], v[192:195], v[120:123], a[152:155]\n"
"v_exp_f32 v236, v236\n"
"v_exp_f32 v237, v237\n"
"v_exp_f32 v238, v238\n"
"v_exp_f32 v239, v239\n"
"v_mfma_f32_16x16x32_bf16 a[152:155], v[196:199], v[136:139], a[152:155]\n"
"v_cvt_pk_bf16_f32 v108, v236, v237\n"
"v_cvt_pk_bf16_f32 v109, v238, v239\n"
"v_mfma_f32_16x16x32_bf16 v[220:223], v[168:171], v[56:59], 0\n"
"buffer_load_dwordx4 v1, s[36:39], s9 offen offset:0 sc1 lds\n"
"v_mfma_f32_16x16x32_bf16 v[220:223], v[172:175], v[60:63], v[220:223]\n"
"ds_read_b64_tr_b16 v[214:215], v23 offset:20480\n"
"v_cvt_pk_bf16_f32 v216, 0, v216\n"
"v_mfma_f32_16x16x32_bf16 v[220:223], v[176:179], v[64:67], v[220:223]\n"
"buffer_load_dwordx4 v1, s[36:39], s9 offen offset:1024 sc1 lds\n"
"v_mfma_f32_16x16x32_bf16 v[220:223], v[180:183], v[68:71], v[220:223]\n"
"ds_read_b128 v[144:147], v18 offset:0\n"
"v_cvt_pk_bf16_f32 v217, 0, v217\n"
"v_mfma_f32_16x16x32_bf16 v[220:223], v[184:187], v[72:75], v[220:223]\n"
"ds_read_b128 v[148:151], v19 offset:0\n"
"v_cvt_pk_bf16_f32 v218, 0, v218\n"
"v_mfma_f32_16x16x32_bf16 v[220:223], v[188:191], v[76:79], v[220:223]\n"
"buffer_load_dwordx4 v1, s[36:39], s9 offen offset:2048 sc1 lds\n"
"v_mfma_f32_16x16x32_bf16 a[156:159], v[192:195], v[124:127], a[156:159]\n"
"s_waitcnt lgkmcnt(6)\n"
"v_cvt_pk_bf16_f32 v219, 0, v219\n"
"v_mul_f32 v216, s85, v216\n"
"v_mfma_f32_16x16x32_bf16 a[156:159], v[196:199], v[140:143], a[156:159]\n"
"v_mul_f32 v217, s85, v217\n"
"v_mul_f32 v218, s85, v218\n"
"v_mul_f32 v219, s85, v219\n"
"v_mfma_f32_16x16x32_bf16 a[160:163], v[200:203], v[112:115], a[160:163]\n"
"v_cvt_pk_bf16_f32 v216, 0, v216\n"
"v_cvt_pk_bf16_f32 v217, 0, v217\n"
"v_cvt_pk_bf16_f32 v218, 0, v218\n"
"v_cvt_pk_bf16_f32 v219, 0, v219\n"
"v_mfma_f32_16x16x32_bf16 a[160:163], v[204:207], v[128:131], a[160:163]\n"
"v_fma_f32 v216, v216, s86, -v28\n"
"v_fma_f32 v217, v217, s86, -v28\n"
"v_fma_f32 v218, v218, s86, -v28\n"
"v_fma_f32 v219, v219, s86, -v28\n"
"v_mfma_f32_16x16x32_bf16 a[164:167], v[200:203], v[116:119], a[164:167]\n"
"v_exp_f32 v216, v216\n"
"v_exp_f32 v217, v217\n"
"v_exp_f32 v218, v218\n"
"v_exp_f32 v219, v219\n"
"v_mfma_f32_16x16x32_bf16 a[164:167], v[204:207], v[132:135], a[164:167]\n"
"v_cvt_pk_bf16_f32 v98, v216, v217\n"
"v_cvt_pk_bf16_f32 v99, v218, v219\n"
"v_mfma_f32_16x16x32_bf16 v[224:227], v[168:171], a[208:211], 0\n"
"buffer_load_dwordx4 v1, s[36:39], s9 offen offset:3072 sc1 lds\n"
"v_mfma_f32_16x16x32_bf16 v[224:227], v[172:175], a[212:215], v[224:227]\n"
"s_add_u32 m0, s7, 86016\n"
"v_cvt_pk_bf16_f32 v220, 0, v220\n"
"v_cvt_pk_bf16_f32 v221, 0, v221\n"
"v_mfma_f32_16x16x32_bf16 v[224:227], v[176:179], a[216:219], v[224:227]\n"
"buffer_load_dwordx4 v4, s[36:39], s9 offen sc1 lds\n"
"v_mfma_f32_16x16x32_bf16 v[224:227], v[180:183], a[220:223], v[224:227]\n"
"s_add_u32 s9, s9, 81920\n"
"s_add_u32 m0, s7, 102400\n"
"v_cvt_pk_bf16_f32 v222, 0, v222\n"
"v_mfma_f32_16x16x32_bf16 v[224:227], v[184:187], a[224:227], v[224:227]\n"
"ds_read_b128 v[152:155], v18 offset:1024\n"
"v_cvt_pk_bf16_f32 v223, 0, v223\n"
"v_mfma_f32_16x16x32_bf16 v[224:227], v[188:191], a[228:231], v[224:227]\n"
"buffer_load_dwordx4 v1, s[36:39], s9 offen offset:0 sc1 lds\n"
"v_mfma_f32_16x16x32_bf16 a[168:171], v[200:203], v[120:123], a[168:171]\n"
"ds_read_b128 v[156:159], v19 offset:1024\n"
"v_mul_f32 v220, s85, v220\n"
"v_mfma_f32_16x16x32_bf16 a[168:171], v[204:207], v[136:139], a[168:171]\n"
"s_waitcnt lgkmcnt(4)\n"
"v_mul_f32 v221, s85, v221\n"
"v_mul_f32 v222, s85, v222\n"
"v_mfma_f32_16x16x32_bf16 a[172:175], v[200:203], v[124:127], a[172:175]\n"
"buffer_load_dwordx4 v1, s[36:39], s9 offen offset:1024 sc1 lds\n"
"v_mfma_f32_16x16x32_bf16 a[172:175], v[204:207], v[140:143], a[172:175]\n"
"v_mul_f32 v223, s85, v223\n"
"v_cvt_pk_bf16_f32 v220, 0, v220\n"
"v_mfma_f32_16x16x32_bf16 a[176:179], v[208:211], v[112:115], a[176:179]\n"
"v_cvt_pk_bf16_f32 v221, 0, v221\n"
"v_cvt_pk_bf16_f32 v222, 0, v222\n"
"v_cvt_pk_bf16_f32 v223, 0, v223\n"
"v_mfma_f32_16x16x32_bf16 a[176:179], v[212:215], v[128:131], a[176:179]\n"
"v_fma_f32 v220, v220, s86, -v29\n"
"v_fma_f32 v221, v221, s86, -v29\n"
"v_fma_f32 v222, v222, s86, -v29\n"
"v_fma_f32 v223, v223, s86, -v29\n"
"v_mfma_f32_16x16x32_bf16 v[228:231], v[168:171], a[232:235], 0\n"
"v_exp_f32 v220, v220\n"
"v_exp_f32 v221, v221\n"
"v_exp_f32 v222, v222\n"
"v_exp_f32 v223, v223\n"
"v_mfma_f32_16x16x32_bf16 v[228:231], v[172:175], a[236:239], v[228:231]\n"
"v_cvt_pk_bf16_f32 v102, v220, v221\n"
"v_cvt_pk_bf16_f32 v103, v222, v223\n"
"v_mfma_f32_16x16x32_bf16 v[228:231], v[176:179], a[240:243], v[228:231]\n"
"buffer_load_dwordx4 v1, s[36:39], s9 offen offset:2048 sc1 lds\n"
"v_mfma_f32_16x16x32_bf16 v[228:231], v[180:183], a[244:247], v[228:231]\n"
"ds_read_b128 v[160:163], v18 offset:2048\n"
"v_cvt_pk_bf16_f32 v224, 0, v224\n"
"v_mfma_f32_16x16x32_bf16 v[228:231], v[184:187], a[248:251], v[228:231]\n"
"buffer_load_dwordx4 v1, s[36:39], s9 offen offset:3072 sc1 lds\n"
"v_mfma_f32_16x16x32_bf16 v[228:231], v[188:191], a[252:255], v[228:231]\n"
"ds_read_b128 v[164:167], v19 offset:2048\n"
"s_add_u32 m0, s7, 106496\n"
"v_mfma_f32_16x16x32_bf16 a[180:183], v[208:211], v[116:119], a[180:183]\n"
"ds_read_b64_tr_b16 v[192:193], v20 offset:24576\n"
"v_cvt_pk_bf16_f32 v225, 0, v225\n"
"v_mfma_f32_16x16x32_bf16 a[180:183], v[212:215], v[132:135], a[180:183]\n"
"buffer_load_dwordx4 v4, s[36:39], s9 offen sc1 lds\n"
"v_mfma_f32_16x16x32_bf16 a[184:187], v[208:211], v[120:123], a[184:187]\n"
"s_add_u32 s9, s9, 81920\n"
"v_cvt_pk_bf16_f32 v226, 0, v226\n"
"v_cvt_pk_bf16_f32 v227, 0, v227\n"
"v_mfma_f32_16x16x32_bf16 a[184:187], v[212:215], v[136:139], a[184:187]\n"
"v_mul_f32 v224, s85, v224\n"
"v_mul_f32 v225, s85, v225\n"
"v_mul_f32 v226, s85, v226\n"
"v_mul_f32 v227, s85, v227\n"
"v_mfma_f32_16x16x32_bf16 a[188:191], v[208:211], v[124:127], a[188:191]\n"
"v_cvt_pk_bf16_f32 v224, 0, v224\n"
"v_cvt_pk_bf16_f32 v225, 0, v225\n"
"v_cvt_pk_bf16_f32 v226, 0, v226\n"
"v_cvt_pk_bf16_f32 v227, 0, v227\n"
"v_mfma_f32_16x16x32_bf16 a[188:191], v[212:215], v[140:143], a[188:191]\n"
"s_waitcnt lgkmcnt(1)\n"
"v_fma_f32 v224, v224, s86, -v30\n"
"v_fma_f32 v225, v225, s86, -v30\n"
"v_fma_f32 v226, v226, s86, -v30\n"
"v_fma_f32 v227, v227, s86, -v30\n"
"v_mfma_f32_16x16x32_bf16 v[232:235], v[144:147], v[32:35], 0\n"
"v_exp_f32 v224, v224\n"
"v_exp_f32 v225, v225\n"
"v_exp_f32 v226, v226\n"
"v_exp_f32 v227, v227\n"
"v_mfma_f32_16x16x32_bf16 v[232:235], v[148:151], v[36:39], v[232:235]\n"
"v_cvt_pk_bf16_f32 v106, v224, v225\n"
"v_cvt_pk_bf16_f32 v107, v226, v227\n"
"v_mfma_f32_16x16x32_bf16 v[232:235], v[152:155], v[40:43], v[232:235]\n"
"ds_read_b64_tr_b16 v[194:195], v20 offset:30720\n"
"v_cvt_pk_bf16_f32 v228, 0, v228\n"
"v_mfma_f32_16x16x32_bf16 v[232:235], v[156:159], v[44:47], v[232:235]\n"
"ds_read_b64_tr_b16 v[196:197], v20 offset:36864\n"
"v_cvt_pk_bf16_f32 v229, 0, v229\n"
"v_mfma_f32_16x16x32_bf16 v[232:235], v[160:163], v[48:51], v[232:235]\n"
"ds_read_b64_tr_b16 v[198:199], v20 offset:43008\n"
"v_cvt_pk_bf16_f32 v230, 0, v230\n"
"v_mfma_f32_16x16x32_bf16 v[232:235], v[164:167], v[52:55], v[232:235]\n"
"ds_read_b64_tr_b16 v[200:201], v21 offset:24576\n"
"v_cvt_pk_bf16_f32 v231, 0, v231\n"
"v_mfma_f32_16x16x32_bf16 a[192:195], v[240:243], v[112:115], a[192:195]\n"
"ds_read_b64_tr_b16 v[202:203], v21 offset:30720\n"
"v_mul_f32 v228, s85, v228\n"
"v_mfma_f32_16x16x32_bf16 a[192:195], v[240:243], v[128:131], a[192:195]\n"
"ds_read_b64_tr_b16 v[204:205], v21 offset:36864\n"
"v_mul_f32 v229, s85, v229\n"
"v_mfma_f32_16x16x32_bf16 a[196:199], v[240:243], v[116:119], a[196:199]\n"
"ds_read_b64_tr_b16 v[206:207], v21 offset:43008\n"
"v_mul_f32 v230, s85, v230\n"
"v_mfma_f32_16x16x32_bf16 a[196:199], v[240:243], v[132:135], a[196:199]\n"
"v_mul_f32 v231, s85, v231\n"
"v_cvt_pk_bf16_f32 v228, 0, v228\n"
"v_mfma_f32_16x16x32_bf16 a[200:203], v[240:243], v[120:123], a[200:203]\n"
"v_cvt_pk_bf16_f32 v229, 0, v229\n"
"v_cvt_pk_bf16_f32 v230, 0, v230\n"
"v_cvt_pk_bf16_f32 v231, 0, v231\n"
"v_mfma_f32_16x16x32_bf16 a[200:203], v[240:243], v[136:139], a[200:203]\n"
"v_fma_f32 v228, v228, s86, -v31\n"
"v_fma_f32 v229, v229, s86, -v31\n"
"v_fma_f32 v230, v230, s86, -v31\n"
"v_fma_f32 v231, v231, s86, -v31\n"
"v_mfma_f32_16x16x32_bf16 a[204:207], v[240:243], v[124:127], a[204:207]\n"
"v_exp_f32 v228, v228\n"
"v_exp_f32 v229, v229\n"
"v_exp_f32 v230, v230\n"
"v_exp_f32 v231, v231\n"
"v_mfma_f32_16x16x32_bf16 a[204:207], v[240:243], v[140:143], a[204:207]\n"
"v_cvt_pk_bf16_f32 v110, v228, v229\n"
"v_cvt_pk_bf16_f32 v111, v230, v231\n"
"v_mfma_f32_16x16x32_bf16 v[236:239], v[144:147], v[56:59], 0\n"
"ds_read_b64_tr_b16 v[208:209], v22 offset:24576\n"
"v_cvt_pk_bf16_f32 v232, 0, v232\n"
"v_mfma_f32_16x16x32_bf16 v[236:239], v[148:151], v[60:63], v[236:239]\n"
"ds_read_b64_tr_b16 v[210:211], v22 offset:30720\n"
"v_cvt_pk_bf16_f32 v233, 0, v233\n"
"v_mfma_f32_16x16x32_bf16 v[236:239], v[152:155], v[64:67], v[236:239]\n"
"ds_read_b64_tr_b16 v[212:213], v22 offset:36864\n"
"v_cvt_pk_bf16_f32 v234, 0, v234\n"
"v_mfma_f32_16x16x32_bf16 v[236:239], v[156:159], v[68:71], v[236:239]\n"
"s_waitcnt lgkmcnt(7)\n"
"v_cvt_pk_bf16_f32 v235, 0, v235\n"
"v_mul_f32 v232, s85, v232\n"
"v_mfma_f32_16x16x32_bf16 v[236:239], v[160:163], v[72:75], v[236:239]\n"
"ds_read_b64_tr_b16 v[214:215], v22 offset:43008\n"
"v_mul_f32 v233, s85, v233\n"
"v_mfma_f32_16x16x32_bf16 v[236:239], v[164:167], v[76:79], v[236:239]\n"
"ds_read_b128 v[168:171], v18 offset:6144\n"
"v_mul_f32 v234, s85, v234\n"
"v_mfma_f32_16x16x32_bf16 a[0:3], v[192:195], v[80:83], a[0:3]\n"
"ds_read_b128 v[172:175], v19 offset:6144\n"
"v_mul_f32 v235, s85, v235\n"
"v_mfma_f32_16x16x32_bf16 a[0:3], v[196:199], v[96:99], a[0:3]\n"
"ds_read_b128 v[176:179], v18 offset:7168\n"
"v_cvt_pk_bf16_f32 v232, 0, v232\n"
"v_mfma_f32_16x16x32_bf16 a[4:7], v[192:195], v[84:87], a[4:7]\n"
"s_barrier\n"
"v_cvt_pk_bf16_f32 v233, 0, v233\n"
"v_cvt_pk_bf16_f32 v234, 0, v234\n"
"v_cvt_pk_bf16_f32 v235, 0, v235\n"
"v_mfma_f32_16x16x32_bf16 a[4:7], v[196:199], v[100:103], a[4:7]\n"
"s_add_u32 m0, s7, 0\n"
"v_fma_f32 v232, v232, s86, -v28\n"
"v_fma_f32 v233, v233, s86, -v28\n"
"v_fma_f32 v234, v234, s86, -v28\n"
"v_fma_f32 v235, v235, s86, -v28\n"
"v_mfma_f32_16x16x32_bf16 a[8:11], v[192:195], v[88:91], a[8:11]\n"
"v_exp_f32 v232, v232\n"
"v_exp_f32 v233, v233\n"
"v_exp_f32 v234, v234\n"
"v_exp_f32 v235, v235\n"
"v_mfma_f32_16x16x32_bf16 a[8:11], v[196:199], v[104:107], a[8:11]\n"
"v_cvt_pk_bf16_f32 v112, v232, v233\n"
"v_cvt_pk_bf16_f32 v113, v234, v235\n"
"v_mfma_f32_16x16x32_bf16 v[216:219], v[144:147], a[208:211], 0\n"
"buffer_load_dwordx4 v1, s[36:39], s9 offen offset:0 sc1 lds\n"
"v_mfma_f32_16x16x32_bf16 v[216:219], v[148:151], a[212:215], v[216:219]\n"
"ds_read_b128 v[180:183], v19 offset:7168\n"
"v_cvt_pk_bf16_f32 v236, 0, v236\n"
"v_mfma_f32_16x16x32_bf16 v[216:219], v[152:155], a[216:219], v[216:219]\n"
"buffer_load_dwordx4 v1, s[36:39], s9 offen offset:1024 sc1 lds\n"
"v_mfma_f32_16x16x32_bf16 v[216:219], v[156:159], a[220:223], v[216:219]\n"
"ds_read_b128 v[184:187], v18 offset:8192\n"
"v_cvt_pk_bf16_f32 v237, 0, v237\n"
"v_mfma_f32_16x16x32_bf16 v[216:219], v[160:163], a[224:227], v[216:219]\n"
"ds_read_b128 v[188:191], v19 offset:8192\n"
"v_cvt_pk_bf16_f32 v238, 0, v238\n"
"v_mfma_f32_16x16x32_bf16 v[216:219], v[164:167], a[228:231], v[216:219]\n"
"buffer_load_dwordx4 v1, s[36:39], s9 offen offset:2048 sc1 lds\n"
"v_mfma_f32_16x16x32_bf16 a[12:15], v[192:195], v[92:95], a[12:15]\n"
"s_waitcnt lgkmcnt(10)\n"
"v_cvt_pk_bf16_f32 v239, 0, v239\n"
"v_mul_f32 v236, s85, v236\n"
"v_mfma_f32_16x16x32_bf16 a[12:15], v[196:199], v[108:111], a[12:15]\n"
"ds_read_b64_tr_b16 v[192:193], v23 offset:24576\n"
"v_mul_f32 v237, s85, v237\n"
"v_mfma_f32_16x16x32_bf16 a[16:19], v[200:203], v[80:83], a[16:19]\n"
"buffer_load_dwordx4 v1, s[36:39], s9 offen offset:3072 sc1 lds\n"
"v_mfma_f32_16x16x32_bf16 a[16:19], v[204:207], v[96:99], a[16:19]\n"
"s_add_u32 m0, s7, 4096\n"
"v_mul_f32 v238, s85, v238\n"
"v_mul_f32 v239, s85, v239\n"
"v_mfma_f32_16x16x32_bf16 a[20:23], v[200:203], v[84:87], a[20:23]\n"
"v_cvt_pk_bf16_f32 v236, 0, v236\n"
"v_cvt_pk_bf16_f32 v237, 0, v237\n"
"v_cvt_pk_bf16_f32 v238, 0, v238\n"
"v_cvt_pk_bf16_f32 v239, 0, v239\n"
"v_mfma_f32_16x16x32_bf16 a[20:23], v[204:207], v[100:103], a[20:23]\n"
"v_fma_f32 v236, v236, s86, -v29\n"
"v_fma_f32 v237, v237, s86, -v29\n"
"v_fma_f32 v238, v238, s86, -v29\n"
"v_fma_f32 v239, v239, s86, -v29\n"
"v_mfma_f32_16x16x32_bf16 v[220:223], v[144:147], a[232:235], 0\n"
"v_exp_f32 v236, v236\n"
"v_exp_f32 v237, v237\n"
"v_exp_f32 v238, v238\n"
"v_exp_f32 v239, v239\n"
"v_mfma_f32_16x16x32_bf16 v[220:223], v[148:151], a[236:239], v[220:223]\n"
"v_cvt_pk_bf16_f32 v116, v236, v237\n"
"v_cvt_pk_bf16_f32 v117, v238, v239\n"
"v_mfma_f32_16x16x32_bf16 v[220:223], v[152:155], a[240:243], v[220:223]\n"
"buffer_load_dwordx4 v4, s[36:39], s9 offen sc1 lds\n"
"v_mfma_f32_16x16x32_bf16 v[220:223], v[156:159], a[244:247], v[220:223]\n"
"s_add_u32 s9, s9, 81920\n"
"v_cvt_pk_bf16_f32 v216, 0, v216\n"
"v_cvt_pk_bf16_f32 v217, 0, v217\n"
"v_mfma_f32_16x16x32_bf16 v[220:223], v[160:163], a[248:251], v[220:223]\n"
"ds_read_b64_tr_b16 v[194:195], v23 offset:30720\n"
"v_cvt_pk_bf16_f32 v218, 0, v218\n"
"v_mfma_f32_16x16x32_bf16 v[220:223], v[164:167], a[252:255], v[220:223]\n"
"ds_read_b64_tr_b16 v[196:197], v23 offset:36864\n"
"v_cvt_pk_bf16_f32 v219, 0, v219\n"
"v_mfma_f32_16x16x32_bf16 a[24:27], v[200:203], v[88:91], a[24:27]\n"
"ds_read_b64_tr_b16 v[198:199], v23 offset:43008\n"
"v_mul_f32 v216, s85, v216\n"
"v_mfma_f32_16x16x32_bf16 a[24:27], v[204:207], v[104:107], a[24:27]\n"
"v_mul_f32 v217, s85, v217\n"
"v_mul_f32 v218, s85, v218\n"
"v_mfma_f32_16x16x32_bf16 a[28:31], v[200:203], v[92:95], a[28:31]\n"
"ds_read_b128 v[144:147], v18 offset:12288\n"
"v_mul_f32 v219, s85, v219\n"
"v_mfma_f32_16x16x32_bf16 a[28:31], v[204:207], v[108:111], a[28:31]\n"
"s_waitcnt lgkmcnt(5)\n"
"v_cvt_pk_bf16_f32 v216, 0, v216\n"
"v_cvt_pk_bf16_f32 v217, 0, v217\n"
"v_mfma_f32_16x16x32_bf16 a[32:35], v[208:211], v[80:83], a[32:35]\n"
"v_cvt_pk_bf16_f32 v218, 0, v218\n"
"v_cvt_pk_bf16_f32 v219, 0, v219\n"
"v_mfma_f32_16x16x32_bf16 a[32:35], v[212:215], v[96:99], a[32:35]\n"
"v_fma_f32 v216, v216, s86, -v30\n"
"v_fma_f32 v217, v217, s86, -v30\n"
"v_fma_f32 v218, v218, s86, -v30\n"
"v_fma_f32 v219, v219, s86, -v30\n"
"v_mfma_f32_16x16x32_bf16 v[224:227], v[168:171], v[32:35], 0\n"
"v_exp_f32 v216, v216\n"
"v_exp_f32 v217, v217\n"
"v_exp_f32 v218, v218\n"
"v_exp_f32 v219, v219\n"
"v_mfma_f32_16x16x32_bf16 v[224:227], v[172:175], v[36:39], v[224:227]\n"
"v_cvt_pk_bf16_f32 v120, v216, v217\n"
"v_cvt_pk_bf16_f32 v121, v218, v219\n"
"v_mfma_f32_16x16x32_bf16 v[224:227], v[176:179], v[40:43], v[224:227]\n"
"ds_read_b64_tr_b16 v[200:201], v20 offset:25600\n"
"v_cvt_pk_bf16_f32 v220, 0, v220\n"
"v_mfma_f32_16x16x32_bf16 v[224:227], v[180:183], v[44:47], v[224:227]\n"
"ds_read_b64_tr_b16 v[202:203], v20 offset:31744\n"
"v_cvt_pk_bf16_f32 v221, 0, v221\n"
"v_mfma_f32_16x16x32_bf16 v[224:227], v[184:187], v[48:51], v[224:227]\n"
"ds_read_b64_tr_b16 v[204:205], v20 offset:37888\n"
"v_cvt_pk_bf16_f32 v222, 0, v222\n"
"v_mfma_f32_16x16x32_bf16 v[224:227], v[188:191], v[52:55], v[224:227]\n"
"ds_read_b64_tr_b16 v[206:207], v20 offset:44032\n"
"v_cvt_pk_bf16_f32 v223, 0, v223\n"
"v_mfma_f32_16x16x32_bf16 a[36:39], v[208:211], v[84:87], a[36:39]\n"
"ds_read_b128 v[148:151], v19 offset:12288\n"
"v_mul_f32 v220, s85, v220\n"
"v_mfma_f32_16x16x32_bf16 a[36:39], v[212:215], v[100:103], a[36:39]\n"
"ds_read_b128 v[152:155], v18 offset:13312\n"
"v_mul_f32 v221, s85, v221\n"
"v_mfma_f32_16x16x32_bf16 a[40:43], v[208:211], v[88:91], a[40:43]\n"
"ds_read_b128 v[156:159], v19 offset:13312\n"
"v_mul_f32 v222, s85, v222\n"
"v_mfma_f32_16x16x32_bf16 a[40:43], v[212:215], v[104:107], a[40:43]\n"
"s_waitcnt lgkmcnt(8)\n"
"v_mul_f32 v223, s85, v223\n"
"v_cvt_pk_bf16_f32 v220, 0, v220\n"
"v_mfma_f32_16x16x32_bf16 a[44:47], v[208:211], v[92:95], a[44:47]\n"
"s_add_u32 vcc_hi, vcc_lo, 81920\n"
"buffer_load_dword v254, v0, s[56:59], vcc_hi idxen\n"
"v_cvt_pk_bf16_f32 v221, 0, v221\n"
"v_cvt_pk_bf16_f32 v222, 0, v222\n"
"v_cvt_pk_bf16_f32 v223, 0, v223\n"
"v_mfma_f32_16x16x32_bf16 a[44:47], v[212:215], v[108:111], a[44:47]\n"
"v_fma_f32 v220, v220, s86, -v31\n"
"v_fma_f32 v221, v221, s86, -v31\n"
"v_fma_f32 v222, v222, s86, -v31\n"
"v_fma_f32 v223, v223, s86, -v31\n"
"v_mfma_f32_16x16x32_bf16 a[48:51], v[192:195], v[80:83], a[48:51]\n"
"v_exp_f32 v220, v220\n"
"v_exp_f32 v221, v221\n"
"v_exp_f32 v222, v222\n"
"v_exp_f32 v223, v223\n"
"v_mfma_f32_16x16x32_bf16 a[48:51], v[196:199], v[96:99], a[48:51]\n"
"v_cvt_pk_bf16_f32 v124, v220, v221\n"
"v_cvt_pk_bf16_f32 v125, v222, v223\n"
"v_mfma_f32_16x16x32_bf16 v[228:231], v[168:171], v[56:59], 0\n"
"ds_read_b64_tr_b16 v[208:209], v21 offset:25600\n"
"v_cvt_pk_bf16_f32 v224, 0, v224\n"
"v_mfma_f32_16x16x32_bf16 v[228:231], v[172:175], v[60:63], v[228:231]\n"
"ds_read_b64_tr_b16 v[210:211], v21 offset:31744\n"
"v_cvt_pk_bf16_f32 v225, 0, v225\n"
"v_mfma_f32_16x16x32_bf16 v[228:231], v[176:179], v[64:67], v[228:231]\n"
"ds_read_b64_tr_b16 v[212:213], v21 offset:37888\n"
"v_cvt_pk_bf16_f32 v226, 0, v226\n"
"v_mfma_f32_16x16x32_bf16 v[228:231], v[180:183], v[68:71], v[228:231]\n"
"ds_read_b64_tr_b16 v[214:215], v21 offset:44032\n"
"v_cvt_pk_bf16_f32 v227, 0, v227\n"
"v_mfma_f32_16x16x32_bf16 v[228:231], v[184:187], v[72:75], v[228:231]\n"
"ds_read_b128 v[160:163], v18 offset:14336\n"
"v_mul_f32 v224, s85, v224\n"
"v_mfma_f32_16x16x32_bf16 v[228:231], v[188:191], v[76:79], v[228:231]\n"
"ds_read_b128 v[164:167], v19 offset:14336\n"
"v_mul_f32 v225, s85, v225\n"
"v_mfma_f32_16x16x32_bf16 a[52:55], v[192:195], v[84:87], a[52:55]\n"
"v_mul_f32 v226, s85, v226\n"
"v_mul_f32 v227, s85, v227\n"
"v_mfma_f32_16x16x32_bf16 a[52:55], v[196:199], v[100:103], a[52:55]\n"
"v_cvt_pk_bf16_f32 v224, 0, v224\n"
"v_cvt_pk_bf16_f32 v225, 0, v225\n"
"v_mfma_f32_16x16x32_bf16 a[56:59], v[192:195], v[88:91], a[56:59]\n"
"v_cvt_pk_bf16_f32 v226, 0, v226\n"
"v_cvt_pk_bf16_f32 v227, 0, v227\n"
"v_mfma_f32_16x16x32_bf16 a[56:59], v[196:199], v[104:107], a[56:59]\n"
"v_fma_f32 v224, v224, s86, -v28\n"
"v_fma_f32 v225, v225, s86, -v28\n"
"v_fma_f32 v226, v226, s86, -v28\n"
"v_fma_f32 v227, v227, s86, -v28\n"
"v_mfma_f32_16x16x32_bf16 a[60:63], v[192:195], v[92:95], a[60:63]\n"
"v_exp_f32 v224, v224\n"
"v_exp_f32 v225, v225\n"
"v_exp_f32 v226, v226\n"
"v_exp_f32 v227, v227\n"
"v_mfma_f32_16x16x32_bf16 a[60:63], v[196:199], v[108:111], a[60:63]\n"
"v_cvt_pk_bf16_f32 v114, v224, v225\n"
"v_cvt_pk_bf16_f32 v115, v226, v227\n"
"v_mfma_f32_16x16x32_bf16 v[232:235], v[168:171], a[208:211], 0\n"
"ds_read_b64_tr_b16 v[192:193], v22 offset:25600\n"
"v_cvt_pk_bf16_f32 v228, 0, v228\n"
"v_mfma_f32_16x16x32_bf16 v[232:235], v[172:175], a[212:215], v[232:235]\n"
"ds_read_b64_tr_b16 v[194:195], v22 offset:31744\n"
"v_cvt_pk_bf16_f32 v229, 0, v229\n"
"v_mfma_f32_16x16x32_bf16 v[232:235], v[176:179], a[216:219], v[232:235]\n"
"ds_read_b64_tr_b16 v[196:197], v22 offset:37888\n"
"v_cvt_pk_bf16_f32 v230, 0, v230\n"
"v_mfma_f32_16x16x32_bf16 v[232:235], v[180:183], a[220:223], v[232:235]\n"
"s_waitcnt lgkmcnt(12)\n"
"v_cvt_pk_bf16_f32 v231, 0, v231\n"
"v_mul_f32 v228, s85, v228\n"
"v_mfma_f32_16x16x32_bf16 v[232:235], v[184:187], a[224:227], v[232:235]\n"
"ds_read_b64_tr_b16 v[198:199], v22 offset:44032\n"
"v_mul_f32 v229, s85, v229\n"
"v_mfma_f32_16x16x32_bf16 v[232:235], v[188:191], a[228:231], v[232:235]\n"
"v_mul_f32 v230, s85, v230\n"
"v_mul_f32 v231, s85, v231\n"
"v_mfma_f32_16x16x32_bf16 a[64:67], v[200:203], v[80:83], a[64:67]\n"
"v_cvt_pk_bf16_f32 v228, 0, v228\n"
"v_cvt_pk_bf16_f32 v229, 0, v229\n"
"v_mfma_f32_16x16x32_bf16 a[64:67], v[204:207], v[96:99], a[64:67]\n"
"v_cvt_pk_bf16_f32 v230, 0, v230\n"
"v_cvt_pk_bf16_f32 v231, 0, v231\n"
"v_mfma_f32_16x16x32_bf16 a[68:71], v[200:203], v[84:87], a[68:71]\n"
"v_fma_f32 v228, v228, s86, -v29\n"
"v_fma_f32 v229, v229, s86, -v29\n"
"v_mfma_f32_16x16x32_bf16 a[68:71], v[204:207], v[100:103], a[68:71]\n"
"v_fma_f32 v230, v230, s86, -v29\n"
"v_fma_f32 v231, v231, s86, -v29\n"
"v_mfma_f32_16x16x32_bf16 a[72:75], v[200:203], v[88:91], a[72:75]\n"
"v_exp_f32 v228, v228\n"
"v_mfma_f32_16x16x32_bf16 a[72:75], v[204:207], v[104:107], a[72:75]\n"
"v_exp_f32 v229, v229\n"
"v_mfma_f32_16x16x32_bf16 v[236:239], v[168:171], a[232:235], 0\n"
"v_exp_f32 v230, v230\n"
"v_exp_f32 v231, v231\n"
"v_mfma_f32_16x16x32_bf16 v[236:239], v[172:175], a[236:239], v[236:239]\n"
"v_cvt_pk_bf16_f32 v118, v228, v229\n"
"v_cvt_pk_bf16_f32 v119, v230, v231\n"
"v_mfma_f32_16x16x32_bf16 v[236:239], v[176:179], a[240:243], v[236:239]\n"
"ds_read_b128 v[168:171], v18 offset:18432\n"
"v_cvt_pk_bf16_f32 v232, 0, v232\n"
"v_mfma_f32_16x16x32_bf16 v[236:239], v[180:183], a[244:247], v[236:239]\n"
"ds_read_b128 v[172:175], v19 offset:18432\n"
"v_cvt_pk_bf16_f32 v233, 0, v233\n"
"v_mfma_f32_16x16x32_bf16 v[236:239], v[184:187], a[248:251], v[236:239]\n"
"ds_read_b128 v[176:179], v18 offset:19456\n"
"v_cvt_pk_bf16_f32 v234, 0, v234\n"
"v_mfma_f32_16x16x32_bf16 v[236:239], v[188:191], a[252:255], v[236:239]\n"
"s_waitcnt lgkmcnt(9)\n"
"v_cvt_pk_bf16_f32 v235, 0, v235\n"
"v_mul_f32 v232, s85, v232\n"
"v_mfma_f32_16x16x32_bf16 a[76:79], v[200:203], v[92:95], a[76:79]\n"
"ds_read_b128 v[180:183], v19 offset:19456\n"
"v_mul_f32 v233, s85, v233\n"
"v_mfma_f32_16x16x32_bf16 a[76:79], v[204:207], v[108:111], a[76:79]\n"
"ds_read_b64_tr_b16 v[200:201], v23 offset:25600\n"
"v_mul_f32 v234, s85, v234\n"
"v_mfma_f32_16x16x32_bf16 a[80:83], v[208:211], v[80:83], a[80:83]\n"
"ds_read_b64_tr_b16 v[202:203], v23 offset:31744\n"
"v_mul_f32 v235, s85, v235\n"
"v_mfma_f32_16x16x32_bf16 a[80:83], v[212:215], v[96:99], a[80:83]\n"
"s_waitcnt lgkmcnt(10)\n"
"v_cvt_pk_bf16_f32 v232, 0, v232\n"
"v_cvt_pk_bf16_f32 v233, 0, v233\n"
"v_mfma_f32_16x16x32_bf16 a[84:87], v[208:211], v[84:87], a[84:87]\n"
"v_cvt_pk_bf16_f32 v234, 0, v234\n"
"v_cvt_pk_bf16_f32 v235, 0, v235\n"
"v_mfma_f32_16x16x32_bf16 a[84:87], v[212:215], v[100:103], a[84:87]\n"
"v_fma_f32 v232, v232, s86, -v30\n"
"v_fma_f32 v233, v233, s86, -v30\n"
"v_fma_f32 v234, v234, s86, -v30\n"
"v_fma_f32 v235, v235, s86, -v30\n"
"v_mfma_f32_16x16x32_bf16 v[216:219], v[144:147], v[32:35], 0\n"
"v_exp_f32 v232, v232\n"
"v_exp_f32 v233, v233\n"
"v_exp_f32 v234, v234\n"
"v_exp_f32 v235, v235\n"
"v_mfma_f32_16x16x32_bf16 v[216:219], v[148:151], v[36:39], v[216:219]\n"
"v_cvt_pk_bf16_f32 v122, v232, v233\n"
"v_cvt_pk_bf16_f32 v123, v234, v235\n"
"v_mfma_f32_16x16x32_bf16 v[216:219], v[152:155], v[40:43], v[216:219]\n"
"ds_read_b64_tr_b16 v[204:205], v23 offset:37888\n"
"v_cvt_pk_bf16_f32 v236, 0, v236\n"
"v_mfma_f32_16x16x32_bf16 v[216:219], v[156:159], v[44:47], v[216:219]\n"
"ds_read_b64_tr_b16 v[206:207], v23 offset:44032\n"
"v_cvt_pk_bf16_f32 v237, 0, v237\n"
"v_mfma_f32_16x16x32_bf16 v[216:219], v[160:163], v[48:51], v[216:219]\n"
"ds_read_b128 v[184:187], v18 offset:20480\n"
"v_cvt_pk_bf16_f32 v238, 0, v238\n"
"v_mfma_f32_16x16x32_bf16 v[216:219], v[164:167], v[52:55], v[216:219]\n"
"ds_read_b128 v[188:191], v19 offset:20480\n"
"v_cvt_pk_bf16_f32 v239, 0, v239\n"
"v_mfma_f32_16x16x32_bf16 a[88:91], v[208:211], v[88:91], a[88:91]\n"
"v_mul_f32 v236, s85, v236\n"
"v_mul_f32 v237, s85, v237\n"
"v_mfma_f32_16x16x32_bf16 a[88:91], v[212:215], v[104:107], a[88:91]\n"
"s_waitcnt lgkmcnt(10)\n"
"v_mul_f32 v238, s85, v238\n"
"v_mul_f32 v239, s85, v239\n"
"v_mfma_f32_16x16x32_bf16 a[92:95], v[208:211], v[92:95], a[92:95]\n"
"v_cvt_pk_bf16_f32 v236, 0, v236\n"
"v_cvt_pk_bf16_f32 v237, 0, v237\n"
"v_mfma_f32_16x16x32_bf16 a[92:95], v[212:215], v[108:111], a[92:95]\n"
"ds_read_b64_tr_b16 v[208:209], v20 offset:26624\n"
"v_cvt_pk_bf16_f32 v238, 0, v238\n"
"v_mfma_f32_16x16x32_bf16 a[96:99], v[192:195], v[80:83], a[96:99]\n"
"v_cvt_pk_bf16_f32 v239, 0, v239\n"
"v_fma_f32 v236, v236, s86, -v31\n"
"v_mfma_f32_16x16x32_bf16 a[96:99], v[196:199], v[96:99], a[96:99]\n"
"v_fma_f32 v237, v237, s86, -v31\n"
"v_fma_f32 v238, v238, s86, -v31\n"
"v_fma_f32 v239, v239, s86, -v31\n"
"v_mfma_f32_16x16x32_bf16 a[100:103], v[192:195], v[84:87], a[100:103]\n"
"v_exp_f32 v236, v236\n"
"v_exp_f32 v237, v237\n"
"v_exp_f32 v238, v238\n"
"v_exp_f32 v239, v239\n"
"v_mfma_f32_16x16x32_bf16 a[100:103], v[196:199], v[100:103], a[100:103]\n"
"v_cvt_pk_bf16_f32 v126, v236, v237\n"
"v_cvt_pk_bf16_f32 v127, v238, v239\n"
"v_mfma_f32_16x16x32_bf16 v[220:223], v[144:147], v[56:59], 0\n"
"ds_read_b64_tr_b16 v[210:211], v20 offset:32768\n"
"v_cvt_pk_bf16_f32 v216, 0, v216\n"
"v_mfma_f32_16x16x32_bf16 v[220:223], v[148:151], v[60:63], v[220:223]\n"
"ds_read_b64_tr_b16 v[212:213], v20 offset:38912\n"
"v_cvt_pk_bf16_f32 v217, 0, v217\n"
"v_mfma_f32_16x16x32_bf16 v[220:223], v[152:155], v[64:67], v[220:223]\n"
"ds_read_b64_tr_b16 v[214:215], v20 offset:45056\n"
"v_cvt_pk_bf16_f32 v218, 0, v218\n"
"v_mfma_f32_16x16x32_bf16 v[220:223], v[156:159], v[68:71], v[220:223]\n"
"v_cvt_pk_bf16_f32 v219, 0, v219\n"
"v_mul_f32 v216, s85, v216\n"
"v_mfma_f32_16x16x32_bf16 v[220:223], v[160:163], v[72:75], v[220:223]\n"
"v_mul_f32 v217, s85, v217\n"
"v_mul_f32 v218, s85, v218\n"
"v_mfma_f32_16x16x32_bf16 v[220:223], v[164:167], v[76:79], v[220:223]\n"
"v_mul_f32 v219, s85, v219\n"
"v_cvt_pk_bf16_f32 v216, 0, v216\n"
"v_mfma_f32_16x16x32_bf16 a[104:107], v[192:195], v[88:91], a[104:107]\n"
"v_cvt_pk_bf16_f32 v217, 0, v217\n"
"v_cvt_pk_bf16_f32 v218, 0, v218\n"
"v_mfma_f32_16x16x32_bf16 a[104:107], v[196:199], v[104:107], a[104:107]\n"
"s_waitcnt lgkmcnt(6)\n"
"v_cvt_pk_bf16_f32 v219, 0, v219\n"
"v_fma_f32 v216, v216, s86, -v28\n"
"v_mfma_f32_16x16x32_bf16 a[108:111], v[192:195], v[92:95], a[108:111]\n"
"v_fma_f32 v217, v217, s86, -v28\n"
"v_fma_f32 v218, v218, s86, -v28\n"
"v_mfma_f32_16x16x32_bf16 a[108:111], v[196:199], v[108:111], a[108:111]\n"
"v_fma_f32 v219, v219, s86, -v28\n"
"v_cvt_pk_bf16_f32 v220, 0, v220\n"
"v_mfma_f32_16x16x32_bf16 a[112:115], v[200:203], v[80:83], a[112:115]\n"
"v_exp_f32 v216, v216\n"
"v_exp_f32 v217, v217\n"
"v_exp_f32 v218, v218\n"
"v_exp_f32 v219, v219\n"
"v_mfma_f32_16x16x32_bf16 a[112:115], v[204:207], v[96:99], a[112:115]\n"
"v_cvt_pk_bf16_f32 v128, v216, v217\n"
"v_cvt_pk_bf16_f32 v129, v218, v219\n"
"v_mfma_f32_16x16x32_bf16 v[224:227], v[144:147], a[208:211], 0\n"
"ds_read_b64_tr_b16 v[192:193], v21 offset:26624\n"
"v_cvt_pk_bf16_f32 v221, 0, v221\n"
"v_mfma_f32_16x16x32_bf16 v[224:227], v[148:151], a[212:215], v[224:227]\n"
"ds_read_b64_tr_b16 v[194:195], v21 offset:32768\n"
"v_cvt_pk_bf16_f32 v222, 0, v222\n"
"v_mfma_f32_16x16x32_bf16 v[224:227], v[152:155], a[216:219], v[224:227]\n"
"ds_read_b64_tr_b16 v[196:197], v21 offset:38912\n"
"v_cvt_pk_bf16_f32 v223, 0, v223\n"
"v_mfma_f32_16x16x32_bf16 v[224:227], v[156:159], a[220:223], v[224:227]\n"
"ds_read_b64_tr_b16 v[198:199], v21 offset:45056\n"
"v_mul_f32 v220, s85, v220\n"
"v_mfma_f32_16x16x32_bf16 v[224:227], v[160:163], a[224:227], v[224:227]\n"
"v_mul_f32 v221, s85, v221\n"
"v_mul_f32 v222, s85, v222\n"
"v_mfma_f32_16x16x32_bf16 v[224:227], v[164:167], a[228:231], v[224:227]\n"
"v_mul_f32 v223, s85, v223\n"
"v_cvt_pk_bf16_f32 v220, 0, v220\n"
"v_mfma_f32_16x16x32_bf16 a[116:119], v[200:203], v[84:87], a[116:119]\n"
"v_cvt_pk_bf16_f32 v221, 0, v221\n"
"v_cvt_pk_bf16_f32 v222, 0, v222\n"
"v_mfma_f32_16x16x32_bf16 a[116:119], v[204:207], v[100:103], a[116:119]\n"
"v_cvt_pk_bf16_f32 v223, 0, v223\n"
"v_fma_f32 v220, v220, s86, -v29\n"
"v_mfma_f32_16x16x32_bf16 a[120:123], v[200:203], v[88:91], a[120:123]\n"
"v_fma_f32 v221, v221, s86, -v29\n"
"v_fma_f32 v222, v222, s86, -v29\n"
"v_mfma_f32_16x16x32_bf16 a[120:123], v[204:207], v[104:107], a[120:123]\n"
"v_fma_f32 v223, v223, s86, -v29\n"
"v_cvt_pk_bf16_f32 v224, 0, v224\n"
"v_mfma_f32_16x16x32_bf16 a[124:127], v[200:203], v[92:95], a[124:127]\n"
"v_exp_f32 v220, v220\n"
"v_mfma_f32_16x16x32_bf16 a[124:127], v[204:207], v[108:111], a[124:127]\n"
"v_exp_f32 v221, v221\n"
"v_mfma_f32_16x16x32_bf16 v[228:231], v[144:147], a[232:235], 0\n"
"v_exp_f32 v222, v222\n"
"v_exp_f32 v223, v223\n"
"v_mfma_f32_16x16x32_bf16 v[228:231], v[148:151], a[236:239], v[228:231]\n"
"v_cvt_pk_bf16_f32 v132, v220, v221\n"
"v_cvt_pk_bf16_f32 v133, v222, v223\n"
"v_mfma_f32_16x16x32_bf16 v[228:231], v[152:155], a[240:243], v[228:231]\n"
"ds_read_b64_tr_b16 v[200:201], v22 offset:26624\n"
"v_cvt_pk_bf16_f32 v225, 0, v225\n"
"v_mfma_f32_16x16x32_bf16 v[228:231], v[156:159], a[244:247], v[228:231]\n"
"s_waitcnt lgkmcnt(5)\n"
"v_cvt_pk_bf16_f32 v226, 0, v226\n"
"v_cvt_pk_bf16_f32 v227, 0, v227\n"
"v_mfma_f32_16x16x32_bf16 v[228:231], v[160:163], a[248:251], v[228:231]\n"
"ds_read_b64_tr_b16 v[202:203], v22 offset:32768\n"
"v_mul_f32 v224, s85, v224\n"
"v_mfma_f32_16x16x32_bf16 v[228:231], v[164:167], a[252:255], v[228:231]\n"
"ds_read_b64_tr_b16 v[204:205], v22 offset:38912\n"
"v_mul_f32 v225, s85, v225\n"
"v_mfma_f32_16x16x32_bf16 a[128:131], v[208:211], v[80:83], a[128:131]\n"
"ds_read_b64_tr_b16 v[206:207], v22 offset:45056\n"
"v_mul_f32 v226, s85, v226\n"
"v_mfma_f32_16x16x32_bf16 a[128:131], v[212:215], v[96:99], a[128:131]\n"
"v_mul_f32 v227, s85, v227\n"
"v_cvt_pk_bf16_f32 v224, 0, v224\n"
"v_mfma_f32_16x16x32_bf16 a[132:135], v[208:211], v[84:87], a[132:135]\n"
"v_cvt_pk_bf16_f32 v225, 0, v225\n"
"v_cvt_pk_bf16_f32 v226, 0, v226\n"
"v_mfma_f32_16x16x32_bf16 a[132:135], v[212:215], v[100:103], a[132:135]\n"
"v_cvt_pk_bf16_f32 v227, 0, v227\n"
"v_fma_f32 v224, v224, s86, -v30\n"
"v_mfma_f32_16x16x32_bf16 a[136:139], v[208:211], v[88:91], a[136:139]\n"
"v_fma_f32 v225, v225, s86, -v30\n"
"v_fma_f32 v226, v226, s86, -v30\n"
"v_mfma_f32_16x16x32_bf16 a[136:139], v[212:215], v[104:107], a[136:139]\n"
"v_fma_f32 v227, v227, s86, -v30\n"
"v_cvt_pk_bf16_f32 v228, 0, v228\n"
"v_mfma_f32_16x16x32_bf16 v[232:235], v[168:171], v[32:35], 0\n"
"v_exp_f32 v224, v224\n"
"v_exp_f32 v225, v225\n"
"v_exp_f32 v226, v226\n"
"v_exp_f32 v227, v227\n"
"v_mfma_f32_16x16x32_bf16 v[232:235], v[172:175], v[36:39], v[232:235]\n"
"v_cvt_pk_bf16_f32 v136, v224, v225\n"
"v_cvt_pk_bf16_f32 v137, v226, v227\n"
"v_mfma_f32_16x16x32_bf16 v[232:235], v[176:179], v[40:43], v[232:235]\n"
"v_cvt_pk_bf16_f32 v229, 0, v229\n"
"v_cvt_pk_bf16_f32 v230, 0, v230\n"
"v_mfma_f32_16x16x32_bf16 v[232:235], v[180:183], v[44:47], v[232:235]\n"
"v_cvt_pk_bf16_f32 v231, 0, v231\n"
"v_mul_f32 v228, s85, v228\n"
"v_mfma_f32_16x16x32_bf16 v[232:235], v[184:187], v[48:51], v[232:235]\n"
"v_mul_f32 v229, s85, v229\n"
"v_mul_f32 v230, s85, v230\n"
"v_mfma_f32_16x16x32_bf16 v[232:235], v[188:191], v[52:55], v[232:235]\n"
"s_waitcnt lgkmcnt(4)\n"
"v_mul_f32 v231, s85, v231\n"
"v_cvt_pk_bf16_f32 v228, 0, v228\n"
"v_mfma_f32_16x16x32_bf16 a[140:143], v[208:211], v[92:95], a[140:143]\n"
"v_cvt_pk_bf16_f32 v229, 0, v229\n"
"v_cvt_pk_bf16_f32 v230, 0, v230\n"
"v_mfma_f32_16x16x32_bf16 a[140:143], v[212:215], v[108:111], a[140:143]\n"
"ds_read_b64_tr_b16 v[208:209], v23 offset:26624\n"
"v_cvt_pk_bf16_f32 v231, 0, v231\n"
"v_mfma_f32_16x16x32_bf16 a[144:147], v[192:195], v[80:83], a[144:147]\n"
"ds_read_b64_tr_b16 v[210:211], v23 offset:32768\n"
"v_fma_f32 v228, v228, s86, -v31\n"
"v_mfma_f32_16x16x32_bf16 a[144:147], v[196:199], v[96:99], a[144:147]\n"
"ds_read_b64_tr_b16 v[212:213], v23 offset:38912\n"
"v_fma_f32 v229, v229, s86, -v31\n"
"v_mfma_f32_16x16x32_bf16 a[148:151], v[192:195], v[84:87], a[148:151]\n"
"s_waitcnt vmcnt(16)\n"
"s_barrier\n"
"s_add_u32 m0, s7, 122880\n"
"v_mfma_f32_16x16x32_bf16 a[148:151], v[196:199], v[100:103], a[148:151]\n"
"v_fma_f32 v230, v230, s86, -v31\n"
"v_fma_f32 v231, v231, s86, -v31\n"
"v_mfma_f32_16x16x32_bf16 a[152:155], v[192:195], v[88:91], a[152:155]\n"
"v_exp_f32 v228, v228\n"
"v_exp_f32 v229, v229\n"
"v_exp_f32 v230, v230\n"
"v_exp_f32 v231, v231\n"
"v_mfma_f32_16x16x32_bf16 a[152:155], v[196:199], v[104:107], a[152:155]\n"
"v_cvt_pk_bf16_f32 v140, v228, v229\n"
"v_cvt_pk_bf16_f32 v141, v230, v231\n"
"v_mfma_f32_16x16x32_bf16 v[236:239], v[168:171], v[56:59], 0\n"
"buffer_load_dwordx4 v1, s[36:39], s9 offen offset:0 sc1 lds\n"
"v_mfma_f32_16x16x32_bf16 v[236:239], v[172:175], v[60:63], v[236:239]\n"
"ds_read_b64_tr_b16 v[214:215], v23 offset:45056\n"
"v_cvt_pk_bf16_f32 v232, 0, v232\n"
"v_mfma_f32_16x16x32_bf16 v[236:239], v[176:179], v[64:67], v[236:239]\n"
"buffer_load_dwordx4 v1, s[36:39], s9 offen offset:1024 sc1 lds\n"
"v_mfma_f32_16x16x32_bf16 v[236:239], v[180:183], v[68:71], v[236:239]\n"
"v_cvt_pk_bf16_f32 v233, 0, v233\n"
"v_cvt_pk_bf16_f32 v234, 0, v234\n"
"v_mfma_f32_16x16x32_bf16 v[236:239], v[184:187], v[72:75], v[236:239]\n"
"v_cvt_pk_bf16_f32 v235, 0, v235\n"
"v_mul_f32 v232, s85, v232\n"
"v_mfma_f32_16x16x32_bf16 v[236:239], v[188:191], v[76:79], v[236:239]\n"
"buffer_load_dwordx4 v1, s[36:39], s9 offen offset:2048 sc1 lds\n"
"v_mfma_f32_16x16x32_bf16 a[156:159], v[192:195], v[92:95], a[156:159]\n"
"s_waitcnt lgkmcnt(4)\n"
"v_mul_f32 v233, s85, v233\n"
"v_mul_f32 v234, s85, v234\n"
"v_mfma_f32_16x16x32_bf16 a[156:159], v[196:199], v[108:111], a[156:159]\n"
"v_mul_f32 v235, s85, v235\n"
"v_cvt_pk_bf16_f32 v232, 0, v232\n"
"v_mfma_f32_16x16x32_bf16 a[160:163], v[200:203], v[80:83], a[160:163]\n"
"v_cvt_pk_bf16_f32 v233, 0, v233\n"
"v_cvt_pk_bf16_f32 v234, 0, v234\n"
"v_cvt_pk_bf16_f32 v235, 0, v235\n"
"v_mfma_f32_16x16x32_bf16 a[160:163], v[204:207], v[96:99], a[160:163]\n"
"v_fma_f32 v232, v232, s86, -v28\n"
"v_fma_f32 v233, v233, s86, -v28\n"
"v_fma_f32 v234, v234, s86, -v28\n"
"v_fma_f32 v235, v235, s86, -v28\n"
"v_mfma_f32_16x16x32_bf16 a[164:167], v[200:203], v[84:87], a[164:167]\n"
"v_exp_f32 v232, v232\n"
"v_exp_f32 v233, v233\n"
"v_exp_f32 v234, v234\n"
"v_exp_f32 v235, v235\n"
"v_mfma_f32_16x16x32_bf16 a[164:167], v[204:207], v[100:103], a[164:167]\n"
"v_cvt_pk_bf16_f32 v130, v232, v233\n"
"v_cvt_pk_bf16_f32 v131, v234, v235\n"
"v_mfma_f32_16x16x32_bf16 v[216:219], v[168:171], a[208:211], 0\n"
"buffer_load_dwordx4 v1, s[36:39], s9 offen offset:3072 sc1 lds\n"
"v_mfma_f32_16x16x32_bf16 v[216:219], v[172:175], a[212:215], v[216:219]\n"
"s_add_u32 m0, s7, 126976\n"
"v_cvt_pk_bf16_f32 v236, 0, v236\n"
"v_cvt_pk_bf16_f32 v237, 0, v237\n"
"v_mfma_f32_16x16x32_bf16 v[216:219], v[176:179], a[216:219], v[216:219]\n"
"buffer_load_dwordx4 v4, s[36:39], s9 offen sc1 lds\n"
"v_mfma_f32_16x16x32_bf16 v[216:219], v[180:183], a[220:223], v[216:219]\n"
"s_add_u32 s9, s9, 81920\n"
"s_add_u32 m0, s7, 143360\n"
"v_cvt_pk_bf16_f32 v238, 0, v238\n"
"v_mfma_f32_16x16x32_bf16 v[216:219], v[184:187], a[224:227], v[216:219]\n"
"ds_read_b64_tr_b16 v[192:193], v24 offset:0\n"
"v_cvt_pk_bf16_f32 v239, 0, v239\n"
"v_mfma_f32_16x16x32_bf16 v[216:219], v[188:191], a[228:231], v[216:219]\n"
"buffer_load_dwordx4 v1, s[36:39], s34 offen offset:0 sc1 lds\n"
"v_mfma_f32_16x16x32_bf16 a[168:171], v[200:203], v[88:91], a[168:171]\n"
"ds_read_b64_tr_b16 v[194:195], v24 offset:6144\n"
"v_mul_f32 v236, s85, v236\n"
"v_mfma_f32_16x16x32_bf16 a[168:171], v[204:207], v[104:107], a[168:171]\n"
"s_waitcnt lgkmcnt(2)\n"
"v_mul_f32 v237, s85, v237\n"
"v_mul_f32 v238, s85, v238\n"
"v_mfma_f32_16x16x32_bf16 a[172:175], v[200:203], v[92:95], a[172:175]\n"
"buffer_load_dwordx4 v1, s[36:39], s34 offen offset:1024 sc1 lds\n"
"v_mfma_f32_16x16x32_bf16 a[172:175], v[204:207], v[108:111], a[172:175]\n"
"v_mul_f32 v239, s85, v239\n"
"v_cvt_pk_bf16_f32 v236, 0, v236\n"
"v_mfma_f32_16x16x32_bf16 a[176:179], v[208:211], v[80:83], a[176:179]\n"
"v_cvt_pk_bf16_f32 v237, 0, v237\n"
"v_cvt_pk_bf16_f32 v238, 0, v238\n"
"v_cvt_pk_bf16_f32 v239, 0, v239\n"
"v_mfma_f32_16x16x32_bf16 a[176:179], v[212:215], v[96:99], a[176:179]\n"
"v_fma_f32 v236, v236, s86, -v29\n"
"v_fma_f32 v237, v237, s86, -v29\n"
"v_fma_f32 v238, v238, s86, -v29\n"
"v_fma_f32 v239, v239, s86, -v29\n"
"v_mfma_f32_16x16x32_bf16 v[220:223], v[168:171], a[232:235], 0\n"
"v_exp_f32 v236, v236\n"
"v_exp_f32 v237, v237\n"
"v_exp_f32 v238, v238\n"
"v_exp_f32 v239, v239\n"
"v_mfma_f32_16x16x32_bf16 v[220:223], v[172:175], a[236:239], v[220:223]\n"
"v_cvt_pk_bf16_f32 v134, v236, v237\n"
"v_cvt_pk_bf16_f32 v135, v238, v239\n"
"v_mfma_f32_16x16x32_bf16 v[220:223], v[176:179], a[240:243], v[220:223]\n"
"buffer_load_dwordx4 v1, s[36:39], s34 offen offset:2048 sc1 lds\n"
"v_mfma_f32_16x16x32_bf16 v[220:223], v[180:183], a[244:247], v[220:223]\n"
"ds_read_b64_tr_b16 v[196:197], v24 offset:12288\n"
"v_cvt_pk_bf16_f32 v216, 0, v216\n"
"v_mfma_f32_16x16x32_bf16 v[220:223], v[184:187], a[248:251], v[220:223]\n"
"buffer_load_dwordx4 v1, s[36:39], s34 offen offset:3072 sc1 lds\n"
"v_mfma_f32_16x16x32_bf16 v[220:223], v[188:191], a[252:255], v[220:223]\n"
"s_add_u32 m0, s7, 147456\n"
"v_cvt_pk_bf16_f32 v217, 0, v217\n"
"v_cvt_pk_bf16_f32 v218, 0, v218\n"
"v_mfma_f32_16x16x32_bf16 a[180:183], v[208:211], v[84:87], a[180:183]\n"
"ds_read_b64_tr_b16 v[198:199], v24 offset:18432\n"
"v_cvt_pk_bf16_f32 v219, 0, v219\n"
"v_mfma_f32_16x16x32_bf16 a[180:183], v[212:215], v[100:103], a[180:183]\n"
"buffer_load_dwordx4 v4, s[36:39], s34 offen sc1 lds\n"
"v_mfma_f32_16x16x32_bf16 a[184:187], v[208:211], v[88:91], a[184:187]\n"
"s_add_u32 s34, s34, 81920\n"
"v_mul_f32 v216, s85, v216\n"
"v_mul_f32 v217, s85, v217\n"
"v_mfma_f32_16x16x32_bf16 a[184:187], v[212:215], v[104:107], a[184:187]\n"
"v_mul_f32 v218, s85, v218\n"
"v_mul_f32 v219, s85, v219\n"
"v_mfma_f32_16x16x32_bf16 a[188:191], v[208:211], v[92:95], a[188:191]\n"
"v_cvt_pk_bf16_f32 v216, 0, v216\n"
"v_cvt_pk_bf16_f32 v217, 0, v217\n"
"v_cvt_pk_bf16_f32 v218, 0, v218\n"
"v_cvt_pk_bf16_f32 v219, 0, v219\n"
"v_mfma_f32_16x16x32_bf16 a[188:191], v[212:215], v[108:111], a[188:191]\n"
"v_fma_f32 v216, v216, s86, -v30\n"
"v_fma_f32 v217, v217, s86, -v30\n"
"v_fma_f32 v218, v218, s86, -v30\n"
"v_fma_f32 v219, v219, s86, -v30\n"
"v_mfma_f32_16x16x32_bf16 a[192:195], v[240:243], v[80:83], a[192:195]\n"
"v_exp_f32 v216, v216\n"
"v_exp_f32 v217, v217\n"
"v_exp_f32 v218, v218\n"
"v_exp_f32 v219, v219\n"
"v_mfma_f32_16x16x32_bf16 a[192:195], v[240:243], v[96:99], a[192:195]\n"
"v_cvt_pk_bf16_f32 v138, v216, v217\n"
"v_cvt_pk_bf16_f32 v139, v218, v219\n"
"v_mfma_f32_16x16x32_bf16 a[196:199], v[240:243], v[84:87], a[196:199]\n"
"v_cvt_pk_bf16_f32 v220, 0, v220\n"
"v_cvt_pk_bf16_f32 v221, 0, v221\n"
"v_cvt_pk_bf16_f32 v222, 0, v222\n"
"v_cvt_pk_bf16_f32 v223, 0, v223\n"
"v_mfma_f32_16x16x32_bf16 a[196:199], v[240:243], v[100:103], a[196:199]\n"
"v_mul_f32 v220, s85, v220\n"
"v_mul_f32 v221, s85, v221\n"
"v_mul_f32 v222, s85, v222\n"
"v_mul_f32 v223, s85, v223\n"
"v_mfma_f32_16x16x32_bf16 a[200:203], v[240:243], v[88:91], a[200:203]\n"
"v_cvt_pk_bf16_f32 v220, 0, v220\n"
"v_cvt_pk_bf16_f32 v221, 0, v221\n"
"v_cvt_pk_bf16_f32 v222, 0, v222\n"
"v_cvt_pk_bf16_f32 v223, 0, v223\n"
"v_mfma_f32_16x16x32_bf16 a[200:203], v[240:243], v[104:107], a[200:203]\n"
"v_fma_f32 v220, v220, s86, -v31\n"
"v_fma_f32 v221, v221, s86, -v31\n"
"v_fma_f32 v222, v222, s86, -v31\n"
"v_fma_f32 v223, v223, s86, -v31\n"
"v_mfma_f32_16x16x32_bf16 a[204:207], v[240:243], v[92:95], a[204:207]\n"
"v_exp_f32 v220, v220\n"
"v_exp_f32 v221, v221\n"
"v_exp_f32 v222, v222\n"
"v_exp_f32 v223, v223\n"
"v_mfma_f32_16x16x32_bf16 a[204:207], v[240:243], v[108:111], a[204:207]\n"
"v_cvt_pk_bf16_f32 v142, v220, v221\n"
"v_cvt_pk_bf16_f32 v143, v222, v223\n"
"v_mfma_f32_16x16x32_bf16 a[192:195], v[240:243], v[112:115], a[192:195]\n"
"ds_read_b64_tr_b16 v[200:201], v25 offset:0\n"
"v_mfma_f32_16x16x32_bf16 a[192:195], v[240:243], v[128:131], a[192:195]\n"
"ds_read_b64_tr_b16 v[202:203], v25 offset:6144\n"
"v_mfma_f32_16x16x32_bf16 a[196:199], v[240:243], v[116:119], a[196:199]\n"
"s_barrier\n"
"s_add_u32 m0, s7, 20480\n"
"v_mfma_f32_16x16x32_bf16 a[196:199], v[240:243], v[132:135], a[196:199]\n"
"buffer_load_dwordx4 v1, s[36:39], s34 offen offset:0 sc1 lds\n"
"v_mfma_f32_16x16x32_bf16 a[200:203], v[240:243], v[120:123], a[200:203]\n"
"ds_read_b64_tr_b16 v[204:205], v25 offset:12288\n"
"v_mfma_f32_16x16x32_bf16 a[200:203], v[240:243], v[136:139], a[200:203]\n"
"buffer_load_dwordx4 v1, s[36:39], s34 offen offset:1024 sc1 lds\n"
"v_mfma_f32_16x16x32_bf16 a[204:207], v[240:243], v[124:127], a[204:207]\n"
"s_waitcnt lgkmcnt(3)\n"
"ds_read_b64_tr_b16 v[206:207], v25 offset:18432\n"
"v_mfma_f32_16x16x32_bf16 a[204:207], v[240:243], v[140:143], a[204:207]\n"
"ds_read_b64_tr_b16 v[208:209], v26 offset:0\n"
"v_accvgpr_read_b32 v244, a192\n"
"v_mfma_f32_16x16x32_bf16 a[0:3], v[192:195], v[112:115], a[0:3]\n"
"buffer_load_dwordx4 v1, s[36:39], s34 offen offset:2048 sc1 lds\n"
"v_mfma_f32_16x16x32_bf16 a[0:3], v[196:199], v[128:131], a[0:3]\n"
"ds_read_b64_tr_b16 v[210:211], v26 offset:6144\n"
"v_accvgpr_read_b32 v245, a196\n"
"v_mfma_f32_16x16x32_bf16 a[4:7], v[192:195], v[116:119], a[4:7]\n"
"ds_read_b64_tr_b16 v[212:213], v26 offset:12288\n"
"v_accvgpr_read_b32 v246, a200\n"
"v_mfma_f32_16x16x32_bf16 a[4:7], v[196:199], v[132:135], a[4:7]\n"
"buffer_load_dwordx4 v1, s[36:39], s34 offen offset:3072 sc1 lds\n"
"v_mfma_f32_16x16x32_bf16 a[8:11], v[192:195], v[120:123], a[8:11]\n"
"s_add_u32 m0, s7, 24576\n"
"ds_read_b64_tr_b16 v[214:215], v26 offset:18432\n"
"v_mfma_f32_16x16x32_bf16 a[8:11], v[196:199], v[136:139], a[8:11]\n"
"s_waitcnt lgkmcnt(4)\n"
"v_accvgpr_read_b32 v247, a204\n"
"v_add_f32 v244, v244, v245\n"
"v_mfma_f32_16x16x32_bf16 a[12:15], v[192:195], v[124:127], a[12:15]\n"
"buffer_load_dwordx4 v4, s[36:39], s34 offen sc1 lds\n"
"v_mfma_f32_16x16x32_bf16 a[12:15], v[196:199], v[140:143], a[12:15]\n"
"s_add_u32 s34, s34, 81920\n"
"v_add_f32 v246, v246, v247\n"
"v_add_f32 v244, v244, v246\n"
"v_mfma_f32_16x16x32_bf16 a[16:19], v[200:203], v[112:115], a[16:19]\n"
"s_mov_b32 s87, 0x5d800000\n"
"v_cmp_ngt_f32 s[92:93], s87, v244\n"
"s_lshl_b32 s87, s76, 2\n"
"v_mfma_f32_16x16x32_bf16 a[16:19], v[204:207], v[128:131], a[16:19]\n"
"s_add_u32 s87, s87, 74752\n"
"v_mov_b32 v245, s87\n"
"s_or_b32 s92, s92, s93\n"
"v_mfma_f32_16x16x32_bf16 a[20:23], v[200:203], v[116:119], a[20:23]\n"
"ds_read_b64_tr_b16 v[192:193], v27 offset:0\n"
"v_mov_b32 v244, s92\n"
"v_mfma_f32_16x16x32_bf16 a[20:23], v[204:207], v[132:135], a[20:23]\n"
"ds_write_b32 v245, v244\n"
"s_waitcnt lgkmcnt(0)\n"
"v_mfma_f32_16x16x32_bf16 a[24:27], v[200:203], v[120:123], a[24:27]\n"
"ds_read_b64_tr_b16 v[194:195], v27 offset:6144\n"
"v_mov_b32 v245, 74752\n"
"v_mfma_f32_16x16x32_bf16 a[24:27], v[204:207], v[136:139], a[24:27]\n"
"ds_read_b64_tr_b16 v[196:197], v27 offset:12288\n"
"v_mfma_f32_16x16x32_bf16 a[28:31], v[200:203], v[124:127], a[28:31]\n"
"s_barrier\n"
"v_mfma_f32_16x16x32_bf16 a[28:31], v[204:207], v[140:143], a[28:31]\n"
"ds_read_b64_tr_b16 v[198:199], v27 offset:18432\n"
"v_mfma_f32_16x16x32_bf16 a[32:35], v[208:211], v[112:115], a[32:35]\n"
"ds_read_b128 v[248:251], v245\n"
"v_mfma_f32_16x16x32_bf16 a[32:35], v[212:215], v[128:131], a[32:35]\n"
"ds_read_b64_tr_b16 v[200:201], v24 offset:1024\n"
"v_mfma_f32_16x16x32_bf16 a[36:39], v[208:211], v[116:119], a[36:39]\n"
"ds_read_b64_tr_b16 v[202:203], v24 offset:7168\n"
"v_mfma_f32_16x16x32_bf16 a[36:39], v[212:215], v[132:135], a[36:39]\n"
"ds_read_b64_tr_b16 v[204:205], v24 offset:13312\n"
"v_mfma_f32_16x16x32_bf16 a[40:43], v[208:211], v[120:123], a[40:43]\n"
"ds_read_b64_tr_b16 v[206:207], v24 offset:19456\n"
"v_mfma_f32_16x16x32_bf16 a[40:43], v[212:215], v[136:139], a[40:43]\n"
"s_waitcnt lgkmcnt(7)\n"
"v_mfma_f32_16x16x32_bf16 a[44:47], v[208:211], v[124:127], a[44:47]\n"
"v_mfma_f32_16x16x32_bf16 a[44:47], v[212:215], v[140:143], a[44:47]\n"
"ds_read_b64_tr_b16 v[208:209], v25 offset:1024\n"
"v_mfma_f32_16x16x32_bf16 a[48:51], v[192:195], v[112:115], a[48:51]\n"
"s_waitcnt lgkmcnt(5)\n"
"v_or3_b32 v248, v248, v249, v250\n"
"v_or_b32 v248, v248, v251\n"
"s_nop 0\n"
"v_readfirstlane_b32 s100, v248\n"
"s_nop 0\n"
"s_cmp_lg_u32 s100, 0\n"
"s_cbranch_scc1 .Lslow\n"
".Lslow_back:\n"
"v_mfma_f32_16x16x32_bf16 a[48:51], v[196:199], v[128:131], a[48:51]\n"
"buffer_load_dwordx4 a[208:211], v10, s[36:39], s12 offen sc1\n"
"v_mfma_f32_16x16x32_bf16 a[52:55], v[192:195], v[116:119], a[52:55]\n"
"ds_read_b64_tr_b16 v[210:211], v25 offset:7168\n"
"v_accvgpr_read_b32 v28, a192\n"
"v_mfma_f32_16x16x32_bf16 a[52:55], v[196:199], v[132:135], a[52:55]\n"
"buffer_load_dwordx4 a[216:219], v11, s[36:39], s12 offen sc1\n"
"v_mfma_f32_16x16x32_bf16 a[56:59], v[192:195], v[120:123], a[56:59]\n"
"s_add_u32 s12, s12, 131072\n"
"v_rcp_f32 v28, v28\n"
"v_mfma_f32_16x16x32_bf16 a[56:59], v[196:199], v[136:139], a[56:59]\n"
"s_waitcnt lgkmcnt(2)\n"
"v_accvgpr_read_b32 v144, a0\n"
"v_accvgpr_read_b32 v145, a1\n"
"v_mfma_f32_16x16x32_bf16 a[60:63], v[192:195], v[124:127], a[60:63]\n"
"v_accvgpr_read_b32 v146, a2\n"
"v_accvgpr_read_b32 v147, a3\n"
"v_accvgpr_read_b32 v148, a16\n"
"v_accvgpr_read_b32 v149, a17\n"
"v_accvgpr_read_b32 v150, a18\n"
"v_accvgpr_read_b32 v151, a19\n"
"v_mfma_f32_16x16x32_bf16 a[60:63], v[196:199], v[140:143], a[60:63]\n"
"v_mul_f32 v144, v28, v144\n"
"v_mul_f32 v145, v28, v145\n"
"v_mul_f32 v146, v28, v146\n"
"v_mul_f32 v147, v28, v147\n"
"v_mul_f32 v148, v28, v148\n"
"v_mul_f32 v149, v28, v149\n"
"v_mul_f32 v150, v28, v150\n"
"v_mul_f32 v151, v28, v151\n"
"v_mfma_f32_16x16x32_bf16 a[64:67], v[200:203], v[112:115], a[64:67]\n"
"v_cvt_pk_bf16_f32 v144, v144, v145\n"
"v_cvt_pk_bf16_f32 v145, v146, v147\n"
"v_cvt_pk_bf16_f32 v146, v148, v149\n"
"v_cvt_pk_bf16_f32 v147, v150, v151\n"
"v_mfma_f32_16x16x32_bf16 a[64:67], v[204:207], v[128:131], a[64:67]\n"
"ds_read_b64_tr_b16 v[212:213], v25 offset:13312\n"
"ds_read_b64_tr_b16 v[214:215], v25 offset:19456\n"
"buffer_store_dwordx4 v[144:147], v1, s[36:39], s91 offen offset:0 sc1\n"
"v_mfma_f32_16x16x32_bf16 a[68:71], v[200:203], v[116:119], a[68:71]\n"
"ds_read_b64_tr_b16 v[192:193], v26 offset:1024\n"
"v_accvgpr_read_b32 v29, a196\n"
"v_mfma_f32_16x16x32_bf16 a[68:71], v[204:207], v[132:135], a[68:71]\n"
"buffer_load_dwordx4 a[212:215], v10, s[36:39], s12 offen sc1\n"
"v_mfma_f32_16x16x32_bf16 a[72:75], v[200:203], v[120:123], a[72:75]\n"
"v_rcp_f32 v29, v29\n"
"v_mfma_f32_16x16x32_bf16 a[72:75], v[204:207], v[136:139], a[72:75]\n"
"ds_read_b64_tr_b16 v[194:195], v26 offset:7168\n"
"v_accvgpr_read_b32 v152, a4\n"
"v_mfma_f32_16x16x32_bf16 a[76:79], v[200:203], v[124:127], a[76:79]\n"
"buffer_load_dwordx4 a[220:223], v11, s[36:39], s12 offen sc1\n"
"s_add_u32 s12, s12, 131072\n"
"v_accvgpr_read_b32 v153, a5\n"
"v_accvgpr_read_b32 v154, a6\n"
"v_accvgpr_read_b32 v155, a7\n"
"v_accvgpr_read_b32 v156, a20\n"
"v_accvgpr_read_b32 v157, a21\n"
"v_accvgpr_read_b32 v158, a22\n"
"v_accvgpr_read_b32 v159, a23\n"
"v_mfma_f32_16x16x32_bf16 a[76:79], v[204:207], v[140:143], a[76:79]\n"
"s_waitcnt lgkmcnt(2)\n"
"v_mul_f32 v152, v29, v152\n"
"v_mul_f32 v153, v29, v153\n"
"v_mul_f32 v154, v29, v154\n"
"v_mul_f32 v155, v29, v155\n"
"v_mul_f32 v156, v29, v156\n"
"v_mul_f32 v157, v29, v157\n"
"v_mul_f32 v158, v29, v158\n"
"v_mul_f32 v159, v29, v159\n"
"v_mfma_f32_16x16x32_bf16 a[80:83], v[208:211], v[112:115], a[80:83]\n"
"v_cvt_pk_bf16_f32 v152, v152, v153\n"
"v_cvt_pk_bf16_f32 v153, v154, v155\n"
"v_cvt_pk_bf16_f32 v154, v156, v157\n"
"v_cvt_pk_bf16_f32 v155, v158, v159\n"
"v_mfma_f32_16x16x32_bf16 a[80:83], v[212:215], v[128:131], a[80:83]\n"
"ds_read_b64_tr_b16 v[196:197], v26 offset:13312\n"
"ds_read_b64_tr_b16 v[198:199], v26 offset:19456\n"
"buffer_store_dwordx4 v[152:155], v1, s[36:39], s91 offen offset:1024 sc1\n"
"v_mfma_f32_16x16x32_bf16 a[84:87], v[208:211], v[116:119], a[84:87]\n"
"ds_read_b64_tr_b16 v[200:201], v27 offset:1024\n"
"v_accvgpr_read_b32 v30, a200\n"
"v_mfma_f32_16x16x32_bf16 a[84:87], v[212:215], v[132:135], a[84:87]\n"
"v_rcp_f32 v30, v30\n"
"v_mfma_f32_16x16x32_bf16 a[88:91], v[208:211], v[120:123], a[88:91]\n"
"buffer_load_dwordx4 a[224:227], v10, s[36:39], s12 offen sc1\n"
"v_mfma_f32_16x16x32_bf16 a[88:91], v[212:215], v[136:139], a[88:91]\n"
"v_accvgpr_read_b32 v160, a8\n"
"v_accvgpr_read_b32 v161, a9\n"
"v_accvgpr_read_b32 v162, a10\n"
"v_accvgpr_read_b32 v163, a11\n"
"v_accvgpr_read_b32 v164, a24\n"
"v_accvgpr_read_b32 v165, a25\n"
"v_accvgpr_read_b32 v166, a26\n"
"v_accvgpr_read_b32 v167, a27\n"
"v_mfma_f32_16x16x32_bf16 a[92:95], v[208:211], v[124:127], a[92:95]\n"
"v_mul_f32 v160, v30, v160\n"
"v_mul_f32 v161, v30, v161\n"
"v_mul_f32 v162, v30, v162\n"
"v_mul_f32 v163, v30, v163\n"
"v_mul_f32 v164, v30, v164\n"
"v_mul_f32 v165, v30, v165\n"
"v_mul_f32 v166, v30, v166\n"
"v_mul_f32 v167, v30, v167\n"
"v_mfma_f32_16x16x32_bf16 a[92:95], v[212:215], v[140:143], a[92:95]\n"
"s_waitcnt lgkmcnt(1)\n"
"v_cvt_pk_bf16_f32 v160, v160, v161\n"
"v_cvt_pk_bf16_f32 v161, v162, v163\n"
"v_cvt_pk_bf16_f32 v162, v164, v165\n"
"v_cvt_pk_bf16_f32 v163, v166, v167\n"
"v_mfma_f32_16x16x32_bf16 a[96:99], v[192:195], v[112:115], a[96:99]\n"
"buffer_store_dwordx4 v[160:163], v1, s[36:39], s91 offen offset:2048 sc1\n"
"v_mfma_f32_16x16x32_bf16 a[96:99], v[196:199], v[128:131], a[96:99]\n"
"ds_read_b64_tr_b16 v[202:203], v27 offset:7168\n"
"ds_read_b64_tr_b16 v[204:205], v27 offset:13312\n"
"ds_read_b64_tr_b16 v[206:207], v27 offset:19456\n"
"v_mfma_f32_16x16x32_bf16 a[100:103], v[192:195], v[116:119], a[100:103]\n"
"buffer_load_dwordx4 a[232:235], v11, s[36:39], s12 offen sc1\n"
"v_mfma_f32_16x16x32_bf16 a[100:103], v[196:199], v[132:135], a[100:103]\n"
"s_add_u32 s12, s12, 131072\n"
"v_accvgpr_read_b32 v31, a204\n"
"v_accvgpr_read_b32 v168, a12\n"
"v_mfma_f32_16x16x32_bf16 a[104:107], v[192:195], v[120:123], a[104:107]\n"
"v_rcp_f32 v31, v31\n"
"v_mfma_f32_16x16x32_bf16 a[104:107], v[196:199], v[136:139], a[104:107]\n"
"v_accvgpr_read_b32 v169, a13\n"
"v_accvgpr_read_b32 v170, a14\n"
"v_accvgpr_read_b32 v171, a15\n"
"v_accvgpr_read_b32 v172, a28\n"
"v_accvgpr_read_b32 v173, a29\n"
"v_accvgpr_read_b32 v174, a30\n"
"v_accvgpr_read_b32 v175, a31\n"
"v_mfma_f32_16x16x32_bf16 a[108:111], v[192:195], v[124:127], a[108:111]\n"
"v_mul_f32 v168, v31, v168\n"
"v_mul_f32 v169, v31, v169\n"
"v_mul_f32 v170, v31, v170\n"
"v_mul_f32 v171, v31, v171\n"
"v_mul_f32 v172, v31, v172\n"
"v_mul_f32 v173, v31, v173\n"
"v_mul_f32 v174, v31, v174\n"
"v_mul_f32 v175, v31, v175\n"
"v_mfma_f32_16x16x32_bf16 a[108:111], v[196:199], v[140:143], a[108:111]\n"
"s_waitcnt lgkmcnt(0)\n"
"v_cvt_pk_bf16_f32 v168, v168, v169\n"
"v_cvt_pk_bf16_f32 v169, v170, v171\n"
"v_cvt_pk_bf16_f32 v170, v172, v173\n"
"v_cvt_pk_bf16_f32 v171, v174, v175\n"
"v_mfma_f32_16x16x32_bf16 a[112:115], v[200:203], v[112:115], a[112:115]\n"
"buffer_store_dwordx4 v[168:171], v1, s[36:39], s91 offen offset:3072 sc1\n"
"v_mfma_f32_16x16x32_bf16 a[112:115], v[204:207], v[128:131], a[112:115]\n"
"ds_read_b64_tr_b16 v[208:209], v24 offset:2048\n"
"ds_read_b64_tr_b16 v[210:211], v24 offset:8192\n"
"ds_read_b64_tr_b16 v[212:213], v24 offset:14336\n"
"ds_read_b64_tr_b16 v[214:215], v24 offset:20480\n"
"v_mfma_f32_16x16x32_bf16 a[116:119], v[200:203], v[116:119], a[116:119]\n"
"buffer_load_dwordx4 a[228:231], v10, s[36:39], s12 offen sc1\n"
"v_mfma_f32_16x16x32_bf16 a[116:119], v[204:207], v[132:135], a[116:119]\n"
"s_add_u32 s91, s91, 131072\n"
"v_accvgpr_read_b32 v144, a32\n"
"v_accvgpr_read_b32 v145, a33\n"
"v_mfma_f32_16x16x32_bf16 a[120:123], v[200:203], v[120:123], a[120:123]\n"
"v_accvgpr_read_b32 v146, a34\n"
"v_accvgpr_read_b32 v147, a35\n"
"v_accvgpr_read_b32 v148, a48\n"
"v_accvgpr_read_b32 v149, a49\n"
"v_accvgpr_read_b32 v150, a50\n"
"v_accvgpr_read_b32 v151, a51\n"
"v_mfma_f32_16x16x32_bf16 a[120:123], v[204:207], v[136:139], a[120:123]\n"
"v_mul_f32 v144, v28, v144\n"
"v_mul_f32 v145, v28, v145\n"
"v_mul_f32 v146, v28, v146\n"
"v_mul_f32 v147, v28, v147\n"
"v_mul_f32 v148, v28, v148\n"
"v_mul_f32 v149, v28, v149\n"
"v_mul_f32 v150, v28, v150\n"
"v_mul_f32 v151, v28, v151\n"
"v_mfma_f32_16x16x32_bf16 a[124:127], v[200:203], v[124:127], a[124:127]\n"
"v_cvt_pk_bf16_f32 v144, v144, v145\n"
"v_cvt_pk_bf16_f32 v145, v146, v147\n"
"v_cvt_pk_bf16_f32 v146, v148, v149\n"
"v_cvt_pk_bf16_f32 v147, v150, v151\n"
"v_mfma_f32_16x16x32_bf16 a[124:127], v[204:207], v[140:143], a[124:127]\n"
"s_waitcnt lgkmcnt(0)\n"
"buffer_store_dwordx4 v[144:147], v1, s[36:39], s91 offen offset:0 sc1\n"
"v_mfma_f32_16x16x32_bf16 a[128:131], v[208:211], v[112:115], a[128:131]\n"
"ds_read_b64_tr_b16 v[192:193], v25 offset:2048\n"
"v_accvgpr_read_b32 v152, a36\n"
"v_mfma_f32_16x16x32_bf16 a[128:131], v[212:215], v[128:131], a[128:131]\n"
"ds_read_b64_tr_b16 v[194:195], v25 offset:8192\n"
"ds_read_b64_tr_b16 v[196:197], v25 offset:14336\n"
"ds_read_b64_tr_b16 v[198:199], v25 offset:20480\n"
"v_mfma_f32_16x16x32_bf16 a[132:135], v[208:211], v[116:119], a[132:135]\n"
"buffer_load_dwordx4 a[236:239], v11, s[36:39], s12 offen sc1\n"
"v_mfma_f32_16x16x32_bf16 a[132:135], v[212:215], v[132:135], a[132:135]\n"
"s_add_u32 s12, s12, 131072\n"
"v_accvgpr_read_b32 v153, a37\n"
"v_accvgpr_read_b32 v154, a38\n"
"v_mfma_f32_16x16x32_bf16 a[136:139], v[208:211], v[120:123], a[136:139]\n"
"v_accvgpr_read_b32 v155, a39\n"
"v_accvgpr_read_b32 v156, a52\n"
"v_accvgpr_read_b32 v157, a53\n"
"v_accvgpr_read_b32 v158, a54\n"
"v_accvgpr_read_b32 v159, a55\n"
"v_mfma_f32_16x16x32_bf16 a[136:139], v[212:215], v[136:139], a[136:139]\n"
"v_mul_f32 v152, v29, v152\n"
"v_mul_f32 v153, v29, v153\n"
"v_mul_f32 v154, v29, v154\n"
"v_mul_f32 v155, v29, v155\n"
"v_mul_f32 v156, v29, v156\n"
"v_mul_f32 v157, v29, v157\n"
"v_mul_f32 v158, v29, v158\n"
"v_mul_f32 v159, v29, v159\n"
"v_mfma_f32_16x16x32_bf16 a[140:143], v[208:211], v[124:127], a[140:143]\n"
"v_cvt_pk_bf16_f32 v152, v152, v153\n"
"v_cvt_pk_bf16_f32 v153, v154, v155\n"
"v_cvt_pk_bf16_f32 v154, v156, v157\n"
"v_cvt_pk_bf16_f32 v155, v158, v159\n"
"v_mfma_f32_16x16x32_bf16 a[140:143], v[212:215], v[140:143], a[140:143]\n"
"s_waitcnt lgkmcnt(0)\n"
"buffer_store_dwordx4 v[152:155], v1, s[36:39], s91 offen offset:1024 sc1\n"
"v_mfma_f32_16x16x32_bf16 a[144:147], v[192:195], v[112:115], a[144:147]\n"
"ds_read_b64_tr_b16 v[200:201], v26 offset:2048\n"
"v_accvgpr_read_b32 v160, a40\n"
"v_mfma_f32_16x16x32_bf16 a[144:147], v[196:199], v[128:131], a[144:147]\n"
"ds_read_b64_tr_b16 v[202:203], v26 offset:8192\n"
"ds_read_b64_tr_b16 v[204:205], v26 offset:14336\n"
"ds_read_b64_tr_b16 v[206:207], v26 offset:20480\n"
"v_mfma_f32_16x16x32_bf16 a[148:151], v[192:195], v[116:119], a[148:151]\n"
"buffer_load_dwordx4 a[240:243], v10, s[36:39], s12 offen sc1\n"
"v_mfma_f32_16x16x32_bf16 a[148:151], v[196:199], v[132:135], a[148:151]\n"
"v_accvgpr_read_b32 v161, a41\n"
"v_accvgpr_read_b32 v162, a42\n"
"v_accvgpr_read_b32 v163, a43\n"
"v_accvgpr_read_b32 v164, a56\n"
"v_accvgpr_read_b32 v165, a57\n"
"v_accvgpr_read_b32 v166, a58\n"
"v_accvgpr_read_b32 v167, a59\n"
"v_mfma_f32_16x16x32_bf16 a[152:155], v[192:195], v[120:123], a[152:155]\n"
"s_waitcnt vmcnt(10)\n"
"s_barrier\n"
"v_mul_f32 v160, v30, v160\n"
"v_mul_f32 v161, v30, v161\n"
"v_mul_f32 v162, v30, v162\n"
"v_mul_f32 v163, v30, v163\n"
"v_mul_f32 v164, v30, v164\n"
"v_mul_f32 v165, v30, v165\n"
"v_mul_f32 v166, v30, v166\n"
"v_mul_f32 v167, v30, v167\n"
"v_mfma_f32_16x16x32_bf16 a[152:155], v[196:199], v[136:139], a[152:155]\n"
"v_cvt_pk_bf16_f32 v160, v160, v161\n"
"v_cvt_pk_bf16_f32 v161, v162, v163\n"
"v_cvt_pk_bf16_f32 v162, v164, v165\n"
"v_cvt_pk_bf16_f32 v163, v166, v167\n"
"v_mfma_f32_16x16x32_bf16 a[156:159], v[192:195], v[124:127], a[156:159]\n"
"buffer_store_dwordx4 v[160:163], v1, s[36:39], s91 offen offset:2048 sc1\n"
"v_mfma_f32_16x16x32_bf16 a[156:159], v[196:199], v[140:143], a[156:159]\n"
"s_waitcnt lgkmcnt(0)\n"
"ds_read_b64_tr_b16 v[208:209], v27 offset:2048\n"
"v_mfma_f32_16x16x32_bf16 a[160:163], v[200:203], v[112:115], a[160:163]\n"
"buffer_load_dwordx4 a[248:251], v11, s[36:39], s12 offen sc1\n"
"v_mfma_f32_16x16x32_bf16 a[160:163], v[204:207], v[128:131], a[160:163]\n"
"ds_read_b64_tr_b16 v[210:211], v27 offset:8192\n"
"ds_read_b64_tr_b16 v[212:213], v27 offset:14336\n"
"ds_read_b64_tr_b16 v[214:215], v27 offset:20480\n"
"v_mfma_f32_16x16x32_bf16 a[164:167], v[200:203], v[116:119], a[164:167]\n"
"s_add_u32 s12, s12, 131072\n"
"v_accvgpr_read_b32 v168, a44\n"
"v_accvgpr_read_b32 v169, a45\n"
"v_mfma_f32_16x16x32_bf16 a[164:167], v[204:207], v[132:135], a[164:167]\n"
"v_accvgpr_read_b32 v170, a46\n"
"v_accvgpr_read_b32 v171, a47\n"
"v_accvgpr_read_b32 v172, a60\n"
"v_accvgpr_read_b32 v173, a61\n"
"v_accvgpr_read_b32 v174, a62\n"
"v_accvgpr_read_b32 v175, a63\n"
"v_mfma_f32_16x16x32_bf16 a[168:171], v[200:203], v[120:123], a[168:171]\n"
"v_mul_f32 v168, v31, v168\n"
"v_mul_f32 v169, v31, v169\n"
"v_mul_f32 v170, v31, v170\n"
"v_mul_f32 v171, v31, v171\n"
"v_mul_f32 v172, v31, v172\n"
"v_mul_f32 v173, v31, v173\n"
"v_mul_f32 v174, v31, v174\n"
"v_mul_f32 v175, v31, v175\n"
"v_mfma_f32_16x16x32_bf16 a[168:171], v[204:207], v[136:139], a[168:171]\n"
"v_cvt_pk_bf16_f32 v168, v168, v169\n"
"v_cvt_pk_bf16_f32 v169, v170, v171\n"
"v_cvt_pk_bf16_f32 v170, v172, v173\n"
"v_cvt_pk_bf16_f32 v171, v174, v175\n"
"v_mfma_f32_16x16x32_bf16 a[172:175], v[200:203], v[124:127], a[172:175]\n"
"buffer_store_dwordx4 v[168:171], v1, s[36:39], s91 offen offset:3072 sc1\n"
"v_mfma_f32_16x16x32_bf16 a[172:175], v[204:207], v[140:143], a[172:175]\n"
"s_waitcnt lgkmcnt(0)\n"
"ds_read_b128 v[80:83], v2 offset:20480\n"
"v_mfma_f32_16x16x32_bf16 a[176:179], v[208:211], v[112:115], a[176:179]\n"
"buffer_load_dwordx4 a[244:247], v10, s[36:39], s12 offen sc1\n"
"v_mfma_f32_16x16x32_bf16 a[176:179], v[212:215], v[128:131], a[176:179]\n"
"ds_read_b128 v[84:87], v2 offset:30720\n"
"v_accvgpr_read_b32 v144, a64\n"
"v_mfma_f32_16x16x32_bf16 a[180:183], v[208:211], v[116:119], a[180:183]\n"
"ds_read_b128 v[88:91], v2 offset:21504\n"
"v_accvgpr_read_b32 v145, a65\n"
"v_mfma_f32_16x16x32_bf16 a[180:183], v[212:215], v[132:135], a[180:183]\n"
"buffer_load_dwordx4 a[252:255], v11, s[36:39], s12 offen sc1\n"
"v_mfma_f32_16x16x32_bf16 a[184:187], v[208:211], v[120:123], a[184:187]\n"
"s_add_u32 s12, s12, 131072\n"
"ds_read_b128 v[92:95], v2 offset:31744\n"
"v_mfma_f32_16x16x32_bf16 a[184:187], v[212:215], v[136:139], a[184:187]\n"
"ds_read_b128 v[96:99], v2 offset:22528\n"
"v_accvgpr_read_b32 v146, a66\n"
"v_mfma_f32_16x16x32_bf16 a[188:191], v[208:211], v[124:127], a[188:191]\n"
"buffer_load_dwordx4 v[32:35], v10, s[36:39], s12 offen sc1\n"
"v_mfma_f32_16x16x32_bf16 a[188:191], v[212:215], v[140:143], a[188:191]\n"
"s_waitcnt lgkmcnt(3)\n"
"ds_read_b128 v[100:103], v2 offset:32768\n"
"v_mfma_f32_16x16x32_bf16 v[112:115], v[80:83], a[208:211], 0\n"
"ds_read_b128 v[104:107], v2 offset:23552\n"
"v_accvgpr_read_b32 v147, a67\n"
"v_mfma_f32_16x16x32_bf16 v[112:115], v[84:87], a[212:215], v[112:115]\n"
"buffer_load_dwordx4 v[40:43], v11, s[36:39], s12 offen sc1\n"
"v_mfma_f32_16x16x32_bf16 v[200:203], v[80:83], a[216:219], 0\n"
"s_add_u32 s12, s12, 131072\n"
"s_waitcnt lgkmcnt(3)\n"
"v_accvgpr_read_b32 v148, a80\n"
"v_mfma_f32_16x16x32_bf16 v[200:203], v[84:87], a[220:223], v[200:203]\n"
"ds_read_b128 v[108:111], v2 offset:33792\n"
"v_accvgpr_read_b32 v149, a81\n"
"v_mfma_f32_16x16x32_bf16 v[116:119], v[88:91], a[208:211], 0\n"
"buffer_load_dwordx4 v[36:39], v10, s[36:39], s12 offen sc1\n"
"v_mfma_f32_16x16x32_bf16 v[116:119], v[92:95], a[212:215], v[116:119]\n"
"s_waitcnt lgkmcnt(2)\n"
"ds_read_b128 v[80:83], v2 offset:24576\n"
"v_mfma_f32_16x16x32_bf16 v[204:207], v[88:91], a[216:219], 0\n"
"ds_read_b128 v[84:87], v2 offset:34816\n"
"v_accvgpr_read_b32 v150, a82\n"
"v_mfma_f32_16x16x32_bf16 v[204:207], v[92:95], a[220:223], v[204:207]\n"
"buffer_load_dwordx4 v[44:47], v11, s[36:39], s12 offen sc1\n"
"v_mfma_f32_16x16x32_bf16 v[120:123], v[96:99], a[208:211], 0\n"
"s_add_u32 s12, s12, 131072\n"
"ds_read_b128 v[88:91], v2 offset:25600\n"
"v_mfma_f32_16x16x32_bf16 v[120:123], v[100:103], a[212:215], v[120:123]\n"
"s_waitcnt lgkmcnt(3)\n"
"ds_read_b128 v[92:95], v2 offset:35840\n"
"v_mfma_f32_16x16x32_bf16 v[208:211], v[96:99], a[216:219], 0\n"
"buffer_load_dwordx2 v[48:49], v14, s[36:39], s13 offen sc1\n"
"v_mfma_f32_16x16x32_bf16 v[208:211], v[100:103], a[220:223], v[208:211]\n"
"s_add_u32 s91, s91, 131072\n"
"v_accvgpr_read_b32 v151, a83\n"
"v_mul_f32 v144, v28, v144\n"
"v_mfma_f32_16x16x32_bf16 v[124:127], v[104:107], a[208:211], 0\n"
"buffer_load_dwordx2 v[56:57], v15, s[36:39], s13 offen sc1\n"
"v_mfma_f32_16x16x32_bf16 v[124:127], v[108:111], a[212:215], v[124:127]\n"
"s_add_u32 s13, s13, 65536\n"
"v_mul_f32 v145, v28, v145\n"
"v_mul_f32 v146, v28, v146\n"
"v_mfma_f32_16x16x32_bf16 v[212:215], v[104:107], a[216:219], 0\n"
"s_waitcnt lgkmcnt(2)\n"
"v_mul_f32 v147, v28, v147\n"
"v_mul_f32 v148, v28, v148\n"
"v_mul_f32 v149, v28, v149\n"
"v_mul_f32 v150, v28, v150\n"
"v_mul_f32 v151, v28, v151\n"
"v_mfma_f32_16x16x32_bf16 v[212:215], v[108:111], a[220:223], v[212:215]\n"
"s_mov_b32 s88, 0xbfb8aa3b\n"
"v_cvt_pk_bf16_f32 v144, v144, v145\n"
"v_cvt_pk_bf16_f32 v145, v146, v147\n"
"v_cvt_pk_bf16_f32 v146, v148, v149\n"
"v_cvt_pk_bf16_f32 v147, v150, v151\n"
"v_mfma_f32_16x16x32_bf16 v[128:131], v[80:83], a[208:211], 0\n"
"buffer_store_dwordx4 v[144:147], v1, s[36:39], s91 offen offset:0 sc1\n"
"v_mfma_f32_16x16x32_bf16 v[128:131], v[84:87], a[212:215], v[128:131]\n"
"ds_read_b128 v[96:99], v2 offset:26624\n"
"ds_read_b128 v[100:103], v2 offset:36864\n"
"v_mfma_f32_16x16x32_bf16 v[216:219], v[80:83], a[216:219], 0\n"
"buffer_load_dwordx2 v[50:51], v14, s[36:39], s13 offen sc1\n"
"v_mfma_f32_16x16x32_bf16 v[216:219], v[84:87], a[220:223], v[216:219]\n"
"s_waitcnt lgkmcnt(2)\n"
"ds_read_b128 v[104:107], v2 offset:27648\n"
"v_mfma_f32_16x16x32_bf16 v[132:135], v[88:91], a[208:211], 0\n"
"s_waitcnt vmcnt(15)\n"
"s_barrier\n"
"s_add_u32 m0, s7, 40960\n"
"v_mfma_f32_16x16x32_bf16 v[132:135], v[92:95], a[212:215], v[132:135]\n"
"ds_read_b128 v[108:111], v2 offset:37888\n"
"v_accvgpr_read_b32 v152, a68\n"
"v_mfma_f32_16x16x32_bf16 v[220:223], v[88:91], a[216:219], 0\n"
"buffer_load_dwordx4 v1, s[36:39], s8 offen offset:0 sc1 lds\n"
"v_mfma_f32_16x16x32_bf16 v[220:223], v[92:95], a[220:223], v[220:223]\n"
"s_waitcnt lgkmcnt(2)\n"
"ds_read_b128 v[80:83], v2 offset:28672\n"
"v_mfma_f32_16x16x32_bf16 v[136:139], v[96:99], a[208:211], 0\n"
"buffer_load_dwordx4 v1, s[36:39], s8 offen offset:1024 sc1 lds\n"
"v_mfma_f32_16x16x32_bf16 v[136:139], v[100:103], a[212:215], v[136:139]\n"
"ds_read_b128 v[84:87], v2 offset:38912\n"
"v_accvgpr_read_b32 v153, a69\n"
"v_mfma_f32_16x16x32_bf16 v[224:227], v[96:99], a[216:219], 0\n"
"ds_read_b128 v[88:91], v2 offset:29696\n"
"v_accvgpr_read_b32 v154, a70\n"
"v_mfma_f32_16x16x32_bf16 v[224:227], v[100:103], a[220:223], v[224:227]\n"
"s_waitcnt lgkmcnt(3)\n"
"ds_read_b128 v[92:95], v2 offset:39936\n"
"v_mfma_f32_16x16x32_bf16 v[140:143], v[104:107], a[208:211], 0\n"
"buffer_load_dwordx4 v1, s[36:39], s8 offen offset:2048 sc1 lds\n"
"v_mfma_f32_16x16x32_bf16 v[140:143], v[108:111], a[212:215], v[140:143]\n"
"ds_read_b128 v[96:99], v2 offset:40960\n"
"v_accvgpr_read_b32 v155, a71\n"
"v_mfma_f32_16x16x32_bf16 v[228:231], v[104:107], a[216:219], 0\n"
"buffer_load_dwordx4 v1, s[36:39], s8 offen offset:3072 sc1 lds\n"
"v_mfma_f32_16x16x32_bf16 v[228:231], v[108:111], a[220:223], v[228:231]\n"
"s_waitcnt lgkmcnt(3)\n"
"s_add_u32 m0, s7, 45056\n"
"v_accvgpr_read_b32 v156, a84\n"
"v_mfma_f32_16x16x32_bf16 v[192:195], v[80:83], a[208:211], 0\n"
"ds_read_b128 v[100:103], v2 offset:51200\n"
"v_accvgpr_read_b32 v157, a85\n"
"v_mfma_f32_16x16x32_bf16 v[192:195], v[84:87], a[212:215], v[192:195]\n"
"buffer_load_dwordx4 v4, s[36:39], s8 offen sc1 lds\n"
"v_mfma_f32_16x16x32_bf16 v[232:235], v[80:83], a[216:219], 0\n"
"s_add_u32 s8, s8, 491520\n"
"s_add_u32 m0, s7, 61440\n"
"s_waitcnt lgkmcnt(2)\n"
"v_mfma_f32_16x16x32_bf16 v[232:235], v[84:87], a[220:223], v[232:235]\n"
"ds_read_b128 v[104:107], v2 offset:41984\n"
"v_accvgpr_read_b32 v158, a86\n"
"v_mfma_f32_16x16x32_bf16 v[196:199], v[88:91], a[208:211], 0\n"
"buffer_load_dwordx2 v[58:59], v15, s[36:39], s13 offen sc1\n"
"v_mfma_f32_16x16x32_bf16 v[196:199], v[92:95], a[212:215], v[196:199]\n"
"ds_read_b128 v[108:111], v2 offset:52224\n"
"s_add_u32 s13, s13, 65536\n"
"v_mfma_f32_16x16x32_bf16 v[236:239], v[88:91], a[216:219], 0\n"
"buffer_load_dwordx2 v[52:53], v14, s[36:39], s13 offen sc1\n"
"v_mfma_f32_16x16x32_bf16 v[236:239], v[92:95], a[220:223], v[236:239]\n"
"s_waitcnt lgkmcnt(2)\n"
"v_accvgpr_read_b32 v159, a87\n"
"v_mul_f32 v152, v29, v152\n"
"v_mfma_f32_16x16x32_bf16 v[112:115], v[96:99], a[224:227], v[112:115]\n"
"v_mul_f32 v153, v29, v153\n"
"v_mul_f32 v154, v29, v154\n"
"v_mul_f32 v155, v29, v155\n"
"v_mul_f32 v156, v29, v156\n"
"v_mul_f32 v157, v29, v157\n"
"v_mul_f32 v158, v29, v158\n"
"v_mul_f32 v159, v29, v159\n"
"v_mfma_f32_16x16x32_bf16 v[112:115], v[100:103], a[228:231], v[112:115]\n"
"ds_read_b128 v[80:83], v2 offset:43008\n"
"ds_read_b128 v[84:87], v2 offset:53248\n"
"v_cvt_pk_bf16_f32 v152, v152, v153\n"
"v_cvt_pk_bf16_f32 v153, v154, v155\n"
"v_cvt_pk_bf16_f32 v154, v156, v157\n"
"v_cvt_pk_bf16_f32 v155, v158, v159\n"
"v_mfma_f32_16x16x32_bf16 v[200:203], v[96:99], a[232:235], v[200:203]\n"
"buffer_store_dwordx4 v[152:155], v1, s[36:39], s91 offen offset:1024 sc1\n"
"v_mfma_f32_16x16x32_bf16 v[200:203], v[100:103], a[236:239], v[200:203]\n"
"s_waitcnt lgkmcnt(2)\n"
"ds_read_b128 v[88:91], v2 offset:44032\n"
"v_mfma_f32_16x16x32_bf16 v[116:119], v[104:107], a[224:227], v[116:119]\n"
"buffer_load_dwordx2 v[60:61], v15, s[36:39], s13 offen sc1\n"
"v_mfma_f32_16x16x32_bf16 v[116:119], v[108:111], a[228:231], v[116:119]\n"
"ds_read_b128 v[92:95], v2 offset:54272\n"
"s_add_u32 s13, s13, 65536\n"
"v_mfma_f32_16x16x32_bf16 v[204:207], v[104:107], a[232:235], v[204:207]\n"
"buffer_load_dwordx2 v[54:55], v14, s[36:39], s13 offen sc1\n"
"v_mfma_f32_16x16x32_bf16 v[204:207], v[108:111], a[236:239], v[204:207]\n"
"s_waitcnt lgkmcnt(2)\n"
"ds_read_b128 v[96:99], v2 offset:45056\n"
"v_mfma_f32_16x16x32_bf16 v[120:123], v[80:83], a[224:227], v[120:123]\n"
"buffer_load_dwordx2 v[62:63], v15, s[36:39], s13 offen sc1\n"
"v_mfma_f32_16x16x32_bf16 v[120:123], v[84:87], a[228:231], v[120:123]\n"
"ds_read_b128 v[100:103], v2 offset:55296\n"
"s_add_u32 s13, s13, 65536\n"
"v_mfma_f32_16x16x32_bf16 v[208:211], v[80:83], a[232:235], v[208:211]\n"
"buffer_load_dwordx4 v1, s[36:39], s8 offen offset:0 sc1 lds\n"
"v_mfma_f32_16x16x32_bf16 v[208:211], v[84:87], a[236:239], v[208:211]\n"
"s_waitcnt lgkmcnt(2)\n"
"ds_read_b128 v[104:107], v2 offset:46080\n"
"v_mfma_f32_16x16x32_bf16 v[124:127], v[88:91], a[224:227], v[124:127]\n"
"ds_read_b128 v[108:111], v2 offset:56320\n"
"v_accvgpr_read_b32 v160, a72\n"
"v_mfma_f32_16x16x32_bf16 v[124:127], v[92:95], a[228:231], v[124:127]\n"
"buffer_load_dwordx4 v1, s[36:39], s8 offen offset:1024 sc1 lds\n"
"v_mfma_f32_16x16x32_bf16 v[212:215], v[88:91], a[232:235], v[212:215]\n"
"ds_read_b128 v[80:83], v2 offset:47104\n"
"v_accvgpr_read_b32 v161, a73\n"
"v_mfma_f32_16x16x32_bf16 v[212:215], v[92:95], a[236:239], v[212:215]\n"
"s_waitcnt lgkmcnt(3)\n"
"ds_read_b128 v[84:87], v2 offset:57344\n"
"v_mfma_f32_16x16x32_bf16 v[128:131], v[96:99], a[224:227], v[128:131]\n"
"buffer_load_dwordx4 v1, s[36:39], s8 offen offset:2048 sc1 lds\n"
"v_mfma_f32_16x16x32_bf16 v[128:131], v[100:103], a[228:231], v[128:131]\n"
"ds_read_b128 v[88:91], v2 offset:48128\n"
"v_accvgpr_read_b32 v162, a74\n"
"v_mfma_f32_16x16x32_bf16 v[216:219], v[96:99], a[232:235], v[216:219]\n"
"s_waitcnt lgkmcnt(3)\n"
"ds_read_b128 v[92:95], v2 offset:58368\n"
"v_mfma_f32_16x16x32_bf16 v[216:219], v[100:103], a[236:239], v[216:219]\n"
"buffer_load_dwordx4 v1, s[36:39], s8 offen offset:3072 sc1 lds\n"
"v_mfma_f32_16x16x32_bf16 v[132:135], v[104:107], a[224:227], v[132:135]\n"
"s_waitcnt vmcnt(23)\n"
"s_barrier\n"
"s_add_u32 m0, s7, 65536\n"
"v_mfma_f32_16x16x32_bf16 v[132:135], v[108:111], a[228:231], v[132:135]\n"
"s_waitcnt lgkmcnt(2)\n"
"ds_read_b128 v[96:99], v2 offset:49152\n"
"v_mfma_f32_16x16x32_bf16 v[220:223], v[104:107], a[232:235], v[220:223]\n"
"s_cmp_lg_u32 s76, 0\n"
"s_cbranch_scc1 .Lsig3\n"
"s_mov_b64 exec, 1\n"
"v_mov_b32 v8, 1\n"
"buffer_atomic_add v8, off, s[36:39], s14 offset:0 sc1\n"
"s_mov_b64 exec, -1\n"
".Lsig3:\n"
"v_mfma_f32_16x16x32_bf16 v[220:223], v[108:111], a[236:239], v[220:223]\n"
"buffer_load_dwordx4 v4, s[36:39], s8 offen sc1 lds\n"
"v_mfma_f32_16x16x32_bf16 v[136:139], v[80:83], a[224:227], v[136:139]\n"
"s_add_u32 s8, s8, 491520\n"
"s_add_u32 m0, s7, 81920\n"
"v_accvgpr_read_b32 v163, a75\n"
"v_mfma_f32_16x16x32_bf16 v[136:139], v[84:87], a[228:231], v[136:139]\n"
"ds_read_b128 v[100:103], v2 offset:59392\n"
"v_accvgpr_read_b32 v164, a88\n"
"v_accvgpr_read_b32 v165, a89\n"
"v_accvgpr_read_b32 v166, a90\n"
"v_accvgpr_read_b32 v167, a91\n"
"v_mfma_f32_16x16x32_bf16 v[224:227], v[80:83], a[232:235], v[224:227]\n"
"s_waitcnt lgkmcnt(2)\n"
"v_mul_f32 v160, v30, v160\n"
"v_mul_f32 v161, v30, v161\n"
"v_mul_f32 v162, v30, v162\n"
"v_mul_f32 v163, v30, v163\n"
"v_mul_f32 v164, v30, v164\n"
"v_mul_f32 v165, v30, v165\n"
"v_mul_f32 v166, v30, v166\n"
"v_mul_f32 v167, v30, v167\n"
"v_mfma_f32_16x16x32_bf16 v[224:227], v[84:87], a[236:239], v[224:227]\n"
"v_cvt_pk_bf16_f32 v160, v160, v161\n"
"v_cvt_pk_bf16_f32 v161, v162, v163\n"
"v_cvt_pk_bf16_f32 v162, v164, v165\n"
"v_cvt_pk_bf16_f32 v163, v166, v167\n"
"v_mfma_f32_16x16x32_bf16 v[140:143], v[88:91], a[224:227], v[140:143]\n"
"buffer_store_dwordx4 v[160:163], v1, s[36:39], s91 offen offset:2048 sc1\n"
"v_mfma_f32_16x16x32_bf16 v[140:143], v[92:95], a[228:231], v[140:143]\n"
"ds_read_b128 v[104:107], v2 offset:50176\n"
"ds_read_b128 v[108:111], v2 offset:60416\n"
"v_mfma_f32_16x16x32_bf16 v[228:231], v[88:91], a[232:235], v[228:231]\n"
"buffer_load_dwordx2 v[64:65], v14, s[36:39], s13 offen sc1\n"
"v_mfma_f32_16x16x32_bf16 v[228:231], v[92:95], a[236:239], v[228:231]\n"
"s_waitcnt lgkmcnt(2)\n"
"ds_read_b128 v[80:83], v1 offset:0\n"
"v_mfma_f32_16x16x32_bf16 v[192:195], v[96:99], a[224:227], v[192:195]\n"
"buffer_load_dwordx2 v[72:73], v15, s[36:39], s13 offen sc1\n"
"v_mfma_f32_16x16x32_bf16 v[192:195], v[100:103], a[228:231], v[192:195]\n"
"ds_read_b128 v[84:87], v1 offset:10240\n"
"s_add_u32 s13, s13, 65536\n"
"v_mfma_f32_16x16x32_bf16 v[232:235], v[96:99], a[232:235], v[232:235]\n"
"buffer_load_dwordx2 v[66:67], v14, s[36:39], s13 offen sc1\n"
"v_mfma_f32_16x16x32_bf16 v[232:235], v[100:103], a[236:239], v[232:235]\n"
"s_waitcnt lgkmcnt(2)\n"
"ds_read_b128 v[88:91], v1 offset:1024\n"
"v_mfma_f32_16x16x32_bf16 v[196:199], v[104:107], a[224:227], v[196:199]\n"
"buffer_load_dwordx2 v[74:75], v15, s[36:39], s13 offen sc1\n"
"v_mfma_f32_16x16x32_bf16 v[196:199], v[108:111], a[228:231], v[196:199]\n"
"ds_read_b128 v[92:95], v1 offset:11264\n"
"s_add_u32 s13, s13, 65536\n"
"v_mfma_f32_16x16x32_bf16 v[236:239], v[104:107], a[232:235], v[236:239]\n"
"buffer_load_dwordx2 v[68:69], v14, s[36:39], s13 offen sc1\n"
"v_mfma_f32_16x16x32_bf16 v[236:239], v[108:111], a[236:239], v[236:239]\n"
"s_waitcnt lgkmcnt(2)\n"
"ds_read_b128 v[96:99], v1 offset:2048\n"
"v_mfma_f32_16x16x32_bf16 v[112:115], v[80:83], a[240:243], v[112:115]\n"
"buffer_load_dwordx2 v[76:77], v15, s[36:39], s13 offen sc1\n"
"v_mfma_f32_16x16x32_bf16 v[112:115], v[84:87], a[244:247], v[112:115]\n"
"ds_read_b128 v[100:103], v1 offset:12288\n"
"s_add_u32 s13, s13, 65536\n"
"v_mfma_f32_16x16x32_bf16 v[200:203], v[80:83], a[248:251], v[200:203]\n"
"s_add_u32 vcc_hi, vcc_lo, 90112\n"
"buffer_load_dword v9, v0, s[56:59], vcc_hi idxen\n"
"buffer_load_dwordx2 v[70:71], v14, s[36:39], s13 offen sc1\n"
"v_mfma_f32_16x16x32_bf16 v[200:203], v[84:87], a[252:255], v[200:203]\n"
"s_waitcnt lgkmcnt(2)\n"
"ds_read_b128 v[104:107], v1 offset:3072\n"
"v_mfma_f32_16x16x32_bf16 v[116:119], v[88:91], a[240:243], v[116:119]\n"
"buffer_load_dwordx2 v[78:79], v15, s[36:39], s13 offen sc1\n"
"v_mfma_f32_16x16x32_bf16 v[116:119], v[92:95], a[244:247], v[116:119]\n"
"ds_read_b128 v[108:111], v1 offset:13312\n"
"s_add_u32 s13, s13, 65536\n"
"v_mfma_f32_16x16x32_bf16 v[204:207], v[88:91], a[248:251], v[204:207]\n"
"buffer_load_dwordx4 v1, s[36:39], s8 offen offset:0 sc1 lds\n"
"v_mfma_f32_16x16x32_bf16 v[204:207], v[92:95], a[252:255], v[204:207]\n"
"s_waitcnt lgkmcnt(2)\n"
"ds_read_b128 v[80:83], v1 offset:4096\n"
"v_mfma_f32_16x16x32_bf16 v[120:123], v[96:99], a[240:243], v[120:123]\n"
"ds_read_b128 v[84:87], v1 offset:14336\n"
"v_accvgpr_read_b32 v168, a76\n"
"v_mfma_f32_16x16x32_bf16 v[120:123], v[100:103], a[244:247], v[120:123]\n"
"buffer_load_dwordx4 v1, s[36:39], s8 offen offset:1024 sc1 lds\n"
"v_mfma_f32_16x16x32_bf16 v[208:211], v[96:99], a[248:251], v[208:211]\n"
"ds_read_b128 v[88:91], v1 offset:5120\n"
"v_accvgpr_read_b32 v169, a77\n"
"v_mfma_f32_16x16x32_bf16 v[208:211], v[100:103], a[252:255], v[208:211]\n"
"s_waitcnt lgkmcnt(3)\n"
"v_accvgpr_read_b32 v170, a78\n"
"v_accvgpr_read_b32 v171, a79\n"
"v_mfma_f32_16x16x32_bf16 v[124:127], v[104:107], a[240:243], v[124:127]\n"
"v_accvgpr_read_b32 v172, a92\n"
"v_accvgpr_read_b32 v173, a93\n"
"v_accvgpr_read_b32 v174, a94\n"
"v_accvgpr_read_b32 v175, a95\n"
"v_mfma_f32_16x16x32_bf16 v[124:127], v[108:111], a[244:247], v[124:127]\n"
"ds_read_b128 v[92:95], v1 offset:15360\n"
"v_mul_f32 v168, v31, v168\n"
"v_mul_f32 v169, v31, v169\n"
"v_mul_f32 v170, v31, v170\n"
"v_mul_f32 v171, v31, v171\n"
"v_mul_f32 v172, v31, v172\n"
"v_mul_f32 v173, v31, v173\n"
"v_mul_f32 v174, v31, v174\n"
"v_mul_f32 v175, v31, v175\n"
"v_mfma_f32_16x16x32_bf16 v[212:215], v[104:107], a[248:251], v[212:215]\n"
"s_waitcnt lgkmcnt(2)\n"
"v_cvt_pk_bf16_f32 v168, v168, v169\n"
"v_cvt_pk_bf16_f32 v169, v170, v171\n"
"v_cvt_pk_bf16_f32 v170, v172, v173\n"
"v_cvt_pk_bf16_f32 v171, v174, v175\n"
"v_mfma_f32_16x16x32_bf16 v[212:215], v[108:111], a[252:255], v[212:215]\n"
"buffer_store_dwordx4 v[168:171], v1, s[36:39], s91 offen offset:3072 sc1\n"
"v_mfma_f32_16x16x32_bf16 v[128:131], v[80:83], a[240:243], v[128:131]\n"
"buffer_load_dword v7, off, s[36:39], s14 offset:0 sc1\n"
"v_mfma_f32_16x16x32_bf16 v[128:131], v[84:87], a[244:247], v[128:131]\n"
"ds_read_b128 v[96:99], v1 offset:6144\n"
"ds_read_b128 v[100:103], v1 offset:16384\n"
"v_mfma_f32_16x16x32_bf16 v[216:219], v[80:83], a[248:251], v[216:219]\n"
"buffer_load_dwordx4 v1, s[36:39], s8 offen offset:2048 sc1 lds\n"
"v_mfma_f32_16x16x32_bf16 v[216:219], v[84:87], a[252:255], v[216:219]\n"
"s_waitcnt lgkmcnt(2)\n"
"ds_read_b128 v[104:107], v1 offset:7168\n"
"v_mfma_f32_16x16x32_bf16 v[132:135], v[88:91], a[240:243], v[132:135]\n"
"s_waitcnt vmcnt(35)\n"
"s_barrier\n"
"v_accvgpr_read_b32 v144, a96\n"
"v_mfma_f32_16x16x32_bf16 v[132:135], v[92:95], a[244:247], v[132:135]\n"
"ds_read_b128 v[108:111], v1 offset:17408\n"
"v_accvgpr_read_b32 v145, a97\n"
"v_mfma_f32_16x16x32_bf16 v[220:223], v[88:91], a[248:251], v[220:223]\n"
"buffer_load_dwordx4 v1, s[36:39], s8 offen offset:3072 sc1 lds\n"
"v_mfma_f32_16x16x32_bf16 v[220:223], v[92:95], a[252:255], v[220:223]\n"
"s_waitcnt lgkmcnt(2)\n"
"s_add_u32 m0, s7, 86016\n"
"v_accvgpr_read_b32 v146, a98\n"
"v_mfma_f32_16x16x32_bf16 v[136:139], v[96:99], a[240:243], v[136:139]\n"
"buffer_load_dwordx4 v4, s[36:39], s8 offen sc1 lds\n"
"v_mfma_f32_16x16x32_bf16 v[136:139], v[100:103], a[244:247], v[136:139]\n"
"ds_read_b128 v[80:83], v1 offset:8192\n"
"ds_read_b128 v[84:87], v1 offset:18432\n"
"v_mfma_f32_16x16x32_bf16 v[224:227], v[96:99], a[248:251], v[224:227]\n"
"s_add_u32 s8, s8, 491520\n"
"s_add_u32 m0, s7, 102400\n"
"v_accvgpr_read_b32 v147, a99\n"
"v_mfma_f32_16x16x32_bf16 v[224:227], v[100:103], a[252:255], v[224:227]\n"
"s_waitcnt lgkmcnt(2)\n"
"ds_read_b128 v[88:91], v1 offset:9216\n"
"v_mfma_f32_16x16x32_bf16 v[140:143], v[104:107], a[240:243], v[140:143]\n"
"buffer_load_dwordx4 v1, s[36:39], s8 offen offset:0 sc1 lds\n"
"v_mfma_f32_16x16x32_bf16 v[140:143], v[108:111], a[244:247], v[140:143]\n"
"ds_read_b128 v[92:95], v1 offset:19456\n"
"v_accvgpr_read_b32 v148, a112\n"
"v_mfma_f32_16x16x32_bf16 v[228:231], v[104:107], a[248:251], v[228:231]\n"
"buffer_load_dwordx4 v1, s[36:39], s8 offen offset:1024 sc1 lds\n"
"v_mfma_f32_16x16x32_bf16 v[228:231], v[108:111], a[252:255], v[228:231]\n"
"s_waitcnt lgkmcnt(2)\n"
"ds_read_b128 v[96:99], v3 offset:0\n"
"v_mfma_f32_16x16x32_bf16 v[192:195], v[80:83], a[240:243], v[192:195]\n"
"ds_read_b128 v[100:103], v3 offset:10240\n"
"v_accvgpr_read_b32 v149, a113\n"
"v_mfma_f32_16x16x32_bf16 v[192:195], v[84:87], a[244:247], v[192:195]\n"
"buffer_load_dwordx4 v1, s[36:39], s8 offen offset:2048 sc1 lds\n"
"v_mfma_f32_16x16x32_bf16 v[232:235], v[80:83], a[248:251], v[232:235]\n"
"ds_read_b128 v[104:107], v3 offset:1024\n"
"v_accvgpr_read_b32 v150, a114\n"
"v_mfma_f32_16x16x32_bf16 v[232:235], v[84:87], a[252:255], v[232:235]\n"
"s_waitcnt lgkmcnt(3)\n"
"ds_read_b128 v[108:111], v3 offset:11264\n"
"v_mfma_f32_16x16x32_bf16 v[196:199], v[88:91], a[240:243], v[196:199]\n"
"buffer_load_dwordx4 v1, s[36:39], s8 offen offset:3072 sc1 lds\n"
"v_mfma_f32_16x16x32_bf16 v[196:199], v[92:95], a[244:247], v[196:199]\n"
"s_add_u32 m0, s7, 106496\n"
"v_accvgpr_read_b32 v151, a115\n"
"v_mul_f32 v144, v28, v144\n"
"v_mfma_f32_16x16x32_bf16 v[236:239], v[88:91], a[248:251], v[236:239]\n"
"s_waitcnt lgkmcnt(2)\n"
"v_mul_f32 v145, v28, v145\n"
"v_mul_f32 v146, v28, v146\n"
"v_mfma_f32_16x16x32_bf16 v[236:239], v[92:95], a[252:255], v[236:239]\n"
"s_add_u32 s91, s91, 131072\n"
"v_mul_f32 v147, v28, v147\n"
"v_mul_f32 v148, v28, v148\n"
"v_mul_f32 v149, v28, v149\n"
"v_mul_f32 v150, v28, v150\n"
"v_mul_f32 v151, v28, v151\n"
"v_mfma_f32_16x16x32_bf16 v[112:115], v[96:99], v[32:35], v[112:115]\n"
"v_cvt_pk_bf16_f32 v144, v144, v145\n"
"v_cvt_pk_bf16_f32 v145, v146, v147\n"
"v_cvt_pk_bf16_f32 v146, v148, v149\n"
"v_cvt_pk_bf16_f32 v147, v150, v151\n"
"v_mfma_f32_16x16x32_bf16 v[112:115], v[100:103], v[36:39], v[112:115]\n"
"ds_read_b128 v[80:83], v3 offset:2048\n"
"ds_read_b128 v[84:87], v3 offset:12288\n"
"buffer_store_dwordx4 v[144:147], v1, s[36:39], s91 offen offset:0 sc1\n"
"v_mfma_f32_16x16x32_bf16 v[200:203], v[96:99], v[40:43], v[200:203]\n"
"s_waitcnt lgkmcnt(2)\n"
"ds_read_b128 v[88:91], v3 offset:3072\n"
"v_mfma_f32_16x16x32_bf16 v[200:203], v[100:103], v[44:47], v[200:203]\n"
"s_waitcnt vmcnt(8)\n"
"v_cmp_gt_u32 s[92:93], 8, v7\n"
"s_nop 0\n"
"s_cmp_eq_u64 s[92:93], 0\n"
"s_cbranch_scc0 .Lspin3\n"
".Lback3:\n"
"v_mfma_f32_16x16x32_bf16 v[116:119], v[104:107], v[32:35], v[116:119]\n"
"buffer_load_dwordx4 a[192:195], v10, s[36:39], s11 offen sc1\n"
"v_mfma_f32_16x16x32_bf16 v[116:119], v[108:111], v[36:39], v[116:119]\n"
"ds_read_b128 v[92:95], v3 offset:13312\n"
"v_accvgpr_read_b32 v152, a100\n"
"v_mfma_f32_16x16x32_bf16 v[204:207], v[104:107], v[40:43], v[204:207]\n"
"buffer_load_dwordx4 a[196:199], v12, s[36:39], s11 offen sc1\n"
"v_mfma_f32_16x16x32_bf16 v[204:207], v[108:111], v[44:47], v[204:207]\n"
"s_waitcnt lgkmcnt(2)\n"
"ds_read_b128 v[96:99], v3 offset:4096\n"
"v_mfma_f32_16x16x32_bf16 v[120:123], v[80:83], v[32:35], v[120:123]\n"
"ds_read_b128 v[100:103], v3 offset:14336\n"
"v_accvgpr_read_b32 v153, a101\n"
"v_mfma_f32_16x16x32_bf16 v[120:123], v[84:87], v[36:39], v[120:123]\n"
"buffer_load_dwordx4 a[200:203], v11, s[36:39], s11 offen sc1\n"
"v_mfma_f32_16x16x32_bf16 v[208:211], v[80:83], v[40:43], v[208:211]\n"
"ds_read_b128 v[104:107], v3 offset:5120\n"
"v_accvgpr_read_b32 v154, a102\n"
"v_mfma_f32_16x16x32_bf16 v[208:211], v[84:87], v[44:47], v[208:211]\n"
"s_waitcnt lgkmcnt(3)\n"
"ds_read_b128 v[108:111], v3 offset:15360\n"
"v_mfma_f32_16x16x32_bf16 v[124:127], v[88:91], v[32:35], v[124:127]\n"
"buffer_load_dwordx4 a[204:207], v13, s[36:39], s11 offen sc1\n"
"v_mfma_f32_16x16x32_bf16 v[124:127], v[92:95], v[36:39], v[124:127]\n"
"s_add_u32 s11, s11, 1572864\n"
"ds_read_b128 v[80:83], v3 offset:6144\n"
"v_mfma_f32_16x16x32_bf16 v[212:215], v[88:91], v[40:43], v[212:215]\n"
"s_waitcnt lgkmcnt(3)\n"
"ds_read_b128 v[84:87], v3 offset:16384\n"
"v_mfma_f32_16x16x32_bf16 v[212:215], v[92:95], v[44:47], v[212:215]\n"
"buffer_load_dwordx4 v4, s[36:39], s8 offen sc1 lds\n"
"v_mfma_f32_16x16x32_bf16 v[128:131], v[96:99], v[32:35], v[128:131]\n"
"s_sub_u32 s8, s8, 1433600\n"
"ds_read_b128 v[88:91], v3 offset:7168\n"
"v_mfma_f32_16x16x32_bf16 v[128:131], v[100:103], v[36:39], v[128:131]\n"
"s_waitcnt lgkmcnt(3)\n"
"ds_read_b128 v[92:95], v3 offset:17408\n"
"v_mfma_f32_16x16x32_bf16 v[216:219], v[96:99], v[40:43], v[216:219]\n"
"buffer_load_dwordx4 a[0:3], v10, s[36:39], s11 offen sc1\n"
"v_mfma_f32_16x16x32_bf16 v[216:219], v[100:103], v[44:47], v[216:219]\n"
"ds_read_b128 v[96:99], v3 offset:8192\n"
"v_accvgpr_read_b32 v155, a103\n"
"v_mfma_f32_16x16x32_bf16 v[132:135], v[104:107], v[32:35], v[132:135]\n"
"s_barrier\n"
"s_add_u32 m0, s7, 0\n"
"v_mfma_f32_16x16x32_bf16 v[132:135], v[108:111], v[36:39], v[132:135]\n"
"buffer_load_dwordx4 a[4:7], v12, s[36:39], s11 offen sc1\n"
"v_mfma_f32_16x16x32_bf16 v[220:223], v[104:107], v[40:43], v[220:223]\n"
"s_waitcnt lgkmcnt(3)\n"
"v_accvgpr_read_b32 v156, a116\n"
"v_accvgpr_read_b32 v157, a117\n"
"v_mfma_f32_16x16x32_bf16 v[220:223], v[108:111], v[44:47], v[220:223]\n"
"ds_read_b128 v[100:103], v3 offset:18432\n"
"v_accvgpr_read_b32 v158, a118\n"
"v_mfma_f32_16x16x32_bf16 v[136:139], v[80:83], v[32:35], v[136:139]\n"
"v_accvgpr_read_b32 v159, a119\n"
"v_mul_f32 v152, v29, v152\n"
"v_mfma_f32_16x16x32_bf16 v[136:139], v[84:87], v[36:39], v[136:139]\n"
"s_waitcnt lgkmcnt(2)\n"
"v_mul_f32 v153, v29, v153\n"
"v_mul_f32 v154, v29, v154\n"
"v_mul_f32 v155, v29, v155\n"
"v_mul_f32 v156, v29, v156\n"
"v_mul_f32 v157, v29, v157\n"
"v_mul_f32 v158, v29, v158\n"
"v_mul_f32 v159, v29, v159\n"
"v_mfma_f32_16x16x32_bf16 v[224:227], v[80:83], v[40:43], v[224:227]\n"
"v_cvt_pk_bf16_f32 v152, v152, v153\n"
"v_cvt_pk_bf16_f32 v153, v154, v155\n"
"v_cvt_pk_bf16_f32 v154, v156, v157\n"
"v_cvt_pk_bf16_f32 v155, v158, v159\n"
"v_mfma_f32_16x16x32_bf16 v[224:227], v[84:87], v[44:47], v[224:227]\n"
"buffer_store_dwordx4 v[152:155], v1, s[36:39], s91 offen offset:1024 sc1\n"
"v_mfma_f32_16x16x32_bf16 v[140:143], v[88:91], v[32:35], v[140:143]\n"
"ds_read_b128 v[104:107], v3 offset:9216\n"
"v_accvgpr_read_b32 v160, a104\n"
"v_mfma_f32_16x16x32_bf16 v[140:143], v[92:95], v[36:39], v[140:143]\n"
"ds_read_b128 v[108:111], v3 offset:19456\n"
"s_waitcnt lgkmcnt(2)\n"
"v_mfma_f32_16x16x32_bf16 v[228:231], v[88:91], v[40:43], v[228:231]\n"
"buffer_load_dwordx4 a[8:11], v11, s[36:39], s11 offen sc1\n"
"v_mfma_f32_16x16x32_bf16 v[228:231], v[92:95], v[44:47], v[228:231]\n"
"ds_read_b128 v[80:83], v3 offset:20480\n"
"v_accvgpr_read_b32 v161, a105\n"
"v_mfma_f32_16x16x32_bf16 v[192:195], v[96:99], v[32:35], v[192:195]\n"
"buffer_load_dwordx4 a[12:15], v13, s[36:39], s11 offen sc1\n"
"v_mfma_f32_16x16x32_bf16 v[192:195], v[100:103], v[36:39], v[192:195]\n"
"ds_read_b128 v[84:87], v3 offset:30720\n"
"s_add_u32 s11, s11, 1572864\n"
"v_mfma_f32_16x16x32_bf16 v[232:235], v[96:99], v[40:43], v[232:235]\n"
"ds_read_b128 v[88:91], v3 offset:21504\n"
"v_accvgpr_read_b32 v162, a106\n"
"v_mfma_f32_16x16x32_bf16 v[232:235], v[100:103], v[44:47], v[232:235]\n"
"s_waitcnt lgkmcnt(3)\n"
"ds_read_b128 v[92:95], v3 offset:31744\n"
"v_mfma_f32_16x16x32_bf16 v[196:199], v[104:107], v[32:35], v[196:199]\n"
"buffer_load_dwordx4 v1, s[36:39], s8 offen offset:0 sc1 lds\n"
"v_mfma_f32_16x16x32_bf16 v[196:199], v[108:111], v[36:39], v[196:199]\n"
"ds_read_b128 v[96:99], v3 offset:22528\n"
"v_accvgpr_read_b32 v163, a107\n"
"v_mfma_f32_16x16x32_bf16 v[236:239], v[104:107], v[40:43], v[236:239]\n"
"buffer_load_dwordx4 v1, s[36:39], s8 offen offset:1024 sc1 lds\n"
"v_mfma_f32_16x16x32_bf16 v[236:239], v[108:111], v[44:47], v[236:239]\n"
"s_waitcnt lgkmcnt(3)\n"
"ds_read_b128 v[100:103], v3 offset:32768\n"
"v_mfma_f32_16x16x32_bf16 v[112:115], v[80:83], v[48:51], v[112:115]\n"
"ds_read_b128 v[104:107], v3 offset:23552\n"
"v_accvgpr_read_b32 v164, a120\n"
"v_mfma_f32_16x16x32_bf16 v[112:115], v[84:87], v[52:55], v[112:115]\n"
"buffer_load_dwordx4 v1, s[36:39], s8 offen offset:2048 sc1 lds\n"
"v_mfma_f32_16x16x32_bf16 v[200:203], v[80:83], v[56:59], v[200:203]\n"
"s_waitcnt lgkmcnt(3)\n"
"ds_read_b128 v[108:111], v3 offset:33792\n"
"v_mfma_f32_16x16x32_bf16 v[200:203], v[84:87], v[60:63], v[200:203]\n"
"ds_read_b128 v[80:83], v3 offset:24576\n"
"v_accvgpr_read_b32 v165, a121\n"
"v_mfma_f32_16x16x32_bf16 v[116:119], v[88:91], v[48:51], v[116:119]\n"
"buffer_load_dwordx4 v1, s[36:39], s8 offen offset:3072 sc1 lds\n"
"v_mfma_f32_16x16x32_bf16 v[116:119], v[92:95], v[52:55], v[116:119]\n"
"s_add_u32 m0, s7, 4096\n"
"s_waitcnt lgkmcnt(3)\n"
"v_accvgpr_read_b32 v166, a122\n"
"v_mfma_f32_16x16x32_bf16 v[204:207], v[88:91], v[56:59], v[204:207]\n"
"ds_read_b128 v[84:87], v3 offset:34816\n"
"v_accvgpr_read_b32 v167, a123\n"
"v_mfma_f32_16x16x32_bf16 v[204:207], v[92:95], v[60:63], v[204:207]\n"
"buffer_load_dwordx4 v4, s[36:39], s8 offen sc1 lds\n"
"v_mfma_f32_16x16x32_bf16 v[120:123], v[96:99], v[48:51], v[120:123]\n"
"s_add_u32 s8, s8, 491520\n"
"v_mul_f32 v160, v30, v160\n"
"v_mul_f32 v161, v30, v161\n"
"v_mfma_f32_16x16x32_bf16 v[120:123], v[100:103], v[52:55], v[120:123]\n"
"s_waitcnt lgkmcnt(2)\n"
"v_mul_f32 v162, v30, v162\n"
"v_mul_f32 v163, v30, v163\n"
"v_mfma_f32_16x16x32_bf16 v[208:211], v[96:99], v[56:59], v[208:211]\n"
"buffer_load_dwordx4 a[16:19], v10, s[36:39], s11 offen sc1\n"
"v_mfma_f32_16x16x32_bf16 v[208:211], v[100:103], v[60:63], v[208:211]\n"
"v_mul_f32 v164, v30, v164\n"
"v_mul_f32 v165, v30, v165\n"
"v_mul_f32 v166, v30, v166\n"
"v_mul_f32 v167, v30, v167\n"
"v_mfma_f32_16x16x32_bf16 v[124:127], v[104:107], v[48:51], v[124:127]\n"
"v_cvt_pk_bf16_f32 v160, v160, v161\n"
"v_cvt_pk_bf16_f32 v161, v162, v163\n"
"v_cvt_pk_bf16_f32 v162, v164, v165\n"
"v_cvt_pk_bf16_f32 v163, v166, v167\n"
"v_mfma_f32_16x16x32_bf16 v[124:127], v[108:111], v[52:55], v[124:127]\n"
"ds_read_b128 v[88:91], v3 offset:25600\n"
"ds_read_b128 v[92:95], v3 offset:35840\n"
"buffer_store_dwordx4 v[160:163], v1, s[36:39], s91 offen offset:2048 sc1\n"
"v_mfma_f32_16x16x32_bf16 v[212:215], v[104:107], v[56:59], v[212:215]\n"
"s_waitcnt lgkmcnt(2)\n"
"ds_read_b128 v[96:99], v3 offset:26624\n"
"v_mfma_f32_16x16x32_bf16 v[212:215], v[108:111], v[60:63], v[212:215]\n"
"ds_read_b128 v[100:103], v3 offset:36864\n"
"v_accvgpr_read_b32 v168, a108\n"
"v_mfma_f32_16x16x32_bf16 v[128:131], v[80:83], v[48:51], v[128:131]\n"
"buffer_load_dwordx4 a[20:23], v12, s[36:39], s11 offen sc1\n"
"v_mfma_f32_16x16x32_bf16 v[128:131], v[84:87], v[52:55], v[128:131]\n"
"ds_read_b128 v[104:107], v3 offset:27648\n"
"v_accvgpr_read_b32 v169, a109\n"
"v_mfma_f32_16x16x32_bf16 v[216:219], v[80:83], v[56:59], v[216:219]\n"
"ds_read_b128 v[108:111], v3 offset:37888\n"
"v_accvgpr_read_b32 v170, a110\n"
"v_mfma_f32_16x16x32_bf16 v[216:219], v[84:87], v[60:63], v[216:219]\n"
"s_waitcnt lgkmcnt(4)\n"
"ds_read_b128 v[80:83], v3 offset:28672\n"
"v_mfma_f32_16x16x32_bf16 v[132:135], v[88:91], v[48:51], v[132:135]\n"
"s_barrier\n"
"s_add_u32 m0, s7, 122880\n"
"v_mfma_f32_16x16x32_bf16 v[132:135], v[92:95], v[52:55], v[132:135]\n"
"buffer_load_dwordx4 a[24:27], v11, s[36:39], s11 offen sc1\n"
"v_mfma_f32_16x16x32_bf16 v[220:223], v[88:91], v[56:59], v[220:223]\n"
"s_waitcnt lgkmcnt(3)\n"
"ds_read_b128 v[84:87], v3 offset:38912\n"
"v_mfma_f32_16x16x32_bf16 v[220:223], v[92:95], v[60:63], v[220:223]\n"
"buffer_load_dwordx4 a[28:31], v13, s[36:39], s11 offen sc1\n"
"v_mfma_f32_16x16x32_bf16 v[136:139], v[96:99], v[48:51], v[136:139]\n"
"s_add_u32 s11, s11, 1572864\n"
"ds_read_b128 v[88:91], v3 offset:29696\n"
"v_mfma_f32_16x16x32_bf16 v[136:139], v[100:103], v[52:55], v[136:139]\n"
"s_waitcnt lgkmcnt(3)\n"
"ds_read_b128 v[92:95], v3 offset:39936\n"
"v_mfma_f32_16x16x32_bf16 v[224:227], v[96:99], v[56:59], v[224:227]\n"
"buffer_load_dwordx4 v1, s[36:39], s8 offen offset:0 sc1 lds\n"
"v_mfma_f32_16x16x32_bf16 v[224:227], v[100:103], v[60:63], v[224:227]\n"
"ds_read_b128 v[96:99], v1 offset:20480\n"
"v_accvgpr_read_b32 v171, a111\n"
"v_mfma_f32_16x16x32_bf16 v[140:143], v[104:107], v[48:51], v[140:143]\n"
"ds_read_b128 v[100:103], v1 offset:30720\n"
"v_accvgpr_read_b32 v172, a124\n"
"v_mfma_f32_16x16x32_bf16 v[140:143], v[108:111], v[52:55], v[140:143]\n"
"buffer_load_dwordx4 v1, s[36:39], s8 offen offset:1024 sc1 lds\n"
"v_mfma_f32_16x16x32_bf16 v[228:231], v[104:107], v[56:59], v[228:231]\n"
"s_waitcnt lgkmcnt(4)\n"
"ds_read_b128 v[104:107], v1 offset:21504\n"
"v_mfma_f32_16x16x32_bf16 v[228:231], v[108:111], v[60:63], v[228:231]\n"
"ds_read_b128 v[108:111], v1 offset:31744\n"
"v_accvgpr_read_b32 v173, a125\n"
"v_mfma_f32_16x16x32_bf16 v[192:195], v[80:83], v[48:51], v[192:195]\n"
"buffer_load_dwordx4 v1, s[36:39], s8 offen offset:2048 sc1 lds\n"
"v_mfma_f32_16x16x32_bf16 v[192:195], v[84:87], v[52:55], v[192:195]\n"
"s_waitcnt lgkmcnt(4)\n"
"v_accvgpr_read_b32 v174, a126\n"
"v_accvgpr_read_b32 v175, a127\n"
"v_mfma_f32_16x16x32_bf16 v[232:235], v[80:83], v[56:59], v[232:235]\n"
"ds_read_b128 v[80:83], v1 offset:22528\n"
"v_mul_f32 v168, v31, v168\n"
"v_mfma_f32_16x16x32_bf16 v[232:235], v[84:87], v[60:63], v[232:235]\n"
"buffer_load_dwordx4 v1, s[36:39], s8 offen offset:3072 sc1 lds\n"
"v_mfma_f32_16x16x32_bf16 v[196:199], v[88:91], v[48:51], v[196:199]\n"
"s_add_u32 m0, s7, 126976\n"
"v_mul_f32 v169, v31, v169\n"
"v_mul_f32 v170, v31, v170\n"
"v_mfma_f32_16x16x32_bf16 v[196:199], v[92:95], v[52:55], v[196:199]\n"
"s_waitcnt lgkmcnt(3)\n"
"v_mul_f32 v171, v31, v171\n"
"v_mul_f32 v172, v31, v172\n"
"v_mfma_f32_16x16x32_bf16 v[236:239], v[88:91], v[56:59], v[236:239]\n"
"v_mul_f32 v173, v31, v173\n"
"v_mul_f32 v174, v31, v174\n"
"v_mul_f32 v175, v31, v175\n"
"v_mfma_f32_16x16x32_bf16 v[236:239], v[92:95], v[60:63], v[236:239]\n"
"v_cvt_pk_bf16_f32 v168, v168, v169\n"
"v_cvt_pk_bf16_f32 v169, v170, v171\n"
"v_cvt_pk_bf16_f32 v170, v172, v173\n"
"v_cvt_pk_bf16_f32 v171, v174, v175\n"
"v_mfma_f32_16x16x32_bf16 v[112:115], v[96:99], v[64:67], v[112:115]\n"
"buffer_store_dwordx4 v[168:171], v1, s[36:39], s91 offen offset:3072 sc1\n"
"v_mfma_f32_16x16x32_bf16 v[112:115], v[100:103], v[68:71], v[112:115]\n"
"ds_read_b128 v[84:87], v1 offset:32768\n"
"s_waitcnt lgkmcnt(2)\n"
"v_mfma_f32_16x16x32_bf16 v[200:203], v[96:99], v[72:75], v[200:203]\n"
"buffer_load_dwordx4 v4, s[36:39], s8 offen sc1 lds\n"
"v_mfma_f32_16x16x32_bf16 v[200:203], v[100:103], v[76:79], v[200:203]\n"
"s_add_u32 s8, s8, 491520\n"
"ds_read_b128 v[88:91], v1 offset:23552\n"
"v_mfma_f32_16x16x32_bf16 v[116:119], v[104:107], v[64:67], v[116:119]\n"
"ds_read_b128 v[92:95], v1 offset:33792\n"
"v_accvgpr_read_b32 v144, a128\n"
"v_mfma_f32_16x16x32_bf16 v[116:119], v[108:111], v[68:71], v[116:119]\n"
"buffer_load_dwordx4 a[32:35], v10, s[36:39], s11 offen sc1\n"
"v_mfma_f32_16x16x32_bf16 v[204:207], v[104:107], v[72:75], v[204:207]\n"
"ds_read_b128 v[96:99], v1 offset:24576\n"
"v_accvgpr_read_b32 v145, a129\n"
"v_mfma_f32_16x16x32_bf16 v[204:207], v[108:111], v[76:79], v[204:207]\n"
"s_waitcnt lgkmcnt(3)\n"
"ds_read_b128 v[100:103], v1 offset:34816\n"
"v_mfma_f32_16x16x32_bf16 v[120:123], v[80:83], v[64:67], v[120:123]\n"
"buffer_load_dwordx4 a[36:39], v12, s[36:39], s11 offen sc1\n"
"v_mfma_f32_16x16x32_bf16 v[120:123], v[84:87], v[68:71], v[120:123]\n"
"ds_read_b128 v[104:107], v1 offset:25600\n"
"v_accvgpr_read_b32 v146, a130\n"
"v_mfma_f32_16x16x32_bf16 v[208:211], v[80:83], v[72:75], v[208:211]\n"
"s_waitcnt lgkmcnt(3)\n"
"ds_read_b128 v[108:111], v1 offset:35840\n"
"v_mfma_f32_16x16x32_bf16 v[208:211], v[84:87], v[76:79], v[208:211]\n"
"buffer_load_dwordx4 a[40:43], v11, s[36:39], s11 offen sc1\n"
"v_mfma_f32_16x16x32_bf16 v[124:127], v[88:91], v[64:67], v[124:127]\n"
"ds_read_b128 v[80:83], v1 offset:26624\n"
"v_accvgpr_read_b32 v147, a131\n"
"v_mfma_f32_16x16x32_bf16 v[124:127], v[92:95], v[68:71], v[124:127]\n"
"s_waitcnt lgkmcnt(3)\n"
"ds_read_b128 v[84:87], v1 offset:36864\n"
"v_mfma_f32_16x16x32_bf16 v[212:215], v[88:91], v[72:75], v[212:215]\n"
"buffer_load_dwordx4 a[44:47], v13, s[36:39], s11 offen sc1\n"
"v_mfma_f32_16x16x32_bf16 v[212:215], v[92:95], v[76:79], v[212:215]\n"
"s_sub_u32 s11, s11, 4587520\n"
"ds_read_b128 v[88:91], v1 offset:27648\n"
"v_mfma_f32_16x16x32_bf16 v[128:131], v[96:99], v[64:67], v[128:131]\n"
"ds_read_b128 v[92:95], v1 offset:37888\n"
"v_accvgpr_read_b32 v148, a144\n"
"v_mfma_f32_16x16x32_bf16 v[128:131], v[100:103], v[68:71], v[128:131]\n"
"s_waitcnt lgkmcnt(4)\n"
"v_accvgpr_read_b32 v149, a145\n"
"v_accvgpr_read_b32 v150, a146\n"
"v_mfma_f32_16x16x32_bf16 v[216:219], v[96:99], v[72:75], v[216:219]\n"
"ds_read_b128 v[96:99], v1 offset:28672\n"
"v_accvgpr_read_b32 v151, a147\n"
"v_mfma_f32_16x16x32_bf16 v[216:219], v[100:103], v[76:79], v[216:219]\n"
"ds_read_b128 v[100:103], v1 offset:38912\n"
"v_mul_f32 v144, v28, v144\n"
"v_mfma_f32_16x16x32_bf16 v[132:135], v[104:107], v[64:67], v[132:135]\n"
"s_waitcnt vmcnt(26)\n"
"s_barrier\n"
"v_mul_f32 v145, v28, v145\n"
"v_mfma_f32_16x16x32_bf16 v[132:135], v[108:111], v[68:71], v[132:135]\n"
"buffer_load_dwordx4 a[48:51], v10, s[36:39], s11 offen sc1\n"
"v_mfma_f32_16x16x32_bf16 v[220:223], v[104:107], v[72:75], v[220:223]\n"
"s_waitcnt lgkmcnt(4)\n"
"ds_read_b128 v[104:107], v1 offset:29696\n"
"v_mfma_f32_16x16x32_bf16 v[220:223], v[108:111], v[76:79], v[220:223]\n"
"buffer_load_dwordx4 a[52:55], v12, s[36:39], s11 offen sc1\n"
"v_mfma_f32_16x16x32_bf16 v[136:139], v[80:83], v[64:67], v[136:139]\n"
"ds_read_b128 v[108:111], v1 offset:39936\n"
"v_mul_f32 v146, v28, v146\n"
"v_mfma_f32_16x16x32_bf16 v[136:139], v[84:87], v[68:71], v[136:139]\n"
"s_waitcnt lgkmcnt(4)\n"
"v_mul_f32 v147, v28, v147\n"
"v_mul_f32 v148, v28, v148\n"
"v_mfma_f32_16x16x32_bf16 v[224:227], v[80:83], v[72:75], v[224:227]\n"
"buffer_load_dwordx4 a[56:59], v11, s[36:39], s11 offen sc1\n"
"v_mfma_f32_16x16x32_bf16 v[224:227], v[84:87], v[76:79], v[224:227]\n"
"ds_read_b128 v[80:83], v1 offset:40960\n"
"v_mul_f32 v149, v28, v149\n"
"v_mfma_f32_16x16x32_bf16 v[140:143], v[88:91], v[64:67], v[140:143]\n"
"ds_read_b128 v[84:87], v1 offset:51200\n"
"v_mul_f32 v150, v28, v150\n"
"v_mfma_f32_16x16x32_bf16 v[140:143], v[92:95], v[68:71], v[140:143]\n"
"buffer_load_dwordx4 a[60:63], v13, s[36:39], s11 offen sc1\n"
"v_mfma_f32_16x16x32_bf16 v[228:231], v[88:91], v[72:75], v[228:231]\n"
"s_add_u32 s11, s11, 1572864\n"
"s_waitcnt lgkmcnt(4)\n"
"v_mul_f32 v151, v28, v151\n"
"v_mfma_f32_16x16x32_bf16 v[228:231], v[92:95], v[76:79], v[228:231]\n"
"s_add_u32 s91, s91, 131072\n"
"v_cvt_pk_bf16_f32 v144, v144, v145\n"
"v_cvt_pk_bf16_f32 v145, v146, v147\n"
"v_mfma_f32_16x16x32_bf16 v[192:195], v[96:99], v[64:67], v[192:195]\n"
"ds_read_b128 v[88:91], v1 offset:41984\n"
"v_cvt_pk_bf16_f32 v146, v148, v149\n"
"v_mfma_f32_16x16x32_bf16 v[192:195], v[100:103], v[68:71], v[192:195]\n"
"s_waitcnt lgkmcnt(3)\n"
"v_cvt_pk_bf16_f32 v147, v150, v151\n"
"v_accvgpr_read_b32 v152, a132\n"
"v_mfma_f32_16x16x32_bf16 v[232:235], v[96:99], v[72:75], v[232:235]\n"
"buffer_store_dwordx4 v[144:147], v1, s[36:39], s91 offen offset:0 sc1\n"
"v_mfma_f32_16x16x32_bf16 v[232:235], v[100:103], v[76:79], v[232:235]\n"
"ds_read_b128 v[92:95], v1 offset:52224\n"
"v_accvgpr_read_b32 v153, a133\n"
"v_mfma_f32_16x16x32_bf16 v[196:199], v[104:107], v[64:67], v[196:199]\n"
"ds_read_b128 v[96:99], v1 offset:43008\n"
"v_accvgpr_read_b32 v154, a134\n"
"v_mfma_f32_16x16x32_bf16 v[196:199], v[108:111], v[68:71], v[196:199]\n"
"s_waitcnt lgkmcnt(3)\n"
"ds_read_b128 v[100:103], v1 offset:53248\n"
"v_mfma_f32_16x16x32_bf16 v[236:239], v[104:107], v[72:75], v[236:239]\n"
"ds_read_b128 v[104:107], v1 offset:44032\n"
"v_accvgpr_read_b32 v155, a135\n"
"v_mfma_f32_16x16x32_bf16 v[236:239], v[108:111], v[76:79], v[236:239]\n"
"ds_read_b128 v[108:111], v1 offset:54272\n"
"v_accvgpr_read_b32 v156, a148\n"
"v_mfma_f32_16x16x32_bf16 v[32:35], v[80:83], a[192:195], 0\n"
"v_accvgpr_read_b32 v157, a149\n"
"v_accvgpr_read_b32 v158, a150\n"
"v_mfma_f32_16x16x32_bf16 v[32:35], v[84:87], a[196:199], v[32:35]\n"
"s_waitcnt lgkmcnt(4)\n"
"v_accvgpr_read_b32 v159, a151\n"
"v_mul_f32 v152, v29, v152\n"
"v_mfma_f32_16x16x32_bf16 v[72:75], v[80:83], a[200:203], 0\n"
"ds_read_b128 v[80:83], v1 offset:45056\n"
"v_mul_f32 v153, v29, v153\n"
"v_mfma_f32_16x16x32_bf16 v[72:75], v[84:87], a[204:207], v[72:75]\n"
"ds_read_b128 v[84:87], v1 offset:55296\n"
"v_mul_f32 v154, v29, v154\n"
"v_mfma_f32_16x16x32_bf16 v[36:39], v[88:91], a[192:195], 0\n"
"v_mul_f32 v155, v29, v155\n"
"v_mul_f32 v156, v29, v156\n"
"v_mfma_f32_16x16x32_bf16 v[36:39], v[92:95], a[196:199], v[36:39]\n"
"s_waitcnt lgkmcnt(4)\n"
"v_mul_f32 v157, v29, v157\n"
"v_mul_f32 v158, v29, v158\n"
"v_mfma_f32_16x16x32_bf16 v[76:79], v[88:91], a[200:203], 0\n"
"ds_read_b128 v[88:91], v1 offset:46080\n"
"v_mul_f32 v159, v29, v159\n"
"v_mfma_f32_16x16x32_bf16 v[76:79], v[92:95], a[204:207], v[76:79]\n"
"ds_read_b128 v[92:95], v1 offset:56320\n"
"v_cvt_pk_bf16_f32 v152, v152, v153\n"
"v_mfma_f32_16x16x32_bf16 v[40:43], v[96:99], a[192:195], 0\n"
"v_cvt_pk_bf16_f32 v153, v154, v155\n"
"v_cvt_pk_bf16_f32 v154, v156, v157\n"
"v_mfma_f32_16x16x32_bf16 v[40:43], v[100:103], a[196:199], v[40:43]\n"
"s_waitcnt lgkmcnt(4)\n"
"v_cvt_pk_bf16_f32 v155, v158, v159\n"
"v_cvt_pk_bf16_f32 v132, v132, v133\n"
"v_mfma_f32_16x16x32_bf16 v[240:243], v[96:99], a[200:203], 0\n"
"buffer_store_dwordx4 v[152:155], v1, s[36:39], s91 offen offset:1024 sc1\n"
"v_mfma_f32_16x16x32_bf16 v[240:243], v[100:103], a[204:207], v[240:243]\n"
"ds_read_b128 v[96:99], v1 offset:47104\n"
"v_cvt_pk_bf16_f32 v133, v134, v135\n"
"v_mfma_f32_16x16x32_bf16 v[44:47], v[104:107], a[192:195], 0\n"
"ds_read_b128 v[100:103], v1 offset:57344\n"
"v_cvt_pk_bf16_f32 v134, v136, v137\n"
"v_mfma_f32_16x16x32_bf16 v[44:47], v[108:111], a[196:199], v[44:47]\n"
"s_waitcnt lgkmcnt(4)\n"
"v_cvt_pk_bf16_f32 v135, v138, v139\n"
"v_accvgpr_read_b32 v160, a136\n"
"v_mfma_f32_16x16x32_bf16 v[244:247], v[104:107], a[200:203], 0\n"
"ds_read_b128 v[104:107], v1 offset:48128\n"
"v_accvgpr_read_b32 v161, a137\n"
"v_mfma_f32_16x16x32_bf16 v[244:247], v[108:111], a[204:207], v[244:247]\n"
"ds_read_b128 v[108:111], v1 offset:58368\n"
"v_accvgpr_read_b32 v162, a138\n"
"v_mfma_f32_16x16x32_bf16 v[48:51], v[80:83], a[192:195], 0\n"
"v_accvgpr_read_b32 v163, a139\n"
"v_accvgpr_read_b32 v164, a152\n"
"v_mfma_f32_16x16x32_bf16 v[48:51], v[84:87], a[196:199], v[48:51]\n"
"s_waitcnt lgkmcnt(4)\n"
"v_accvgpr_read_b32 v165, a153\n"
"v_accvgpr_read_b32 v166, a154\n"
"v_mfma_f32_16x16x32_bf16 v[248:251], v[80:83], a[200:203], 0\n"
"ds_read_b128 v[80:83], v1 offset:49152\n"
"v_accvgpr_read_b32 v167, a155\n"
"v_mfma_f32_16x16x32_bf16 v[248:251], v[84:87], a[204:207], v[248:251]\n"
"ds_read_b128 v[84:87], v1 offset:59392\n"
"v_mul_f32 v160, v30, v160\n"
"v_mfma_f32_16x16x32_bf16 v[52:55], v[88:91], a[192:195], 0\n"
"s_waitcnt vmcnt(11)\n"
"s_barrier\n"
"v_mul_f32 v161, v30, v161\n"
"v_mfma_f32_16x16x32_bf16 v[52:55], v[92:95], a[196:199], v[52:55]\n"
"buffer_load_dwordx4 a[208:211], v10, s[36:39], s11 offen sc1\n"
"v_mfma_f32_16x16x32_bf16 v[252:255], v[88:91], a[200:203], 0\n"
"s_cmp_lg_u32 s76, 0\n"
"s_cbranch_scc1 .Lsig4\n"
"s_mov_b64 exec, 1\n"
"v_mov_b32 v8, 1\n"
"buffer_atomic_add v8, off, s[36:39], s14 offset:1024 sc1\n"
"s_mov_b64 exec, -1\n"
".Lsig4:\n"
"v_mfma_f32_16x16x32_bf16 v[252:255], v[92:95], a[204:207], v[252:255]\n"
"s_waitcnt lgkmcnt(4)\n"
"ds_read_b128 v[88:91], v1 offset:50176\n"
"v_mfma_f32_16x16x32_bf16 v[56:59], v[96:99], a[192:195], 0\n"
"buffer_store_dwordx4 v[132:135], v1, s[36:39], s64 offen offset:0 sc1\n"
"v_mfma_f32_16x16x32_bf16 v[56:59], v[100:103], a[196:199], v[56:59]\n"
"s_waitcnt lgkmcnt(3)\n"
"ds_read_b128 v[92:95], v1 offset:60416\n"
"v_mfma_f32_16x16x32_bf16 v[176:179], v[96:99], a[200:203], 0\n"
"buffer_load_dwordx4 a[212:215], v12, s[36:39], s11 offen sc1\n"
"v_mfma_f32_16x16x32_bf16 v[176:179], v[100:103], a[204:207], v[176:179]\n"
"ds_read_b128 v[96:99], v2 offset:0\n"
"v_mul_f32 v162, v30, v162\n"
"v_mfma_f32_16x16x32_bf16 v[60:63], v[104:107], a[192:195], 0\n"
"ds_read_b128 v[100:103], v2 offset:10240\n"
"v_mul_f32 v163, v30, v163\n"
"v_mfma_f32_16x16x32_bf16 v[60:63], v[108:111], a[196:199], v[60:63]\n"
"buffer_load_dwordx4 a[216:219], v11, s[36:39], s11 offen sc1\n"
"v_mfma_f32_16x16x32_bf16 v[180:183], v[104:107], a[200:203], 0\n"
"s_waitcnt lgkmcnt(4)\n"
"ds_read_b128 v[104:107], v2 offset:1024\n"
"v_mfma_f32_16x16x32_bf16 v[180:183], v[108:111], a[204:207], v[180:183]\n"
"ds_read_b128 v[108:111], v2 offset:11264\n"
"v_mul_f32 v164, v30, v164\n"
"v_mfma_f32_16x16x32_bf16 v[64:67], v[80:83], a[192:195], 0\n"
"buffer_load_dwordx4 a[220:223], v13, s[36:39], s11 offen sc1\n"
"v_mfma_f32_16x16x32_bf16 v[64:67], v[84:87], a[196:199], v[64:67]\n"
"s_add_u32 s11, s11, 1572864\n"
"s_waitcnt lgkmcnt(4)\n"
"v_mul_f32 v165, v30, v165\n"
"v_mfma_f32_16x16x32_bf16 v[184:187], v[80:83], a[200:203], 0\n"
"ds_read_b128 v[80:83], v2 offset:2048\n"
"v_mul_f32 v166, v30, v166\n"
"v_mfma_f32_16x16x32_bf16 v[184:187], v[84:87], a[204:207], v[184:187]\n"
"ds_read_b128 v[84:87], v2 offset:12288\n"
"v_mul_f32 v167, v30, v167\n"
"v_mfma_f32_16x16x32_bf16 v[68:71], v[88:91], a[192:195], 0\n"
"v_cvt_pk_bf16_f32 v160, v160, v161\n"
"v_cvt_pk_bf16_f32 v161, v162, v163\n"
"v_mfma_f32_16x16x32_bf16 v[68:71], v[92:95], a[196:199], v[68:71]\n"
"s_waitcnt lgkmcnt(4)\n"
"v_cvt_pk_bf16_f32 v162, v164, v165\n"
"v_cvt_pk_bf16_f32 v163, v166, v167\n"
"v_mfma_f32_16x16x32_bf16 v[188:191], v[88:91], a[200:203], 0\n"
"buffer_store_dwordx4 v[160:163], v1, s[36:39], s91 offen offset:2048 sc1\n"
"v_mfma_f32_16x16x32_bf16 v[188:191], v[92:95], a[204:207], v[188:191]\n"
"ds_read_b128 v[88:91], v2 offset:3072\n"
"v_cvt_pk_bf16_f32 v136, v140, v141\n"
"v_mfma_f32_16x16x32_bf16 v[32:35], v[96:99], a[0:3], v[32:35]\n"
"ds_read_b128 v[92:95], v2 offset:13312\n"
"v_cvt_pk_bf16_f32 v137, v142, v143\n"
"v_mfma_f32_16x16x32_bf16 v[32:35], v[100:103], a[4:7], v[32:35]\n"
"s_waitcnt lgkmcnt(4)\n"
"v_cvt_pk_bf16_f32 v138, v192, v193\n"
"v_cvt_pk_bf16_f32 v139, v194, v195\n"
"v_mfma_f32_16x16x32_bf16 v[72:75], v[96:99], a[8:11], v[72:75]\n"
"ds_read_b128 v[96:99], v2 offset:4096\n"
"v_cvt_pk_bf16_f32 v140, v196, v197\n"
"v_mfma_f32_16x16x32_bf16 v[72:75], v[100:103], a[12:15], v[72:75]\n"
"ds_read_b128 v[100:103], v2 offset:14336\n"
"v_cvt_pk_bf16_f32 v141, v198, v199\n"
"v_mfma_f32_16x16x32_bf16 v[36:39], v[104:107], a[0:3], v[36:39]\n"
"s_add_u32 m0, s7, 143360\n"
"v_accvgpr_read_b32 v168, a140\n"
"v_accvgpr_read_b32 v169, a141\n"
"v_mfma_f32_16x16x32_bf16 v[36:39], v[108:111], a[4:7], v[36:39]\n"
"buffer_load_dwordx4 v1, s[36:39], s8 offen offset:0 sc1 lds\n"
"v_mfma_f32_16x16x32_bf16 v[76:79], v[104:107], a[8:11], v[76:79]\n"
"s_waitcnt lgkmcnt(4)\n"
"ds_read_b128 v[104:107], v2 offset:5120\n"
"v_mfma_f32_16x16x32_bf16 v[76:79], v[108:111], a[12:15], v[76:79]\n"
"buffer_load_dwordx4 v1, s[36:39], s8 offen offset:1024 sc1 lds\n"
"v_mfma_f32_16x16x32_bf16 v[40:43], v[80:83], a[0:3], v[40:43]\n"
"ds_read_b128 v[108:111], v2 offset:15360\n"
"v_accvgpr_read_b32 v170, a142\n"
"v_mfma_f32_16x16x32_bf16 v[40:43], v[84:87], a[4:7], v[40:43]\n"
"s_waitcnt lgkmcnt(4)\n"
"v_accvgpr_read_b32 v171, a143\n"
"v_accvgpr_read_b32 v172, a156\n"
"v_mfma_f32_16x16x32_bf16 v[240:243], v[80:83], a[8:11], v[240:243]\n"
"buffer_load_dwordx4 v1, s[36:39], s8 offen offset:2048 sc1 lds\n"
"v_mfma_f32_16x16x32_bf16 v[240:243], v[84:87], a[12:15], v[240:243]\n"
"ds_read_b128 v[80:83], v2 offset:6144\n"
"v_accvgpr_read_b32 v173, a157\n"
"v_mfma_f32_16x16x32_bf16 v[44:47], v[88:91], a[0:3], v[44:47]\n"
"ds_read_b128 v[84:87], v2 offset:16384\n"
"v_accvgpr_read_b32 v174, a158\n"
"v_mfma_f32_16x16x32_bf16 v[44:47], v[92:95], a[4:7], v[44:47]\n"
"buffer_load_dwordx4 v1, s[36:39], s8 offen offset:3072 sc1 lds\n"
"v_mfma_f32_16x16x32_bf16 v[244:247], v[88:91], a[8:11], v[244:247]\n"
"s_add_u32 m0, s7, 147456\n"
"s_waitcnt lgkmcnt(4)\n"
"v_accvgpr_read_b32 v175, a159\n"
"v_mfma_f32_16x16x32_bf16 v[244:247], v[92:95], a[12:15], v[244:247]\n"
"ds_read_b128 v[88:91], v2 offset:7168\n"
"v_mul_f32 v168, v31, v168\n"
"v_mfma_f32_16x16x32_bf16 v[48:51], v[96:99], a[0:3], v[48:51]\n"
"buffer_store_dwordx4 v[136:139], v1, s[36:39], s64 offen offset:1024 sc1\n"
"v_mfma_f32_16x16x32_bf16 v[48:51], v[100:103], a[4:7], v[48:51]\n"
"s_waitcnt lgkmcnt(3)\n"
"ds_read_b128 v[92:95], v2 offset:17408\n"
"v_mfma_f32_16x16x32_bf16 v[248:251], v[96:99], a[8:11], v[248:251]\n"
"ds_read_b128 v[96:99], v2 offset:8192\n"
"v_mul_f32 v169, v31, v169\n"
"v_mfma_f32_16x16x32_bf16 v[248:251], v[100:103], a[12:15], v[248:251]\n"
"buffer_load_dwordx4 v4, s[36:39], s8 offen sc1 lds\n"
"v_mfma_f32_16x16x32_bf16 v[52:55], v[104:107], a[0:3], v[52:55]\n"
"s_barrier\n"
"s_add_u32 s8, s8, 491520\n"
"v_mfma_f32_16x16x32_bf16 v[52:55], v[108:111], a[4:7], v[52:55]\n"
"s_waitcnt lgkmcnt(3)\n"
"ds_read_b128 v[100:103], v2 offset:18432\n"
"v_mfma_f32_16x16x32_bf16 v[252:255], v[104:107], a[8:11], v[252:255]\n"
"buffer_load_dwordx4 a[224:227], v10, s[36:39], s11 offen sc1\n"
"v_mfma_f32_16x16x32_bf16 v[252:255], v[108:111], a[12:15], v[252:255]\n"
"ds_read_b128 v[104:107], v2 offset:9216\n"
"v_mul_f32 v170, v31, v170\n"
"v_mfma_f32_16x16x32_bf16 v[56:59], v[80:83], a[0:3], v[56:59]\n"
"ds_read_b128 v[108:111], v2 offset:19456\n"
"v_mul_f32 v171, v31, v171\n"
"v_mfma_f32_16x16x32_bf16 v[56:59], v[84:87], a[4:7], v[56:59]\n"
"s_waitcnt lgkmcnt(4)\n"
"v_mul_f32 v172, v31, v172\n"
"v_mul_f32 v173, v31, v173\n"
"v_mfma_f32_16x16x32_bf16 v[176:179], v[80:83], a[8:11], v[176:179]\n"
"buffer_load_dwordx4 a[228:231], v12, s[36:39], s11 offen sc1\n"
"v_mfma_f32_16x16x32_bf16 v[176:179], v[84:87], a[12:15], v[176:179]\n"
"ds_read_b128 v[80:83], v2 offset:20480\n"
"v_mul_f32 v174, v31, v174\n"
"v_mfma_f32_16x16x32_bf16 v[60:63], v[88:91], a[0:3], v[60:63]\n"
"ds_read_b128 v[84:87], v2 offset:30720\n"
"v_mul_f32 v175, v31, v175\n"
"v_mfma_f32_16x16x32_bf16 v[60:63], v[92:95], a[4:7], v[60:63]\n"
"s_waitcnt lgkmcnt(4)\n"
"v_cvt_pk_bf16_f32 v168, v168, v169\n"
"v_cvt_pk_bf16_f32 v169, v170, v171\n"
"v_mfma_f32_16x16x32_bf16 v[180:183], v[88:91], a[8:11], v[180:183]\n"
"buffer_store_dwordx2 v[140:141], v5, s[36:39], s64 offen offset:0 sc1\n"
"v_mfma_f32_16x16x32_bf16 v[180:183], v[92:95], a[12:15], v[180:183]\n"
"ds_read_b128 v[88:91], v2 offset:21504\n"
"v_cvt_pk_bf16_f32 v170, v172, v173\n"
"v_mfma_f32_16x16x32_bf16 v[64:67], v[96:99], a[0:3], v[64:67]\n"
"ds_read_b128 v[92:95], v2 offset:31744\n"
"v_cvt_pk_bf16_f32 v171, v174, v175\n"
"v_mfma_f32_16x16x32_bf16 v[64:67], v[100:103], a[4:7], v[64:67]\n"
"s_waitcnt lgkmcnt(4)\n"
"v_cvt_pk_bf16_f32 v200, v200, v201\n"
"v_cvt_pk_bf16_f32 v201, v202, v203\n"
"v_mfma_f32_16x16x32_bf16 v[184:187], v[96:99], a[8:11], v[184:187]\n"
"buffer_load_dwordx4 a[232:235], v11, s[36:39], s11 offen sc1\n"
"v_mfma_f32_16x16x32_bf16 v[184:187], v[100:103], a[12:15], v[184:187]\n"
"ds_read_b128 v[96:99], v2 offset:22528\n"
"v_cvt_pk_bf16_f32 v202, v204, v205\n"
"v_mfma_f32_16x16x32_bf16 v[68:71], v[104:107], a[0:3], v[68:71]\n"
"ds_read_b128 v[100:103], v2 offset:32768\n"
"v_cvt_pk_bf16_f32 v203, v206, v207\n"
"v_mfma_f32_16x16x32_bf16 v[68:71], v[108:111], a[4:7], v[68:71]\n"
"s_waitcnt lgkmcnt(4)\n"
"v_cvt_pk_bf16_f32 v204, v208, v209\n"
"v_cvt_pk_bf16_f32 v205, v210, v211\n"
"v_mfma_f32_16x16x32_bf16 v[188:191], v[104:107], a[8:11], v[188:191]\n"
"buffer_load_dwordx4 a[236:239], v13, s[36:39], s11 offen sc1\n"
"v_mfma_f32_16x16x32_bf16 v[188:191], v[108:111], a[12:15], v[188:191]\n"
"s_add_u32 s11, s11, 1572864\n"
"ds_read_b128 v[104:107], v2 offset:23552\n"
"v_mfma_f32_16x16x32_bf16 v[32:35], v[80:83], a[16:19], v[32:35]\n"
"ds_read_b128 v[108:111], v2 offset:33792\n"
"v_cvt_pk_bf16_f32 v206, v212, v213\n"
"v_mfma_f32_16x16x32_bf16 v[32:35], v[84:87], a[20:23], v[32:35]\n"
"s_waitcnt lgkmcnt(4)\n"
"v_cvt_pk_bf16_f32 v207, v214, v215\n"
"v_cvt_pk_bf16_f32 v208, v216, v217\n"
"v_mfma_f32_16x16x32_bf16 v[72:75], v[80:83], a[24:27], v[72:75]\n"
"ds_read_b128 v[80:83], v2 offset:24576\n"
"v_cvt_pk_bf16_f32 v209, v218, v219\n"
"v_mfma_f32_16x16x32_bf16 v[72:75], v[84:87], a[28:31], v[72:75]\n"
"ds_read_b128 v[84:87], v2 offset:34816\n"
"v_accvgpr_read_b32 v144, a160\n"
"v_mfma_f32_16x16x32_bf16 v[36:39], v[88:91], a[16:19], v[36:39]\n"
"s_add_u32 m0, s7, 20480\n"
"v_accvgpr_read_b32 v145, a161\n"
"v_accvgpr_read_b32 v146, a162\n"
"v_mfma_f32_16x16x32_bf16 v[36:39], v[92:95], a[20:23], v[36:39]\n"
"buffer_load_dwordx4 v1, s[36:39], s8 offen offset:0 sc1 lds\n"
"v_mfma_f32_16x16x32_bf16 v[76:79], v[88:91], a[24:27], v[76:79]\n"
"s_waitcnt lgkmcnt(4)\n"
"ds_read_b128 v[88:91], v2 offset:25600\n"
"v_mfma_f32_16x16x32_bf16 v[76:79], v[92:95], a[28:31], v[76:79]\n"
"ds_read_b128 v[92:95], v2 offset:35840\n"
"v_accvgpr_read_b32 v147, a163\n"
"v_mfma_f32_16x16x32_bf16 v[40:43], v[96:99], a[16:19], v[40:43]\n"
"buffer_store_dwordx4 v[168:171], v1, s[36:39], s91 offen offset:3072 sc1\n"
"v_mfma_f32_16x16x32_bf16 v[40:43], v[100:103], a[20:23], v[40:43]\n"
"s_waitcnt lgkmcnt(4)\n"
"v_accvgpr_read_b32 v148, a176\n"
"v_accvgpr_read_b32 v149, a177\n"
"v_mfma_f32_16x16x32_bf16 v[240:243], v[96:99], a[24:27], v[240:243]\n"
"ds_read_b128 v[96:99], v2 offset:26624\n"
"v_accvgpr_read_b32 v150, a178\n"
"v_mfma_f32_16x16x32_bf16 v[240:243], v[100:103], a[28:31], v[240:243]\n"
"ds_read_b128 v[100:103], v2 offset:36864\n"
"v_accvgpr_read_b32 v151, a179\n"
"v_mfma_f32_16x16x32_bf16 v[44:47], v[104:107], a[16:19], v[44:47]\n"
"buffer_load_dwordx4 v1, s[36:39], s8 offen offset:1024 sc1 lds\n"
"v_mfma_f32_16x16x32_bf16 v[44:47], v[108:111], a[20:23], v[44:47]\n"
"s_waitcnt lgkmcnt(4)\n"
"v_mul_f32 v144, v28, v144\n"
"v_mul_f32 v145, v28, v145\n"
"v_mfma_f32_16x16x32_bf16 v[244:247], v[104:107], a[24:27], v[244:247]\n"
"ds_read_b128 v[104:107], v2 offset:27648\n"
"v_mul_f32 v146, v28, v146\n"
"v_mfma_f32_16x16x32_bf16 v[244:247], v[108:111], a[28:31], v[244:247]\n"
"ds_read_b128 v[108:111], v2 offset:37888\n"
"v_mul_f32 v147, v28, v147\n"
"v_mfma_f32_16x16x32_bf16 v[48:51], v[80:83], a[16:19], v[48:51]\n"
"buffer_load_dwordx4 v1, s[36:39], s8 offen offset:2048 sc1 lds\n"
"v_mfma_f32_16x16x32_bf16 v[48:51], v[84:87], a[20:23], v[48:51]\n"
"s_waitcnt lgkmcnt(4)\n"
"v_mul_f32 v148, v28, v148\n"
"v_mul_f32 v149, v28, v149\n"
"v_mfma_f32_16x16x32_bf16 v[248:251], v[80:83], a[24:27], v[248:251]\n"
"ds_read_b128 v[80:83], v2 offset:28672\n"
"v_mul_f32 v150, v28, v150\n"
"v_mfma_f32_16x16x32_bf16 v[248:251], v[84:87], a[28:31], v[248:251]\n"
"ds_read_b128 v[84:87], v2 offset:38912\n"
"v_mul_f32 v151, v28, v151\n"
"v_mfma_f32_16x16x32_bf16 v[52:55], v[88:91], a[16:19], v[52:55]\n"
"s_waitcnt vmcnt(27)\n"
"s_barrier\n"
"v_cvt_pk_bf16_f32 v144, v144, v145\n"
"v_mfma_f32_16x16x32_bf16 v[52:55], v[92:95], a[20:23], v[52:55]\n"
"buffer_load_dwordx4 v1, s[36:39], s8 offen offset:3072 sc1 lds\n"
"v_mfma_f32_16x16x32_bf16 v[252:255], v[88:91], a[24:27], v[252:255]\n"
"buffer_store_dwordx4 v[200:203], v1, s[36:39], s65 offen offset:0 sc1\n"
"v_mfma_f32_16x16x32_bf16 v[252:255], v[92:95], a[28:31], v[252:255]\n"
"s_waitcnt lgkmcnt(4)\n"
"s_add_u32 m0, s7, 24576\n"
"v_cvt_pk_bf16_f32 v145, v146, v147\n"
"v_mfma_f32_16x16x32_bf16 v[56:59], v[96:99], a[16:19], v[56:59]\n"
"ds_read_b128 v[88:91], v2 offset:29696\n"
"v_cvt_pk_bf16_f32 v146, v148, v149\n"
"v_mfma_f32_16x16x32_bf16 v[56:59], v[100:103], a[20:23], v[56:59]\n"
"s_waitcnt lgkmcnt(3)\n"
"ds_read_b128 v[92:95], v2 offset:39936\n"
"v_mfma_f32_16x16x32_bf16 v[176:179], v[96:99], a[24:27], v[176:179]\n"
"ds_read_b128 v[96:99], v2 offset:40960\n"
"v_cvt_pk_bf16_f32 v147, v150, v151\n"
"v_mfma_f32_16x16x32_bf16 v[176:179], v[100:103], a[28:31], v[176:179]\n"
"ds_read_b128 v[100:103], v2 offset:51200\n"
"s_add_u32 s91, s91, 131072\n"
"v_mfma_f32_16x16x32_bf16 v[60:63], v[104:107], a[16:19], v[60:63]\n"
"buffer_load_dwordx4 v4, s[36:39], s8 offen sc1 lds\n"
"v_mfma_f32_16x16x32_bf16 v[60:63], v[108:111], a[20:23], v[60:63]\n"
"s_sub_u32 s8, s8, 1433600\n"
"s_waitcnt lgkmcnt(4)\n"
"v_cvt_pk_bf16_f32 v220, v220, v221\n"
"v_mfma_f32_16x16x32_bf16 v[180:183], v[104:107], a[24:27], v[180:183]\n"
"ds_read_b128 v[104:107], v2 offset:41984\n"
"v_cvt_pk_bf16_f32 v221, v222, v223\n"
"v_mfma_f32_16x16x32_bf16 v[180:183], v[108:111], a[28:31], v[180:183]\n"
"ds_read_b128 v[108:111], v2 offset:52224\n"
"v_cvt_pk_bf16_f32 v222, v224, v225\n"
"v_mfma_f32_16x16x32_bf16 v[64:67], v[80:83], a[16:19], v[64:67]\n"
"buffer_load_dwordx4 a[240:243], v10, s[36:39], s11 offen sc1\n"
"v_mfma_f32_16x16x32_bf16 v[64:67], v[84:87], a[20:23], v[64:67]\n"
"s_waitcnt lgkmcnt(4)\n"
"v_cvt_pk_bf16_f32 v223, v226, v227\n"
"v_cvt_pk_bf16_f32 v224, v228, v229\n"
"v_mfma_f32_16x16x32_bf16 v[184:187], v[80:83], a[24:27], v[184:187]\n"
"ds_read_b128 v[80:83], v2 offset:43008\n"
"v_cvt_pk_bf16_f32 v225, v230, v231\n"
"v_mfma_f32_16x16x32_bf16 v[184:187], v[84:87], a[28:31], v[184:187]\n"
"ds_read_b128 v[84:87], v2 offset:53248\n"
"v_cvt_pk_bf16_f32 v226, v232, v233\n"
"v_mfma_f32_16x16x32_bf16 v[68:71], v[88:91], a[16:19], v[68:71]\n"
"buffer_store_dwordx4 v[204:207], v1, s[36:39], s65 offen offset:1024 sc1\n"
"v_mfma_f32_16x16x32_bf16 v[68:71], v[92:95], a[20:23], v[68:71]\n"
"s_waitcnt lgkmcnt(4)\n"
"v_cvt_pk_bf16_f32 v227, v234, v235\n"
"v_accvgpr_read_b32 v152, a164\n"
"v_mfma_f32_16x16x32_bf16 v[188:191], v[88:91], a[24:27], v[188:191]\n"
"ds_read_b128 v[88:91], v2 offset:44032\n"
"v_accvgpr_read_b32 v153, a165\n"
"v_mfma_f32_16x16x32_bf16 v[188:191], v[92:95], a[28:31], v[188:191]\n"
"ds_read_b128 v[92:95], v2 offset:54272\n"
"v_accvgpr_read_b32 v154, a166\n"
"v_mfma_f32_16x16x32_bf16 v[32:35], v[96:99], a[32:35], v[32:35]\n"
"buffer_load_dwordx4 a[244:247], v12, s[36:39], s11 offen sc1\n"
"v_mfma_f32_16x16x32_bf16 v[32:35], v[100:103], a[36:39], v[32:35]\n"
"s_waitcnt lgkmcnt(4)\n"
"v_accvgpr_read_b32 v155, a167\n"
"v_accvgpr_read_b32 v156, a180\n"
"v_mfma_f32_16x16x32_bf16 v[72:75], v[96:99], a[40:43], v[72:75]\n"
"ds_read_b128 v[96:99], v2 offset:45056\n"
"v_accvgpr_read_b32 v157, a181\n"
"v_mfma_f32_16x16x32_bf16 v[72:75], v[100:103], a[44:47], v[72:75]\n"
"ds_read_b128 v[100:103], v2 offset:55296\n"
"v_accvgpr_read_b32 v158, a182\n"
"v_mfma_f32_16x16x32_bf16 v[36:39], v[104:107], a[32:35], v[36:39]\n"
"s_add_u32 m0, s7, 40960\n"
"v_accvgpr_read_b32 v159, a183\n"
"v_mul_f32 v152, v29, v152\n"
"v_mfma_f32_16x16x32_bf16 v[36:39], v[108:111], a[36:39], v[36:39]\n"
"buffer_load_dwordx4 v1, s[36:39], s8 offen offset:0 sc1 lds\n"
"v_mfma_f32_16x16x32_bf16 v[76:79], v[104:107], a[40:43], v[76:79]\n"
"buffer_load_dword v7, off, s[36:39], s14 offset:1024 sc1\n"
"v_mfma_f32_16x16x32_bf16 v[76:79], v[108:111], a[44:47], v[76:79]\n"
"s_waitcnt lgkmcnt(4)\n"
"ds_read_b128 v[104:107], v2 offset:46080\n"
"v_mfma_f32_16x16x32_bf16 v[40:43], v[80:83], a[32:35], v[40:43]\n"
"ds_read_b128 v[108:111], v2 offset:56320\n"
"v_mul_f32 v153, v29, v153\n"
"v_mfma_f32_16x16x32_bf16 v[40:43], v[84:87], a[36:39], v[40:43]\n"
"buffer_load_dwordx4 v1, s[36:39], s8 offen offset:1024 sc1 lds\n"
"v_mfma_f32_16x16x32_bf16 v[240:243], v[80:83], a[40:43], v[240:243]\n"
"buffer_store_dwordx2 v[208:209], v5, s[36:39], s65 offen offset:0 sc1\n"
"v_mfma_f32_16x16x32_bf16 v[240:243], v[84:87], a[44:47], v[240:243]\n"
"s_waitcnt lgkmcnt(4)\n"
"ds_read_b128 v[80:83], v2 offset:47104\n"
"v_mfma_f32_16x16x32_bf16 v[44:47], v[88:91], a[32:35], v[44:47]\n"
"ds_read_b128 v[84:87], v2 offset:57344\n"
"v_mul_f32 v154, v29, v154\n"
"v_mfma_f32_16x16x32_bf16 v[44:47], v[92:95], a[36:39], v[44:47]\n"
"s_waitcnt lgkmcnt(4)\n"
"v_mul_f32 v155, v29, v155\n"
"v_mul_f32 v156, v29, v156\n"
"v_mfma_f32_16x16x32_bf16 v[244:247], v[88:91], a[40:43], v[244:247]\n"
"ds_read_b128 v[88:91], v2 offset:48128\n"
"v_mul_f32 v157, v29, v157\n"
"v_mfma_f32_16x16x32_bf16 v[244:247], v[92:95], a[44:47], v[244:247]\n"
"buffer_store_dwordx4 v[144:147], v1, s[36:39], s91 offen offset:0 sc1\n"
"v_mfma_f32_16x16x32_bf16 v[48:51], v[96:99], a[32:35], v[48:51]\n"
"ds_read_b128 v[92:95], v2 offset:58368\n"
"v_mul_f32 v158, v29, v158\n"
"v_mfma_f32_16x16x32_bf16 v[48:51], v[100:103], a[36:39], v[48:51]\n"
"s_waitcnt lgkmcnt(4)\n"
"v_mul_f32 v159, v29, v159\n"
"v_cvt_pk_bf16_f32 v152, v152, v153\n"
"v_mfma_f32_16x16x32_bf16 v[248:251], v[96:99], a[40:43], v[248:251]\n"
"ds_read_b128 v[96:99], v2 offset:49152\n"
"v_cvt_pk_bf16_f32 v153, v154, v155\n"
"v_mfma_f32_16x16x32_bf16 v[248:251], v[100:103], a[44:47], v[248:251]\n"
"ds_read_b128 v[100:103], v2 offset:59392\n"
"v_cvt_pk_bf16_f32 v154, v156, v157\n"
"v_mfma_f32_16x16x32_bf16 v[52:55], v[104:107], a[32:35], v[52:55]\n"
"s_waitcnt vmcnt(34)\n"
"s_barrier\n"
"v_cvt_pk_bf16_f32 v155, v158, v159\n"
"v_mfma_f32_16x16x32_bf16 v[52:55], v[108:111], a[36:39], v[52:55]\n"
"buffer_load_dwordx4 v1, s[36:39], s8 offen offset:2048 sc1 lds\n"
"v_mfma_f32_16x16x32_bf16 v[252:255], v[104:107], a[40:43], v[252:255]\n"
"s_waitcnt lgkmcnt(4)\n"
"ds_read_b128 v[104:107], v2 offset:50176\n"
"v_mfma_f32_16x16x32_bf16 v[252:255], v[108:111], a[44:47], v[252:255]\n"
"ds_read_b128 v[108:111], v2 offset:60416\n"
"v_cvt_pk_bf16_f32 v228, v236, v237\n"
"v_mfma_f32_16x16x32_bf16 v[56:59], v[80:83], a[32:35], v[56:59]\n"
"buffer_store_dwordx4 v[220:223], v1, s[36:39], s66 offen offset:0 sc1\n"
"v_mfma_f32_16x16x32_bf16 v[56:59], v[84:87], a[36:39], v[56:59]\n"
"s_waitcnt lgkmcnt(4)\n"
"v_cvt_pk_bf16_f32 v229, v238, v239\n"
"v_accvgpr_read_b32 v160, a168\n"
"v_mfma_f32_16x16x32_bf16 v[176:179], v[80:83], a[40:43], v[176:179]\n"
"ds_read_b128 v[80:83], v1 offset:0\n"
"v_accvgpr_read_b32 v161, a169\n"
"v_mfma_f32_16x16x32_bf16 v[176:179], v[84:87], a[44:47], v[176:179]\n"
"ds_read_b128 v[84:87], v1 offset:10240\n"
"v_accvgpr_read_b32 v162, a170\n"
"v_mfma_f32_16x16x32_bf16 v[60:63], v[88:91], a[32:35], v[60:63]\n"
"v_accvgpr_read_b32 v163, a171\n"
"v_accvgpr_read_b32 v164, a184\n"
"v_mfma_f32_16x16x32_bf16 v[60:63], v[92:95], a[36:39], v[60:63]\n"
"buffer_load_dwordx4 v1, s[36:39], s8 offen offset:3072 sc1 lds\n"
"v_mfma_f32_16x16x32_bf16 v[180:183], v[88:91], a[40:43], v[180:183]\n"
"s_add_u32 m0, s7, 45056\n"
"s_waitcnt lgkmcnt(4)\n"
"v_accvgpr_read_b32 v165, a185\n"
"v_mfma_f32_16x16x32_bf16 v[180:183], v[92:95], a[44:47], v[180:183]\n"
"ds_read_b128 v[88:91], v1 offset:1024\n"
"v_accvgpr_read_b32 v166, a186\n"
"v_mfma_f32_16x16x32_bf16 v[64:67], v[96:99], a[32:35], v[64:67]\n"
"ds_read_b128 v[92:95], v1 offset:11264\n"
"v_accvgpr_read_b32 v167, a187\n"
"v_mfma_f32_16x16x32_bf16 v[64:67], v[100:103], a[36:39], v[64:67]\n"
"buffer_load_dwordx4 v4, s[36:39], s8 offen sc1 lds\n"
"v_mfma_f32_16x16x32_bf16 v[184:187], v[96:99], a[40:43], v[184:187]\n"
"s_add_u32 s8, s8, 491520\n"
"s_waitcnt lgkmcnt(4)\n"
"v_mul_f32 v160, v30, v160\n"
"v_mfma_f32_16x16x32_bf16 v[184:187], v[100:103], a[44:47], v[184:187]\n"
"ds_read_b128 v[96:99], v1 offset:2048\n"
"v_mul_f32 v161, v30, v161\n"
"v_mfma_f32_16x16x32_bf16 v[68:71], v[104:107], a[32:35], v[68:71]\n"
"ds_read_b128 v[100:103], v1 offset:12288\n"
"v_mul_f32 v162, v30, v162\n"
"v_mfma_f32_16x16x32_bf16 v[68:71], v[108:111], a[36:39], v[68:71]\n"
"buffer_load_dwordx4 a[248:251], v11, s[36:39], s11 offen sc1\n"
"v_mfma_f32_16x16x32_bf16 v[188:191], v[104:107], a[40:43], v[188:191]\n"
"buffer_store_dwordx4 v[224:227], v1, s[36:39], s66 offen offset:1024 sc1\n"
"v_mfma_f32_16x16x32_bf16 v[188:191], v[108:111], a[44:47], v[188:191]\n"
"s_waitcnt lgkmcnt(4)\n"
"ds_read_b128 v[104:107], v1 offset:3072\n"
"v_mfma_f32_16x16x32_bf16 v[32:35], v[80:83], a[48:51], v[32:35]\n"
"ds_read_b128 v[108:111], v1 offset:13312\n"
"v_mul_f32 v163, v30, v163\n"
"v_mfma_f32_16x16x32_bf16 v[32:35], v[84:87], a[52:55], v[32:35]\n"
"s_waitcnt lgkmcnt(4)\n"
"v_mul_f32 v164, v30, v164\n"
"v_mul_f32 v165, v30, v165\n"
"v_mfma_f32_16x16x32_bf16 v[72:75], v[80:83], a[56:59], v[72:75]\n"
"ds_read_b128 v[80:83], v1 offset:4096\n"
"v_mul_f32 v166, v30, v166\n"
"v_mfma_f32_16x16x32_bf16 v[72:75], v[84:87], a[60:63], v[72:75]\n"
"ds_read_b128 v[84:87], v1 offset:14336\n"
"v_mul_f32 v167, v30, v167\n"
"v_mfma_f32_16x16x32_bf16 v[36:39], v[88:91], a[48:51], v[36:39]\n"
"s_add_u32 m0, s7, 61440\n"
"v_cvt_pk_bf16_f32 v160, v160, v161\n"
"v_cvt_pk_bf16_f32 v161, v162, v163\n"
"v_mfma_f32_16x16x32_bf16 v[36:39], v[92:95], a[52:55], v[36:39]\n"
"buffer_load_dwordx4 v1, s[36:39], s8 offen offset:0 sc1 lds\n"
"v_mfma_f32_16x16x32_bf16 v[76:79], v[88:91], a[56:59], v[76:79]\n"
"s_waitcnt lgkmcnt(4)\n"
"ds_read_b128 v[88:91], v1 offset:5120\n"
"v_mfma_f32_16x16x32_bf16 v[76:79], v[92:95], a[60:63], v[76:79]\n"
"ds_read_b128 v[92:95], v1 offset:15360\n"
"v_cvt_pk_bf16_f32 v162, v164, v165\n"
"v_mfma_f32_16x16x32_bf16 v[40:43], v[96:99], a[48:51], v[40:43]\n"
"v_cvt_pk_bf16_f32 v163, v166, v167\n"
"v_accvgpr_read_b32 v168, a172\n"
"v_mfma_f32_16x16x32_bf16 v[40:43], v[100:103], a[52:55], v[40:43]\n"
"buffer_load_dwordx4 v1, s[36:39], s8 offen offset:1024 sc1 lds\n"
"v_mfma_f32_16x16x32_bf16 v[240:243], v[96:99], a[56:59], v[240:243]\n"
"buffer_store_dwordx4 v[152:155], v1, s[36:39], s91 offen offset:1024 sc1\n"
"v_mfma_f32_16x16x32_bf16 v[240:243], v[100:103], a[60:63], v[240:243]\n"
"s_waitcnt lgkmcnt(4)\n"
"ds_read_b128 v[96:99], v1 offset:6144\n"
"v_mfma_f32_16x16x32_bf16 v[44:47], v[104:107], a[48:51], v[44:47]\n"
"buffer_store_dwordx2 v[228:229], v5, s[36:39], s66 offen offset:0 sc1\n"
"v_mfma_f32_16x16x32_bf16 v[44:47], v[108:111], a[52:55], v[44:47]\n"
"s_waitcnt lgkmcnt(3)\n"
"ds_read_b128 v[100:103], v1 offset:16384\n"
"v_mfma_f32_16x16x32_bf16 v[244:247], v[104:107], a[56:59], v[244:247]\n"
"ds_read_b128 v[104:107], v1 offset:7168\n"
"v_accvgpr_read_b32 v169, a173\n"
"v_mfma_f32_16x16x32_bf16 v[244:247], v[108:111], a[60:63], v[244:247]\n"
"ds_read_b128 v[108:111], v1 offset:17408\n"
"v_accvgpr_read_b32 v170, a174\n"
"v_mfma_f32_16x16x32_bf16 v[48:51], v[80:83], a[48:51], v[48:51]\n"
"s_add_u32 s64, s64, 20971520\n"
"v_accvgpr_read_b32 v171, a175\n"
"v_accvgpr_read_b32 v172, a188\n"
"v_mfma_f32_16x16x32_bf16 v[48:51], v[84:87], a[52:55], v[48:51]\n"
"s_waitcnt lgkmcnt(4)\n"
"v_accvgpr_read_b32 v173, a189\n"
"v_accvgpr_read_b32 v174, a190\n"
"v_mfma_f32_16x16x32_bf16 v[248:251], v[80:83], a[56:59], v[248:251]\n"
"ds_read_b128 v[80:83], v1 offset:8192\n"
"v_accvgpr_read_b32 v175, a191\n"
"v_mfma_f32_16x16x32_bf16 v[248:251], v[84:87], a[60:63], v[248:251]\n"
"ds_read_b128 v[84:87], v1 offset:18432\n"
"v_mul_f32 v168, v31, v168\n"
"v_mfma_f32_16x16x32_bf16 v[52:55], v[88:91], a[48:51], v[52:55]\n"
"s_barrier\n"
"v_mul_f32 v169, v31, v169\n"
"v_mfma_f32_16x16x32_bf16 v[52:55], v[92:95], a[52:55], v[52:55]\n"
"buffer_load_dwordx4 v1, s[36:39], s8 offen offset:2048 sc1 lds\n"
"v_mfma_f32_16x16x32_bf16 v[252:255], v[88:91], a[56:59], v[252:255]\n"
"s_waitcnt lgkmcnt(4)\n"
"ds_read_b128 v[88:91], v1 offset:9216\n"
"v_mfma_f32_16x16x32_bf16 v[252:255], v[92:95], a[60:63], v[252:255]\n"
"s_waitcnt vmcnt(14)\n"
"v_cmp_gt_u32 s[92:93], 8, v7\n"
"s_nop 0\n"
"s_cmp_eq_u64 s[92:93], 0\n"
"s_cbranch_scc0 .Lspin4\n"
".Lback4:\n"
"v_mfma_f32_16x16x32_bf16 v[56:59], v[96:99], a[48:51], v[56:59]\n"
"ds_read_b128 v[92:95], v1 offset:19456\n"
"v_mul_f32 v170, v31, v170\n"
"v_mfma_f32_16x16x32_bf16 v[56:59], v[100:103], a[52:55], v[56:59]\n"
"buffer_load_dwordx4 v1, s[36:39], s8 offen offset:3072 sc1 lds\n"
"v_mfma_f32_16x16x32_bf16 v[176:179], v[96:99], a[56:59], v[176:179]\n"
"s_add_u32 m0, s7, 65536\n"
"s_waitcnt lgkmcnt(4)\n"
"v_mul_f32 v171, v31, v171\n"
"v_mfma_f32_16x16x32_bf16 v[176:179], v[100:103], a[60:63], v[176:179]\n"
"ds_read_b128 v[96:99], v3 offset:0\n"
"v_mul_f32 v172, v31, v172\n"
"v_mfma_f32_16x16x32_bf16 v[60:63], v[104:107], a[48:51], v[60:63]\n"
"ds_read_b128 v[100:103], v3 offset:10240\n"
"v_mul_f32 v173, v31, v173\n"
"v_mfma_f32_16x16x32_bf16 v[60:63], v[108:111], a[52:55], v[60:63]\n"
"buffer_load_dwordx4 v4, s[36:39], s8 offen sc1 lds\n"
"v_mfma_f32_16x16x32_bf16 v[180:183], v[104:107], a[56:59], v[180:183]\n"
"s_add_u32 s8, s8, 491520\n"
"s_waitcnt lgkmcnt(4)\n"
"v_mul_f32 v174, v31, v174\n"
"v_mfma_f32_16x16x32_bf16 v[180:183], v[108:111], a[60:63], v[180:183]\n"
"ds_read_b128 v[104:107], v3 offset:1024\n"
"v_mul_f32 v175, v31, v175\n"
"v_mfma_f32_16x16x32_bf16 v[64:67], v[80:83], a[48:51], v[64:67]\n"
"ds_read_b128 v[108:111], v3 offset:11264\n"
"v_cvt_pk_bf16_f32 v168, v168, v169\n"
"v_mfma_f32_16x16x32_bf16 v[64:67], v[84:87], a[52:55], v[64:67]\n"
"buffer_load_dwordx4 a[252:255], v13, s[36:39], s11 offen sc1\n"
"v_mfma_f32_16x16x32_bf16 v[184:187], v[80:83], a[56:59], v[184:187]\n"
"s_sub_u32 s11, s11, 4587520\n"
"s_waitcnt lgkmcnt(4)\n"
"v_cvt_pk_bf16_f32 v169, v170, v171\n"
"v_mfma_f32_16x16x32_bf16 v[184:187], v[84:87], a[60:63], v[184:187]\n"
"ds_read_b128 v[80:83], v3 offset:2048\n"
"v_cvt_pk_bf16_f32 v170, v172, v173\n"
"v_mfma_f32_16x16x32_bf16 v[68:71], v[88:91], a[48:51], v[68:71]\n"
"ds_read_b128 v[84:87], v3 offset:12288\n"
"v_cvt_pk_bf16_f32 v171, v174, v175\n"
"v_mfma_f32_16x16x32_bf16 v[68:71], v[92:95], a[52:55], v[68:71]\n"
"buffer_load_dwordx4 a[192:195], v10, s[36:39], s11 offen sc1\n"
"v_mfma_f32_16x16x32_bf16 v[188:191], v[88:91], a[56:59], v[188:191]\n"
"s_waitcnt lgkmcnt(4)\n"
"ds_read_b128 v[88:91], v3 offset:3072\n"
"v_mfma_f32_16x16x32_bf16 v[188:191], v[92:95], a[60:63], v[188:191]\n"
"ds_read_b128 v[92:95], v3 offset:13312\n"
"s_add_u32 s65, s65, 20971520\n"
"v_mfma_f32_16x16x32_bf16 v[32:35], v[96:99], a[208:211], v[32:35]\n"
"s_add_u32 s66, s66, 20971520\n"
"v_mfma_f32_16x16x32_bf16 v[32:35], v[100:103], a[212:215], v[32:35]\n"
"buffer_load_dwordx4 a[196:199], v12, s[36:39], s11 offen sc1\n"
"v_mfma_f32_16x16x32_bf16 v[72:75], v[96:99], a[216:219], v[72:75]\n"
"s_waitcnt lgkmcnt(4)\n"
"ds_read_b128 v[96:99], v3 offset:4096\n"
"v_mfma_f32_16x16x32_bf16 v[72:75], v[100:103], a[220:223], v[72:75]\n"
"ds_read_b128 v[100:103], v3 offset:14336\n"
"v_mfma_f32_16x16x32_bf16 v[36:39], v[104:107], a[208:211], v[36:39]\n"
"s_add_u32 m0, s7, 81920\n"
"v_mfma_f32_16x16x32_bf16 v[36:39], v[108:111], a[212:215], v[36:39]\n"
"buffer_store_dwordx4 v[160:163], v1, s[36:39], s91 offen offset:2048 sc1\n"
"v_mfma_f32_16x16x32_bf16 v[76:79], v[104:107], a[216:219], v[76:79]\n"
"s_waitcnt lgkmcnt(4)\n"
"ds_read_b128 v[104:107], v3 offset:5120\n"
"v_mfma_f32_16x16x32_bf16 v[76:79], v[108:111], a[220:223], v[76:79]\n"
"ds_read_b128 v[108:111], v3 offset:15360\n"
"v_mfma_f32_16x16x32_bf16 v[40:43], v[80:83], a[208:211], v[40:43]\n"
"v_mfma_f32_16x16x32_bf16 v[40:43], v[84:87], a[212:215], v[40:43]\n"
"buffer_load_dwordx4 v1, s[36:39], s8 offen offset:0 sc1 lds\n"
"v_mfma_f32_16x16x32_bf16 v[240:243], v[80:83], a[216:219], v[240:243]\n"
"s_waitcnt lgkmcnt(4)\n"
"ds_read_b128 v[80:83], v3 offset:6144\n"
"v_mfma_f32_16x16x32_bf16 v[240:243], v[84:87], a[220:223], v[240:243]\n"
"ds_read_b128 v[84:87], v3 offset:16384\n"
"v_mfma_f32_16x16x32_bf16 v[44:47], v[88:91], a[208:211], v[44:47]\n"
"v_mfma_f32_16x16x32_bf16 v[44:47], v[92:95], a[212:215], v[44:47]\n"
"buffer_load_dwordx4 v1, s[36:39], s8 offen offset:1024 sc1 lds\n"
"v_mfma_f32_16x16x32_bf16 v[244:247], v[88:91], a[216:219], v[244:247]\n"
"s_waitcnt lgkmcnt(4)\n"
"ds_read_b128 v[88:91], v3 offset:7168\n"
"v_mfma_f32_16x16x32_bf16 v[244:247], v[92:95], a[220:223], v[244:247]\n"
"ds_read_b128 v[92:95], v3 offset:17408\n"
"v_mfma_f32_16x16x32_bf16 v[48:51], v[96:99], a[208:211], v[48:51]\n"
"v_mfma_f32_16x16x32_bf16 v[48:51], v[100:103], a[212:215], v[48:51]\n"
"buffer_load_dwordx4 v1, s[36:39], s8 offen offset:2048 sc1 lds\n"
"v_mfma_f32_16x16x32_bf16 v[248:251], v[96:99], a[216:219], v[248:251]\n"
"s_waitcnt lgkmcnt(4)\n"
"ds_read_b128 v[96:99], v3 offset:8192\n"
"v_mfma_f32_16x16x32_bf16 v[248:251], v[100:103], a[220:223], v[248:251]\n"
"ds_read_b128 v[100:103], v3 offset:18432\n"
"v_mfma_f32_16x16x32_bf16 v[52:55], v[104:107], a[208:211], v[52:55]\n"
"s_waitcnt vmcnt(10)\n"
"s_barrier\n"
"v_mfma_f32_16x16x32_bf16 v[52:55], v[108:111], a[212:215], v[52:55]\n"
"buffer_load_dwordx4 v1, s[36:39], s8 offen offset:3072 sc1 lds\n"
"v_mfma_f32_16x16x32_bf16 v[252:255], v[104:107], a[216:219], v[252:255]\n"
"s_cmp_lg_u32 s76, 0\n"
"s_cbranch_scc1 .Lsig6\n"
"s_mov_b64 exec, 1\n"
"v_mov_b32 v26, 1\n"
"buffer_atomic_add v26, off, s[36:39], s16 offset:0 sc1\n"
"s_mov_b64 exec, -1\n"
".Lsig6:\n"
"v_mfma_f32_16x16x32_bf16 v[252:255], v[108:111], a[220:223], v[252:255]\n"
"s_waitcnt lgkmcnt(4)\n"
"s_add_u32 m0, s7, 86016\n"
"v_mfma_f32_16x16x32_bf16 v[56:59], v[80:83], a[208:211], v[56:59]\n"
"ds_read_b128 v[104:107], v3 offset:9216\n"
"v_mfma_f32_16x16x32_bf16 v[56:59], v[84:87], a[212:215], v[56:59]\n"
"buffer_load_dwordx4 v4, s[36:39], s8 offen sc1 lds\n"
"v_mfma_f32_16x16x32_bf16 v[176:179], v[80:83], a[216:219], v[176:179]\n"
"s_add_u32 s8, s8, 491520\n"
"s_waitcnt lgkmcnt(3)\n"
"v_mfma_f32_16x16x32_bf16 v[176:179], v[84:87], a[220:223], v[176:179]\n"
"ds_read_b128 v[108:111], v3 offset:19456\n"
"v_mfma_f32_16x16x32_bf16 v[60:63], v[88:91], a[208:211], v[60:63]\n"
"ds_read_b128 v[80:83], v3 offset:20480\n"
"v_mfma_f32_16x16x32_bf16 v[60:63], v[92:95], a[212:215], v[60:63]\n"
"buffer_load_dwordx4 a[200:203], v11, s[36:39], s11 offen sc1\n"
"v_mfma_f32_16x16x32_bf16 v[180:183], v[88:91], a[216:219], v[180:183]\n"
"s_waitcnt lgkmcnt(3)\n"
"ds_read_b128 v[84:87], v3 offset:30720\n"
"v_mfma_f32_16x16x32_bf16 v[180:183], v[92:95], a[220:223], v[180:183]\n"
"ds_read_b128 v[88:91], v3 offset:21504\n"
"v_mfma_f32_16x16x32_bf16 v[64:67], v[96:99], a[208:211], v[64:67]\n"
"ds_read_b128 v[92:95], v3 offset:31744\n"
"v_mfma_f32_16x16x32_bf16 v[64:67], v[100:103], a[212:215], v[64:67]\n"
"buffer_load_dwordx4 a[204:207], v13, s[36:39], s11 offen sc1\n"
"v_mfma_f32_16x16x32_bf16 v[184:187], v[96:99], a[216:219], v[184:187]\n"
"s_add_u32 s11, s11, 1572864\n"
"s_waitcnt lgkmcnt(4)\n"
"v_mfma_f32_16x16x32_bf16 v[184:187], v[100:103], a[220:223], v[184:187]\n"
"ds_read_b128 v[96:99], v3 offset:22528\n"
"v_mfma_f32_16x16x32_bf16 v[68:71], v[104:107], a[208:211], v[68:71]\n"
"ds_read_b128 v[100:103], v3 offset:32768\n"
"v_mfma_f32_16x16x32_bf16 v[68:71], v[108:111], a[212:215], v[68:71]\n"
"buffer_load_dwordx4 a[0:3], v10, s[36:39], s11 offen sc1\n"
"v_mfma_f32_16x16x32_bf16 v[188:191], v[104:107], a[216:219], v[188:191]\n"
"s_waitcnt lgkmcnt(4)\n"
"ds_read_b128 v[104:107], v3 offset:23552\n"
"v_mfma_f32_16x16x32_bf16 v[188:191], v[108:111], a[220:223], v[188:191]\n"
"ds_read_b128 v[108:111], v3 offset:33792\n"
"v_mfma_f32_16x16x32_bf16 v[32:35], v[80:83], a[224:227], v[32:35]\n"
"buffer_store_dwordx4 v[168:171], v1, s[36:39], s91 offen offset:3072 sc1\n"
"v_mfma_f32_16x16x32_bf16 v[32:35], v[84:87], a[228:231], v[32:35]\n"
"s_waitcnt lgkmcnt(4)\n"
"v_mfma_f32_16x16x32_bf16 v[72:75], v[80:83], a[232:235], v[72:75]\n"
"ds_read_b128 v[80:83], v3 offset:24576\n"
"v_mfma_f32_16x16x32_bf16 v[72:75], v[84:87], a[236:239], v[72:75]\n"
"ds_read_b128 v[84:87], v3 offset:34816\n"
"v_mfma_f32_16x16x32_bf16 v[36:39], v[88:91], a[224:227], v[36:39]\n"
"s_add_u32 m0, s7, 102400\n"
"v_mfma_f32_16x16x32_bf16 v[36:39], v[92:95], a[228:231], v[36:39]\n"
"buffer_load_dwordx4 v1, s[36:39], s8 offen offset:0 sc1 lds\n"
"v_mfma_f32_16x16x32_bf16 v[76:79], v[88:91], a[232:235], v[76:79]\n"
"s_waitcnt lgkmcnt(4)\n"
"ds_read_b128 v[88:91], v3 offset:25600\n"
"v_mfma_f32_16x16x32_bf16 v[76:79], v[92:95], a[236:239], v[76:79]\n"
"ds_read_b128 v[92:95], v3 offset:35840\n"
"v_mfma_f32_16x16x32_bf16 v[40:43], v[96:99], a[224:227], v[40:43]\n"
"v_mfma_f32_16x16x32_bf16 v[40:43], v[100:103], a[228:231], v[40:43]\n"
"buffer_load_dwordx4 v1, s[36:39], s8 offen offset:1024 sc1 lds\n"
"v_mfma_f32_16x16x32_bf16 v[240:243], v[96:99], a[232:235], v[240:243]\n"
"s_waitcnt lgkmcnt(4)\n"
"ds_read_b128 v[96:99], v3 offset:26624\n"
"v_mfma_f32_16x16x32_bf16 v[240:243], v[100:103], a[236:239], v[240:243]\n"
"ds_read_b128 v[100:103], v3 offset:36864\n"
"v_mfma_f32_16x16x32_bf16 v[44:47], v[104:107], a[224:227], v[44:47]\n"
"v_mfma_f32_16x16x32_bf16 v[44:47], v[108:111], a[228:231], v[44:47]\n"
"buffer_load_dwordx4 v1, s[36:39], s8 offen offset:2048 sc1 lds\n"
"v_mfma_f32_16x16x32_bf16 v[244:247], v[104:107], a[232:235], v[244:247]\n"
"s_waitcnt lgkmcnt(4)\n"
"ds_read_b128 v[104:107], v3 offset:27648\n"
"v_mfma_f32_16x16x32_bf16 v[244:247], v[108:111], a[236:239], v[244:247]\n"
"ds_read_b128 v[108:111], v3 offset:37888\n"
"v_mfma_f32_16x16x32_bf16 v[48:51], v[80:83], a[224:227], v[48:51]\n"
"v_mfma_f32_16x16x32_bf16 v[48:51], v[84:87], a[228:231], v[48:51]\n"
"buffer_load_dwordx4 v1, s[36:39], s8 offen offset:3072 sc1 lds\n"
"v_mfma_f32_16x16x32_bf16 v[248:251], v[80:83], a[232:235], v[248:251]\n"
"s_add_u32 m0, s7, 106496\n"
"s_waitcnt lgkmcnt(4)\n"
"v_mfma_f32_16x16x32_bf16 v[248:251], v[84:87], a[236:239], v[248:251]\n"
"ds_read_b128 v[80:83], v3 offset:28672\n"
"v_mfma_f32_16x16x32_bf16 v[52:55], v[88:91], a[224:227], v[52:55]\n"
"s_waitcnt vmcnt(16)\n"
"s_barrier\n"
"v_mfma_f32_16x16x32_bf16 v[52:55], v[92:95], a[228:231], v[52:55]\n"
"buffer_load_dwordx4 v4, s[36:39], s8 offen sc1 lds\n"
"v_mfma_f32_16x16x32_bf16 v[252:255], v[88:91], a[232:235], v[252:255]\n"
"s_sub_u32 s8, s8, 1433600\n"
"s_waitcnt lgkmcnt(3)\n"
"v_mfma_f32_16x16x32_bf16 v[252:255], v[92:95], a[236:239], v[252:255]\n"
"ds_read_b128 v[84:87], v3 offset:38912\n"
"v_mfma_f32_16x16x32_bf16 v[56:59], v[96:99], a[224:227], v[56:59]\n"
"ds_read_b128 v[88:91], v3 offset:29696\n"
"v_mfma_f32_16x16x32_bf16 v[56:59], v[100:103], a[228:231], v[56:59]\n"
"buffer_load_dwordx4 a[4:7], v12, s[36:39], s11 offen sc1\n"
"v_mfma_f32_16x16x32_bf16 v[176:179], v[96:99], a[232:235], v[176:179]\n"
"s_waitcnt lgkmcnt(3)\n"
"ds_read_b128 v[92:95], v3 offset:39936\n"
"v_mfma_f32_16x16x32_bf16 v[176:179], v[100:103], a[236:239], v[176:179]\n"
"ds_read_b128 v[96:99], v1 offset:20480\n"
"v_mfma_f32_16x16x32_bf16 v[60:63], v[104:107], a[224:227], v[60:63]\n"
"ds_read_b128 v[100:103], v1 offset:30720\n"
"v_mfma_f32_16x16x32_bf16 v[60:63], v[108:111], a[228:231], v[60:63]\n"
"buffer_load_dwordx4 a[8:11], v11, s[36:39], s11 offen sc1\n"
"v_mfma_f32_16x16x32_bf16 v[180:183], v[104:107], a[232:235], v[180:183]\n"
"s_waitcnt lgkmcnt(4)\n"
"ds_read_b128 v[104:107], v1 offset:21504\n"
"v_mfma_f32_16x16x32_bf16 v[180:183], v[108:111], a[236:239], v[180:183]\n"
"ds_read_b128 v[108:111], v1 offset:31744\n"
"v_mfma_f32_16x16x32_bf16 v[64:67], v[80:83], a[224:227], v[64:67]\n"
"v_mfma_f32_16x16x32_bf16 v[64:67], v[84:87], a[228:231], v[64:67]\n"
"buffer_load_dwordx4 a[12:15], v13, s[36:39], s11 offen sc1\n"
"v_mfma_f32_16x16x32_bf16 v[184:187], v[80:83], a[232:235], v[184:187]\n"
"s_add_u32 s11, s11, 1572864\n"
"s_waitcnt lgkmcnt(4)\n"
"v_mfma_f32_16x16x32_bf16 v[184:187], v[84:87], a[236:239], v[184:187]\n"
"ds_read_b128 v[80:83], v1 offset:22528\n"
"v_mfma_f32_16x16x32_bf16 v[68:71], v[88:91], a[224:227], v[68:71]\n"
"ds_read_b128 v[84:87], v1 offset:32768\n"
"v_mfma_f32_16x16x32_bf16 v[68:71], v[92:95], a[228:231], v[68:71]\n"
"buffer_load_dwordx4 a[16:19], v10, s[36:39], s11 offen sc1\n"
"v_mfma_f32_16x16x32_bf16 v[188:191], v[88:91], a[232:235], v[188:191]\n"
"s_waitcnt lgkmcnt(4)\n"
"ds_read_b128 v[88:91], v1 offset:23552\n"
"v_mfma_f32_16x16x32_bf16 v[188:191], v[92:95], a[236:239], v[188:191]\n"
"ds_read_b128 v[92:95], v1 offset:33792\n"
"v_mfma_f32_16x16x32_bf16 v[32:35], v[96:99], a[240:243], v[32:35]\n"
"v_mfma_f32_16x16x32_bf16 v[32:35], v[100:103], a[244:247], v[32:35]\n"
"buffer_load_dwordx4 a[20:23], v12, s[36:39], s11 offen sc1\n"
"v_mfma_f32_16x16x32_bf16 v[72:75], v[96:99], a[248:251], v[72:75]\n"
"s_waitcnt lgkmcnt(4)\n"
"ds_read_b128 v[96:99], v1 offset:24576\n"
"v_mfma_f32_16x16x32_bf16 v[72:75], v[100:103], a[252:255], v[72:75]\n"
"ds_read_b128 v[100:103], v1 offset:34816\n"
"v_mfma_f32_16x16x32_bf16 v[36:39], v[104:107], a[240:243], v[36:39]\n"
"s_add_u32 m0, s7, 0\n"
"v_mfma_f32_16x16x32_bf16 v[36:39], v[108:111], a[244:247], v[36:39]\n"
"buffer_load_dwordx4 v1, s[36:39], s8 offen offset:0 sc1 lds\n"
"v_mfma_f32_16x16x32_bf16 v[76:79], v[104:107], a[248:251], v[76:79]\n"
"s_waitcnt lgkmcnt(4)\n"
"ds_read_b128 v[104:107], v1 offset:25600\n"
"v_mfma_f32_16x16x32_bf16 v[76:79], v[108:111], a[252:255], v[76:79]\n"
"ds_read_b128 v[108:111], v1 offset:35840\n"
"v_mfma_f32_16x16x32_bf16 v[40:43], v[80:83], a[240:243], v[40:43]\n"
"v_mfma_f32_16x16x32_bf16 v[40:43], v[84:87], a[244:247], v[40:43]\n"
"buffer_load_dwordx4 v1, s[36:39], s8 offen offset:1024 sc1 lds\n"
"v_mfma_f32_16x16x32_bf16 v[240:243], v[80:83], a[248:251], v[240:243]\n"
"s_waitcnt lgkmcnt(4)\n"
"ds_read_b128 v[80:83], v1 offset:26624\n"
"v_mfma_f32_16x16x32_bf16 v[240:243], v[84:87], a[252:255], v[240:243]\n"
"ds_read_b128 v[84:87], v1 offset:36864\n"
"v_mfma_f32_16x16x32_bf16 v[44:47], v[88:91], a[240:243], v[44:47]\n"
"v_mfma_f32_16x16x32_bf16 v[44:47], v[92:95], a[244:247], v[44:47]\n"
"buffer_load_dwordx4 v1, s[36:39], s8 offen offset:2048 sc1 lds\n"
"v_mfma_f32_16x16x32_bf16 v[244:247], v[88:91], a[248:251], v[244:247]\n"
"s_waitcnt lgkmcnt(4)\n"
"ds_read_b128 v[88:91], v1 offset:27648\n"
"v_mfma_f32_16x16x32_bf16 v[244:247], v[92:95], a[252:255], v[244:247]\n"
"ds_read_b128 v[92:95], v1 offset:37888\n"
"v_mfma_f32_16x16x32_bf16 v[48:51], v[96:99], a[240:243], v[48:51]\n"
"v_mfma_f32_16x16x32_bf16 v[48:51], v[100:103], a[244:247], v[48:51]\n"
"buffer_load_dwordx4 v1, s[36:39], s8 offen offset:3072 sc1 lds\n"
"v_mfma_f32_16x16x32_bf16 v[248:251], v[96:99], a[248:251], v[248:251]\n"
"s_add_u32 m0, s7, 4096\n"
"s_waitcnt lgkmcnt(4)\n"
"v_mfma_f32_16x16x32_bf16 v[248:251], v[100:103], a[252:255], v[248:251]\n"
"ds_read_b128 v[96:99], v1 offset:28672\n"
"v_mfma_f32_16x16x32_bf16 v[52:55], v[104:107], a[240:243], v[52:55]\n"
"s_waitcnt vmcnt(14)\n"
"s_barrier\n"
"v_mfma_f32_16x16x32_bf16 v[52:55], v[108:111], a[244:247], v[52:55]\n"
"buffer_load_dwordx4 v4, s[36:39], s8 offen sc1 lds\n"
"v_mfma_f32_16x16x32_bf16 v[252:255], v[104:107], a[248:251], v[252:255]\n"
"s_cmp_lg_u32 s76, 0\n"
"s_cbranch_scc1 .Lsig5\n"
"s_mov_b64 exec, 1\n"
"v_mov_b32 v8, 1\n"
"buffer_atomic_add v8, off, s[36:39], s14 offset:2048 sc1\n"
"s_mov_b64 exec, -1\n"
".Lsig5:\n"
"v_mfma_f32_16x16x32_bf16 v[252:255], v[108:111], a[252:255], v[252:255]\n"
"s_waitcnt lgkmcnt(3)\n"
"s_add_u32 s8, s8, 491520\n"
"v_mfma_f32_16x16x32_bf16 v[56:59], v[80:83], a[240:243], v[56:59]\n"
"ds_read_b128 v[100:103], v1 offset:38912\n"
"v_mfma_f32_16x16x32_bf16 v[56:59], v[84:87], a[244:247], v[56:59]\n"
"buffer_load_dwordx4 a[24:27], v11, s[36:39], s11 offen sc1\n"
"v_mfma_f32_16x16x32_bf16 v[176:179], v[80:83], a[248:251], v[176:179]\n"
"s_waitcnt lgkmcnt(2)\n"
"ds_read_b128 v[104:107], v1 offset:29696\n"
"v_mfma_f32_16x16x32_bf16 v[176:179], v[84:87], a[252:255], v[176:179]\n"
"ds_read_b128 v[108:111], v1 offset:39936\n"
"v_mfma_f32_16x16x32_bf16 v[60:63], v[88:91], a[240:243], v[60:63]\n"
"ds_read_b128 v[80:83], v1 offset:40960\n"
"v_mfma_f32_16x16x32_bf16 v[60:63], v[92:95], a[244:247], v[60:63]\n"
"buffer_load_dwordx4 a[28:31], v13, s[36:39], s11 offen sc1\n"
"v_mfma_f32_16x16x32_bf16 v[180:183], v[88:91], a[248:251], v[180:183]\n"
"s_add_u32 s11, s11, 1572864\n"
"s_waitcnt lgkmcnt(3)\n"
"v_mfma_f32_16x16x32_bf16 v[180:183], v[92:95], a[252:255], v[180:183]\n"
"ds_read_b128 v[84:87], v1 offset:51200\n"
"v_mfma_f32_16x16x32_bf16 v[64:67], v[96:99], a[240:243], v[64:67]\n"
"ds_read_b128 v[88:91], v1 offset:41984\n"
"v_mfma_f32_16x16x32_bf16 v[64:67], v[100:103], a[244:247], v[64:67]\n"
"buffer_load_dwordx4 a[32:35], v10, s[36:39], s11 offen sc1\n"
"v_mfma_f32_16x16x32_bf16 v[184:187], v[96:99], a[248:251], v[184:187]\n"
"s_waitcnt lgkmcnt(3)\n"
"ds_read_b128 v[92:95], v1 offset:52224\n"
"v_mfma_f32_16x16x32_bf16 v[184:187], v[100:103], a[252:255], v[184:187]\n"
"ds_read_b128 v[96:99], v1 offset:43008\n"
"v_mfma_f32_16x16x32_bf16 v[68:71], v[104:107], a[240:243], v[68:71]\n"
"ds_read_b128 v[100:103], v1 offset:53248\n"
"v_mfma_f32_16x16x32_bf16 v[68:71], v[108:111], a[244:247], v[68:71]\n"
"buffer_load_dwordx4 a[36:39], v12, s[36:39], s11 offen sc1\n"
"v_mfma_f32_16x16x32_bf16 v[188:191], v[104:107], a[248:251], v[188:191]\n"
"s_waitcnt lgkmcnt(4)\n"
"ds_read_b128 v[104:107], v1 offset:44032\n"
"v_mfma_f32_16x16x32_bf16 v[188:191], v[108:111], a[252:255], v[188:191]\n"
"ds_read_b128 v[108:111], v1 offset:54272\n"
"v_mfma_f32_16x16x32_bf16 v[32:35], v[80:83], a[192:195], v[32:35]\n"
"v_mfma_f32_16x16x32_bf16 v[32:35], v[84:87], a[196:199], v[32:35]\n"
"buffer_load_dwordx4 a[40:43], v11, s[36:39], s11 offen sc1\n"
"v_mfma_f32_16x16x32_bf16 v[72:75], v[80:83], a[200:203], v[72:75]\n"
"s_waitcnt lgkmcnt(4)\n"
"ds_read_b128 v[80:83], v1 offset:45056\n"
"v_mfma_f32_16x16x32_bf16 v[72:75], v[84:87], a[204:207], v[72:75]\n"
"ds_read_b128 v[84:87], v1 offset:55296\n"
"v_mfma_f32_16x16x32_bf16 v[36:39], v[88:91], a[192:195], v[36:39]\n"
"s_add_u32 m0, s7, 122880\n"
"v_mfma_f32_16x16x32_bf16 v[36:39], v[92:95], a[196:199], v[36:39]\n"
"buffer_load_dwordx4 v1, s[36:39], s8 offen offset:0 sc1 lds\n"
"v_mfma_f32_16x16x32_bf16 v[76:79], v[88:91], a[200:203], v[76:79]\n"
"s_waitcnt lgkmcnt(4)\n"
"ds_read_b128 v[88:91], v1 offset:46080\n"
"v_mfma_f32_16x16x32_bf16 v[76:79], v[92:95], a[204:207], v[76:79]\n"
"ds_read_b128 v[92:95], v1 offset:56320\n"
"v_mfma_f32_16x16x32_bf16 v[40:43], v[96:99], a[192:195], v[40:43]\n"
"v_mfma_f32_16x16x32_bf16 v[40:43], v[100:103], a[196:199], v[40:43]\n"
"buffer_load_dwordx4 v1, s[36:39], s8 offen offset:1024 sc1 lds\n"
"v_mfma_f32_16x16x32_bf16 v[240:243], v[96:99], a[200:203], v[240:243]\n"
"s_waitcnt lgkmcnt(4)\n"
"ds_read_b128 v[96:99], v1 offset:47104\n"
"v_mfma_f32_16x16x32_bf16 v[240:243], v[100:103], a[204:207], v[240:243]\n"
"ds_read_b128 v[100:103], v1 offset:57344\n"
"v_mfma_f32_16x16x32_bf16 v[44:47], v[104:107], a[192:195], v[44:47]\n"
"buffer_load_dword v27, off, s[36:39], s16 offset:0 sc1\n"
"v_mfma_f32_16x16x32_bf16 v[44:47], v[108:111], a[196:199], v[44:47]\n"
"s_waitcnt lgkmcnt(4)\n"
"v_mfma_f32_16x16x32_bf16 v[244:247], v[104:107], a[200:203], v[244:247]\n"
"buffer_load_dwordx4 v1, s[36:39], s8 offen offset:2048 sc1 lds\n"
"v_mfma_f32_16x16x32_bf16 v[244:247], v[108:111], a[204:207], v[244:247]\n"
"ds_read_b128 v[104:107], v1 offset:48128\n"
"v_mfma_f32_16x16x32_bf16 v[48:51], v[80:83], a[192:195], v[48:51]\n"
"ds_read_b128 v[108:111], v1 offset:58368\n"
"v_mfma_f32_16x16x32_bf16 v[48:51], v[84:87], a[196:199], v[48:51]\n"
"s_waitcnt lgkmcnt(4)\n"
"v_mfma_f32_16x16x32_bf16 v[248:251], v[80:83], a[200:203], v[248:251]\n"
"buffer_load_dwordx4 v1, s[36:39], s8 offen offset:3072 sc1 lds\n"
"v_mfma_f32_16x16x32_bf16 v[248:251], v[84:87], a[204:207], v[248:251]\n"
"s_add_u32 m0, s7, 126976\n"
"ds_read_b128 v[80:83], v1 offset:49152\n"
"v_mfma_f32_16x16x32_bf16 v[52:55], v[88:91], a[192:195], v[52:55]\n"
"s_waitcnt vmcnt(17)\n"
"s_barrier\n"
"v_mfma_f32_16x16x32_bf16 v[52:55], v[92:95], a[196:199], v[52:55]\n"
"s_waitcnt lgkmcnt(3)\n"
"ds_read_b128 v[84:87], v1 offset:59392\n"
"v_mfma_f32_16x16x32_bf16 v[252:255], v[88:91], a[200:203], v[252:255]\n"
"buffer_load_dwordx4 v4, s[36:39], s8 offen sc1 lds\n"
"v_mfma_f32_16x16x32_bf16 v[252:255], v[92:95], a[204:207], v[252:255]\n"
"s_add_u32 s8, s8, 491520\n"
"ds_read_b128 v[88:91], v1 offset:50176\n"
"v_mfma_f32_16x16x32_bf16 v[56:59], v[96:99], a[192:195], v[56:59]\n"
"ds_read_b128 v[92:95], v1 offset:60416\n"
"v_mfma_f32_16x16x32_bf16 v[56:59], v[100:103], a[196:199], v[56:59]\n"
"s_waitcnt lgkmcnt(4)\n"
"v_mfma_f32_16x16x32_bf16 v[176:179], v[96:99], a[200:203], v[176:179]\n"
"buffer_load_dwordx4 a[44:47], v13, s[36:39], s11 offen sc1\n"
"v_mfma_f32_16x16x32_bf16 v[176:179], v[100:103], a[204:207], v[176:179]\n"
"s_sub_u32 s11, s11, 4587520\n"
"ds_read_b128 v[96:99], v2 offset:0\n"
"v_mfma_f32_16x16x32_bf16 v[60:63], v[104:107], a[192:195], v[60:63]\n"
"ds_read_b128 v[100:103], v2 offset:10240\n"
"v_mfma_f32_16x16x32_bf16 v[60:63], v[108:111], a[196:199], v[60:63]\n"
"s_waitcnt lgkmcnt(4)\n"
"v_mfma_f32_16x16x32_bf16 v[180:183], v[104:107], a[200:203], v[180:183]\n"
"buffer_load_dwordx2 v[16:17], v6, s[68:71], s67 offen offset:0\n"
"v_mfma_f32_16x16x32_bf16 v[180:183], v[108:111], a[204:207], v[180:183]\n"
"ds_read_b128 v[104:107], v2 offset:1024\n"
"v_mfma_f32_16x16x32_bf16 v[64:67], v[80:83], a[192:195], v[64:67]\n"
"ds_read_b128 v[108:111], v2 offset:11264\n"
"v_mfma_f32_16x16x32_bf16 v[64:67], v[84:87], a[196:199], v[64:67]\n"
"s_waitcnt lgkmcnt(4)\n"
"v_mfma_f32_16x16x32_bf16 v[184:187], v[80:83], a[200:203], v[184:187]\n"
"buffer_load_dwordx4 a[48:51], v10, s[36:39], s11 offen sc1\n"
"v_mfma_f32_16x16x32_bf16 v[184:187], v[84:87], a[204:207], v[184:187]\n"
"ds_read_b128 v[80:83], v2 offset:2048\n"
"v_mfma_f32_16x16x32_bf16 v[68:71], v[88:91], a[192:195], v[68:71]\n"
"ds_read_b128 v[84:87], v2 offset:12288\n"
"v_mfma_f32_16x16x32_bf16 v[68:71], v[92:95], a[196:199], v[68:71]\n"
"s_waitcnt lgkmcnt(4)\n"
"v_mfma_f32_16x16x32_bf16 v[188:191], v[88:91], a[200:203], v[188:191]\n"
"buffer_load_dwordx4 a[52:55], v12, s[36:39], s11 offen sc1\n"
"v_mfma_f32_16x16x32_bf16 v[188:191], v[92:95], a[204:207], v[188:191]\n"
"ds_read_b128 v[88:91], v2 offset:3072\n"
"v_mfma_f32_16x16x32_bf16 v[32:35], v[96:99], a[0:3], v[32:35]\n"
"ds_read_b128 v[92:95], v2 offset:13312\n"
"v_mfma_f32_16x16x32_bf16 v[32:35], v[100:103], a[4:7], v[32:35]\n"
"s_waitcnt lgkmcnt(4)\n"
"v_mfma_f32_16x16x32_bf16 v[72:75], v[96:99], a[8:11], v[72:75]\n"
"buffer_load_dwordx4 a[56:59], v11, s[36:39], s11 offen sc1\n"
"v_mfma_f32_16x16x32_bf16 v[72:75], v[100:103], a[12:15], v[72:75]\n"
"ds_read_b128 v[96:99], v2 offset:4096\n"
"v_mfma_f32_16x16x32_bf16 v[36:39], v[104:107], a[0:3], v[36:39]\n"
"s_add_u32 m0, s7, 143360\n"
"ds_read_b128 v[100:103], v2 offset:14336\n"
"v_mfma_f32_16x16x32_bf16 v[36:39], v[108:111], a[4:7], v[36:39]\n"
"s_waitcnt lgkmcnt(4)\n"
"v_mfma_f32_16x16x32_bf16 v[76:79], v[104:107], a[8:11], v[76:79]\n"
"buffer_load_dwordx4 v1, s[36:39], s8 offen offset:0 sc1 lds\n"
"v_mfma_f32_16x16x32_bf16 v[76:79], v[108:111], a[12:15], v[76:79]\n"
"ds_read_b128 v[104:107], v2 offset:5120\n"
"v_mfma_f32_16x16x32_bf16 v[40:43], v[80:83], a[0:3], v[40:43]\n"
"ds_read_b128 v[108:111], v2 offset:15360\n"
"v_mfma_f32_16x16x32_bf16 v[40:43], v[84:87], a[4:7], v[40:43]\n"
"s_waitcnt lgkmcnt(4)\n"
"v_mfma_f32_16x16x32_bf16 v[240:243], v[80:83], a[8:11], v[240:243]\n"
"buffer_load_dwordx4 v1, s[36:39], s8 offen offset:1024 sc1 lds\n"
"v_mfma_f32_16x16x32_bf16 v[240:243], v[84:87], a[12:15], v[240:243]\n"
"ds_read_b128 v[80:83], v2 offset:6144\n"
"v_mfma_f32_16x16x32_bf16 v[44:47], v[88:91], a[0:3], v[44:47]\n"
"ds_read_b128 v[84:87], v2 offset:16384\n"
"v_mfma_f32_16x16x32_bf16 v[44:47], v[92:95], a[4:7], v[44:47]\n"
"s_waitcnt lgkmcnt(4)\n"
"v_mfma_f32_16x16x32_bf16 v[244:247], v[88:91], a[8:11], v[244:247]\n"
"buffer_load_dwordx4 v1, s[36:39], s8 offen offset:2048 sc1 lds\n"
"v_mfma_f32_16x16x32_bf16 v[244:247], v[92:95], a[12:15], v[244:247]\n"
"ds_read_b128 v[88:91], v2 offset:7168\n"
"v_mfma_f32_16x16x32_bf16 v[48:51], v[96:99], a[0:3], v[48:51]\n"
"ds_read_b128 v[92:95], v2 offset:17408\n"
"v_mfma_f32_16x16x32_bf16 v[48:51], v[100:103], a[4:7], v[48:51]\n"
"s_waitcnt lgkmcnt(4)\n"
"v_mfma_f32_16x16x32_bf16 v[248:251], v[96:99], a[8:11], v[248:251]\n"
"buffer_load_dwordx4 v1, s[36:39], s8 offen offset:3072 sc1 lds\n"
"v_mfma_f32_16x16x32_bf16 v[248:251], v[100:103], a[12:15], v[248:251]\n"
"s_add_u32 m0, s7, 147456\n"
"ds_read_b128 v[96:99], v2 offset:8192\n"
"v_mfma_f32_16x16x32_bf16 v[52:55], v[104:107], a[0:3], v[52:55]\n"
"s_waitcnt vmcnt(18)\n"
"s_barrier\n"
"buffer_load_dwordx2 v[18:19], v6, s[68:71], s67 offen offset:32\n"
"v_mfma_f32_16x16x32_bf16 v[52:55], v[108:111], a[4:7], v[52:55]\n"
"s_waitcnt lgkmcnt(3)\n"
"ds_read_b128 v[100:103], v2 offset:18432\n"
"v_mfma_f32_16x16x32_bf16 v[252:255], v[104:107], a[8:11], v[252:255]\n"
"ds_read_b128 v[104:107], v2 offset:9216\n"
"v_mfma_f32_16x16x32_bf16 v[252:255], v[108:111], a[12:15], v[252:255]\n"
"ds_read_b128 v[108:111], v2 offset:19456\n"
"v_mfma_f32_16x16x32_bf16 v[56:59], v[80:83], a[0:3], v[56:59]\n"
"v_mfma_f32_16x16x32_bf16 v[56:59], v[84:87], a[4:7], v[56:59]\n"
"s_waitcnt lgkmcnt(4)\n"
"v_mfma_f32_16x16x32_bf16 v[176:179], v[80:83], a[8:11], v[176:179]\n"
"buffer_load_dwordx4 v4, s[36:39], s8 offen sc1 lds\n"
"v_mfma_f32_16x16x32_bf16 v[176:179], v[84:87], a[12:15], v[176:179]\n"
"s_add_u32 s8, s8, 491520\n"
"ds_read_b128 v[80:83], v2 offset:20480\n"
"v_mfma_f32_16x16x32_bf16 v[60:63], v[88:91], a[0:3], v[60:63]\n"
"ds_read_b128 v[84:87], v2 offset:30720\n"
"v_mfma_f32_16x16x32_bf16 v[60:63], v[92:95], a[4:7], v[60:63]\n"
"s_waitcnt lgkmcnt(4)\n"
"v_mfma_f32_16x16x32_bf16 v[180:183], v[88:91], a[8:11], v[180:183]\n"
"buffer_load_dwordx4 a[60:63], v13, s[36:39], s11 offen sc1\n"
"v_mfma_f32_16x16x32_bf16 v[180:183], v[92:95], a[12:15], v[180:183]\n"
"s_add_u32 s11, s11, 1572864\n"
"ds_read_b128 v[88:91], v2 offset:21504\n"
"v_mfma_f32_16x16x32_bf16 v[64:67], v[96:99], a[0:3], v[64:67]\n"
"ds_read_b128 v[92:95], v2 offset:31744\n"
"v_mfma_f32_16x16x32_bf16 v[64:67], v[100:103], a[4:7], v[64:67]\n"
"s_waitcnt lgkmcnt(4)\n"
"v_mfma_f32_16x16x32_bf16 v[184:187], v[96:99], a[8:11], v[184:187]\n"
"buffer_load_dwordx4 a[208:211], v10, s[36:39], s11 offen sc1\n"
"v_mfma_f32_16x16x32_bf16 v[184:187], v[100:103], a[12:15], v[184:187]\n"
"ds_read_b128 v[96:99], v2 offset:22528\n"
"v_mfma_f32_16x16x32_bf16 v[68:71], v[104:107], a[0:3], v[68:71]\n"
"ds_read_b128 v[100:103], v2 offset:32768\n"
"v_mfma_f32_16x16x32_bf16 v[68:71], v[108:111], a[4:7], v[68:71]\n"
"s_waitcnt lgkmcnt(4)\n"
"v_mfma_f32_16x16x32_bf16 v[188:191], v[104:107], a[8:11], v[188:191]\n"
"buffer_load_dwordx4 a[212:215], v12, s[36:39], s11 offen sc1\n"
"v_mfma_f32_16x16x32_bf16 v[188:191], v[108:111], a[12:15], v[188:191]\n"
"ds_read_b128 v[104:107], v2 offset:23552\n"
"v_mfma_f32_16x16x32_bf16 v[32:35], v[80:83], a[16:19], v[32:35]\n"
"ds_read_b128 v[108:111], v2 offset:33792\n"
"v_mfma_f32_16x16x32_bf16 v[32:35], v[84:87], a[20:23], v[32:35]\n"
"s_waitcnt lgkmcnt(4)\n"
"v_mfma_f32_16x16x32_bf16 v[72:75], v[80:83], a[24:27], v[72:75]\n"
"buffer_load_dwordx4 a[216:219], v11, s[36:39], s11 offen sc1\n"
"v_mfma_f32_16x16x32_bf16 v[72:75], v[84:87], a[28:31], v[72:75]\n"
"ds_read_b128 v[80:83], v2 offset:24576\n"
"v_mfma_f32_16x16x32_bf16 v[36:39], v[88:91], a[16:19], v[36:39]\n"
"s_add_u32 m0, s7, 20480\n"
"ds_read_b128 v[84:87], v2 offset:34816\n"
"v_mfma_f32_16x16x32_bf16 v[36:39], v[92:95], a[20:23], v[36:39]\n"
"s_waitcnt lgkmcnt(4)\n"
"v_mfma_f32_16x16x32_bf16 v[76:79], v[88:91], a[24:27], v[76:79]\n"
"buffer_load_dwordx4 v1, s[36:39], s8 offen offset:0 sc1 lds\n"
"v_mfma_f32_16x16x32_bf16 v[76:79], v[92:95], a[28:31], v[76:79]\n"
"ds_read_b128 v[88:91], v2 offset:25600\n"
"v_mfma_f32_16x16x32_bf16 v[40:43], v[96:99], a[16:19], v[40:43]\n"
"ds_read_b128 v[92:95], v2 offset:35840\n"
"v_mfma_f32_16x16x32_bf16 v[40:43], v[100:103], a[20:23], v[40:43]\n"
"s_waitcnt lgkmcnt(4)\n"
"v_mfma_f32_16x16x32_bf16 v[240:243], v[96:99], a[24:27], v[240:243]\n"
"buffer_load_dwordx2 v[20:21], v6, s[68:71], s67 offen offset:64\n"
"v_mfma_f32_16x16x32_bf16 v[240:243], v[100:103], a[28:31], v[240:243]\n"
"ds_read_b128 v[96:99], v2 offset:26624\n"
"v_mfma_f32_16x16x32_bf16 v[44:47], v[104:107], a[16:19], v[44:47]\n"
"ds_read_b128 v[100:103], v2 offset:36864\n"
"v_mfma_f32_16x16x32_bf16 v[44:47], v[108:111], a[20:23], v[44:47]\n"
"s_waitcnt lgkmcnt(4)\n"
"v_mfma_f32_16x16x32_bf16 v[244:247], v[104:107], a[24:27], v[244:247]\n"
"buffer_load_dwordx4 v1, s[36:39], s8 offen offset:1024 sc1 lds\n"
"v_mfma_f32_16x16x32_bf16 v[244:247], v[108:111], a[28:31], v[244:247]\n"
"ds_read_b128 v[104:107], v2 offset:27648\n"
"v_mfma_f32_16x16x32_bf16 v[48:51], v[80:83], a[16:19], v[48:51]\n"
"ds_read_b128 v[108:111], v2 offset:37888\n"
"v_mfma_f32_16x16x32_bf16 v[48:51], v[84:87], a[20:23], v[48:51]\n"
"s_waitcnt lgkmcnt(4)\n"
"v_mfma_f32_16x16x32_bf16 v[248:251], v[80:83], a[24:27], v[248:251]\n"
"buffer_load_dwordx4 v1, s[36:39], s8 offen offset:2048 sc1 lds\n"
"v_mfma_f32_16x16x32_bf16 v[248:251], v[84:87], a[28:31], v[248:251]\n"
"ds_read_b128 v[80:83], v2 offset:28672\n"
"v_mfma_f32_16x16x32_bf16 v[52:55], v[88:91], a[16:19], v[52:55]\n"
"s_barrier\n"
"v_mfma_f32_16x16x32_bf16 v[52:55], v[92:95], a[20:23], v[52:55]\n"
"s_waitcnt lgkmcnt(3)\n"
"ds_read_b128 v[84:87], v2 offset:38912\n"
"v_mfma_f32_16x16x32_bf16 v[252:255], v[88:91], a[24:27], v[252:255]\n"
"buffer_load_dwordx4 v1, s[36:39], s8 offen offset:3072 sc1 lds\n"
"v_mfma_f32_16x16x32_bf16 v[252:255], v[92:95], a[28:31], v[252:255]\n"
"s_waitcnt vmcnt(19)\n"
"v_cmp_gt_u32 s[92:93], 4, v27\n"
"s_nop 0\n"
"s_cmp_eq_u64 s[92:93], 0\n"
"s_cbranch_scc0 .Lspin6\n"
".Lback6:\n"
"v_mfma_f32_16x16x32_bf16 v[56:59], v[96:99], a[16:19], v[56:59]\n"
"s_add_u32 m0, s7, 24576\n"
"ds_read_b128 v[88:91], v2 offset:29696\n"
"v_mfma_f32_16x16x32_bf16 v[56:59], v[100:103], a[20:23], v[56:59]\n"
"s_waitcnt lgkmcnt(3)\n"
"ds_read_b128 v[92:95], v2 offset:39936\n"
"v_mfma_f32_16x16x32_bf16 v[176:179], v[96:99], a[24:27], v[176:179]\n"
"buffer_load_dwordx4 v4, s[36:39], s8 offen sc1 lds\n"
"v_mfma_f32_16x16x32_bf16 v[176:179], v[100:103], a[28:31], v[176:179]\n"
"s_sub_u32 s8, s8, 1433600\n"
"ds_read_b128 v[96:99], v2 offset:40960\n"
"v_mfma_f32_16x16x32_bf16 v[60:63], v[104:107], a[16:19], v[60:63]\n"
"ds_read_b128 v[100:103], v2 offset:51200\n"
"s_add_u32 s17, s17, 5120\n"
"v_mfma_f32_16x16x32_bf16 v[60:63], v[108:111], a[20:23], v[60:63]\n"
"s_waitcnt lgkmcnt(4)\n"
"v_mfma_f32_16x16x32_bf16 v[180:183], v[104:107], a[24:27], v[180:183]\n"
"buffer_load_dwordx4 a[220:223], v13, s[36:39], s11 offen sc1\n"
"v_mfma_f32_16x16x32_bf16 v[180:183], v[108:111], a[28:31], v[180:183]\n"
"s_add_u32 s11, s11, 1572864\n"
"ds_read_b128 v[104:107], v2 offset:41984\n"
"v_mfma_f32_16x16x32_bf16 v[64:67], v[80:83], a[16:19], v[64:67]\n"
"ds_read_b128 v[108:111], v2 offset:52224\n"
"v_mfma_f32_16x16x32_bf16 v[64:67], v[84:87], a[20:23], v[64:67]\n"
"s_waitcnt lgkmcnt(4)\n"
"v_mfma_f32_16x16x32_bf16 v[184:187], v[80:83], a[24:27], v[184:187]\n"
"buffer_load_dwordx4 v[132:135], v1, s[36:39], s17 offen offset:0 sc1\n"
"v_mfma_f32_16x16x32_bf16 v[184:187], v[84:87], a[28:31], v[184:187]\n"
"ds_read_b128 v[80:83], v2 offset:43008\n"
"v_mfma_f32_16x16x32_bf16 v[68:71], v[88:91], a[16:19], v[68:71]\n"
"ds_read_b128 v[84:87], v2 offset:53248\n"
"v_mfma_f32_16x16x32_bf16 v[68:71], v[92:95], a[20:23], v[68:71]\n"
"s_waitcnt lgkmcnt(4)\n"
"v_mfma_f32_16x16x32_bf16 v[188:191], v[88:91], a[24:27], v[188:191]\n"
"buffer_load_dwordx4 a[224:227], v10, s[36:39], s11 offen sc1\n"
"v_mfma_f32_16x16x32_bf16 v[188:191], v[92:95], a[28:31], v[188:191]\n"
"ds_read_b128 v[88:91], v2 offset:44032\n"
"v_mfma_f32_16x16x32_bf16 v[32:35], v[96:99], a[32:35], v[32:35]\n"
"buffer_load_dwordx2 v[22:23], v6, s[68:71], s67 offen offset:96\n"
"v_mfma_f32_16x16x32_bf16 v[32:35], v[100:103], a[36:39], v[32:35]\n"
"s_waitcnt lgkmcnt(3)\n"
"ds_read_b128 v[92:95], v2 offset:54272\n"
"v_mfma_f32_16x16x32_bf16 v[72:75], v[96:99], a[40:43], v[72:75]\n"
"ds_read_b128 v[96:99], v2 offset:45056\n"
"v_mfma_f32_16x16x32_bf16 v[72:75], v[100:103], a[44:47], v[72:75]\n"
"ds_read_b128 v[100:103], v2 offset:55296\n"
"v_mfma_f32_16x16x32_bf16 v[36:39], v[104:107], a[32:35], v[36:39]\n"
"s_add_u32 m0, s7, 40960\n"
"v_mfma_f32_16x16x32_bf16 v[36:39], v[108:111], a[36:39], v[36:39]\n"
"s_waitcnt lgkmcnt(4)\n"
"v_mfma_f32_16x16x32_bf16 v[76:79], v[104:107], a[40:43], v[76:79]\n"
"buffer_load_dword v7, off, s[36:39], s14 offset:2048 sc1\n"
"v_mfma_f32_16x16x32_bf16 v[76:79], v[108:111], a[44:47], v[76:79]\n"
"buffer_load_dwordx4 v1, s[36:39], s8 offen offset:0 sc1 lds\n"
"v_mfma_f32_16x16x32_bf16 v[40:43], v[80:83], a[32:35], v[40:43]\n"
"buffer_load_dwordx4 v[136:139], v1, s[36:39], s17 offen offset:1024 sc1\n"
"v_mfma_f32_16x16x32_bf16 v[40:43], v[84:87], a[36:39], v[40:43]\n"
"s_waitcnt lgkmcnt(2)\n"
"ds_read_b128 v[104:107], v2 offset:46080\n"
"v_mfma_f32_16x16x32_bf16 v[240:243], v[80:83], a[40:43], v[240:243]\n"
"ds_read_b128 v[108:111], v2 offset:56320\n"
"v_mfma_f32_16x16x32_bf16 v[240:243], v[84:87], a[44:47], v[240:243]\n"
"ds_read_b128 v[80:83], v2 offset:47104\n"
"v_mfma_f32_16x16x32_bf16 v[44:47], v[88:91], a[32:35], v[44:47]\n"
"ds_read_b128 v[84:87], v2 offset:57344\n"
"v_mfma_f32_16x16x32_bf16 v[44:47], v[92:95], a[36:39], v[44:47]\n"
"s_waitcnt lgkmcnt(4)\n"
"v_mfma_f32_16x16x32_bf16 v[244:247], v[88:91], a[40:43], v[244:247]\n"
"ds_read_b128 v[88:91], v2 offset:48128\n"
"v_mfma_f32_16x16x32_bf16 v[244:247], v[92:95], a[44:47], v[244:247]\n"
"buffer_load_dwordx4 v1, s[36:39], s8 offen offset:1024 sc1 lds\n"
"v_mfma_f32_16x16x32_bf16 v[48:51], v[96:99], a[32:35], v[48:51]\n"
"ds_read_b128 v[92:95], v2 offset:58368\n"
"v_mfma_f32_16x16x32_bf16 v[48:51], v[100:103], a[36:39], v[48:51]\n"
"s_waitcnt lgkmcnt(4)\n"
"v_mfma_f32_16x16x32_bf16 v[248:251], v[96:99], a[40:43], v[248:251]\n"
"ds_read_b128 v[96:99], v2 offset:49152\n"
"v_mfma_f32_16x16x32_bf16 v[248:251], v[100:103], a[44:47], v[248:251]\n"
"buffer_load_dwordx4 v1, s[36:39], s8 offen offset:2048 sc1 lds\n"
"v_mfma_f32_16x16x32_bf16 v[52:55], v[104:107], a[32:35], v[52:55]\n"
"s_waitcnt vmcnt(18)\n"
"s_barrier\n"
"v_mfma_f32_16x16x32_bf16 v[52:55], v[108:111], a[36:39], v[52:55]\n"
"s_waitcnt lgkmcnt(3)\n"
"ds_read_b128 v[100:103], v2 offset:59392\n"
"v_mfma_f32_16x16x32_bf16 v[252:255], v[104:107], a[40:43], v[252:255]\n"
"buffer_load_dwordx2 v[140:141], v5, s[36:39], s17 offen offset:0 sc1\n"
"v_mfma_f32_16x16x32_bf16 v[252:255], v[108:111], a[44:47], v[252:255]\n"
"ds_read_b128 v[104:107], v2 offset:50176\n"
"s_add_u32 s17, s17, 5120\n"
"v_mfma_f32_16x16x32_bf16 v[56:59], v[80:83], a[32:35], v[56:59]\n"
"ds_read_b128 v[108:111], v2 offset:60416\n"
"v_mfma_f32_16x16x32_bf16 v[56:59], v[84:87], a[36:39], v[56:59]\n"
"s_waitcnt lgkmcnt(4)\n"
"v_mfma_f32_16x16x32_bf16 v[176:179], v[80:83], a[40:43], v[176:179]\n"
"ds_read_b128 v[80:83], v1 offset:0\n"
"v_mfma_f32_16x16x32_bf16 v[176:179], v[84:87], a[44:47], v[176:179]\n"
"buffer_load_dwordx4 v1, s[36:39], s8 offen offset:3072 sc1 lds\n"
"v_mfma_f32_16x16x32_bf16 v[60:63], v[88:91], a[32:35], v[60:63]\n"
"s_add_u32 m0, s7, 45056\n"
"ds_read_b128 v[84:87], v1 offset:10240\n"
"v_mfma_f32_16x16x32_bf16 v[60:63], v[92:95], a[36:39], v[60:63]\n"
"s_waitcnt lgkmcnt(4)\n"
"v_mfma_f32_16x16x32_bf16 v[180:183], v[88:91], a[40:43], v[180:183]\n"
"buffer_load_dwordx2 v[24:25], v6, s[68:71], s67 offen offset:128\n"
"v_mfma_f32_16x16x32_bf16 v[180:183], v[92:95], a[44:47], v[180:183]\n"
"ds_read_b128 v[88:91], v1 offset:1024\n"
"v_mfma_f32_16x16x32_bf16 v[64:67], v[96:99], a[32:35], v[64:67]\n"
"ds_read_b128 v[92:95], v1 offset:11264\n"
"v_mfma_f32_16x16x32_bf16 v[64:67], v[100:103], a[36:39], v[64:67]\n"
"s_waitcnt lgkmcnt(4)\n"
"v_mfma_f32_16x16x32_bf16 v[184:187], v[96:99], a[40:43], v[184:187]\n"
"ds_read_b128 v[96:99], v1 offset:2048\n"
"v_mfma_f32_16x16x32_bf16 v[184:187], v[100:103], a[44:47], v[184:187]\n"
"buffer_load_dwordx4 v4, s[36:39], s8 offen sc1 lds\n"
"v_mfma_f32_16x16x32_bf16 v[68:71], v[104:107], a[32:35], v[68:71]\n"
"buffer_load_dwordx4 v[200:203], v1, s[36:39], s17 offen offset:0 sc1\n"
"v_mfma_f32_16x16x32_bf16 v[68:71], v[108:111], a[36:39], v[68:71]\n"
"s_add_u32 s8, s8, 491520\n"
"s_waitcnt lgkmcnt(3)\n"
"v_mfma_f32_16x16x32_bf16 v[188:191], v[104:107], a[40:43], v[188:191]\n"
"ds_read_b128 v[100:103], v1 offset:12288\n"
"v_mfma_f32_16x16x32_bf16 v[188:191], v[108:111], a[44:47], v[188:191]\n"
"ds_read_b128 v[104:107], v1 offset:3072\n"
"v_mfma_f32_16x16x32_bf16 v[32:35], v[80:83], a[48:51], v[32:35]\n"
"ds_read_b128 v[108:111], v1 offset:13312\n"
"v_mfma_f32_16x16x32_bf16 v[32:35], v[84:87], a[52:55], v[32:35]\n"
"s_waitcnt lgkmcnt(4)\n"
"v_mfma_f32_16x16x32_bf16 v[72:75], v[80:83], a[56:59], v[72:75]\n"
"ds_read_b128 v[80:83], v1 offset:4096\n"
"v_mfma_f32_16x16x32_bf16 v[72:75], v[84:87], a[60:63], v[72:75]\n"
"buffer_load_dwordx4 a[228:231], v12, s[36:39], s11 offen sc1\n"
"v_mfma_f32_16x16x32_bf16 v[36:39], v[88:91], a[48:51], v[36:39]\n"
"s_add_u32 m0, s7, 61440\n"
"ds_read_b128 v[84:87], v1 offset:14336\n"
"v_mfma_f32_16x16x32_bf16 v[36:39], v[92:95], a[52:55], v[36:39]\n"
"s_waitcnt lgkmcnt(4)\n"
"v_mfma_f32_16x16x32_bf16 v[76:79], v[88:91], a[56:59], v[76:79]\n"
"ds_read_b128 v[88:91], v1 offset:5120\n"
"v_mfma_f32_16x16x32_bf16 v[76:79], v[92:95], a[60:63], v[76:79]\n"
"buffer_load_dwordx4 a[232:235], v11, s[36:39], s11 offen sc1\n"
"v_mfma_f32_16x16x32_bf16 v[40:43], v[96:99], a[48:51], v[40:43]\n"
"ds_read_b128 v[92:95], v1 offset:15360\n"
"v_mfma_f32_16x16x32_bf16 v[40:43], v[100:103], a[52:55], v[40:43]\n"
"s_waitcnt lgkmcnt(4)\n"
"v_mfma_f32_16x16x32_bf16 v[240:243], v[96:99], a[56:59], v[240:243]\n"
"buffer_load_dwordx4 v[204:207], v1, s[36:39], s17 offen offset:1024 sc1\n"
"v_mfma_f32_16x16x32_bf16 v[240:243], v[100:103], a[60:63], v[240:243]\n"
"ds_read_b128 v[96:99], v1 offset:6144\n"
"v_mfma_f32_16x16x32_bf16 v[44:47], v[104:107], a[48:51], v[44:47]\n"
"ds_read_b128 v[100:103], v1 offset:16384\n"
"v_mfma_f32_16x16x32_bf16 v[44:47], v[108:111], a[52:55], v[44:47]\n"
"s_waitcnt lgkmcnt(4)\n"
"v_mfma_f32_16x16x32_bf16 v[244:247], v[104:107], a[56:59], v[244:247]\n"
"ds_read_b128 v[104:107], v1 offset:7168\n"
"v_mfma_f32_16x16x32_bf16 v[244:247], v[108:111], a[60:63], v[244:247]\n"
"buffer_load_dwordx4 a[236:239], v13, s[36:39], s11 offen sc1\n"
"v_mfma_f32_16x16x32_bf16 v[48:51], v[80:83], a[48:51], v[48:51]\n"
"s_add_u32 s11, s11, 1572864\n"
"ds_read_b128 v[108:111], v1 offset:17408\n"
"v_mfma_f32_16x16x32_bf16 v[48:51], v[84:87], a[52:55], v[48:51]\n"
"s_waitcnt lgkmcnt(4)\n"
"v_mfma_f32_16x16x32_bf16 v[248:251], v[80:83], a[56:59], v[248:251]\n"
"ds_read_b128 v[80:83], v1 offset:8192\n"
"v_mfma_f32_16x16x32_bf16 v[248:251], v[84:87], a[60:63], v[248:251]\n"
"buffer_load_dwordx4 v1, s[36:39], s8 offen offset:0 sc1 lds\n"
"v_mfma_f32_16x16x32_bf16 v[52:55], v[88:91], a[48:51], v[52:55]\n"
"s_barrier\n"
"v_mfma_f32_16x16x32_bf16 v[52:55], v[92:95], a[52:55], v[52:55]\n"
"s_waitcnt lgkmcnt(3)\n"
"ds_read_b128 v[84:87], v1 offset:18432\n"
"v_mfma_f32_16x16x32_bf16 v[252:255], v[88:91], a[56:59], v[252:255]\n"
"ds_read_b128 v[88:91], v1 offset:9216\n"
"v_mfma_f32_16x16x32_bf16 v[252:255], v[92:95], a[60:63], v[252:255]\n"
"s_waitcnt vmcnt(14)\n"
"v_cmp_gt_u32 s[92:93], 8, v7\n"
"s_nop 0\n"
"s_cmp_eq_u64 s[92:93], 0\n"
"s_cbranch_scc0 .Lspin5\n"
".Lback5:\n"
"v_mfma_f32_16x16x32_bf16 v[56:59], v[96:99], a[48:51], v[56:59]\n"
"buffer_load_dwordx2 v[208:209], v5, s[36:39], s17 offen offset:0 sc1\n"
"v_mfma_f32_16x16x32_bf16 v[56:59], v[100:103], a[52:55], v[56:59]\n"
"s_waitcnt lgkmcnt(3)\n"
"ds_read_b128 v[92:95], v1 offset:19456\n"
"v_mfma_f32_16x16x32_bf16 v[176:179], v[96:99], a[56:59], v[176:179]\n"
"ds_read_b128 v[96:99], v3 offset:0\n"
"s_add_u32 s17, s17, 5120\n"
"v_mfma_f32_16x16x32_bf16 v[176:179], v[100:103], a[60:63], v[176:179]\n"
"buffer_load_dwordx4 v1, s[36:39], s8 offen offset:1024 sc1 lds\n"
"v_mfma_f32_16x16x32_bf16 v[60:63], v[104:107], a[48:51], v[60:63]\n"
"ds_read_b128 v[100:103], v3 offset:10240\n"
"v_mfma_f32_16x16x32_bf16 v[60:63], v[108:111], a[52:55], v[60:63]\n"
"s_waitcnt lgkmcnt(4)\n"
"v_mfma_f32_16x16x32_bf16 v[180:183], v[104:107], a[56:59], v[180:183]\n"
"ds_read_b128 v[104:107], v3 offset:1024\n"
"v_mfma_f32_16x16x32_bf16 v[180:183], v[108:111], a[60:63], v[180:183]\n"
"buffer_load_dwordx4 v1, s[36:39], s8 offen offset:2048 sc1 lds\n"
"v_mfma_f32_16x16x32_bf16 v[64:67], v[80:83], a[48:51], v[64:67]\n"
"ds_read_b128 v[108:111], v3 offset:11264\n"
"v_mfma_f32_16x16x32_bf16 v[64:67], v[84:87], a[52:55], v[64:67]\n"
"s_waitcnt lgkmcnt(4)\n"
"v_mfma_f32_16x16x32_bf16 v[184:187], v[80:83], a[56:59], v[184:187]\n"
"ds_read_b128 v[80:83], v3 offset:2048\n"
"v_mfma_f32_16x16x32_bf16 v[184:187], v[84:87], a[60:63], v[184:187]\n"
"buffer_load_dwordx4 v1, s[36:39], s8 offen offset:3072 sc1 lds\n"
"v_mfma_f32_16x16x32_bf16 v[68:71], v[88:91], a[48:51], v[68:71]\n"
"s_add_u32 m0, s7, 65536\n"
"ds_read_b128 v[84:87], v3 offset:12288\n"
"v_mfma_f32_16x16x32_bf16 v[68:71], v[92:95], a[52:55], v[68:71]\n"
"s_waitcnt lgkmcnt(4)\n"
"v_mfma_f32_16x16x32_bf16 v[188:191], v[88:91], a[56:59], v[188:191]\n"
"buffer_load_dwordx4 v[220:223], v1, s[36:39], s17 offen offset:0 sc1\n"
"v_mfma_f32_16x16x32_bf16 v[188:191], v[92:95], a[60:63], v[188:191]\n"
"ds_read_b128 v[88:91], v3 offset:3072\n"
"v_mfma_f32_16x16x32_bf16 v[32:35], v[96:99], a[208:211], v[32:35]\n"
"ds_read_b128 v[92:95], v3 offset:13312\n"
"v_mfma_f32_16x16x32_bf16 v[32:35], v[100:103], a[212:215], v[32:35]\n"
"s_waitcnt lgkmcnt(4)\n"
"v_mfma_f32_16x16x32_bf16 v[72:75], v[96:99], a[216:219], v[72:75]\n"
"ds_read_b128 v[96:99], v3 offset:4096\n"
"v_mfma_f32_16x16x32_bf16 v[72:75], v[100:103], a[220:223], v[72:75]\n"
"buffer_load_dwordx4 v4, s[36:39], s8 offen sc1 lds\n"
"v_mfma_f32_16x16x32_bf16 v[36:39], v[104:107], a[208:211], v[36:39]\n"
"s_add_u32 s8, s8, 491520\n"
"s_add_u32 m0, s7, 81920\n"
"v_mfma_f32_16x16x32_bf16 v[36:39], v[108:111], a[212:215], v[36:39]\n"
"s_waitcnt lgkmcnt(3)\n"
"ds_read_b128 v[100:103], v3 offset:14336\n"
"v_mfma_f32_16x16x32_bf16 v[76:79], v[104:107], a[216:219], v[76:79]\n"
"ds_read_b128 v[104:107], v3 offset:5120\n"
"v_mfma_f32_16x16x32_bf16 v[76:79], v[108:111], a[220:223], v[76:79]\n"
"buffer_load_dwordx4 a[240:243], v10, s[36:39], s11 offen sc1\n"
"v_mfma_f32_16x16x32_bf16 v[40:43], v[80:83], a[208:211], v[40:43]\n"
"ds_read_b128 v[108:111], v3 offset:15360\n"
"v_mfma_f32_16x16x32_bf16 v[40:43], v[84:87], a[212:215], v[40:43]\n"
"s_waitcnt lgkmcnt(4)\n"
"v_mfma_f32_16x16x32_bf16 v[240:243], v[80:83], a[216:219], v[240:243]\n"
"ds_read_b128 v[80:83], v3 offset:6144\n"
"v_mfma_f32_16x16x32_bf16 v[240:243], v[84:87], a[220:223], v[240:243]\n"
"buffer_load_dwordx4 a[244:247], v12, s[36:39], s11 offen sc1\n"
"v_mfma_f32_16x16x32_bf16 v[44:47], v[88:91], a[208:211], v[44:47]\n"
"buffer_load_dwordx4 v[224:227], v1, s[36:39], s17 offen offset:1024 sc1\n"
"v_mfma_f32_16x16x32_bf16 v[44:47], v[92:95], a[212:215], v[44:47]\n"
"s_waitcnt lgkmcnt(3)\n"
"ds_read_b128 v[84:87], v3 offset:16384\n"
"v_mfma_f32_16x16x32_bf16 v[244:247], v[88:91], a[216:219], v[244:247]\n"
"ds_read_b128 v[88:91], v3 offset:7168\n"
"v_mfma_f32_16x16x32_bf16 v[244:247], v[92:95], a[220:223], v[244:247]\n"
"ds_read_b128 v[92:95], v3 offset:17408\n"
"v_mfma_f32_16x16x32_bf16 v[48:51], v[96:99], a[208:211], v[48:51]\n"
"v_mfma_f32_16x16x32_bf16 v[48:51], v[100:103], a[212:215], v[48:51]\n"
"s_waitcnt lgkmcnt(4)\n"
"v_mfma_f32_16x16x32_bf16 v[248:251], v[96:99], a[216:219], v[248:251]\n"
"ds_read_b128 v[96:99], v3 offset:8192\n"
"v_mfma_f32_16x16x32_bf16 v[248:251], v[100:103], a[220:223], v[248:251]\n"
"buffer_load_dwordx4 a[248:251], v11, s[36:39], s11 offen sc1\n"
"v_mfma_f32_16x16x32_bf16 v[52:55], v[104:107], a[208:211], v[52:55]\n"
"s_waitcnt vmcnt(11)\n"
"s_barrier\n"
"v_mfma_f32_16x16x32_bf16 v[52:55], v[108:111], a[212:215], v[52:55]\n"
"s_waitcnt lgkmcnt(3)\n"
"ds_read_b128 v[100:103], v3 offset:18432\n"
"v_mfma_f32_16x16x32_bf16 v[252:255], v[104:107], a[216:219], v[252:255]\n"
"ds_read_b128 v[104:107], v3 offset:9216\n"
"v_mfma_f32_16x16x32_bf16 v[252:255], v[108:111], a[220:223], v[252:255]\n"
"buffer_load_dwordx4 a[252:255], v13, s[36:39], s11 offen sc1\n"
"s_sub_u32 s11, s11, 4587520\n"
"v_mfma_f32_16x16x32_bf16 v[56:59], v[80:83], a[208:211], v[56:59]\n"
"ds_read_b128 v[108:111], v3 offset:19456\n"
"v_mfma_f32_16x16x32_bf16 v[56:59], v[84:87], a[212:215], v[56:59]\n"
"s_waitcnt lgkmcnt(4)\n"
"v_mfma_f32_16x16x32_bf16 v[176:179], v[80:83], a[216:219], v[176:179]\n"
"buffer_load_dwordx2 v[228:229], v5, s[36:39], s17 offen offset:0 sc1\n"
"v_mfma_f32_16x16x32_bf16 v[176:179], v[84:87], a[220:223], v[176:179]\n"
"ds_read_b128 v[80:83], v3 offset:20480\n"
"s_add_u32 s17, s17, 20956160\n"
"v_mfma_f32_16x16x32_bf16 v[60:63], v[88:91], a[208:211], v[60:63]\n"
"ds_read_b128 v[84:87], v3 offset:30720\n"
"v_mfma_f32_16x16x32_bf16 v[60:63], v[92:95], a[212:215], v[60:63]\n"
"s_waitcnt lgkmcnt(4)\n"
"v_mfma_f32_16x16x32_bf16 v[180:183], v[88:91], a[216:219], v[180:183]\n"
"ds_read_b128 v[88:91], v3 offset:21504\n"
"v_mfma_f32_16x16x32_bf16 v[180:183], v[92:95], a[220:223], v[180:183]\n"
"buffer_load_dwordx4 v1, s[36:39], s8 offen offset:0 sc1 lds\n"
"v_mfma_f32_16x16x32_bf16 v[64:67], v[96:99], a[208:211], v[64:67]\n"
"ds_read_b128 v[92:95], v3 offset:31744\n"
"v_mfma_f32_16x16x32_bf16 v[64:67], v[100:103], a[212:215], v[64:67]\n"
"s_waitcnt lgkmcnt(4)\n"
"v_mfma_f32_16x16x32_bf16 v[184:187], v[96:99], a[216:219], v[184:187]\n"
"ds_read_b128 v[96:99], v3 offset:22528\n"
"v_mfma_f32_16x16x32_bf16 v[184:187], v[100:103], a[220:223], v[184:187]\n"
"buffer_load_dwordx4 v1, s[36:39], s8 offen offset:1024 sc1 lds\n"
"v_mfma_f32_16x16x32_bf16 v[68:71], v[104:107], a[208:211], v[68:71]\n"
"ds_read_b128 v[100:103], v3 offset:32768\n"
"v_mfma_f32_16x16x32_bf16 v[68:71], v[108:111], a[212:215], v[68:71]\n"
"s_waitcnt lgkmcnt(4)\n"
"v_mfma_f32_16x16x32_bf16 v[188:191], v[104:107], a[216:219], v[188:191]\n"
"ds_read_b128 v[104:107], v3 offset:23552\n"
"v_mfma_f32_16x16x32_bf16 v[188:191], v[108:111], a[220:223], v[188:191]\n"
"buffer_load_dwordx4 v1, s[36:39], s8 offen offset:2048 sc1 lds\n"
"v_mfma_f32_16x16x32_bf16 v[32:35], v[80:83], a[224:227], v[32:35]\n"
"ds_read_b128 v[108:111], v3 offset:33792\n"
"v_mfma_f32_16x16x32_bf16 v[32:35], v[84:87], a[228:231], v[32:35]\n"
"s_waitcnt lgkmcnt(4)\n"
"v_mfma_f32_16x16x32_bf16 v[72:75], v[80:83], a[232:235], v[72:75]\n"
"ds_read_b128 v[80:83], v3 offset:24576\n"
"v_mfma_f32_16x16x32_bf16 v[72:75], v[84:87], a[236:239], v[72:75]\n"
"buffer_load_dwordx4 v1, s[36:39], s8 offen offset:3072 sc1 lds\n"
"v_mfma_f32_16x16x32_bf16 v[36:39], v[88:91], a[224:227], v[36:39]\n"
"s_add_u32 m0, s7, 86016\n"
"ds_read_b128 v[84:87], v3 offset:34816\n"
"v_mfma_f32_16x16x32_bf16 v[36:39], v[92:95], a[228:231], v[36:39]\n"
"s_waitcnt lgkmcnt(4)\n"
"v_mfma_f32_16x16x32_bf16 v[76:79], v[88:91], a[232:235], v[76:79]\n"
"ds_read_b128 v[88:91], v3 offset:25600\n"
"v_mfma_f32_16x16x32_bf16 v[76:79], v[92:95], a[236:239], v[76:79]\n"
"buffer_load_dwordx4 v4, s[36:39], s8 offen sc1 lds\n"
"v_mfma_f32_16x16x32_bf16 v[40:43], v[96:99], a[224:227], v[40:43]\n"
"s_add_u32 s8, s8, 491520\n"
"s_add_u32 m0, s7, 102400\n"
"v_mfma_f32_16x16x32_bf16 v[40:43], v[100:103], a[228:231], v[40:43]\n"
"s_waitcnt lgkmcnt(3)\n"
"ds_read_b128 v[92:95], v3 offset:35840\n"
"v_mfma_f32_16x16x32_bf16 v[240:243], v[96:99], a[232:235], v[240:243]\n"
"ds_read_b128 v[96:99], v3 offset:26624\n"
"v_mfma_f32_16x16x32_bf16 v[240:243], v[100:103], a[236:239], v[240:243]\n"
"buffer_load_dwordx4 a[192:195], v10, s[36:39], s11 offen sc1\n"
"v_mfma_f32_16x16x32_bf16 v[44:47], v[104:107], a[224:227], v[44:47]\n"
"ds_read_b128 v[100:103], v3 offset:36864\n"
"v_mfma_f32_16x16x32_bf16 v[44:47], v[108:111], a[228:231], v[44:47]\n"
"s_waitcnt lgkmcnt(4)\n"
"v_mfma_f32_16x16x32_bf16 v[244:247], v[104:107], a[232:235], v[244:247]\n"
"ds_read_b128 v[104:107], v3 offset:27648\n"
"v_mfma_f32_16x16x32_bf16 v[244:247], v[108:111], a[236:239], v[244:247]\n"
"buffer_load_dwordx4 a[196:199], v12, s[36:39], s11 offen sc1\n"
"v_mfma_f32_16x16x32_bf16 v[48:51], v[80:83], a[224:227], v[48:51]\n"
"ds_read_b128 v[108:111], v3 offset:37888\n"
"v_mfma_f32_16x16x32_bf16 v[48:51], v[84:87], a[228:231], v[48:51]\n"
"s_waitcnt lgkmcnt(4)\n"
"v_mfma_f32_16x16x32_bf16 v[248:251], v[80:83], a[232:235], v[248:251]\n"
"ds_read_b128 v[80:83], v3 offset:28672\n"
"v_mfma_f32_16x16x32_bf16 v[248:251], v[84:87], a[236:239], v[248:251]\n"
"buffer_load_dwordx4 a[200:203], v11, s[36:39], s11 offen sc1\n"
"v_mfma_f32_16x16x32_bf16 v[52:55], v[88:91], a[224:227], v[52:55]\n"
"s_waitcnt vmcnt(9)\n"
"s_barrier\n"
"v_mfma_f32_16x16x32_bf16 v[52:55], v[92:95], a[228:231], v[52:55]\n"
"s_waitcnt lgkmcnt(3)\n"
"ds_read_b128 v[84:87], v3 offset:38912\n"
"v_mfma_f32_16x16x32_bf16 v[252:255], v[88:91], a[232:235], v[252:255]\n"
"ds_read_b128 v[88:91], v3 offset:29696\n"
"v_mfma_f32_16x16x32_bf16 v[252:255], v[92:95], a[236:239], v[252:255]\n"
"buffer_load_dwordx4 a[204:207], v13, s[36:39], s11 offen sc1\n"
"s_add_u32 s11, s11, 1572864\n"
"v_mfma_f32_16x16x32_bf16 v[56:59], v[96:99], a[224:227], v[56:59]\n"
"ds_read_b128 v[92:95], v3 offset:39936\n"
"v_mfma_f32_16x16x32_bf16 v[56:59], v[100:103], a[228:231], v[56:59]\n"
"s_waitcnt lgkmcnt(4)\n"
"v_mfma_f32_16x16x32_bf16 v[176:179], v[96:99], a[232:235], v[176:179]\n"
"ds_read_b128 v[96:99], v1 offset:20480\n"
"v_mfma_f32_16x16x32_bf16 v[176:179], v[100:103], a[236:239], v[176:179]\n"
"buffer_load_dwordx4 v1, s[36:39], s8 offen offset:0 sc1 lds\n"
"v_mfma_f32_16x16x32_bf16 v[60:63], v[104:107], a[224:227], v[60:63]\n"
"ds_read_b128 v[100:103], v1 offset:30720\n"
"v_mfma_f32_16x16x32_bf16 v[60:63], v[108:111], a[228:231], v[60:63]\n"
"s_waitcnt lgkmcnt(4)\n"
"v_mfma_f32_16x16x32_bf16 v[180:183], v[104:107], a[232:235], v[180:183]\n"
"ds_read_b128 v[104:107], v1 offset:21504\n"
"v_mfma_f32_16x16x32_bf16 v[180:183], v[108:111], a[236:239], v[180:183]\n"
"buffer_load_dwordx4 v1, s[36:39], s8 offen offset:1024 sc1 lds\n"
"v_mfma_f32_16x16x32_bf16 v[64:67], v[80:83], a[224:227], v[64:67]\n"
"ds_read_b128 v[108:111], v1 offset:31744\n"
"v_mfma_f32_16x16x32_bf16 v[64:67], v[84:87], a[228:231], v[64:67]\n"
"s_waitcnt lgkmcnt(4)\n"
"v_mfma_f32_16x16x32_bf16 v[184:187], v[80:83], a[232:235], v[184:187]\n"
"ds_read_b128 v[80:83], v1 offset:22528\n"
"v_mfma_f32_16x16x32_bf16 v[184:187], v[84:87], a[236:239], v[184:187]\n"
"buffer_load_dwordx4 v1, s[36:39], s8 offen offset:2048 sc1 lds\n"
"v_mfma_f32_16x16x32_bf16 v[68:71], v[88:91], a[224:227], v[68:71]\n"
"ds_read_b128 v[84:87], v1 offset:32768\n"
"v_mfma_f32_16x16x32_bf16 v[68:71], v[92:95], a[228:231], v[68:71]\n"
"s_waitcnt lgkmcnt(4)\n"
"v_mfma_f32_16x16x32_bf16 v[188:191], v[88:91], a[232:235], v[188:191]\n"
"ds_read_b128 v[88:91], v1 offset:23552\n"
"v_mfma_f32_16x16x32_bf16 v[188:191], v[92:95], a[236:239], v[188:191]\n"
"buffer_load_dwordx4 v1, s[36:39], s8 offen offset:3072 sc1 lds\n"
"v_mfma_f32_16x16x32_bf16 v[32:35], v[96:99], a[240:243], v[32:35]\n"
"s_add_u32 m0, s7, 106496\n"
"ds_read_b128 v[92:95], v1 offset:33792\n"
"v_mfma_f32_16x16x32_bf16 v[32:35], v[100:103], a[244:247], v[32:35]\n"
"s_waitcnt lgkmcnt(4)\n"
"v_mfma_f32_16x16x32_bf16 v[72:75], v[96:99], a[248:251], v[72:75]\n"
"ds_read_b128 v[96:99], v1 offset:24576\n"
"v_mfma_f32_16x16x32_bf16 v[72:75], v[100:103], a[252:255], v[72:75]\n"
"buffer_load_dwordx4 v4, s[36:39], s8 offen sc1 lds\n"
"v_mfma_f32_16x16x32_bf16 v[36:39], v[104:107], a[240:243], v[36:39]\n"
"s_sub_u32 s8, s8, 1433600\n"
"s_add_u32 m0, s7, 0\n"
"v_mfma_f32_16x16x32_bf16 v[36:39], v[108:111], a[244:247], v[36:39]\n"
"s_waitcnt lgkmcnt(3)\n"
"ds_read_b128 v[100:103], v1 offset:34816\n"
"v_mfma_f32_16x16x32_bf16 v[76:79], v[104:107], a[248:251], v[76:79]\n"
"ds_read_b128 v[104:107], v1 offset:25600\n"
"v_mfma_f32_16x16x32_bf16 v[76:79], v[108:111], a[252:255], v[76:79]\n"
"buffer_load_dwordx4 a[0:3], v10, s[36:39], s11 offen sc1\n"
"v_mfma_f32_16x16x32_bf16 v[40:43], v[80:83], a[240:243], v[40:43]\n"
"ds_read_b128 v[108:111], v1 offset:35840\n"
"v_mfma_f32_16x16x32_bf16 v[40:43], v[84:87], a[244:247], v[40:43]\n"
"s_waitcnt lgkmcnt(4)\n"
"v_mfma_f32_16x16x32_bf16 v[240:243], v[80:83], a[248:251], v[240:243]\n"
"ds_read_b128 v[80:83], v1 offset:26624\n"
"v_mfma_f32_16x16x32_bf16 v[240:243], v[84:87], a[252:255], v[240:243]\n"
"buffer_load_dwordx4 a[4:7], v12, s[36:39], s11 offen sc1\n"
"v_mfma_f32_16x16x32_bf16 v[44:47], v[88:91], a[240:243], v[44:47]\n"
"ds_read_b128 v[84:87], v1 offset:36864\n"
"v_mfma_f32_16x16x32_bf16 v[44:47], v[92:95], a[244:247], v[44:47]\n"
"s_waitcnt lgkmcnt(4)\n"
"v_mfma_f32_16x16x32_bf16 v[244:247], v[88:91], a[248:251], v[244:247]\n"
"ds_read_b128 v[88:91], v1 offset:27648\n"
"v_mfma_f32_16x16x32_bf16 v[244:247], v[92:95], a[252:255], v[244:247]\n"
"buffer_load_dwordx4 a[8:11], v11, s[36:39], s11 offen sc1\n"
"v_mfma_f32_16x16x32_bf16 v[48:51], v[96:99], a[240:243], v[48:51]\n"
"ds_read_b128 v[92:95], v1 offset:37888\n"
"v_mfma_f32_16x16x32_bf16 v[48:51], v[100:103], a[244:247], v[48:51]\n"
"s_waitcnt lgkmcnt(4)\n"
"v_mfma_f32_16x16x32_bf16 v[248:251], v[96:99], a[248:251], v[248:251]\n"
"ds_read_b128 v[96:99], v1 offset:28672\n"
"v_mfma_f32_16x16x32_bf16 v[248:251], v[100:103], a[252:255], v[248:251]\n"
"buffer_load_dwordx4 a[12:15], v13, s[36:39], s11 offen sc1\n"
"v_mfma_f32_16x16x32_bf16 v[52:55], v[104:107], a[240:243], v[52:55]\n"
"s_barrier\n"
"s_add_u32 s11, s11, 1572864\n"
"v_mfma_f32_16x16x32_bf16 v[52:55], v[108:111], a[244:247], v[52:55]\n"
"s_waitcnt lgkmcnt(3)\n"
"ds_read_b128 v[100:103], v1 offset:38912\n"
"v_mfma_f32_16x16x32_bf16 v[252:255], v[104:107], a[248:251], v[252:255]\n"
"ds_read_b128 v[104:107], v1 offset:29696\n"
"v_mfma_f32_16x16x32_bf16 v[252:255], v[108:111], a[252:255], v[252:255]\n"
"buffer_load_dwordx4 v1, s[36:39], s8 offen offset:0 sc1 lds\n"
"v_mfma_f32_16x16x32_bf16 v[56:59], v[80:83], a[240:243], v[56:59]\n"
"ds_read_b128 v[108:111], v1 offset:39936\n"
"v_mfma_f32_16x16x32_bf16 v[56:59], v[84:87], a[244:247], v[56:59]\n"
"s_waitcnt lgkmcnt(4)\n"
"v_mfma_f32_16x16x32_bf16 v[176:179], v[80:83], a[248:251], v[176:179]\n"
"ds_read_b128 v[80:83], v1 offset:40960\n"
"v_mfma_f32_16x16x32_bf16 v[176:179], v[84:87], a[252:255], v[176:179]\n"
"buffer_load_dwordx4 v1, s[36:39], s8 offen offset:1024 sc1 lds\n"
"v_mfma_f32_16x16x32_bf16 v[60:63], v[88:91], a[240:243], v[60:63]\n"
"ds_read_b128 v[84:87], v1 offset:51200\n"
"v_mfma_f32_16x16x32_bf16 v[60:63], v[92:95], a[244:247], v[60:63]\n"
"s_waitcnt lgkmcnt(4)\n"
"v_mfma_f32_16x16x32_bf16 v[180:183], v[88:91], a[248:251], v[180:183]\n"
"s_waitcnt vmcnt(11)\n"
"ds_read_b128 v[88:91], v1 offset:41984\n"
"v_mfma_f32_16x16x32_bf16 v[180:183], v[92:95], a[252:255], v[180:183]\n"
"buffer_load_dwordx4 v1, s[36:39], s8 offen offset:2048 sc1 lds\n"
"v_mfma_f32_16x16x32_bf16 v[64:67], v[96:99], a[240:243], v[64:67]\n"
"ds_read_b128 v[92:95], v1 offset:52224\n"
"v_lshlrev_b32 v210, 16, v132\n"
"v_mfma_f32_16x16x32_bf16 v[64:67], v[100:103], a[244:247], v[64:67]\n"
"s_waitcnt lgkmcnt(4)\n"
"v_and_b32 v211, 0xffff0000, v132\n"
"v_lshlrev_b32 v212, 16, v133\n"
"v_mfma_f32_16x16x32_bf16 v[184:187], v[96:99], a[248:251], v[184:187]\n"
"ds_read_b128 v[96:99], v1 offset:43008\n"
"v_and_b32 v213, 0xffff0000, v133\n"
"v_mfma_f32_16x16x32_bf16 v[184:187], v[100:103], a[252:255], v[184:187]\n"
"buffer_load_dwordx4 v1, s[36:39], s8 offen offset:3072 sc1 lds\n"
"v_mfma_f32_16x16x32_bf16 v[68:71], v[104:107], a[240:243], v[68:71]\n"
"s_add_u32 m0, s7, 4096\n"
"ds_read_b128 v[100:103], v1 offset:53248\n"
"v_mfma_f32_16x16x32_bf16 v[68:71], v[108:111], a[244:247], v[68:71]\n"
"s_waitcnt lgkmcnt(4)\n"
"v_lshlrev_b32 v214, 16, v134\n"
"v_and_b32 v215, 0xffff0000, v134\n"
"v_mfma_f32_16x16x32_bf16 v[188:191], v[104:107], a[248:251], v[188:191]\n"
"ds_read_b128 v[104:107], v1 offset:44032\n"
"v_lshlrev_b32 v216, 16, v135\n"
"v_mfma_f32_16x16x32_bf16 v[188:191], v[108:111], a[252:255], v[188:191]\n"
"buffer_load_dwordx4 v4, s[36:39], s8 offen sc1 lds\n"
"v_mfma_f32_16x16x32_bf16 v[32:35], v[80:83], a[192:195], v[32:35]\n"
"s_add_u32 s8, s8, 491520\n"
"ds_read_b128 v[108:111], v1 offset:54272\n"
"v_mfma_f32_16x16x32_bf16 v[32:35], v[84:87], a[196:199], v[32:35]\n"
"s_waitcnt lgkmcnt(4)\n"
"v_and_b32 v217, 0xffff0000, v135\n"
"v_lshlrev_b32 v218, 16, v136\n"
"v_mfma_f32_16x16x32_bf16 v[72:75], v[80:83], a[200:203], v[72:75]\n"
"ds_read_b128 v[80:83], v1 offset:45056\n"
"v_and_b32 v219, 0xffff0000, v136\n"
"v_mfma_f32_16x16x32_bf16 v[72:75], v[84:87], a[204:207], v[72:75]\n"
"buffer_load_dwordx4 a[16:19], v10, s[36:39], s11 offen sc1\n"
"v_mfma_f32_16x16x32_bf16 v[36:39], v[88:91], a[192:195], v[36:39]\n"
"s_add_u32 m0, s7, 122880\n"
"ds_read_b128 v[84:87], v1 offset:55296\n"
"v_mfma_f32_16x16x32_bf16 v[36:39], v[92:95], a[196:199], v[36:39]\n"
"s_waitcnt lgkmcnt(4)\n"
"v_lshlrev_b32 v230, 16, v137\n"
"v_and_b32 v231, 0xffff0000, v137\n"
"v_mfma_f32_16x16x32_bf16 v[76:79], v[88:91], a[200:203], v[76:79]\n"
"ds_read_b128 v[88:91], v1 offset:46080\n"
"v_lshlrev_b32 v232, 16, v138\n"
"v_mfma_f32_16x16x32_bf16 v[76:79], v[92:95], a[204:207], v[76:79]\n"
"buffer_load_dwordx4 a[20:23], v12, s[36:39], s11 offen sc1\n"
"v_mfma_f32_16x16x32_bf16 v[40:43], v[96:99], a[192:195], v[40:43]\n"
"ds_read_b128 v[92:95], v1 offset:56320\n"
"v_and_b32 v233, 0xffff0000, v138\n"
"v_mfma_f32_16x16x32_bf16 v[40:43], v[100:103], a[196:199], v[40:43]\n"
"s_waitcnt lgkmcnt(4)\n"
"v_lshlrev_b32 v234, 16, v139\n"
"v_and_b32 v235, 0xffff0000, v139\n"
"v_mfma_f32_16x16x32_bf16 v[240:243], v[96:99], a[200:203], v[240:243]\n"
"ds_read_b128 v[96:99], v1 offset:47104\n"
"v_lshlrev_b32 v236, 16, v140\n"
"v_mfma_f32_16x16x32_bf16 v[240:243], v[100:103], a[204:207], v[240:243]\n"
"buffer_load_dwordx4 a[24:27], v11, s[36:39], s11 offen sc1\n"
"v_mfma_f32_16x16x32_bf16 v[44:47], v[104:107], a[192:195], v[44:47]\n"
"ds_read_b128 v[100:103], v1 offset:57344\n"
"v_and_b32 v237, 0xffff0000, v140\n"
"v_mfma_f32_16x16x32_bf16 v[44:47], v[108:111], a[196:199], v[44:47]\n"
"s_waitcnt lgkmcnt(4)\n"
"v_lshlrev_b32 v238, 16, v141\n"
"v_and_b32 v239, 0xffff0000, v141\n"
"v_mfma_f32_16x16x32_bf16 v[244:247], v[104:107], a[200:203], v[244:247]\n"
"ds_read_b128 v[104:107], v1 offset:48128\n"
"v_add_f32 v112, v112, v210\n"
"v_mfma_f32_16x16x32_bf16 v[244:247], v[108:111], a[204:207], v[244:247]\n"
"buffer_load_dwordx4 a[28:31], v13, s[36:39], s11 offen sc1\n"
"v_mfma_f32_16x16x32_bf16 v[48:51], v[80:83], a[192:195], v[48:51]\n"
"s_add_u32 s11, s11, 1572864\n"
"ds_read_b128 v[108:111], v1 offset:58368\n"
"v_mfma_f32_16x16x32_bf16 v[48:51], v[84:87], a[196:199], v[48:51]\n"
"s_waitcnt lgkmcnt(4)\n"
"v_add_f32 v113, v113, v211\n"
"v_add_f32 v114, v114, v212\n"
"v_mfma_f32_16x16x32_bf16 v[248:251], v[80:83], a[200:203], v[248:251]\n"
"ds_read_b128 v[80:83], v1 offset:49152\n"
"v_add_f32 v115, v115, v213\n"
"v_mfma_f32_16x16x32_bf16 v[248:251], v[84:87], a[204:207], v[248:251]\n"
"buffer_load_dwordx4 v1, s[36:39], s8 offen offset:0 sc1 lds\n"
"v_mfma_f32_16x16x32_bf16 v[52:55], v[88:91], a[192:195], v[52:55]\n"
"s_waitcnt vmcnt(10)\n"
"s_barrier\n"
"v_add_f32 v116, v116, v214\n"
"v_mfma_f32_16x16x32_bf16 v[52:55], v[92:95], a[196:199], v[52:55]\n"
"s_waitcnt lgkmcnt(3)\n"
"ds_read_b128 v[84:87], v1 offset:59392\n"
"v_mfma_f32_16x16x32_bf16 v[252:255], v[88:91], a[200:203], v[252:255]\n"
"ds_read_b128 v[88:91], v1 offset:50176\n"
"v_add_f32 v117, v117, v215\n"
"v_mfma_f32_16x16x32_bf16 v[252:255], v[92:95], a[204:207], v[252:255]\n"
"buffer_load_dwordx4 v1, s[36:39], s8 offen offset:1024 sc1 lds\n"
"v_mfma_f32_16x16x32_bf16 v[56:59], v[96:99], a[192:195], v[56:59]\n"
"ds_read_b128 v[92:95], v1 offset:60416\n"
"v_add_f32 v118, v118, v216\n"
"v_mfma_f32_16x16x32_bf16 v[56:59], v[100:103], a[196:199], v[56:59]\n"
"s_waitcnt lgkmcnt(4)\n"
"v_add_f32 v119, v119, v217\n"
"v_add_f32 v120, v120, v218\n"
"v_mfma_f32_16x16x32_bf16 v[176:179], v[96:99], a[200:203], v[176:179]\n"
"ds_read_b128 v[96:99], v2 offset:0\n"
"v_add_f32 v121, v121, v219\n"
"v_mfma_f32_16x16x32_bf16 v[176:179], v[100:103], a[204:207], v[176:179]\n"
"buffer_load_dwordx4 v1, s[36:39], s8 offen offset:2048 sc1 lds\n"
"v_mfma_f32_16x16x32_bf16 v[60:63], v[104:107], a[192:195], v[60:63]\n"
"ds_read_b128 v[100:103], v2 offset:10240\n"
"v_add_f32 v122, v122, v230\n"
"v_mfma_f32_16x16x32_bf16 v[60:63], v[108:111], a[196:199], v[60:63]\n"
"s_waitcnt lgkmcnt(4)\n"
"v_add_f32 v123, v123, v231\n"
"v_add_f32 v124, v124, v232\n"
"v_mfma_f32_16x16x32_bf16 v[180:183], v[104:107], a[200:203], v[180:183]\n"
"ds_read_b128 v[104:107], v2 offset:1024\n"
"v_add_f32 v125, v125, v233\n"
"v_mfma_f32_16x16x32_bf16 v[180:183], v[108:111], a[204:207], v[180:183]\n"
"buffer_load_dwordx4 v1, s[36:39], s8 offen offset:3072 sc1 lds\n"
"v_mfma_f32_16x16x32_bf16 v[64:67], v[80:83], a[192:195], v[64:67]\n"
"s_add_u32 m0, s7, 126976\n"
"ds_read_b128 v[108:111], v2 offset:11264\n"
"v_mfma_f32_16x16x32_bf16 v[64:67], v[84:87], a[196:199], v[64:67]\n"
"s_waitcnt lgkmcnt(4)\n"
"v_add_f32 v126, v126, v234\n"
"v_add_f32 v127, v127, v235\n"
"v_mfma_f32_16x16x32_bf16 v[184:187], v[80:83], a[200:203], v[184:187]\n"
"ds_read_b128 v[80:83], v2 offset:2048\n"
"v_add_f32 v128, v128, v236\n"
"v_mfma_f32_16x16x32_bf16 v[184:187], v[84:87], a[204:207], v[184:187]\n"
"buffer_load_dwordx4 v4, s[36:39], s8 offen sc1 lds\n"
"v_mfma_f32_16x16x32_bf16 v[68:71], v[88:91], a[192:195], v[68:71]\n"
"s_add_u32 s8, s8, 491520\n"
"ds_read_b128 v[84:87], v2 offset:12288\n"
"v_mfma_f32_16x16x32_bf16 v[68:71], v[92:95], a[196:199], v[68:71]\n"
"s_waitcnt lgkmcnt(4)\n"
"v_add_f32 v129, v129, v237\n"
"v_add_f32 v130, v130, v238\n"
"v_mfma_f32_16x16x32_bf16 v[188:191], v[88:91], a[200:203], v[188:191]\n"
"ds_read_b128 v[88:91], v2 offset:3072\n"
"v_add_f32 v131, v131, v239\n"
"v_mfma_f32_16x16x32_bf16 v[188:191], v[92:95], a[204:207], v[188:191]\n"
"buffer_load_dwordx4 a[32:35], v10, s[36:39], s11 offen sc1\n"
"v_mfma_f32_16x16x32_bf16 v[32:35], v[96:99], a[0:3], v[32:35]\n"
"ds_read_b128 v[92:95], v2 offset:13312\n"
"v_lshlrev_b32 v210, 16, v200\n"
"v_mfma_f32_16x16x32_bf16 v[32:35], v[100:103], a[4:7], v[32:35]\n"
"s_waitcnt lgkmcnt(4)\n"
"v_and_b32 v211, 0xffff0000, v200\n"
"v_lshlrev_b32 v212, 16, v201\n"
"v_mfma_f32_16x16x32_bf16 v[72:75], v[96:99], a[8:11], v[72:75]\n"
"ds_read_b128 v[96:99], v2 offset:4096\n"
"v_and_b32 v213, 0xffff0000, v201\n"
"v_mfma_f32_16x16x32_bf16 v[72:75], v[100:103], a[12:15], v[72:75]\n"
"buffer_load_dwordx4 a[36:39], v12, s[36:39], s11 offen sc1\n"
"v_mfma_f32_16x16x32_bf16 v[36:39], v[104:107], a[0:3], v[36:39]\n"
"s_add_u32 m0, s7, 143360\n"
"ds_read_b128 v[100:103], v2 offset:14336\n"
"v_mfma_f32_16x16x32_bf16 v[36:39], v[108:111], a[4:7], v[36:39]\n"
"s_waitcnt lgkmcnt(4)\n"
"v_lshlrev_b32 v214, 16, v202\n"
"v_and_b32 v215, 0xffff0000, v202\n"
"v_mfma_f32_16x16x32_bf16 v[76:79], v[104:107], a[8:11], v[76:79]\n"
"ds_read_b128 v[104:107], v2 offset:5120\n"
"v_lshlrev_b32 v216, 16, v203\n"
"v_mfma_f32_16x16x32_bf16 v[76:79], v[108:111], a[12:15], v[76:79]\n"
"buffer_load_dwordx4 a[40:43], v11, s[36:39], s11 offen sc1\n"
"v_mfma_f32_16x16x32_bf16 v[40:43], v[80:83], a[0:3], v[40:43]\n"
"ds_read_b128 v[108:111], v2 offset:15360\n"
"v_and_b32 v217, 0xffff0000, v203\n"
"v_mfma_f32_16x16x32_bf16 v[40:43], v[84:87], a[4:7], v[40:43]\n"
"s_waitcnt lgkmcnt(4)\n"
"v_lshlrev_b32 v218, 16, v204\n"
"v_and_b32 v219, 0xffff0000, v204\n"
"v_mfma_f32_16x16x32_bf16 v[240:243], v[80:83], a[8:11], v[240:243]\n"
"ds_read_b128 v[80:83], v2 offset:6144\n"
"v_lshlrev_b32 v230, 16, v205\n"
"v_mfma_f32_16x16x32_bf16 v[240:243], v[84:87], a[12:15], v[240:243]\n"
"buffer_load_dwordx4 a[44:47], v13, s[36:39], s11 offen sc1\n"
"v_mfma_f32_16x16x32_bf16 v[44:47], v[88:91], a[0:3], v[44:47]\n"
"s_sub_u32 s11, s11, 4587520\n"
"ds_read_b128 v[84:87], v2 offset:16384\n"
"v_mfma_f32_16x16x32_bf16 v[44:47], v[92:95], a[4:7], v[44:47]\n"
"s_waitcnt lgkmcnt(4)\n"
"v_and_b32 v231, 0xffff0000, v205\n"
"v_lshlrev_b32 v232, 16, v206\n"
"v_mfma_f32_16x16x32_bf16 v[244:247], v[88:91], a[8:11], v[244:247]\n"
"ds_read_b128 v[88:91], v2 offset:7168\n"
"v_and_b32 v233, 0xffff0000, v206\n"
"v_mfma_f32_16x16x32_bf16 v[244:247], v[92:95], a[12:15], v[244:247]\n"
"buffer_load_dwordx4 v1, s[36:39], s8 offen offset:0 sc1 lds\n"
"v_mfma_f32_16x16x32_bf16 v[48:51], v[96:99], a[0:3], v[48:51]\n"
"ds_read_b128 v[92:95], v2 offset:17408\n"
"v_lshlrev_b32 v234, 16, v207\n"
"v_mfma_f32_16x16x32_bf16 v[48:51], v[100:103], a[4:7], v[48:51]\n"
"s_waitcnt lgkmcnt(4)\n"
"v_and_b32 v235, 0xffff0000, v207\n"
"v_lshlrev_b32 v236, 16, v208\n"
"v_mfma_f32_16x16x32_bf16 v[248:251], v[96:99], a[8:11], v[248:251]\n"
"ds_read_b128 v[96:99], v2 offset:8192\n"
"v_and_b32 v237, 0xffff0000, v208\n"
"v_mfma_f32_16x16x32_bf16 v[248:251], v[100:103], a[12:15], v[248:251]\n"
"buffer_load_dwordx4 v1, s[36:39], s8 offen offset:1024 sc1 lds\n"
"v_mfma_f32_16x16x32_bf16 v[52:55], v[104:107], a[0:3], v[52:55]\n"
"s_waitcnt vmcnt(11)\n"
"s_barrier\n"
"v_lshlrev_b32 v238, 16, v209\n"
"v_mfma_f32_16x16x32_bf16 v[52:55], v[108:111], a[4:7], v[52:55]\n"
"s_waitcnt lgkmcnt(3)\n"
"ds_read_b128 v[100:103], v2 offset:18432\n"
"v_mfma_f32_16x16x32_bf16 v[252:255], v[104:107], a[8:11], v[252:255]\n"
"ds_read_b128 v[104:107], v2 offset:9216\n"
"v_and_b32 v239, 0xffff0000, v209\n"
"v_mfma_f32_16x16x32_bf16 v[252:255], v[108:111], a[12:15], v[252:255]\n"
"buffer_load_dwordx4 v1, s[36:39], s8 offen offset:2048 sc1 lds\n"
"v_mfma_f32_16x16x32_bf16 v[56:59], v[80:83], a[0:3], v[56:59]\n"
"ds_read_b128 v[108:111], v2 offset:19456\n"
"v_add_f32 v112, v112, v210\n"
"v_mfma_f32_16x16x32_bf16 v[56:59], v[84:87], a[4:7], v[56:59]\n"
"s_waitcnt lgkmcnt(4)\n"
"v_add_f32 v113, v113, v211\n"
"v_add_f32 v114, v114, v212\n"
"v_mfma_f32_16x16x32_bf16 v[176:179], v[80:83], a[8:11], v[176:179]\n"
"ds_read_b128 v[80:83], v2 offset:20480\n"
"v_add_f32 v115, v115, v213\n"
"v_mfma_f32_16x16x32_bf16 v[176:179], v[84:87], a[12:15], v[176:179]\n"
"buffer_load_dwordx4 v1, s[36:39], s8 offen offset:3072 sc1 lds\n"
"v_mfma_f32_16x16x32_bf16 v[60:63], v[88:91], a[0:3], v[60:63]\n"
"s_add_u32 m0, s7, 147456\n"
"ds_read_b128 v[84:87], v2 offset:30720\n"
"v_mfma_f32_16x16x32_bf16 v[60:63], v[92:95], a[4:7], v[60:63]\n"
"s_waitcnt lgkmcnt(4)\n"
"v_add_f32 v116, v116, v214\n"
"v_add_f32 v117, v117, v215\n"
"v_mfma_f32_16x16x32_bf16 v[180:183], v[88:91], a[8:11], v[180:183]\n"
"ds_read_b128 v[88:91], v2 offset:21504\n"
"v_add_f32 v118, v118, v216\n"
"v_mfma_f32_16x16x32_bf16 v[180:183], v[92:95], a[12:15], v[180:183]\n"
"buffer_load_dwordx4 v4, s[36:39], s8 offen sc1 lds\n"
"v_mfma_f32_16x16x32_bf16 v[64:67], v[96:99], a[0:3], v[64:67]\n"
"s_add_u32 s8, s8, 491520\n"
"ds_read_b128 v[92:95], v2 offset:31744\n"
"v_mfma_f32_16x16x32_bf16 v[64:67], v[100:103], a[4:7], v[64:67]\n"
"s_waitcnt lgkmcnt(4)\n"
"v_add_f32 v119, v119, v217\n"
"v_add_f32 v120, v120, v218\n"
"v_mfma_f32_16x16x32_bf16 v[184:187], v[96:99], a[8:11], v[184:187]\n"
"ds_read_b128 v[96:99], v2 offset:22528\n"
"v_add_f32 v121, v121, v219\n"
"v_mfma_f32_16x16x32_bf16 v[184:187], v[100:103], a[12:15], v[184:187]\n"
"buffer_load_dwordx4 a[48:51], v10, s[36:39], s11 offen sc1\n"
"v_mfma_f32_16x16x32_bf16 v[68:71], v[104:107], a[0:3], v[68:71]\n"
"ds_read_b128 v[100:103], v2 offset:32768\n"
"v_add_f32 v122, v122, v230\n"
"v_mfma_f32_16x16x32_bf16 v[68:71], v[108:111], a[4:7], v[68:71]\n"
"s_waitcnt lgkmcnt(4)\n"
"v_add_f32 v123, v123, v231\n"
"v_add_f32 v124, v124, v232\n"
"v_mfma_f32_16x16x32_bf16 v[188:191], v[104:107], a[8:11], v[188:191]\n"
"ds_read_b128 v[104:107], v2 offset:23552\n"
"v_add_f32 v125, v125, v233\n"
"v_mfma_f32_16x16x32_bf16 v[188:191], v[108:111], a[12:15], v[188:191]\n"
"buffer_load_dwordx4 a[52:55], v12, s[36:39], s11 offen sc1\n"
"v_mfma_f32_16x16x32_bf16 v[32:35], v[80:83], a[16:19], v[32:35]\n"
"ds_read_b128 v[108:111], v2 offset:33792\n"
"v_add_f32 v126, v126, v234\n"
"v_mfma_f32_16x16x32_bf16 v[32:35], v[84:87], a[20:23], v[32:35]\n"
"s_waitcnt lgkmcnt(4)\n"
"v_add_f32 v127, v127, v235\n"
"v_add_f32 v128, v128, v236\n"
"v_mfma_f32_16x16x32_bf16 v[72:75], v[80:83], a[24:27], v[72:75]\n"
"ds_read_b128 v[80:83], v2 offset:24576\n"
"v_add_f32 v129, v129, v237\n"
"v_mfma_f32_16x16x32_bf16 v[72:75], v[84:87], a[28:31], v[72:75]\n"
"buffer_load_dwordx4 a[56:59], v11, s[36:39], s11 offen sc1\n"
"v_mfma_f32_16x16x32_bf16 v[36:39], v[88:91], a[16:19], v[36:39]\n"
"s_add_u32 m0, s7, 20480\n"
"ds_read_b128 v[84:87], v2 offset:34816\n"
"v_mfma_f32_16x16x32_bf16 v[36:39], v[92:95], a[20:23], v[36:39]\n"
"s_waitcnt lgkmcnt(4)\n"
"v_add_f32 v130, v130, v238\n"
"v_add_f32 v131, v131, v239\n"
"v_mfma_f32_16x16x32_bf16 v[76:79], v[88:91], a[24:27], v[76:79]\n"
"ds_read_b128 v[88:91], v2 offset:25600\n"
"v_lshlrev_b32 v210, 16, v220\n"
"v_mfma_f32_16x16x32_bf16 v[76:79], v[92:95], a[28:31], v[76:79]\n"
"buffer_load_dwordx4 a[60:63], v13, s[36:39], s11 offen sc1\n"
"v_mfma_f32_16x16x32_bf16 v[40:43], v[96:99], a[16:19], v[40:43]\n"
"s_add_u32 s11, s11, 1572864\n"
"ds_read_b128 v[92:95], v2 offset:35840\n"
"v_mfma_f32_16x16x32_bf16 v[40:43], v[100:103], a[20:23], v[40:43]\n"
"s_waitcnt lgkmcnt(4)\n"
"v_and_b32 v211, 0xffff0000, v220\n"
"v_lshlrev_b32 v212, 16, v221\n"
"v_mfma_f32_16x16x32_bf16 v[240:243], v[96:99], a[24:27], v[240:243]\n"
"ds_read_b128 v[96:99], v2 offset:26624\n"
"v_and_b32 v213, 0xffff0000, v221\n"
"v_mfma_f32_16x16x32_bf16 v[240:243], v[100:103], a[28:31], v[240:243]\n"
"buffer_load_dwordx4 v1, s[36:39], s8 offen offset:0 sc1 lds\n"
"v_mfma_f32_16x16x32_bf16 v[44:47], v[104:107], a[16:19], v[44:47]\n"
"ds_read_b128 v[100:103], v2 offset:36864\n"
"v_lshlrev_b32 v214, 16, v222\n"
"v_mfma_f32_16x16x32_bf16 v[44:47], v[108:111], a[20:23], v[44:47]\n"
"s_waitcnt lgkmcnt(4)\n"
"v_and_b32 v215, 0xffff0000, v222\n"
"v_lshlrev_b32 v216, 16, v223\n"
"v_mfma_f32_16x16x32_bf16 v[244:247], v[104:107], a[24:27], v[244:247]\n"
"ds_read_b128 v[104:107], v2 offset:27648\n"
"v_and_b32 v217, 0xffff0000, v223\n"
"v_mfma_f32_16x16x32_bf16 v[244:247], v[108:111], a[28:31], v[244:247]\n"
"buffer_load_dwordx4 v1, s[36:39], s8 offen offset:1024 sc1 lds\n"
"v_mfma_f32_16x16x32_bf16 v[48:51], v[80:83], a[16:19], v[48:51]\n"
"ds_read_b128 v[108:111], v2 offset:37888\n"
"v_lshlrev_b32 v218, 16, v224\n"
"v_mfma_f32_16x16x32_bf16 v[48:51], v[84:87], a[20:23], v[48:51]\n"
"s_waitcnt lgkmcnt(4)\n"
"v_and_b32 v219, 0xffff0000, v224\n"
"v_lshlrev_b32 v230, 16, v225\n"
"v_mfma_f32_16x16x32_bf16 v[248:251], v[80:83], a[24:27], v[248:251]\n"
"ds_read_b128 v[80:83], v2 offset:28672\n"
"v_and_b32 v231, 0xffff0000, v225\n"
"v_mfma_f32_16x16x32_bf16 v[248:251], v[84:87], a[28:31], v[248:251]\n"
"buffer_load_dwordx4 v1, s[36:39], s8 offen offset:2048 sc1 lds\n"
"v_mfma_f32_16x16x32_bf16 v[52:55], v[88:91], a[16:19], v[52:55]\n"
"s_waitcnt vmcnt(12)\n"
"s_barrier\n"
"v_lshlrev_b32 v232, 16, v226\n"
"v_mfma_f32_16x16x32_bf16 v[52:55], v[92:95], a[20:23], v[52:55]\n"
"s_waitcnt lgkmcnt(3)\n"
"ds_read_b128 v[84:87], v2 offset:38912\n"
"v_mfma_f32_16x16x32_bf16 v[252:255], v[88:91], a[24:27], v[252:255]\n"
"ds_read_b128 v[88:91], v2 offset:29696\n"
"v_and_b32 v233, 0xffff0000, v226\n"
"v_mfma_f32_16x16x32_bf16 v[252:255], v[92:95], a[28:31], v[252:255]\n"
"buffer_load_dwordx4 v1, s[36:39], s8 offen offset:3072 sc1 lds\n"
"v_mfma_f32_16x16x32_bf16 v[56:59], v[96:99], a[16:19], v[56:59]\n"
"s_add_u32 m0, s7, 24576\n"
"ds_read_b128 v[92:95], v2 offset:39936\n"
"v_mfma_f32_16x16x32_bf16 v[56:59], v[100:103], a[20:23], v[56:59]\n"
"s_waitcnt lgkmcnt(4)\n"
"v_lshlrev_b32 v234, 16, v227\n"
"v_and_b32 v235, 0xffff0000, v227\n"
"v_mfma_f32_16x16x32_bf16 v[176:179], v[96:99], a[24:27], v[176:179]\n"
"ds_read_b128 v[96:99], v2 offset:40960\n"
"v_lshlrev_b32 v236, 16, v228\n"
"v_mfma_f32_16x16x32_bf16 v[176:179], v[100:103], a[28:31], v[176:179]\n"
"buffer_load_dwordx4 v4, s[36:39], s8 offen sc1 lds\n"
"v_mfma_f32_16x16x32_bf16 v[60:63], v[104:107], a[16:19], v[60:63]\n"
"s_sub_u32 s8, s8, 1433600\n"
"ds_read_b128 v[100:103], v2 offset:51200\n"
"v_mfma_f32_16x16x32_bf16 v[60:63], v[108:111], a[20:23], v[60:63]\n"
"s_waitcnt lgkmcnt(4)\n"
"v_and_b32 v237, 0xffff0000, v228\n"
"v_lshlrev_b32 v238, 16, v229\n"
"v_mfma_f32_16x16x32_bf16 v[180:183], v[104:107], a[24:27], v[180:183]\n"
"ds_read_b128 v[104:107], v2 offset:41984\n"
"v_and_b32 v239, 0xffff0000, v229\n"
"v_mfma_f32_16x16x32_bf16 v[180:183], v[108:111], a[28:31], v[180:183]\n"
"buffer_load_dwordx4 a[208:211], v10, s[36:39], s11 offen sc1\n"
"v_mfma_f32_16x16x32_bf16 v[64:67], v[80:83], a[16:19], v[64:67]\n"
"ds_read_b128 v[108:111], v2 offset:52224\n"
"v_add_f32 v112, v112, v210\n"
"v_mfma_f32_16x16x32_bf16 v[64:67], v[84:87], a[20:23], v[64:67]\n"
"s_waitcnt lgkmcnt(4)\n"
"v_add_f32 v113, v113, v211\n"
"v_add_f32 v114, v114, v212\n"
"v_mfma_f32_16x16x32_bf16 v[184:187], v[80:83], a[24:27], v[184:187]\n"
"ds_read_b128 v[80:83], v2 offset:43008\n"
"v_add_f32 v115, v115, v213\n"
"v_mfma_f32_16x16x32_bf16 v[184:187], v[84:87], a[28:31], v[184:187]\n"
"buffer_load_dwordx4 a[212:215], v12, s[36:39], s11 offen sc1\n"
"v_mfma_f32_16x16x32_bf16 v[68:71], v[88:91], a[16:19], v[68:71]\n"
"ds_read_b128 v[84:87], v2 offset:53248\n"
"v_add_f32 v116, v116, v214\n"
"v_mfma_f32_16x16x32_bf16 v[68:71], v[92:95], a[20:23], v[68:71]\n"
"s_waitcnt lgkmcnt(4)\n"
"v_add_f32 v117, v117, v215\n"
"v_add_f32 v118, v118, v216\n"
"v_mfma_f32_16x16x32_bf16 v[188:191], v[88:91], a[24:27], v[188:191]\n"
"ds_read_b128 v[88:91], v2 offset:44032\n"
"v_add_f32 v119, v119, v217\n"
"v_mfma_f32_16x16x32_bf16 v[188:191], v[92:95], a[28:31], v[188:191]\n"
"buffer_load_dwordx4 a[216:219], v11, s[36:39], s11 offen sc1\n"
"v_mfma_f32_16x16x32_bf16 v[32:35], v[96:99], a[32:35], v[32:35]\n"
"ds_read_b128 v[92:95], v2 offset:54272\n"
"v_add_f32 v120, v120, v218\n"
"v_mfma_f32_16x16x32_bf16 v[32:35], v[100:103], a[36:39], v[32:35]\n"
"s_waitcnt lgkmcnt(4)\n"
"v_add_f32 v121, v121, v219\n"
"v_add_f32 v122, v122, v230\n"
"v_mfma_f32_16x16x32_bf16 v[72:75], v[96:99], a[40:43], v[72:75]\n"
"ds_read_b128 v[96:99], v2 offset:45056\n"
"v_add_f32 v123, v123, v231\n"
"v_mfma_f32_16x16x32_bf16 v[72:75], v[100:103], a[44:47], v[72:75]\n"
"buffer_load_dwordx4 a[220:223], v13, s[36:39], s11 offen sc1\n"
"v_mfma_f32_16x16x32_bf16 v[36:39], v[104:107], a[32:35], v[36:39]\n"
"s_add_u32 s11, s11, 1572864\n"
"ds_read_b128 v[100:103], v2 offset:55296\n"
"v_mfma_f32_16x16x32_bf16 v[36:39], v[108:111], a[36:39], v[36:39]\n"
"s_waitcnt lgkmcnt(4)\n"
"v_add_f32 v124, v124, v232\n"
"v_add_f32 v125, v125, v233\n"
"v_mfma_f32_16x16x32_bf16 v[76:79], v[104:107], a[40:43], v[76:79]\n"
"ds_read_b128 v[104:107], v2 offset:46080\n"
"v_add_f32 v126, v126, v234\n"
"v_mfma_f32_16x16x32_bf16 v[76:79], v[108:111], a[44:47], v[76:79]\n"
"buffer_load_dwordx4 a[224:227], v10, s[36:39], s11 offen sc1\n"
"v_mfma_f32_16x16x32_bf16 v[40:43], v[80:83], a[32:35], v[40:43]\n"
"ds_read_b128 v[108:111], v2 offset:56320\n"
"v_add_f32 v127, v127, v235\n"
"v_mfma_f32_16x16x32_bf16 v[40:43], v[84:87], a[36:39], v[40:43]\n"
"s_waitcnt lgkmcnt(4)\n"
"v_add_f32 v128, v128, v236\n"
"v_add_f32 v129, v129, v237\n"
"v_mfma_f32_16x16x32_bf16 v[240:243], v[80:83], a[40:43], v[240:243]\n"
"ds_read_b128 v[80:83], v2 offset:47104\n"
"v_add_f32 v130, v130, v238\n"
"v_mfma_f32_16x16x32_bf16 v[240:243], v[84:87], a[44:47], v[240:243]\n"
"buffer_load_dwordx4 a[228:231], v12, s[36:39], s11 offen sc1\n"
"v_mfma_f32_16x16x32_bf16 v[44:47], v[88:91], a[32:35], v[44:47]\n"
"ds_read_b128 v[84:87], v2 offset:57344\n"
"v_add_f32 v131, v131, v239\n"
"v_mfma_f32_16x16x32_bf16 v[44:47], v[92:95], a[36:39], v[44:47]\n"
"s_waitcnt lgkmcnt(4)\n"
"v_cvt_pk_bf16_f32 v112, 0, v112\n"
"v_cvt_pk_bf16_f32 v113, 0, v113\n"
"v_mfma_f32_16x16x32_bf16 v[244:247], v[88:91], a[40:43], v[244:247]\n"
"ds_read_b128 v[88:91], v2 offset:48128\n"
"v_cvt_pk_bf16_f32 v114, 0, v114\n"
"v_mfma_f32_16x16x32_bf16 v[244:247], v[92:95], a[44:47], v[244:247]\n"
"buffer_load_dwordx4 a[232:235], v11, s[36:39], s11 offen sc1\n"
"v_mfma_f32_16x16x32_bf16 v[48:51], v[96:99], a[32:35], v[48:51]\n"
"ds_read_b128 v[92:95], v2 offset:58368\n"
"v_cvt_pk_bf16_f32 v115, 0, v115\n"
"v_mfma_f32_16x16x32_bf16 v[48:51], v[100:103], a[36:39], v[48:51]\n"
"s_waitcnt lgkmcnt(4)\n"
"v_cvt_pk_bf16_f32 v116, 0, v116\n"
"v_cvt_pk_bf16_f32 v117, 0, v117\n"
"v_mfma_f32_16x16x32_bf16 v[248:251], v[96:99], a[40:43], v[248:251]\n"
"ds_read_b128 v[96:99], v2 offset:49152\n"
"v_cvt_pk_bf16_f32 v118, 0, v118\n"
"v_mfma_f32_16x16x32_bf16 v[248:251], v[100:103], a[44:47], v[248:251]\n"
"buffer_load_dwordx4 a[236:239], v13, s[36:39], s11 offen sc1\n"
"v_mfma_f32_16x16x32_bf16 v[52:55], v[104:107], a[32:35], v[52:55]\n"
"s_waitcnt vmcnt(13)\n"
"s_barrier\n"
"s_add_u32 s11, s11, 1572864\n"
"v_mfma_f32_16x16x32_bf16 v[52:55], v[108:111], a[36:39], v[52:55]\n"
"s_waitcnt lgkmcnt(3)\n"
"ds_read_b128 v[100:103], v2 offset:59392\n"
"v_mfma_f32_16x16x32_bf16 v[252:255], v[104:107], a[40:43], v[252:255]\n"
"ds_read_b128 v[104:107], v2 offset:50176\n"
"v_cvt_pk_bf16_f32 v119, 0, v119\n"
"v_mfma_f32_16x16x32_bf16 v[252:255], v[108:111], a[44:47], v[252:255]\n"
"buffer_load_dwordx4 a[240:243], v10, s[36:39], s11 offen sc1\n"
"v_mfma_f32_16x16x32_bf16 v[56:59], v[80:83], a[32:35], v[56:59]\n"
"ds_read_b128 v[108:111], v2 offset:60416\n"
"v_cvt_pk_bf16_f32 v120, 0, v120\n"
"v_mfma_f32_16x16x32_bf16 v[56:59], v[84:87], a[36:39], v[56:59]\n"
"s_waitcnt lgkmcnt(4)\n"
"v_cvt_pk_bf16_f32 v121, 0, v121\n"
"v_cvt_pk_bf16_f32 v122, 0, v122\n"
"v_mfma_f32_16x16x32_bf16 v[176:179], v[80:83], a[40:43], v[176:179]\n"
"ds_read_b128 v[80:83], v1 offset:0\n"
"v_cvt_pk_bf16_f32 v123, 0, v123\n"
"v_mfma_f32_16x16x32_bf16 v[176:179], v[84:87], a[44:47], v[176:179]\n"
"buffer_load_dwordx4 a[244:247], v12, s[36:39], s11 offen sc1\n"
"v_mfma_f32_16x16x32_bf16 v[60:63], v[88:91], a[32:35], v[60:63]\n"
"ds_read_b128 v[84:87], v1 offset:10240\n"
"v_cvt_pk_bf16_f32 v124, 0, v124\n"
"v_mfma_f32_16x16x32_bf16 v[60:63], v[92:95], a[36:39], v[60:63]\n"
"s_waitcnt lgkmcnt(4)\n"
"v_cvt_pk_bf16_f32 v125, 0, v125\n"
"v_cvt_pk_bf16_f32 v126, 0, v126\n"
"v_mfma_f32_16x16x32_bf16 v[180:183], v[88:91], a[40:43], v[180:183]\n"
"ds_read_b128 v[88:91], v1 offset:1024\n"
"v_cvt_pk_bf16_f32 v127, 0, v127\n"
"v_mfma_f32_16x16x32_bf16 v[180:183], v[92:95], a[44:47], v[180:183]\n"
"buffer_load_dwordx4 a[248:251], v11, s[36:39], s11 offen sc1\n"
"v_mfma_f32_16x16x32_bf16 v[64:67], v[96:99], a[32:35], v[64:67]\n"
"ds_read_b128 v[92:95], v1 offset:11264\n"
"v_cvt_pk_bf16_f32 v128, 0, v128\n"
"v_mfma_f32_16x16x32_bf16 v[64:67], v[100:103], a[36:39], v[64:67]\n"
"s_waitcnt lgkmcnt(4)\n"
"v_cvt_pk_bf16_f32 v129, 0, v129\n"
"v_cvt_pk_bf16_f32 v130, 0, v130\n"
"v_mfma_f32_16x16x32_bf16 v[184:187], v[96:99], a[40:43], v[184:187]\n"
"ds_read_b128 v[96:99], v1 offset:2048\n"
"v_cvt_pk_bf16_f32 v131, 0, v131\n"
"v_mfma_f32_16x16x32_bf16 v[184:187], v[100:103], a[44:47], v[184:187]\n"
"buffer_load_dwordx4 a[252:255], v13, s[36:39], s11 offen sc1\n"
"v_mfma_f32_16x16x32_bf16 v[68:71], v[104:107], a[32:35], v[68:71]\n"
"s_sub_u32 s11, s11, 4587520\n"
"ds_read_b128 v[100:103], v1 offset:12288\n"
"v_mfma_f32_16x16x32_bf16 v[68:71], v[108:111], a[36:39], v[68:71]\n"
"s_waitcnt lgkmcnt(4)\n"
"v_mul_f32 v210, s88, v112\n"
"v_mul_f32 v211, s88, v113\n"
"v_mfma_f32_16x16x32_bf16 v[188:191], v[104:107], a[40:43], v[188:191]\n"
"ds_read_b128 v[104:107], v1 offset:3072\n"
"v_mul_f32 v212, s88, v114\n"
"v_mfma_f32_16x16x32_bf16 v[188:191], v[108:111], a[44:47], v[188:191]\n"
"ds_read_b128 v[108:111], v1 offset:13312\n"
"v_mul_f32 v213, s88, v115\n"
"v_mfma_f32_16x16x32_bf16 v[32:35], v[80:83], a[48:51], v[32:35]\n"
"v_mul_f32 v214, s88, v116\n"
"v_mul_f32 v215, s88, v117\n"
"v_mfma_f32_16x16x32_bf16 v[32:35], v[84:87], a[52:55], v[32:35]\n"
"s_waitcnt lgkmcnt(4)\n"
"v_mul_f32 v216, s88, v118\n"
"v_mul_f32 v217, s88, v119\n"
"v_mfma_f32_16x16x32_bf16 v[72:75], v[80:83], a[56:59], v[72:75]\n"
"ds_read_b128 v[80:83], v1 offset:4096\n"
"v_mul_f32 v218, s88, v120\n"
"v_mfma_f32_16x16x32_bf16 v[72:75], v[84:87], a[60:63], v[72:75]\n"
"ds_read_b128 v[84:87], v1 offset:14336\n"
"v_mul_f32 v219, s88, v121\n"
"v_mfma_f32_16x16x32_bf16 v[36:39], v[88:91], a[48:51], v[36:39]\n"
"v_mul_f32 v230, s88, v122\n"
"v_mul_f32 v231, s88, v123\n"
"v_mfma_f32_16x16x32_bf16 v[36:39], v[92:95], a[52:55], v[36:39]\n"
"s_waitcnt lgkmcnt(4)\n"
"v_mul_f32 v232, s88, v124\n"
"v_mul_f32 v233, s88, v125\n"
"v_mfma_f32_16x16x32_bf16 v[76:79], v[88:91], a[56:59], v[76:79]\n"
"ds_read_b128 v[88:91], v1 offset:5120\n"
"v_mul_f32 v234, s88, v126\n"
"v_mfma_f32_16x16x32_bf16 v[76:79], v[92:95], a[60:63], v[76:79]\n"
"ds_read_b128 v[92:95], v1 offset:15360\n"
"v_mul_f32 v235, s88, v127\n"
"v_mfma_f32_16x16x32_bf16 v[40:43], v[96:99], a[48:51], v[40:43]\n"
"v_mul_f32 v236, s88, v128\n"
"v_mul_f32 v237, s88, v129\n"
"v_mfma_f32_16x16x32_bf16 v[40:43], v[100:103], a[52:55], v[40:43]\n"
"s_waitcnt lgkmcnt(4)\n"
"v_mul_f32 v238, s88, v130\n"
"v_mul_f32 v239, s88, v131\n"
"v_mfma_f32_16x16x32_bf16 v[240:243], v[96:99], a[56:59], v[240:243]\n"
"ds_read_b128 v[96:99], v1 offset:6144\n"
"v_lshlrev_b32 v144, 16, v16\n"
"v_mfma_f32_16x16x32_bf16 v[240:243], v[100:103], a[60:63], v[240:243]\n"
"ds_read_b128 v[100:103], v1 offset:16384\n"
"v_and_b32 v145, 0xffff0000, v16\n"
"v_mfma_f32_16x16x32_bf16 v[44:47], v[104:107], a[48:51], v[44:47]\n"
"v_exp_f32 v210, v210\n"
"v_mfma_f32_16x16x32_bf16 v[44:47], v[108:111], a[52:55], v[44:47]\n"
"s_waitcnt lgkmcnt(4)\n"
"v_exp_f32 v211, v211\n"
"v_mfma_f32_16x16x32_bf16 v[244:247], v[104:107], a[56:59], v[244:247]\n"
"ds_read_b128 v[104:107], v1 offset:7168\n"
"v_add_f32 v210, 1.0, v210\n"
"v_mfma_f32_16x16x32_bf16 v[244:247], v[108:111], a[60:63], v[244:247]\n"
"ds_read_b128 v[108:111], v1 offset:17408\n"
"v_add_f32 v211, 1.0, v211\n"
"v_mfma_f32_16x16x32_bf16 v[48:51], v[80:83], a[48:51], v[48:51]\n"
"v_exp_f32 v212, v212\n"
"v_mfma_f32_16x16x32_bf16 v[48:51], v[84:87], a[52:55], v[48:51]\n"
"s_waitcnt lgkmcnt(4)\n"
"v_exp_f32 v213, v213\n"
"v_mfma_f32_16x16x32_bf16 v[248:251], v[80:83], a[56:59], v[248:251]\n"
"ds_read_b128 v[80:83], v1 offset:8192\n"
"v_add_f32 v212, 1.0, v212\n"
"v_mfma_f32_16x16x32_bf16 v[248:251], v[84:87], a[60:63], v[248:251]\n"
"ds_read_b128 v[84:87], v1 offset:18432\n"
"v_add_f32 v213, 1.0, v213\n"
"v_mfma_f32_16x16x32_bf16 v[52:55], v[88:91], a[48:51], v[52:55]\n"
"s_waitcnt vmcnt(8)\n"
"s_barrier\n"
"v_lshlrev_b32 v146, 16, v17\n"
"v_mfma_f32_16x16x32_bf16 v[52:55], v[92:95], a[52:55], v[52:55]\n"
"s_waitcnt lgkmcnt(4)\n"
"v_exp_f32 v214, v214\n"
"v_mfma_f32_16x16x32_bf16 v[252:255], v[88:91], a[56:59], v[252:255]\n"
"ds_read_b128 v[88:91], v1 offset:9216\n"
"v_add_f32 v214, 1.0, v214\n"
"v_mfma_f32_16x16x32_bf16 v[252:255], v[92:95], a[60:63], v[252:255]\n"
"ds_read_b128 v[92:95], v1 offset:19456\n"
"v_and_b32 v147, 0xffff0000, v17\n"
"v_mfma_f32_16x16x32_bf16 v[56:59], v[96:99], a[48:51], v[56:59]\n"
"v_exp_f32 v215, v215\n"
"v_mfma_f32_16x16x32_bf16 v[56:59], v[100:103], a[52:55], v[56:59]\n"
"s_waitcnt lgkmcnt(4)\n"
"v_exp_f32 v216, v216\n"
"v_mfma_f32_16x16x32_bf16 v[176:179], v[96:99], a[56:59], v[176:179]\n"
"ds_read_b128 v[96:99], v3 offset:0\n"
"v_add_f32 v215, 1.0, v215\n"
"v_mfma_f32_16x16x32_bf16 v[176:179], v[100:103], a[60:63], v[176:179]\n"
"ds_read_b128 v[100:103], v3 offset:10240\n"
"v_add_f32 v216, 1.0, v216\n"
"v_mfma_f32_16x16x32_bf16 v[60:63], v[104:107], a[48:51], v[60:63]\n"
"v_exp_f32 v217, v217\n"
"v_mfma_f32_16x16x32_bf16 v[60:63], v[108:111], a[52:55], v[60:63]\n"
"s_waitcnt lgkmcnt(4)\n"
"v_exp_f32 v218, v218\n"
"v_mfma_f32_16x16x32_bf16 v[180:183], v[104:107], a[56:59], v[180:183]\n"
"ds_read_b128 v[104:107], v3 offset:1024\n"
"v_add_f32 v217, 1.0, v217\n"
"v_mfma_f32_16x16x32_bf16 v[180:183], v[108:111], a[60:63], v[180:183]\n"
"ds_read_b128 v[108:111], v3 offset:11264\n"
"v_add_f32 v218, 1.0, v218\n"
"v_mfma_f32_16x16x32_bf16 v[64:67], v[80:83], a[48:51], v[64:67]\n"
"v_exp_f32 v219, v219\n"
"v_mfma_f32_16x16x32_bf16 v[64:67], v[84:87], a[52:55], v[64:67]\n"
"s_waitcnt lgkmcnt(4)\n"
"v_exp_f32 v230, v230\n"
"v_mfma_f32_16x16x32_bf16 v[184:187], v[80:83], a[56:59], v[184:187]\n"
"ds_read_b128 v[80:83], v3 offset:2048\n"
"v_add_f32 v219, 1.0, v219\n"
"v_mfma_f32_16x16x32_bf16 v[184:187], v[84:87], a[60:63], v[184:187]\n"
"ds_read_b128 v[84:87], v3 offset:12288\n"
"v_add_f32 v230, 1.0, v230\n"
"v_mfma_f32_16x16x32_bf16 v[68:71], v[88:91], a[48:51], v[68:71]\n"
"v_exp_f32 v231, v231\n"
"v_mfma_f32_16x16x32_bf16 v[68:71], v[92:95], a[52:55], v[68:71]\n"
"s_waitcnt lgkmcnt(4)\n"
"v_exp_f32 v232, v232\n"
"v_mfma_f32_16x16x32_bf16 v[188:191], v[88:91], a[56:59], v[188:191]\n"
"ds_read_b128 v[88:91], v3 offset:3072\n"
"v_add_f32 v231, 1.0, v231\n"
"v_mfma_f32_16x16x32_bf16 v[188:191], v[92:95], a[60:63], v[188:191]\n"
"ds_read_b128 v[92:95], v3 offset:13312\n"
"v_add_f32 v232, 1.0, v232\n"
"v_mfma_f32_16x16x32_bf16 v[32:35], v[96:99], a[208:211], v[32:35]\n"
"v_exp_f32 v233, v233\n"
"v_mfma_f32_16x16x32_bf16 v[32:35], v[100:103], a[212:215], v[32:35]\n"
"s_waitcnt lgkmcnt(4)\n"
"v_exp_f32 v234, v234\n"
"v_mfma_f32_16x16x32_bf16 v[72:75], v[96:99], a[216:219], v[72:75]\n"
"ds_read_b128 v[96:99], v3 offset:4096\n"
"v_add_f32 v233, 1.0, v233\n"
"v_mfma_f32_16x16x32_bf16 v[72:75], v[100:103], a[220:223], v[72:75]\n"
"ds_read_b128 v[100:103], v3 offset:14336\n"
"v_add_f32 v234, 1.0, v234\n"
"v_mfma_f32_16x16x32_bf16 v[36:39], v[104:107], a[208:211], v[36:39]\n"
"v_exp_f32 v235, v235\n"
"v_mfma_f32_16x16x32_bf16 v[36:39], v[108:111], a[212:215], v[36:39]\n"
"s_waitcnt lgkmcnt(4)\n"
"v_exp_f32 v236, v236\n"
"v_mfma_f32_16x16x32_bf16 v[76:79], v[104:107], a[216:219], v[76:79]\n"
"ds_read_b128 v[104:107], v3 offset:5120\n"
"v_add_f32 v235, 1.0, v235\n"
"v_mfma_f32_16x16x32_bf16 v[76:79], v[108:111], a[220:223], v[76:79]\n"
"ds_read_b128 v[108:111], v3 offset:15360\n"
"v_add_f32 v236, 1.0, v236\n"
"v_mfma_f32_16x16x32_bf16 v[40:43], v[80:83], a[208:211], v[40:43]\n"
"v_exp_f32 v237, v237\n"
"v_mfma_f32_16x16x32_bf16 v[40:43], v[84:87], a[212:215], v[40:43]\n"
"s_waitcnt lgkmcnt(4)\n"
"v_exp_f32 v238, v238\n"
"v_mfma_f32_16x16x32_bf16 v[240:243], v[80:83], a[216:219], v[240:243]\n"
"ds_read_b128 v[80:83], v3 offset:6144\n"
"v_add_f32 v237, 1.0, v237\n"
"v_mfma_f32_16x16x32_bf16 v[240:243], v[84:87], a[220:223], v[240:243]\n"
"ds_read_b128 v[84:87], v3 offset:16384\n"
"v_add_f32 v238, 1.0, v238\n"
"v_mfma_f32_16x16x32_bf16 v[44:47], v[88:91], a[208:211], v[44:47]\n"
"v_exp_f32 v239, v239\n"
"v_mfma_f32_16x16x32_bf16 v[44:47], v[92:95], a[212:215], v[44:47]\n"
"s_waitcnt lgkmcnt(4)\n"
"v_add_f32 v239, 1.0, v239\n"
"v_lshlrev_b32 v148, 16, v18\n"
"v_mfma_f32_16x16x32_bf16 v[244:247], v[88:91], a[216:219], v[244:247]\n"
"ds_read_b128 v[88:91], v3 offset:7168\n"
"v_and_b32 v149, 0xffff0000, v18\n"
"v_mfma_f32_16x16x32_bf16 v[244:247], v[92:95], a[220:223], v[244:247]\n"
"ds_read_b128 v[92:95], v3 offset:17408\n"
"v_lshlrev_b32 v150, 16, v19\n"
"v_mfma_f32_16x16x32_bf16 v[48:51], v[96:99], a[208:211], v[48:51]\n"
"v_rcp_f32 v210, v210\n"
"v_mfma_f32_16x16x32_bf16 v[48:51], v[100:103], a[212:215], v[48:51]\n"
"s_waitcnt lgkmcnt(4)\n"
"v_rcp_f32 v211, v211\n"
"v_mfma_f32_16x16x32_bf16 v[248:251], v[96:99], a[216:219], v[248:251]\n"
"ds_read_b128 v[96:99], v3 offset:8192\n"
"v_mul_f32 v112, v112, v210\n"
"v_mfma_f32_16x16x32_bf16 v[248:251], v[100:103], a[220:223], v[248:251]\n"
"ds_read_b128 v[100:103], v3 offset:18432\n"
"v_mul_f32 v113, v113, v211\n"
"v_mfma_f32_16x16x32_bf16 v[52:55], v[104:107], a[208:211], v[52:55]\n"
"s_waitcnt vmcnt(4)\n"
"s_barrier\n"
"v_cvt_pk_bf16_f32 v112, 0, v112\n"
"v_mfma_f32_16x16x32_bf16 v[52:55], v[108:111], a[212:215], v[52:55]\n"
"s_waitcnt lgkmcnt(4)\n"
"v_rcp_f32 v212, v212\n"
"v_mfma_f32_16x16x32_bf16 v[252:255], v[104:107], a[216:219], v[252:255]\n"
"ds_read_b128 v[104:107], v3 offset:9216\n"
"v_mul_f32 v114, v114, v212\n"
"v_mfma_f32_16x16x32_bf16 v[252:255], v[108:111], a[220:223], v[252:255]\n"
"ds_read_b128 v[108:111], v3 offset:19456\n"
"v_cvt_pk_bf16_f32 v113, 0, v113\n"
"v_mfma_f32_16x16x32_bf16 v[56:59], v[80:83], a[208:211], v[56:59]\n"
"v_rcp_f32 v213, v213\n"
"v_mfma_f32_16x16x32_bf16 v[56:59], v[84:87], a[212:215], v[56:59]\n"
"s_waitcnt lgkmcnt(4)\n"
"v_rcp_f32 v214, v214\n"
"v_mfma_f32_16x16x32_bf16 v[176:179], v[80:83], a[216:219], v[176:179]\n"
"ds_read_b128 v[80:83], v3 offset:20480\n"
"v_mul_f32 v115, v115, v213\n"
"v_mfma_f32_16x16x32_bf16 v[176:179], v[84:87], a[220:223], v[176:179]\n"
"ds_read_b128 v[84:87], v3 offset:30720\n"
"v_mul_f32 v116, v116, v214\n"
"v_mfma_f32_16x16x32_bf16 v[60:63], v[88:91], a[208:211], v[60:63]\n"
"v_rcp_f32 v215, v215\n"
"v_mfma_f32_16x16x32_bf16 v[60:63], v[92:95], a[212:215], v[60:63]\n"
"s_waitcnt lgkmcnt(4)\n"
"v_rcp_f32 v216, v216\n"
"v_mfma_f32_16x16x32_bf16 v[180:183], v[88:91], a[216:219], v[180:183]\n"
"ds_read_b128 v[88:91], v3 offset:21504\n"
"v_mul_f32 v117, v117, v215\n"
"v_mfma_f32_16x16x32_bf16 v[180:183], v[92:95], a[220:223], v[180:183]\n"
"ds_read_b128 v[92:95], v3 offset:31744\n"
"v_mul_f32 v118, v118, v216\n"
"v_mfma_f32_16x16x32_bf16 v[64:67], v[96:99], a[208:211], v[64:67]\n"
"v_rcp_f32 v217, v217\n"
"v_mfma_f32_16x16x32_bf16 v[64:67], v[100:103], a[212:215], v[64:67]\n"
"s_waitcnt lgkmcnt(4)\n"
"v_rcp_f32 v218, v218\n"
"v_mfma_f32_16x16x32_bf16 v[184:187], v[96:99], a[216:219], v[184:187]\n"
"ds_read_b128 v[96:99], v3 offset:22528\n"
"v_mul_f32 v119, v119, v217\n"
"v_mfma_f32_16x16x32_bf16 v[184:187], v[100:103], a[220:223], v[184:187]\n"
"ds_read_b128 v[100:103], v3 offset:32768\n"
"v_mul_f32 v120, v120, v218\n"
"v_mfma_f32_16x16x32_bf16 v[68:71], v[104:107], a[208:211], v[68:71]\n"
"v_rcp_f32 v219, v219\n"
"v_mfma_f32_16x16x32_bf16 v[68:71], v[108:111], a[212:215], v[68:71]\n"
"s_waitcnt lgkmcnt(4)\n"
"v_rcp_f32 v230, v230\n"
"v_mfma_f32_16x16x32_bf16 v[188:191], v[104:107], a[216:219], v[188:191]\n"
"ds_read_b128 v[104:107], v3 offset:23552\n"
"v_mul_f32 v121, v121, v219\n"
"v_mfma_f32_16x16x32_bf16 v[188:191], v[108:111], a[220:223], v[188:191]\n"
"ds_read_b128 v[108:111], v3 offset:33792\n"
"v_mul_f32 v122, v122, v230\n"
"v_mfma_f32_16x16x32_bf16 v[32:35], v[80:83], a[224:227], v[32:35]\n"
"v_rcp_f32 v231, v231\n"
"v_mfma_f32_16x16x32_bf16 v[32:35], v[84:87], a[228:231], v[32:35]\n"
"s_waitcnt lgkmcnt(4)\n"
"v_rcp_f32 v232, v232\n"
"v_mfma_f32_16x16x32_bf16 v[72:75], v[80:83], a[232:235], v[72:75]\n"
"ds_read_b128 v[80:83], v3 offset:24576\n"
"v_mul_f32 v123, v123, v231\n"
"v_mfma_f32_16x16x32_bf16 v[72:75], v[84:87], a[236:239], v[72:75]\n"
"ds_read_b128 v[84:87], v3 offset:34816\n"
"v_mul_f32 v124, v124, v232\n"
"v_mfma_f32_16x16x32_bf16 v[36:39], v[88:91], a[224:227], v[36:39]\n"
"v_rcp_f32 v233, v233\n"
"v_mfma_f32_16x16x32_bf16 v[36:39], v[92:95], a[228:231], v[36:39]\n"
"s_waitcnt lgkmcnt(4)\n"
"v_rcp_f32 v234, v234\n"
"v_mfma_f32_16x16x32_bf16 v[76:79], v[88:91], a[232:235], v[76:79]\n"
"ds_read_b128 v[88:91], v3 offset:25600\n"
"v_mul_f32 v125, v125, v233\n"
"v_mfma_f32_16x16x32_bf16 v[76:79], v[92:95], a[236:239], v[76:79]\n"
"ds_read_b128 v[92:95], v3 offset:35840\n"
"v_mul_f32 v126, v126, v234\n"
"v_mfma_f32_16x16x32_bf16 v[40:43], v[96:99], a[224:227], v[40:43]\n"
"v_rcp_f32 v235, v235\n"
"v_mfma_f32_16x16x32_bf16 v[40:43], v[100:103], a[228:231], v[40:43]\n"
"s_waitcnt lgkmcnt(4)\n"
"v_rcp_f32 v236, v236\n"
"v_mfma_f32_16x16x32_bf16 v[240:243], v[96:99], a[232:235], v[240:243]\n"
"ds_read_b128 v[96:99], v3 offset:26624\n"
"v_mul_f32 v127, v127, v235\n"
"v_mfma_f32_16x16x32_bf16 v[240:243], v[100:103], a[236:239], v[240:243]\n"
"ds_read_b128 v[100:103], v3 offset:36864\n"
"v_mul_f32 v128, v128, v236\n"
"v_mfma_f32_16x16x32_bf16 v[44:47], v[104:107], a[224:227], v[44:47]\n"
"v_rcp_f32 v237, v237\n"
"v_mfma_f32_16x16x32_bf16 v[44:47], v[108:111], a[228:231], v[44:47]\n"
"s_waitcnt lgkmcnt(4)\n"
"v_rcp_f32 v238, v238\n"
"v_mfma_f32_16x16x32_bf16 v[244:247], v[104:107], a[232:235], v[244:247]\n"
"ds_read_b128 v[104:107], v3 offset:27648\n"
"v_mul_f32 v129, v129, v237\n"
"v_mfma_f32_16x16x32_bf16 v[244:247], v[108:111], a[236:239], v[244:247]\n"
"ds_read_b128 v[108:111], v3 offset:37888\n"
"v_mul_f32 v130, v130, v238\n"
"v_mfma_f32_16x16x32_bf16 v[48:51], v[80:83], a[224:227], v[48:51]\n"
"v_rcp_f32 v239, v239\n"
"v_mfma_f32_16x16x32_bf16 v[48:51], v[84:87], a[228:231], v[48:51]\n"
"s_waitcnt lgkmcnt(4)\n"
"v_mul_f32 v131, v131, v239\n"
"v_cvt_pk_bf16_f32 v114, 0, v114\n"
"v_mfma_f32_16x16x32_bf16 v[248:251], v[80:83], a[232:235], v[248:251]\n"
"ds_read_b128 v[80:83], v3 offset:28672\n"
"v_cvt_pk_bf16_f32 v115, 0, v115\n"
"v_mfma_f32_16x16x32_bf16 v[248:251], v[84:87], a[236:239], v[248:251]\n"
"ds_read_b128 v[84:87], v3 offset:38912\n"
"v_cvt_pk_bf16_f32 v116, 0, v116\n"
"v_mfma_f32_16x16x32_bf16 v[52:55], v[88:91], a[224:227], v[52:55]\n"
"s_waitcnt vmcnt(0)\n"
"s_barrier\n"
"v_cvt_pk_bf16_f32 v117, 0, v117\n"
"v_mfma_f32_16x16x32_bf16 v[52:55], v[92:95], a[228:231], v[52:55]\n"
"s_waitcnt lgkmcnt(4)\n"
"v_cvt_pk_bf16_f32 v118, 0, v118\n"
"v_cvt_pk_bf16_f32 v119, 0, v119\n"
"v_mfma_f32_16x16x32_bf16 v[252:255], v[88:91], a[232:235], v[252:255]\n"
"ds_read_b128 v[88:91], v3 offset:29696\n"
"v_and_b32 v151, 0xffff0000, v19\n"
"v_mfma_f32_16x16x32_bf16 v[252:255], v[92:95], a[236:239], v[252:255]\n"
"ds_read_b128 v[92:95], v3 offset:39936\n"
"v_cvt_pk_bf16_f32 v120, 0, v120\n"
"v_mfma_f32_16x16x32_bf16 v[56:59], v[96:99], a[224:227], v[56:59]\n"
"v_lshlrev_b32 v152, 16, v20\n"
"v_cvt_pk_bf16_f32 v121, 0, v121\n"
"v_mfma_f32_16x16x32_bf16 v[56:59], v[100:103], a[228:231], v[56:59]\n"
"s_waitcnt lgkmcnt(4)\n"
"v_and_b32 v153, 0xffff0000, v20\n"
"v_cvt_pk_bf16_f32 v122, 0, v122\n"
"v_mfma_f32_16x16x32_bf16 v[176:179], v[96:99], a[232:235], v[176:179]\n"
"ds_read_b128 v[96:99], v1 offset:20480\n"
"v_lshlrev_b32 v154, 16, v21\n"
"v_mfma_f32_16x16x32_bf16 v[176:179], v[100:103], a[236:239], v[176:179]\n"
"ds_read_b128 v[100:103], v1 offset:30720\n"
"v_cvt_pk_bf16_f32 v123, 0, v123\n"
"v_mfma_f32_16x16x32_bf16 v[60:63], v[104:107], a[224:227], v[60:63]\n"
"v_and_b32 v155, 0xffff0000, v21\n"
"v_cvt_pk_bf16_f32 v124, 0, v124\n"
"v_mfma_f32_16x16x32_bf16 v[60:63], v[108:111], a[228:231], v[60:63]\n"
"s_waitcnt lgkmcnt(4)\n"
"v_lshlrev_b32 v156, 16, v22\n"
"v_cvt_pk_bf16_f32 v125, 0, v125\n"
"v_mfma_f32_16x16x32_bf16 v[180:183], v[104:107], a[232:235], v[180:183]\n"
"ds_read_b128 v[104:107], v1 offset:21504\n"
"v_and_b32 v157, 0xffff0000, v22\n"
"v_mfma_f32_16x16x32_bf16 v[180:183], v[108:111], a[236:239], v[180:183]\n"
"ds_read_b128 v[108:111], v1 offset:31744\n"
"v_cvt_pk_bf16_f32 v126, 0, v126\n"
"v_mfma_f32_16x16x32_bf16 v[64:67], v[80:83], a[224:227], v[64:67]\n"
"v_lshlrev_b32 v158, 16, v23\n"
"v_cvt_pk_bf16_f32 v127, 0, v127\n"
"v_mfma_f32_16x16x32_bf16 v[64:67], v[84:87], a[228:231], v[64:67]\n"
"s_waitcnt lgkmcnt(4)\n"
"v_and_b32 v159, 0xffff0000, v23\n"
"v_cvt_pk_bf16_f32 v128, 0, v128\n"
"v_mfma_f32_16x16x32_bf16 v[184:187], v[80:83], a[232:235], v[184:187]\n"
"ds_read_b128 v[80:83], v1 offset:22528\n"
"v_lshlrev_b32 v160, 16, v24\n"
"v_mfma_f32_16x16x32_bf16 v[184:187], v[84:87], a[236:239], v[184:187]\n"
"v_cvt_pk_bf16_f32 v129, 0, v129\n"
"v_and_b32 v161, 0xffff0000, v24\n"
"v_cvt_pk_bf16_f32 v130, 0, v130\n"
"v_lshlrev_b32 v162, 16, v25\n"
"v_cvt_pk_bf16_f32 v131, 0, v131\n"
"v_and_b32 v163, 0xffff0000, v25\n"
"v_mfma_f32_16x16x32_bf16 v[68:71], v[88:91], a[224:227], v[68:71]\n"
"ds_read_b128 v[84:87], v1 offset:32768\n"
"v_mfma_f32_16x16x32_bf16 v[68:71], v[92:95], a[228:231], v[68:71]\n"
"s_waitcnt lgkmcnt(4)\n"
"v_mfma_f32_16x16x32_bf16 v[188:191], v[88:91], a[232:235], v[188:191]\n"
"ds_read_b128 v[88:91], v1 offset:23552\n"
"v_mfma_f32_16x16x32_bf16 v[188:191], v[92:95], a[236:239], v[188:191]\n"
"ds_read_b128 v[92:95], v1 offset:33792\n"
"v_mfma_f32_16x16x32_bf16 v[32:35], v[96:99], a[240:243], v[32:35]\n"
"v_mfma_f32_16x16x32_bf16 v[32:35], v[100:103], a[244:247], v[32:35]\n"
"s_waitcnt lgkmcnt(4)\n"
"v_mfma_f32_16x16x32_bf16 v[72:75], v[96:99], a[248:251], v[72:75]\n"
"ds_read_b128 v[96:99], v1 offset:24576\n"
"v_mfma_f32_16x16x32_bf16 v[72:75], v[100:103], a[252:255], v[72:75]\n"
"ds_read_b128 v[100:103], v1 offset:34816\n"
"v_mfma_f32_16x16x32_bf16 v[36:39], v[104:107], a[240:243], v[36:39]\n"
"v_mfma_f32_16x16x32_bf16 v[36:39], v[108:111], a[244:247], v[36:39]\n"
"s_waitcnt lgkmcnt(4)\n"
"v_mfma_f32_16x16x32_bf16 v[76:79], v[104:107], a[248:251], v[76:79]\n"
"s_nop 2\n"
"v_cvt_pk_bf16_f32 v200, v72, v73\n"
"v_cvt_pk_bf16_f32 v201, v74, v75\n"
"v_mfma_f32_16x16x32_bf16 v[76:79], v[108:111], a[252:255], v[76:79]\n"
"ds_read_b128 v[104:107], v1 offset:25600\n"
"v_mfma_f32_16x16x32_bf16 v[40:43], v[80:83], a[240:243], v[40:43]\n"
"ds_read_b128 v[108:111], v1 offset:35840\n"
"v_mfma_f32_16x16x32_bf16 v[40:43], v[84:87], a[244:247], v[40:43]\n"
"s_waitcnt lgkmcnt(4)\n"
"v_mfma_f32_16x16x32_bf16 v[240:243], v[80:83], a[248:251], v[240:243]\n"
"s_nop 1\n"
"v_cvt_pk_bf16_f32 v202, v76, v77\n"
"v_cvt_pk_bf16_f32 v203, v78, v79\n"
"v_mfma_f32_16x16x32_bf16 v[240:243], v[84:87], a[252:255], v[240:243]\n"
"buffer_store_dwordx4 v[200:203], v1, s[36:39], s65 offen offset:0 sc1\n"
"v_mfma_f32_16x16x32_bf16 v[44:47], v[88:91], a[240:243], v[44:47]\n"
"ds_read_b128 v[80:83], v1 offset:26624\n"
"v_mfma_f32_16x16x32_bf16 v[44:47], v[92:95], a[244:247], v[44:47]\n"
"s_waitcnt lgkmcnt(3)\n"
"ds_read_b128 v[84:87], v1 offset:36864\n"
"v_mfma_f32_16x16x32_bf16 v[244:247], v[88:91], a[248:251], v[244:247]\n"
"s_nop 0\n"
"v_cvt_pk_bf16_f32 v204, v240, v241\n"
"v_cvt_pk_bf16_f32 v205, v242, v243\n"
"v_mfma_f32_16x16x32_bf16 v[244:247], v[92:95], a[252:255], v[244:247]\n"
"ds_read_b128 v[88:91], v1 offset:27648\n"
"v_mfma_f32_16x16x32_bf16 v[48:51], v[96:99], a[240:243], v[48:51]\n"
"ds_read_b128 v[92:95], v1 offset:37888\n"
"v_mfma_f32_16x16x32_bf16 v[48:51], v[100:103], a[244:247], v[48:51]\n"
"s_waitcnt lgkmcnt(4)\n"
"v_mfma_f32_16x16x32_bf16 v[248:251], v[96:99], a[248:251], v[248:251]\n"
"s_nop 1\n"
"v_cvt_pk_bf16_f32 v206, v244, v245\n"
"v_cvt_pk_bf16_f32 v207, v246, v247\n"
"v_mfma_f32_16x16x32_bf16 v[248:251], v[100:103], a[252:255], v[248:251]\n"
"buffer_store_dwordx4 v[204:207], v1, s[36:39], s65 offen offset:1024 sc1\n"
"v_mfma_f32_16x16x32_bf16 v[52:55], v[104:107], a[240:243], v[52:55]\n"
"ds_read_b128 v[96:99], v1 offset:28672\n"
"v_mfma_f32_16x16x32_bf16 v[52:55], v[108:111], a[244:247], v[52:55]\n"
"s_waitcnt lgkmcnt(3)\n"
"ds_read_b128 v[100:103], v1 offset:38912\n"
"v_mfma_f32_16x16x32_bf16 v[252:255], v[104:107], a[248:251], v[252:255]\n"
"s_nop 0\n"
"v_cvt_pk_bf16_f32 v208, v248, v249\n"
"v_cvt_pk_bf16_f32 v209, v250, v251\n"
"v_mfma_f32_16x16x32_bf16 v[252:255], v[108:111], a[252:255], v[252:255]\n"
"buffer_store_dwordx2 v[208:209], v5, s[36:39], s65 offen offset:0 sc1\n"
"v_mfma_f32_16x16x32_bf16 v[56:59], v[80:83], a[240:243], v[56:59]\n"
"v_cvt_pk_bf16_f32 v132, v52, v53\n"
"v_cvt_pk_bf16_f32 v133, v54, v55\n"
"v_mfma_f32_16x16x32_bf16 v[56:59], v[84:87], a[244:247], v[56:59]\n"
"s_waitcnt lgkmcnt(2)\n"
"ds_read_b128 v[104:107], v1 offset:29696\n"
"v_mfma_f32_16x16x32_bf16 v[176:179], v[80:83], a[248:251], v[176:179]\n"
"v_cvt_pk_bf16_f32 v220, v252, v253\n"
"v_cvt_pk_bf16_f32 v221, v254, v255\n"
"v_mfma_f32_16x16x32_bf16 v[176:179], v[84:87], a[252:255], v[176:179]\n"
"ds_read_b128 v[108:111], v1 offset:39936\n"
"v_mfma_f32_16x16x32_bf16 v[60:63], v[88:91], a[240:243], v[60:63]\n"
"v_cvt_pk_bf16_f32 v134, v56, v57\n"
"v_cvt_pk_bf16_f32 v135, v58, v59\n"
"v_mfma_f32_16x16x32_bf16 v[60:63], v[92:95], a[244:247], v[60:63]\n"
"buffer_store_dwordx4 v[132:135], v1, s[36:39], s64 offen offset:0 sc1\n"
"v_mfma_f32_16x16x32_bf16 v[180:183], v[88:91], a[248:251], v[180:183]\n"
"s_nop 0\n"
"v_cvt_pk_bf16_f32 v222, v176, v177\n"
"v_cvt_pk_bf16_f32 v223, v178, v179\n"
"v_mfma_f32_16x16x32_bf16 v[180:183], v[92:95], a[252:255], v[180:183]\n"
"s_waitcnt lgkmcnt(2)\n"
"v_mfma_f32_16x16x32_bf16 v[64:67], v[96:99], a[240:243], v[64:67]\n"
"v_cvt_pk_bf16_f32 v136, v60, v61\n"
"v_cvt_pk_bf16_f32 v137, v62, v63\n"
"v_mfma_f32_16x16x32_bf16 v[64:67], v[100:103], a[244:247], v[64:67]\n"
"buffer_store_dwordx4 v[220:223], v1, s[36:39], s66 offen offset:0 sc1\n"
"v_mfma_f32_16x16x32_bf16 v[184:187], v[96:99], a[248:251], v[184:187]\n"
"s_nop 0\n"
"v_cvt_pk_bf16_f32 v224, v180, v181\n"
"v_cvt_pk_bf16_f32 v225, v182, v183\n"
"v_mfma_f32_16x16x32_bf16 v[184:187], v[100:103], a[252:255], v[184:187]\n"
"s_waitcnt lgkmcnt(0)\n"
"v_mfma_f32_16x16x32_bf16 v[68:71], v[104:107], a[240:243], v[68:71]\n"
"v_cvt_pk_bf16_f32 v138, v64, v65\n"
"v_cvt_pk_bf16_f32 v139, v66, v67\n"
"v_mfma_f32_16x16x32_bf16 v[68:71], v[108:111], a[244:247], v[68:71]\n"
"buffer_store_dwordx4 v[136:139], v1, s[36:39], s64 offen offset:1024 sc1\n"
"v_mfma_f32_16x16x32_bf16 v[188:191], v[104:107], a[248:251], v[188:191]\n"
"s_nop 0\n"
"v_cvt_pk_bf16_f32 v226, v184, v185\n"
"v_cvt_pk_bf16_f32 v227, v186, v187\n"
"v_mfma_f32_16x16x32_bf16 v[188:191], v[108:111], a[252:255], v[188:191]\n"
"buffer_store_dwordx4 v[224:227], v1, s[36:39], s66 offen offset:1024 sc1\n"
"s_nop 0\n"
"v_cvt_pk_bf16_f32 v140, v68, v69\n"
"v_cvt_pk_bf16_f32 v141, v70, v71\n"
"s_nop 3\n"
"v_cvt_pk_bf16_f32 v228, v188, v189\n"
"v_cvt_pk_bf16_f32 v229, v190, v191\n"
"buffer_store_dwordx2 v[140:141], v5, s[36:39], s64 offen offset:0 sc1\n"
"buffer_store_dwordx2 v[228:229], v5, s[36:39], s66 offen offset:0 sc1\n"
"v_bfe_u32 v26, v0, 4, 1\n"
"v_mul_u32_u24 v26, 24, v26\n"
"v_add_u32 v26, v26, v6\n"
"s_add_u32 s64, s17, 5120\n"
"s_add_u32 s65, s17, 10240\n"
"s_add_u32 s66, s17, 15360\n"
"s_mov_b32 s101, 0\n"
"v_and_b32 v81, 3, v0\n"
"v_max_u32 v81, 1, v81\n"
"v_mul_u32_u24 v81, 5120, v81\n"
".Lfp:\n"
"buffer_load_dword v80, v81, s[36:39], s17 offen offset:2048 sc1\n"
"s_waitcnt vmcnt(0)\n"
"v_cmp_eq_u32 s[92:93], -1, v80\n"
"s_add_u32 s101, s101, 1\n"
"s_nop 0\n"
"s_cmp_eq_u64 s[92:93], 0\n"
"s_cbranch_scc1 .Lfin\n"
"s_sleep 2\n"
"s_cmp_lt_u32 s101, 100000\n"
"s_cbranch_scc1 .Lfp\n"
".Lfin:\n"
"buffer_load_dwordx4 v[210:213], v1, s[36:39], s64 offen offset:0 sc1\n"
"buffer_load_dwordx4 v[214:217], v1, s[36:39], s64 offen offset:1024 sc1\n"
"buffer_load_dwordx2 v[218:219], v5, s[36:39], s64 offen offset:0 sc1\n"
"buffer_load_dwordx4 v[230:233], v1, s[36:39], s65 offen offset:0 sc1\n"
"buffer_load_dwordx4 v[234:237], v1, s[36:39], s65 offen offset:1024 sc1\n"
"buffer_load_dwordx2 v[238:239], v5, s[36:39], s65 offen offset:0 sc1\n"
"buffer_load_dwordx4 v[164:167], v1, s[36:39], s66 offen offset:0 sc1\n"
"buffer_load_dwordx4 v[168:171], v1, s[36:39], s66 offen offset:1024 sc1\n"
"buffer_load_dwordx2 v[172:173], v5, s[36:39], s66 offen offset:0 sc1\n"
"s_waitcnt vmcnt(6)\n"
"v_max_u32 v80, v210, v211\n"
"v_max3_u32 v80, v80, v212, v213\n"
"v_max3_u32 v80, v80, v214, v215\n"
"v_max3_u32 v80, v80, v216, v217\n"
"v_max3_u32 v80, v80, v218, v219\n"
"s_waitcnt vmcnt(3)\n"
"v_max3_u32 v80, v80, v230, v231\n"
"v_max3_u32 v80, v80, v232, v233\n"
"v_max3_u32 v80, v80, v234, v235\n"
"v_max3_u32 v80, v80, v236, v237\n"
"v_max3_u32 v80, v80, v238, v239\n"
"s_waitcnt vmcnt(0)\n"
"v_max3_u32 v80, v80, v164, v165\n"
"v_max3_u32 v80, v80, v166, v167\n"
"v_max3_u32 v80, v80, v168, v169\n"
"v_max3_u32 v80, v80, v170, v171\n"
"v_max3_u32 v80, v80, v172, v173\n"
"v_cmp_eq_u32 s[92:93], -1, v80\n"
"s_add_u32 s101, s101, 1\n"
"s_nop 0\n"
"s_cmp_eq_u64 s[92:93], 0\n"
"s_cbranch_scc1 .Lfin2\n"
"s_cmp_lt_u32 s101, 100000\n"
"s_cbranch_scc1 .Lfin\n"
".Lfin2:\n"
"v_lshlrev_b32 v80, 16, v210\n"
"v_and_b32 v81, 0xffff0000, v210\n"
"v_lshlrev_b32 v82, 16, v211\n"
"v_and_b32 v83, 0xffff0000, v211\n"
"v_lshlrev_b32 v84, 16, v212\n"
"v_and_b32 v85, 0xffff0000, v212\n"
"v_lshlrev_b32 v86, 16, v213\n"
"v_and_b32 v87, 0xffff0000, v213\n"
"v_lshlrev_b32 v88, 16, v214\n"
"v_and_b32 v89, 0xffff0000, v214\n"
"v_lshlrev_b32 v90, 16, v215\n"
"v_and_b32 v91, 0xffff0000, v215\n"
"v_lshlrev_b32 v92, 16, v216\n"
"v_and_b32 v93, 0xffff0000, v216\n"
"v_lshlrev_b32 v94, 16, v217\n"
"v_and_b32 v95, 0xffff0000, v217\n"
"v_lshlrev_b32 v96, 16, v218\n"
"v_and_b32 v97, 0xffff0000, v218\n"
"v_lshlrev_b32 v98, 16, v219\n"
"v_and_b32 v99, 0xffff0000, v219\n"
"v_add_f32 v32, v32, v80\n"
"v_add_f32 v33, v33, v81\n"
"v_add_f32 v34, v34, v82\n"
"v_add_f32 v35, v35, v83\n"
"v_add_f32 v36, v36, v84\n"
"v_add_f32 v37, v37, v85\n"
"v_add_f32 v38, v38, v86\n"
"v_add_f32 v39, v39, v87\n"
"v_add_f32 v40, v40, v88\n"
"v_add_f32 v41, v41, v89\n"
"v_add_f32 v42, v42, v90\n"
"v_add_f32 v43, v43, v91\n"
"v_add_f32 v44, v44, v92\n"
"v_add_f32 v45, v45, v93\n"
"v_add_f32 v46, v46, v94\n"
"v_add_f32 v47, v47, v95\n"
"v_add_f32 v48, v48, v96\n"
"v_add_f32 v49, v49, v97\n"
"v_add_f32 v50, v50, v98\n"
"v_add_f32 v51, v51, v99\n"
"v_lshlrev_b32 v80, 16, v230\n"
"v_and_b32 v81, 0xffff0000, v230\n"
"v_lshlrev_b32 v82, 16, v231\n"
"v_and_b32 v83, 0xffff0000, v231\n"
"v_lshlrev_b32 v84, 16, v232\n"
"v_and_b32 v85, 0xffff0000, v232\n"
"v_lshlrev_b32 v86, 16, v233\n"
"v_and_b32 v87, 0xffff0000, v233\n"
"v_lshlrev_b32 v88, 16, v234\n"
"v_and_b32 v89, 0xffff0000, v234\n"
"v_lshlrev_b32 v90, 16, v235\n"
"v_and_b32 v91, 0xffff0000, v235\n"
"v_lshlrev_b32 v92, 16, v236\n"
"v_and_b32 v93, 0xffff0000, v236\n"
"v_lshlrev_b32 v94, 16, v237\n"
"v_and_b32 v95, 0xffff0000, v237\n"
"v_lshlrev_b32 v96, 16, v238\n"
"v_and_b32 v97, 0xffff0000, v238\n"
"v_lshlrev_b32 v98, 16, v239\n"
"v_and_b32 v99, 0xffff0000, v239\n"
"v_add_f32 v32, v32, v80\n"
"v_add_f32 v33, v33, v81\n"
"v_add_f32 v34, v34, v82\n"
"v_add_f32 v35, v35, v83\n"
"v_add_f32 v36, v36, v84\n"
"v_add_f32 v37, v37, v85\n"
"v_add_f32 v38, v38, v86\n"
"v_add_f32 v39, v39, v87\n"
"v_add_f32 v40, v40, v88\n"
"v_add_f32 v41, v41, v89\n"
"v_add_f32 v42, v42, v90\n"
"v_add_f32 v43, v43, v91\n"
"v_add_f32 v44, v44, v92\n"
"v_add_f32 v45, v45, v93\n"
"v_add_f32 v46, v46, v94\n"
"v_add_f32 v47, v47, v95\n"
"v_add_f32 v48, v48, v96\n"
"v_add_f32 v49, v49, v97\n"
"v_add_f32 v50, v50, v98\n"
"v_add_f32 v51, v51, v99\n"
"v_lshlrev_b32 v80, 16, v164\n"
"v_and_b32 v81, 0xffff0000, v164\n"
"v_lshlrev_b32 v82, 16, v165\n"
"v_and_b32 v83, 0xffff0000, v165\n"
"v_lshlrev_b32 v84, 16, v166\n"
"v_and_b32 v85, 0xffff0000, v166\n"
"v_lshlrev_b32 v86, 16, v167\n"
"v_and_b32 v87, 0xffff0000, v167\n"
"v_lshlrev_b32 v88, 16, v168\n"
"v_and_b32 v89, 0xffff0000, v168\n"
"v_lshlrev_b32 v90, 16, v169\n"
"v_and_b32 v91, 0xffff0000, v169\n"
"v_lshlrev_b32 v92, 16, v170\n"
"v_and_b32 v93, 0xffff0000, v170\n"
"v_lshlrev_b32 v94, 16, v171\n"
"v_and_b32 v95, 0xffff0000, v171\n"
"v_lshlrev_b32 v96, 16, v172\n"
"v_and_b32 v97, 0xffff0000, v172\n"
"v_lshlrev_b32 v98, 16, v173\n"
"v_and_b32 v99, 0xffff0000, v173\n"
"v_add_f32 v32, v32, v80\n"
"v_add_f32 v33, v33, v81\n"
"v_add_f32 v34, v34, v82\n"
"v_add_f32 v35, v35, v83\n"
"v_add_f32 v36, v36, v84\n"
"v_add_f32 v37, v37, v85\n"
"v_add_f32 v38, v38, v86\n"
"v_add_f32 v39, v39, v87\n"
"v_add_f32 v40, v40, v88\n"
"v_add_f32 v41, v41, v89\n"
"v_add_f32 v42, v42, v90\n"
"v_add_f32 v43, v43, v91\n"
"v_add_f32 v44, v44, v92\n"
"v_add_f32 v45, v45, v93\n"
"v_add_f32 v46, v46, v94\n"
"v_add_f32 v47, v47, v95\n"
"v_add_f32 v48, v48, v96\n"
"v_add_f32 v49, v49, v97\n"
"v_add_f32 v50, v50, v98\n"
"v_add_f32 v51, v51, v99\n"
"v_cvt_pk_bf16_f32 v32, 0, v32\n"
"v_cvt_pk_bf16_f32 v33, 0, v33\n"
"v_cvt_pk_bf16_f32 v34, 0, v34\n"
"v_cvt_pk_bf16_f32 v35, 0, v35\n"
"v_cvt_pk_bf16_f32 v36, 0, v36\n"
"v_cvt_pk_bf16_f32 v37, 0, v37\n"
"v_cvt_pk_bf16_f32 v38, 0, v38\n"
"v_cvt_pk_bf16_f32 v39, 0, v39\n"
"v_cvt_pk_bf16_f32 v40, 0, v40\n"
"v_cvt_pk_bf16_f32 v41, 0, v41\n"
"v_cvt_pk_bf16_f32 v42, 0, v42\n"
"v_cvt_pk_bf16_f32 v43, 0, v43\n"
"v_cvt_pk_bf16_f32 v44, 0, v44\n"
"v_cvt_pk_bf16_f32 v45, 0, v45\n"
"v_cvt_pk_bf16_f32 v46, 0, v46\n"
"v_cvt_pk_bf16_f32 v47, 0, v47\n"
"v_cvt_pk_bf16_f32 v48, 0, v48\n"
"v_cvt_pk_bf16_f32 v49, 0, v49\n"
"v_cvt_pk_bf16_f32 v50, 0, v50\n"
"v_cvt_pk_bf16_f32 v51, 0, v51\n"
"v_mul_f32 v32, v32, v112\n"
"v_mul_f32 v33, v33, v113\n"
"v_mul_f32 v34, v34, v114\n"
"v_mul_f32 v35, v35, v115\n"
"v_mul_f32 v36, v36, v116\n"
"v_mul_f32 v37, v37, v117\n"
"v_mul_f32 v38, v38, v118\n"
"v_mul_f32 v39, v39, v119\n"
"v_mul_f32 v40, v40, v120\n"
"v_mul_f32 v41, v41, v121\n"
"v_mul_f32 v42, v42, v122\n"
"v_mul_f32 v43, v43, v123\n"
"v_mul_f32 v44, v44, v124\n"
"v_mul_f32 v45, v45, v125\n"
"v_mul_f32 v46, v46, v126\n"
"v_mul_f32 v47, v47, v127\n"
"v_mul_f32 v48, v48, v128\n"
"v_mul_f32 v49, v49, v129\n"
"v_mul_f32 v50, v50, v130\n"
"v_mul_f32 v51, v51, v131\n"
"v_cvt_pk_bf16_f32 v32, 0, v32\n"
"v_cvt_pk_bf16_f32 v33, 0, v33\n"
"v_cvt_pk_bf16_f32 v34, 0, v34\n"
"v_cvt_pk_bf16_f32 v35, 0, v35\n"
"v_cvt_pk_bf16_f32 v36, 0, v36\n"
"v_cvt_pk_bf16_f32 v37, 0, v37\n"
"v_cvt_pk_bf16_f32 v38, 0, v38\n"
"v_cvt_pk_bf16_f32 v39, 0, v39\n"
"v_cvt_pk_bf16_f32 v40, 0, v40\n"
"v_cvt_pk_bf16_f32 v41, 0, v41\n"
"v_cvt_pk_bf16_f32 v42, 0, v42\n"
"v_cvt_pk_bf16_f32 v43, 0, v43\n"
"v_cvt_pk_bf16_f32 v44, 0, v44\n"
"v_cvt_pk_bf16_f32 v45, 0, v45\n"
"v_cvt_pk_bf16_f32 v46, 0, v46\n"
"v_cvt_pk_bf16_f32 v47, 0, v47\n"
"v_cvt_pk_bf16_f32 v48, 0, v48\n"
"v_cvt_pk_bf16_f32 v49, 0, v49\n"
"v_cvt_pk_bf16_f32 v50, 0, v50\n"
"v_cvt_pk_bf16_f32 v51, 0, v51\n"
"v_add_f32 v32, v32, v144\n"
"v_add_f32 v33, v33, v145\n"
"v_add_f32 v34, v34, v146\n"
"v_add_f32 v35, v35, v147\n"
"v_add_f32 v36, v36, v148\n"
"v_add_f32 v37, v37, v149\n"
"v_add_f32 v38, v38, v150\n"
"v_add_f32 v39, v39, v151\n"
"v_add_f32 v40, v40, v152\n"
"v_add_f32 v41, v41, v153\n"
"v_add_f32 v42, v42, v154\n"
"v_add_f32 v43, v43, v155\n"
"v_add_f32 v44, v44, v156\n"
"v_add_f32 v45, v45, v157\n"
"v_add_f32 v46, v46, v158\n"
"v_add_f32 v47, v47, v159\n"
"v_add_f32 v48, v48, v160\n"
"v_add_f32 v49, v49, v161\n"
"v_add_f32 v50, v50, v162\n"
"v_add_f32 v51, v51, v163\n"
"v_cvt_pk_bf16_f32 v80, v32, v33\n"
"v_cvt_pk_bf16_f32 v81, v34, v35\n"
"v_cvt_pk_bf16_f32 v82, v36, v37\n"
"v_cvt_pk_bf16_f32 v83, v38, v39\n"
"s_nop 0\n"
"v_permlane16_swap_b32 v80, v82\n"
"v_permlane16_swap_b32 v81, v83\n"
"buffer_store_dwordx4 v[80:83], v26, s[72:75], s67 offen offset:0 sc1\n"
"v_cvt_pk_bf16_f32 v84, v40, v41\n"
"v_cvt_pk_bf16_f32 v85, v42, v43\n"
"v_cvt_pk_bf16_f32 v86, v44, v45\n"
"v_cvt_pk_bf16_f32 v87, v46, v47\n"
"s_nop 0\n"
"v_permlane16_swap_b32 v84, v86\n"
"v_permlane16_swap_b32 v85, v87\n"
"buffer_store_dwordx4 v[84:87], v26, s[72:75], s67 offen offset:64 sc1\n"
"v_cvt_pk_bf16_f32 v88, v48, v49\n"
"v_cvt_pk_bf16_f32 v89, v50, v51\n"
"buffer_store_dwordx2 v[88:89], v6, s[72:75], s67 offen offset:128 sc1\n"
"s_endpgm\n"
".Lspin0:\n"
"s_mov_b32 s101, 0\n"
".Lspinl0:\n"
"s_sleep 1\n"
"buffer_load_dword v252, off, s[36:39], s15 offset:0 sc1\n"
"s_waitcnt vmcnt(0)\n"
"s_add_u32 s101, s101, 1\n"
"v_cmp_gt_u32 s[92:93], 32, v252\n"
"s_nop 0\n"
"s_cmp_eq_u64 s[92:93], 0\n"
"s_cbranch_scc1 .Lback0\n"
"s_cmp_lt_u32 s101, 200000\n"
"s_cbranch_scc1 .Lspinl0\n"
"s_branch .Lback0\n"
".Lspin1:\n"
"s_mov_b32 s101, 0\n"
".Lspinl1:\n"
"s_sleep 1\n"
"buffer_load_dword v252, v255, s[36:39], s90 offen offset:0 sc1\n"
"s_waitcnt vmcnt(0)\n"
"s_add_u32 s101, s101, 1\n"
"v_cmp_gt_u32 s[92:93], 1, v252\n"
"s_nop 0\n"
"s_cmp_eq_u64 s[92:93], 0\n"
"s_cbranch_scc1 .Lback1\n"
"s_cmp_lt_u32 s101, 200000\n"
"s_cbranch_scc1 .Lspinl1\n"
"s_branch .Lback1\n"
".Lspin2:\n"
"s_mov_b32 s101, 0\n"
".Lspinl2:\n"
"s_sleep 1\n"
"buffer_load_dword v253, off, s[36:39], s14 offset:3072 sc1\n"
"s_waitcnt vmcnt(0)\n"
"s_add_u32 s101, s101, 1\n"
"v_cmp_gt_u32 s[92:93], 8, v253\n"
"s_nop 0\n"
"s_cmp_eq_u64 s[92:93], 0\n"
"s_cbranch_scc1 .Lback2\n"
"s_cmp_lt_u32 s101, 200000\n"
"s_cbranch_scc1 .Lspinl2\n"
"s_branch .Lback2\n"
".Lspin3:\n"
"s_mov_b32 s101, 0\n"
".Lspinl3:\n"
"s_sleep 1\n"
"buffer_load_dword v7, off, s[36:39], s14 offset:0 sc1\n"
"s_waitcnt vmcnt(0)\n"
"s_add_u32 s101, s101, 1\n"
"v_cmp_gt_u32 s[92:93], 8, v7\n"
"s_nop 0\n"
"s_cmp_eq_u64 s[92:93], 0\n"
"s_cbranch_scc1 .Lback3\n"
"s_cmp_lt_u32 s101, 200000\n"
"s_cbranch_scc1 .Lspinl3\n"
"s_branch .Lback3\n"
".Lspin4:\n"
"s_mov_b32 s101, 0\n"
".Lspinl4:\n"
"s_sleep 1\n"
"buffer_load_dword v7, off, s[36:39], s14 offset:1024 sc1\n"
"s_waitcnt vmcnt(0)\n"
"s_add_u32 s101, s101, 1\n"
"v_cmp_gt_u32 s[92:93], 8, v7\n"
"s_nop 0\n"
"s_cmp_eq_u64 s[92:93], 0\n"
"s_cbranch_scc1 .Lback4\n"
"s_cmp_lt_u32 s101, 200000\n"
"s_cbranch_scc1 .Lspinl4\n"
"s_branch .Lback4\n"
".Lspin5:\n"
"s_mov_b32 s101, 0\n"
".Lspinl5:\n"
"s_sleep 1\n"
"buffer_load_dword v7, off, s[36:39], s14 offset:2048 sc1\n"
"s_waitcnt vmcnt(0)\n"
"s_add_u32 s101, s101, 1\n"
"v_cmp_gt_u32 s[92:93], 8, v7\n"
"s_nop 0\n"
"s_cmp_eq_u64 s[92:93], 0\n"
"s_cbranch_scc1 .Lback5\n"
"s_cmp_lt_u32 s101, 200000\n"
"s_cbranch_scc1 .Lspinl5\n"
"s_branch .Lback5\n"
".Lspin6:\n"
"s_mov_b32 s101, 0\n"
".Lspinl6:\n"
"s_sleep 1\n"
"buffer_load_dword v27, off, s[36:39], s16 offset:0 sc1\n"
"s_waitcnt vmcnt(0)\n"
"s_add_u32 s101, s101, 1\n"
"v_cmp_gt_u32 s[92:93], 4, v27\n"
"s_nop 0\n"
"s_cmp_eq_u64 s[92:93], 0\n"
"s_cbranch_scc1 .Lback6\n"
"s_cmp_lt_u32 s101, 200000\n"
"s_cbranch_scc1 .Lspinl6\n"
"s_branch .Lback6\n"
".Lslow:\n"
"s_nop 7\n"
"s_nop 7\n"
"s_waitcnt vmcnt(0) lgkmcnt(0)\n"
"s_mov_b32 s93, m0\n"
"v_add_u32 v244, -24576, v16\n"
"v_add_u32 v245, -24576, v17\n"
"v_add_u32 v246, 49152, v20\n"
"v_add_u32 v247, 49152, v21\n"
"v_add_u32 v248, 49152, v22\n"
"v_add_u32 v249, 49152, v23\n"
"v_mov_b32 v28, 0xff800000\n"
"v_mov_b32 v29, 0xff800000\n"
"v_mov_b32 v30, 0xff800000\n"
"v_mov_b32 v31, 0xff800000\n"
"s_mov_b32 s84, s83\n"
"s_mov_b32 s92, 0\n"
".Lslow1:\n"
"s_barrier\n"
"s_add_u32 m0, s83, 49152\n"
"s_nop 0\n"
"buffer_load_dwordx4 v8, s[44:47], s84 offen offset:0 lds\n"
"buffer_load_dwordx4 v9, s[44:47], s84 offen offset:3072 lds\n"
"s_add_u32 m0, s83, 50048\n"
"s_nop 0\n"
"buffer_load_dwordx4 v8, s[44:47], s84 offen offset:128 lds\n"
"buffer_load_dwordx4 v9, s[44:47], s84 offen offset:3200 lds\n"
"s_add_u32 m0, s83, 50944\n"
"s_nop 0\n"
"buffer_load_dwordx4 v8, s[44:47], s84 offen offset:256 lds\n"
"buffer_load_dwordx4 v9, s[44:47], s84 offen offset:3328 lds\n"
"s_waitcnt vmcnt(0)\n"
"s_barrier\n"
"s_mov_b32 s87, 0\n"
".Lslow1k:\n"
"ds_read_b128 v[144:147], v244 offset:0\n"
"ds_read_b128 v[148:151], v245 offset:0\n"
"ds_read_b128 v[152:155], v244 offset:1024\n"
"ds_read_b128 v[156:159], v245 offset:1024\n"
"ds_read_b128 v[160:163], v244 offset:2048\n"
"ds_read_b128 v[164:167], v245 offset:2048\n"
"s_waitcnt lgkmcnt(0)\n"
"v_mfma_f32_16x16x32_bf16 v[216:219], v[144:147], v[32:35], 0\n"
"v_mfma_f32_16x16x32_bf16 v[216:219], v[148:151], v[36:39], v[216:219]\n"
"v_mfma_f32_16x16x32_bf16 v[216:219], v[152:155], v[40:43], v[216:219]\n"
"v_mfma_f32_16x16x32_bf16 v[216:219], v[156:159], v[44:47], v[216:219]\n"
"v_mfma_f32_16x16x32_bf16 v[216:219], v[160:163], v[48:51], v[216:219]\n"
"v_mfma_f32_16x16x32_bf16 v[216:219], v[164:167], v[52:55], v[216:219]\n"
"v_mfma_f32_16x16x32_bf16 v[220:223], v[144:147], v[56:59], 0\n"
"v_mfma_f32_16x16x32_bf16 v[220:223], v[148:151], v[60:63], v[220:223]\n"
"v_mfma_f32_16x16x32_bf16 v[220:223], v[152:155], v[64:67], v[220:223]\n"
"v_mfma_f32_16x16x32_bf16 v[220:223], v[156:159], v[68:71], v[220:223]\n"
"v_mfma_f32_16x16x32_bf16 v[220:223], v[160:163], v[72:75], v[220:223]\n"
"v_mfma_f32_16x16x32_bf16 v[220:223], v[164:167], v[76:79], v[220:223]\n"
"v_mfma_f32_16x16x32_bf16 v[224:227], v[144:147], a[208:211], 0\n"
"v_mfma_f32_16x16x32_bf16 v[224:227], v[148:151], a[212:215], v[224:227]\n"
"v_mfma_f32_16x16x32_bf16 v[224:227], v[152:155], a[216:219], v[224:227]\n"
"v_mfma_f32_16x16x32_bf16 v[224:227], v[156:159], a[220:223], v[224:227]\n"
"v_mfma_f32_16x16x32_bf16 v[224:227], v[160:163], a[224:227], v[224:227]\n"
"v_mfma_f32_16x16x32_bf16 v[224:227], v[164:167], a[228:231], v[224:227]\n"
"v_mfma_f32_16x16x32_bf16 v[228:231], v[144:147], a[232:235], 0\n"
"v_mfma_f32_16x16x32_bf16 v[228:231], v[148:151], a[236:239], v[228:231]\n"
"v_mfma_f32_16x16x32_bf16 v[228:231], v[152:155], a[240:243], v[228:231]\n"
"v_mfma_f32_16x16x32_bf16 v[228:231], v[156:159], a[244:247], v[228:231]\n"
"v_mfma_f32_16x16x32_bf16 v[228:231], v[160:163], a[248:251], v[228:231]\n"
"v_mfma_f32_16x16x32_bf16 v[228:231], v[164:167], a[252:255], v[228:231]\n"
"v_max3_f32 v28, v28, v216, v217\n"
"v_max3_f32 v28, v28, v218, v219\n"
"v_max3_f32 v29, v29, v220, v221\n"
"v_max3_f32 v29, v29, v222, v223\n"
"v_max3_f32 v30, v30, v224, v225\n"
"v_max3_f32 v30, v30, v226, v227\n"
"s_nop 1\n"
"v_max3_f32 v31, v31, v228, v229\n"
"v_max3_f32 v31, v31, v230, v231\n"
"v_add_u32 v244, 6144, v244\n"
"v_add_u32 v245, 6144, v245\n"
"s_add_u32 s87, s87, 1\n"
"s_cmp_lt_u32 s87, 4\n"
"s_cbranch_scc1 .Lslow1k\n"
"v_subrev_u32 v244, 24576, v244\n"
"v_subrev_u32 v245, 24576, v245\n"
"s_add_u32 s84, s84, 24576\n"
"s_add_u32 s92, s92, 1\n"
"s_cmp_lt_u32 s92, 8\n"
"s_nop 7\n"
"s_cbranch_scc1 .Lslow1\n"
"v_mov_b32 v250, v28\n"
"s_nop 1\n"
"v_permlane32_swap_b32 v28, v250\n"
"v_max_f32 v28, v28, v250\n"
"v_mov_b32 v250, v28\n"
"s_nop 1\n"
"v_permlane16_swap_b32 v28, v250\n"
"v_max_f32 v28, v28, v250\n"
"v_cvt_pk_bf16_f32 v28, 0, v28\n"
"v_mul_f32 v28, s85, v28\n"
"v_cvt_pk_bf16_f32 v28, 0, v28\n"
"v_mul_f32 v28, s86, v28\n"
"v_mov_b32 v250, v29\n"
"s_nop 1\n"
"v_permlane32_swap_b32 v29, v250\n"
"v_max_f32 v29, v29, v250\n"
"v_mov_b32 v250, v29\n"
"s_nop 1\n"
"v_permlane16_swap_b32 v29, v250\n"
"v_max_f32 v29, v29, v250\n"
"v_cvt_pk_bf16_f32 v29, 0, v29\n"
"v_mul_f32 v29, s85, v29\n"
"v_cvt_pk_bf16_f32 v29, 0, v29\n"
"v_mul_f32 v29, s86, v29\n"
"v_mov_b32 v250, v30\n"
"s_nop 1\n"
"v_permlane32_swap_b32 v30, v250\n"
"v_max_f32 v30, v30, v250\n"
"v_mov_b32 v250, v30\n"
"s_nop 1\n"
"v_permlane16_swap_b32 v30, v250\n"
"v_max_f32 v30, v30, v250\n"
"v_cvt_pk_bf16_f32 v30, 0, v30\n"
"v_mul_f32 v30, s85, v30\n"
"v_cvt_pk_bf16_f32 v30, 0, v30\n"
"v_mul_f32 v30, s86, v30\n"
"v_mov_b32 v250, v31\n"
"s_nop 1\n"
"v_permlane32_swap_b32 v31, v250\n"
"v_max_f32 v31, v31, v250\n"
"v_mov_b32 v250, v31\n"
"s_nop 1\n"
"v_permlane16_swap_b32 v31, v250\n"
"v_max_f32 v31, v31, v250\n"
"v_cvt_pk_bf16_f32 v31, 0, v31\n"
"v_mul_f32 v31, s85, v31\n"
"v_cvt_pk_bf16_f32 v31, 0, v31\n"
"v_mul_f32 v31, s86, v31\n"
"v_accvgpr_write_b32 a0, 0\n"
"v_accvgpr_write_b32 a1, 0\n"
"v_accvgpr_write_b32 a2, 0\n"
"v_accvgpr_write_b32 a3, 0\n"
"v_accvgpr_write_b32 a4, 0\n"
"v_accvgpr_write_b32 a5, 0\n"
"v_accvgpr_write_b32 a6, 0\n"
"v_accvgpr_write_b32 a7, 0\n"
"v_accvgpr_write_b32 a8, 0\n"
"v_accvgpr_write_b32 a9, 0\n"
"v_accvgpr_write_b32 a10, 0\n"
"v_accvgpr_write_b32 a11, 0\n"
"v_accvgpr_write_b32 a12, 0\n"
"v_accvgpr_write_b32 a13, 0\n"
"v_accvgpr_write_b32 a14, 0\n"
"v_accvgpr_write_b32 a15, 0\n"
"v_accvgpr_write_b32 a16, 0\n"
"v_accvgpr_write_b32 a17, 0\n"
"v_accvgpr_write_b32 a18, 0\n"
"v_accvgpr_write_b32 a19, 0\n"
"v_accvgpr_write_b32 a20, 0\n"
"v_accvgpr_write_b32 a21, 0\n"
"v_accvgpr_write_b32 a22, 0\n"
"v_accvgpr_write_b32 a23, 0\n"
"v_accvgpr_write_b32 a24, 0\n"
"v_accvgpr_write_b32 a25, 0\n"
"v_accvgpr_write_b32 a26, 0\n"
"v_accvgpr_write_b32 a27, 0\n"
"v_accvgpr_write_b32 a28, 0\n"
"v_accvgpr_write_b32 a29, 0\n"
"v_accvgpr_write_b32 a30, 0\n"
"v_accvgpr_write_b32 a31, 0\n"
"v_accvgpr_write_b32 a32, 0\n"
"v_accvgpr_write_b32 a33, 0\n"
"v_accvgpr_write_b32 a34, 0\n"
"v_accvgpr_write_b32 a35, 0\n"
"v_accvgpr_write_b32 a36, 0\n"
"v_accvgpr_write_b32 a37, 0\n"
"v_accvgpr_write_b32 a38, 0\n"
"v_accvgpr_write_b32 a39, 0\n"
"v_accvgpr_write_b32 a40, 0\n"
"v_accvgpr_write_b32 a41, 0\n"
"v_accvgpr_write_b32 a42, 0\n"
"v_accvgpr_write_b32 a43, 0\n"
"v_accvgpr_write_b32 a44, 0\n"
"v_accvgpr_write_b32 a45, 0\n"
"v_accvgpr_write_b32 a46, 0\n"
"v_accvgpr_write_b32 a47, 0\n"
"v_accvgpr_write_b32 a48, 0\n"
"v_accvgpr_write_b32 a49, 0\n"
"v_accvgpr_write_b32 a50, 0\n"
"v_accvgpr_write_b32 a51, 0\n"
"v_accvgpr_write_b32 a52, 0\n"
"v_accvgpr_write_b32 a53, 0\n"
"v_accvgpr_write_b32 a54, 0\n"
"v_accvgpr_write_b32 a55, 0\n"
"v_accvgpr_write_b32 a56, 0\n"
"v_accvgpr_write_b32 a57, 0\n"
"v_accvgpr_write_b32 a58, 0\n"
"v_accvgpr_write_b32 a59, 0\n"
"v_accvgpr_write_b32 a60, 0\n"
"v_accvgpr_write_b32 a61, 0\n"
"v_accvgpr_write_b32 a62, 0\n"
"v_accvgpr_write_b32 a63, 0\n"
"v_accvgpr_write_b32 a64, 0\n"
"v_accvgpr_write_b32 a65, 0\n"
"v_accvgpr_write_b32 a66, 0\n"
"v_accvgpr_write_b32 a67, 0\n"
"v_accvgpr_write_b32 a68, 0\n"
"v_accvgpr_write_b32 a69, 0\n"
"v_accvgpr_write_b32 a70, 0\n"
"v_accvgpr_write_b32 a71, 0\n"
"v_accvgpr_write_b32 a72, 0\n"
"v_accvgpr_write_b32 a73, 0\n"
"v_accvgpr_write_b32 a74, 0\n"
"v_accvgpr_write_b32 a75, 0\n"
"v_accvgpr_write_b32 a76, 0\n"
"v_accvgpr_write_b32 a77, 0\n"
"v_accvgpr_write_b32 a78, 0\n"
"v_accvgpr_write_b32 a79, 0\n"
"v_accvgpr_write_b32 a80, 0\n"
"v_accvgpr_write_b32 a81, 0\n"
"v_accvgpr_write_b32 a82, 0\n"
"v_accvgpr_write_b32 a83, 0\n"
"v_accvgpr_write_b32 a84, 0\n"
"v_accvgpr_write_b32 a85, 0\n"
"v_accvgpr_write_b32 a86, 0\n"
"v_accvgpr_write_b32 a87, 0\n"
"v_accvgpr_write_b32 a88, 0\n"
"v_accvgpr_write_b32 a89, 0\n"
"v_accvgpr_write_b32 a90, 0\n"
"v_accvgpr_write_b32 a91, 0\n"
"v_accvgpr_write_b32 a92, 0\n"
"v_accvgpr_write_b32 a93, 0\n"
"v_accvgpr_write_b32 a94, 0\n"
"v_accvgpr_write_b32 a95, 0\n"
"v_accvgpr_write_b32 a96, 0\n"
"v_accvgpr_write_b32 a97, 0\n"
"v_accvgpr_write_b32 a98, 0\n"
"v_accvgpr_write_b32 a99, 0\n"
"v_accvgpr_write_b32 a100, 0\n"
"v_accvgpr_write_b32 a101, 0\n"
"v_accvgpr_write_b32 a102, 0\n"
"v_accvgpr_write_b32 a103, 0\n"
"v_accvgpr_write_b32 a104, 0\n"
"v_accvgpr_write_b32 a105, 0\n"
"v_accvgpr_write_b32 a106, 0\n"
"v_accvgpr_write_b32 a107, 0\n"
"v_accvgpr_write_b32 a108, 0\n"
"v_accvgpr_write_b32 a109, 0\n"
"v_accvgpr_write_b32 a110, 0\n"
"v_accvgpr_write_b32 a111, 0\n"
"v_accvgpr_write_b32 a112, 0\n"
"v_accvgpr_write_b32 a113, 0\n"
"v_accvgpr_write_b32 a114, 0\n"
"v_accvgpr_write_b32 a115, 0\n"
"v_accvgpr_write_b32 a116, 0\n"
"v_accvgpr_write_b32 a117, 0\n"
"v_accvgpr_write_b32 a118, 0\n"
"v_accvgpr_write_b32 a119, 0\n"
"v_accvgpr_write_b32 a120, 0\n"
"v_accvgpr_write_b32 a121, 0\n"
"v_accvgpr_write_b32 a122, 0\n"
"v_accvgpr_write_b32 a123, 0\n"
"v_accvgpr_write_b32 a124, 0\n"
"v_accvgpr_write_b32 a125, 0\n"
"v_accvgpr_write_b32 a126, 0\n"
"v_accvgpr_write_b32 a127, 0\n"
"v_accvgpr_write_b32 a128, 0\n"
"v_accvgpr_write_b32 a129, 0\n"
"v_accvgpr_write_b32 a130, 0\n"
"v_accvgpr_write_b32 a131, 0\n"
"v_accvgpr_write_b32 a132, 0\n"
"v_accvgpr_write_b32 a133, 0\n"
"v_accvgpr_write_b32 a134, 0\n"
"v_accvgpr_write_b32 a135, 0\n"
"v_accvgpr_write_b32 a136, 0\n"
"v_accvgpr_write_b32 a137, 0\n"
"v_accvgpr_write_b32 a138, 0\n"
"v_accvgpr_write_b32 a139, 0\n"
"v_accvgpr_write_b32 a140, 0\n"
"v_accvgpr_write_b32 a141, 0\n"
"v_accvgpr_write_b32 a142, 0\n"
"v_accvgpr_write_b32 a143, 0\n"
"v_accvgpr_write_b32 a144, 0\n"
"v_accvgpr_write_b32 a145, 0\n"
"v_accvgpr_write_b32 a146, 0\n"
"v_accvgpr_write_b32 a147, 0\n"
"v_accvgpr_write_b32 a148, 0\n"
"v_accvgpr_write_b32 a149, 0\n"
"v_accvgpr_write_b32 a150, 0\n"
"v_accvgpr_write_b32 a151, 0\n"
"v_accvgpr_write_b32 a152, 0\n"
"v_accvgpr_write_b32 a153, 0\n"
"v_accvgpr_write_b32 a154, 0\n"
"v_accvgpr_write_b32 a155, 0\n"
"v_accvgpr_write_b32 a156, 0\n"
"v_accvgpr_write_b32 a157, 0\n"
"v_accvgpr_write_b32 a158, 0\n"
"v_accvgpr_write_b32 a159, 0\n"
"v_accvgpr_write_b32 a160, 0\n"
"v_accvgpr_write_b32 a161, 0\n"
"v_accvgpr_write_b32 a162, 0\n"
"v_accvgpr_write_b32 a163, 0\n"
"v_accvgpr_write_b32 a164, 0\n"
"v_accvgpr_write_b32 a165, 0\n"
"v_accvgpr_write_b32 a166, 0\n"
"v_accvgpr_write_b32 a167, 0\n"
"v_accvgpr_write_b32 a168, 0\n"
"v_accvgpr_write_b32 a169, 0\n"
"v_accvgpr_write_b32 a170, 0\n"
"v_accvgpr_write_b32 a171, 0\n"
"v_accvgpr_write_b32 a172, 0\n"
"v_accvgpr_write_b32 a173, 0\n"
"v_accvgpr_write_b32 a174, 0\n"
"v_accvgpr_write_b32 a175, 0\n"
"v_accvgpr_write_b32 a176, 0\n"
"v_accvgpr_write_b32 a177, 0\n"
"v_accvgpr_write_b32 a178, 0\n"
"v_accvgpr_write_b32 a179, 0\n"
"v_accvgpr_write_b32 a180, 0\n"
"v_accvgpr_write_b32 a181, 0\n"
"v_accvgpr_write_b32 a182, 0\n"
"v_accvgpr_write_b32 a183, 0\n"
"v_accvgpr_write_b32 a184, 0\n"
"v_accvgpr_write_b32 a185, 0\n"
"v_accvgpr_write_b32 a186, 0\n"
"v_accvgpr_write_b32 a187, 0\n"
"v_accvgpr_write_b32 a188, 0\n"
"v_accvgpr_write_b32 a189, 0\n"
"v_accvgpr_write_b32 a190, 0\n"
"v_accvgpr_write_b32 a191, 0\n"
"v_accvgpr_write_b32 a192, 0\n"
"v_accvgpr_write_b32 a193, 0\n"
"v_accvgpr_write_b32 a194, 0\n"
"v_accvgpr_write_b32 a195, 0\n"
"v_accvgpr_write_b32 a196, 0\n"
"v_accvgpr_write_b32 a197, 0\n"
"v_accvgpr_write_b32 a198, 0\n"
"v_accvgpr_write_b32 a199, 0\n"
"v_accvgpr_write_b32 a200, 0\n"
"v_accvgpr_write_b32 a201, 0\n"
"v_accvgpr_write_b32 a202, 0\n"
"v_accvgpr_write_b32 a203, 0\n"
"v_accvgpr_write_b32 a204, 0\n"
"v_accvgpr_write_b32 a205, 0\n"
"v_accvgpr_write_b32 a206, 0\n"
"v_accvgpr_write_b32 a207, 0\n"
"s_mov_b32 s84, s83\n"
"s_mov_b32 s92, 0\n"
".Lslow2:\n"
"s_barrier\n"
"s_add_u32 m0, s83, 49152\n"
"s_nop 0\n"
"buffer_load_dwordx4 v8, s[44:47], s84 offen offset:0 lds\n"
"buffer_load_dwordx4 v9, s[44:47], s84 offen offset:3072 lds\n"
"s_add_u32 m0, s83, 50048\n"
"s_nop 0\n"
"buffer_load_dwordx4 v8, s[44:47], s84 offen offset:128 lds\n"
"buffer_load_dwordx4 v9, s[44:47], s84 offen offset:3200 lds\n"
"s_add_u32 m0, s83, 50944\n"
"s_nop 0\n"
"buffer_load_dwordx4 v8, s[44:47], s84 offen offset:256 lds\n"
"buffer_load_dwordx4 v9, s[44:47], s84 offen offset:3328 lds\n"
"s_waitcnt vmcnt(0)\n"
"s_barrier\n"
"ds_read_b128 v[144:147], v244 offset:0\n"
"ds_read_b128 v[148:151], v245 offset:0\n"
"ds_read_b128 v[152:155], v244 offset:1024\n"
"ds_read_b128 v[156:159], v245 offset:1024\n"
"ds_read_b128 v[160:163], v244 offset:2048\n"
"ds_read_b128 v[164:167], v245 offset:2048\n"
"s_waitcnt lgkmcnt(0)\n"
"v_mfma_f32_16x16x32_bf16 v[216:219], v[144:147], v[32:35], 0\n"
"v_mfma_f32_16x16x32_bf16 v[216:219], v[148:151], v[36:39], v[216:219]\n"
"v_mfma_f32_16x16x32_bf16 v[216:219], v[152:155], v[40:43], v[216:219]\n"
"v_mfma_f32_16x16x32_bf16 v[216:219], v[156:159], v[44:47], v[216:219]\n"
"v_mfma_f32_16x16x32_bf16 v[216:219], v[160:163], v[48:51], v[216:219]\n"
"v_mfma_f32_16x16x32_bf16 v[216:219], v[164:167], v[52:55], v[216:219]\n"
"v_mfma_f32_16x16x32_bf16 v[220:223], v[144:147], v[56:59], 0\n"
"v_mfma_f32_16x16x32_bf16 v[220:223], v[148:151], v[60:63], v[220:223]\n"
"v_mfma_f32_16x16x32_bf16 v[220:223], v[152:155], v[64:67], v[220:223]\n"
"v_mfma_f32_16x16x32_bf16 v[220:223], v[156:159], v[68:71], v[220:223]\n"
"v_mfma_f32_16x16x32_bf16 v[220:223], v[160:163], v[72:75], v[220:223]\n"
"v_mfma_f32_16x16x32_bf16 v[220:223], v[164:167], v[76:79], v[220:223]\n"
"v_mfma_f32_16x16x32_bf16 v[224:227], v[144:147], a[208:211], 0\n"
"v_mfma_f32_16x16x32_bf16 v[224:227], v[148:151], a[212:215], v[224:227]\n"
"v_mfma_f32_16x16x32_bf16 v[224:227], v[152:155], a[216:219], v[224:227]\n"
"v_mfma_f32_16x16x32_bf16 v[224:227], v[156:159], a[220:223], v[224:227]\n"
"v_mfma_f32_16x16x32_bf16 v[224:227], v[160:163], a[224:227], v[224:227]\n"
"v_mfma_f32_16x16x32_bf16 v[224:227], v[164:167], a[228:231], v[224:227]\n"
"v_mfma_f32_16x16x32_bf16 v[228:231], v[144:147], a[232:235], 0\n"
"v_mfma_f32_16x16x32_bf16 v[228:231], v[148:151], a[236:239], v[228:231]\n"
"v_mfma_f32_16x16x32_bf16 v[228:231], v[152:155], a[240:243], v[228:231]\n"
"v_mfma_f32_16x16x32_bf16 v[228:231], v[156:159], a[244:247], v[228:231]\n"
"v_mfma_f32_16x16x32_bf16 v[228:231], v[160:163], a[248:251], v[228:231]\n"
"v_mfma_f32_16x16x32_bf16 v[228:231], v[164:167], a[252:255], v[228:231]\n"
"v_cvt_pk_bf16_f32 v216, 0, v216\n"
"v_cvt_pk_bf16_f32 v217, 0, v217\n"
"v_cvt_pk_bf16_f32 v218, 0, v218\n"
"v_cvt_pk_bf16_f32 v219, 0, v219\n"
"v_cvt_pk_bf16_f32 v220, 0, v220\n"
"v_cvt_pk_bf16_f32 v221, 0, v221\n"
"v_cvt_pk_bf16_f32 v222, 0, v222\n"
"v_cvt_pk_bf16_f32 v223, 0, v223\n"
"v_cvt_pk_bf16_f32 v224, 0, v224\n"
"v_cvt_pk_bf16_f32 v225, 0, v225\n"
"v_cvt_pk_bf16_f32 v226, 0, v226\n"
"v_cvt_pk_bf16_f32 v227, 0, v227\n"
"v_cvt_pk_bf16_f32 v228, 0, v228\n"
"v_cvt_pk_bf16_f32 v229, 0, v229\n"
"v_cvt_pk_bf16_f32 v230, 0, v230\n"
"v_cvt_pk_bf16_f32 v231, 0, v231\n"
"v_mul_f32 v216, s85, v216\n"
"v_mul_f32 v217, s85, v217\n"
"v_mul_f32 v218, s85, v218\n"
"v_mul_f32 v219, s85, v219\n"
"v_mul_f32 v220, s85, v220\n"
"v_mul_f32 v221, s85, v221\n"
"v_mul_f32 v222, s85, v222\n"
"v_mul_f32 v223, s85, v223\n"
"v_mul_f32 v224, s85, v224\n"
"v_mul_f32 v225, s85, v225\n"
"v_mul_f32 v226, s85, v226\n"
"v_mul_f32 v227, s85, v227\n"
"v_mul_f32 v228, s85, v228\n"
"v_mul_f32 v229, s85, v229\n"
"v_mul_f32 v230, s85, v230\n"
"v_mul_f32 v231, s85, v231\n"
"v_cvt_pk_bf16_f32 v216, 0, v216\n"
"v_cvt_pk_bf16_f32 v217, 0, v217\n"
"v_cvt_pk_bf16_f32 v218, 0, v218\n"
"v_cvt_pk_bf16_f32 v219, 0, v219\n"
"v_cvt_pk_bf16_f32 v220, 0, v220\n"
"v_cvt_pk_bf16_f32 v221, 0, v221\n"
"v_cvt_pk_bf16_f32 v222, 0, v222\n"
"v_cvt_pk_bf16_f32 v223, 0, v223\n"
"v_cvt_pk_bf16_f32 v224, 0, v224\n"
"v_cvt_pk_bf16_f32 v225, 0, v225\n"
"v_cvt_pk_bf16_f32 v226, 0, v226\n"
"v_cvt_pk_bf16_f32 v227, 0, v227\n"
"v_cvt_pk_bf16_f32 v228, 0, v228\n"
"v_cvt_pk_bf16_f32 v229, 0, v229\n"
"v_cvt_pk_bf16_f32 v230, 0, v230\n"
"v_cvt_pk_bf16_f32 v231, 0, v231\n"
"v_fma_f32 v216, v216, s86, -v28\n"
"v_fma_f32 v217, v217, s86, -v28\n"
"v_fma_f32 v218, v218, s86, -v28\n"
"v_fma_f32 v219, v219, s86, -v28\n"
"v_fma_f32 v220, v220, s86, -v29\n"
"v_fma_f32 v221, v221, s86, -v29\n"
"v_fma_f32 v222, v222, s86, -v29\n"
"v_fma_f32 v223, v223, s86, -v29\n"
"v_fma_f32 v224, v224, s86, -v30\n"
"v_fma_f32 v225, v225, s86, -v30\n"
"v_fma_f32 v226, v226, s86, -v30\n"
"v_fma_f32 v227, v227, s86, -v30\n"
"v_fma_f32 v228, v228, s86, -v31\n"
"v_fma_f32 v229, v229, s86, -v31\n"
"v_fma_f32 v230, v230, s86, -v31\n"
"v_fma_f32 v231, v231, s86, -v31\n"
"v_exp_f32 v216, v216\n"
"v_exp_f32 v217, v217\n"
"v_exp_f32 v218, v218\n"
"v_exp_f32 v219, v219\n"
"v_exp_f32 v220, v220\n"
"v_exp_f32 v221, v221\n"
"v_exp_f32 v222, v222\n"
"v_exp_f32 v223, v223\n"
"v_exp_f32 v224, v224\n"
"v_exp_f32 v225, v225\n"
"v_exp_f32 v226, v226\n"
"v_exp_f32 v227, v227\n"
"v_exp_f32 v228, v228\n"
"v_exp_f32 v229, v229\n"
"v_exp_f32 v230, v230\n"
"v_exp_f32 v231, v231\n"
"v_cvt_pk_bf16_f32 v80, v216, v217\n"
"v_cvt_pk_bf16_f32 v81, v218, v219\n"
"v_cvt_pk_bf16_f32 v84, v220, v221\n"
"v_cvt_pk_bf16_f32 v85, v222, v223\n"
"v_cvt_pk_bf16_f32 v88, v224, v225\n"
"v_cvt_pk_bf16_f32 v89, v226, v227\n"
"v_cvt_pk_bf16_f32 v92, v228, v229\n"
"v_cvt_pk_bf16_f32 v93, v230, v231\n"
"ds_read_b128 v[144:147], v244 offset:6144\n"
"ds_read_b128 v[148:151], v245 offset:6144\n"
"ds_read_b128 v[152:155], v244 offset:7168\n"
"ds_read_b128 v[156:159], v245 offset:7168\n"
"ds_read_b128 v[160:163], v244 offset:8192\n"
"ds_read_b128 v[164:167], v245 offset:8192\n"
"s_waitcnt lgkmcnt(0)\n"
"v_mfma_f32_16x16x32_bf16 v[216:219], v[144:147], v[32:35], 0\n"
"v_mfma_f32_16x16x32_bf16 v[216:219], v[148:151], v[36:39], v[216:219]\n"
"v_mfma_f32_16x16x32_bf16 v[216:219], v[152:155], v[40:43], v[216:219]\n"
"v_mfma_f32_16x16x32_bf16 v[216:219], v[156:159], v[44:47], v[216:219]\n"
"v_mfma_f32_16x16x32_bf16 v[216:219], v[160:163], v[48:51], v[216:219]\n"
"v_mfma_f32_16x16x32_bf16 v[216:219], v[164:167], v[52:55], v[216:219]\n"
"v_mfma_f32_16x16x32_bf16 v[220:223], v[144:147], v[56:59], 0\n"
"v_mfma_f32_16x16x32_bf16 v[220:223], v[148:151], v[60:63], v[220:223]\n"
"v_mfma_f32_16x16x32_bf16 v[220:223], v[152:155], v[64:67], v[220:223]\n"
"v_mfma_f32_16x16x32_bf16 v[220:223], v[156:159], v[68:71], v[220:223]\n"
"v_mfma_f32_16x16x32_bf16 v[220:223], v[160:163], v[72:75], v[220:223]\n"
"v_mfma_f32_16x16x32_bf16 v[220:223], v[164:167], v[76:79], v[220:223]\n"
"v_mfma_f32_16x16x32_bf16 v[224:227], v[144:147], a[208:211], 0\n"
"v_mfma_f32_16x16x32_bf16 v[224:227], v[148:151], a[212:215], v[224:227]\n"
"v_mfma_f32_16x16x32_bf16 v[224:227], v[152:155], a[216:219], v[224:227]\n"
"v_mfma_f32_16x16x32_bf16 v[224:227], v[156:159], a[220:223], v[224:227]\n"
"v_mfma_f32_16x16x32_bf16 v[224:227], v[160:163], a[224:227], v[224:227]\n"
"v_mfma_f32_16x16x32_bf16 v[224:227], v[164:167], a[228:231], v[224:227]\n"
"v_mfma_f32_16x16x32_bf16 v[228:231], v[144:147], a[232:235], 0\n"
"v_mfma_f32_16x16x32_bf16 v[228:231], v[148:151], a[236:239], v[228:231]\n"
"v_mfma_f32_16x16x32_bf16 v[228:231], v[152:155], a[240:243], v[228:231]\n"
"v_mfma_f32_16x16x32_bf16 v[228:231], v[156:159], a[244:247], v[228:231]\n"
"v_mfma_f32_16x16x32_bf16 v[228:231], v[160:163], a[248:251], v[228:231]\n"
"v_mfma_f32_16x16x32_bf16 v[228:231], v[164:167], a[252:255], v[228:231]\n"
"v_cvt_pk_bf16_f32 v216, 0, v216\n"
"v_cvt_pk_bf16_f32 v217, 0, v217\n"
"v_cvt_pk_bf16_f32 v218, 0, v218\n"
"v_cvt_pk_bf16_f32 v219, 0, v219\n"
"v_cvt_pk_bf16_f32 v220, 0, v220\n"
"v_cvt_pk_bf16_f32 v221, 0, v221\n"
"v_cvt_pk_bf16_f32 v222, 0, v222\n"
"v_cvt_pk_bf16_f32 v223, 0, v223\n"
"v_cvt_pk_bf16_f32 v224, 0, v224\n"
"v_cvt_pk_bf16_f32 v225, 0, v225\n"
"v_cvt_pk_bf16_f32 v226, 0, v226\n"
"v_cvt_pk_bf16_f32 v227, 0, v227\n"
"v_cvt_pk_bf16_f32 v228, 0, v228\n"
"v_cvt_pk_bf16_f32 v229, 0, v229\n"
"v_cvt_pk_bf16_f32 v230, 0, v230\n"
"v_cvt_pk_bf16_f32 v231, 0, v231\n"
"v_mul_f32 v216, s85, v216\n"
"v_mul_f32 v217, s85, v217\n"
"v_mul_f32 v218, s85, v218\n"
"v_mul_f32 v219, s85, v219\n"
"v_mul_f32 v220, s85, v220\n"
"v_mul_f32 v221, s85, v221\n"
"v_mul_f32 v222, s85, v222\n"
"v_mul_f32 v223, s85, v223\n"
"v_mul_f32 v224, s85, v224\n"
"v_mul_f32 v225, s85, v225\n"
"v_mul_f32 v226, s85, v226\n"
"v_mul_f32 v227, s85, v227\n"
"v_mul_f32 v228, s85, v228\n"
"v_mul_f32 v229, s85, v229\n"
"v_mul_f32 v230, s85, v230\n"
"v_mul_f32 v231, s85, v231\n"
"v_cvt_pk_bf16_f32 v216, 0, v216\n"
"v_cvt_pk_bf16_f32 v217, 0, v217\n"
"v_cvt_pk_bf16_f32 v218, 0, v218\n"
"v_cvt_pk_bf16_f32 v219, 0, v219\n"
"v_cvt_pk_bf16_f32 v220, 0, v220\n"
"v_cvt_pk_bf16_f32 v221, 0, v221\n"
"v_cvt_pk_bf16_f32 v222, 0, v222\n"
"v_cvt_pk_bf16_f32 v223, 0, v223\n"
"v_cvt_pk_bf16_f32 v224, 0, v224\n"
"v_cvt_pk_bf16_f32 v225, 0, v225\n"
"v_cvt_pk_bf16_f32 v226, 0, v226\n"
"v_cvt_pk_bf16_f32 v227, 0, v227\n"
"v_cvt_pk_bf16_f32 v228, 0, v228\n"
"v_cvt_pk_bf16_f32 v229, 0, v229\n"
"v_cvt_pk_bf16_f32 v230, 0, v230\n"
"v_cvt_pk_bf16_f32 v231, 0, v231\n"
"v_fma_f32 v216, v216, s86, -v28\n"
"v_fma_f32 v217, v217, s86, -v28\n"
"v_fma_f32 v218, v218, s86, -v28\n"
"v_fma_f32 v219, v219, s86, -v28\n"
"v_fma_f32 v220, v220, s86, -v29\n"
"v_fma_f32 v221, v221, s86, -v29\n"
"v_fma_f32 v222, v222, s86, -v29\n"
"v_fma_f32 v223, v223, s86, -v29\n"
"v_fma_f32 v224, v224, s86, -v30\n"
"v_fma_f32 v225, v225, s86, -v30\n"
"v_fma_f32 v226, v226, s86, -v30\n"
"v_fma_f32 v227, v227, s86, -v30\n"
"v_fma_f32 v228, v228, s86, -v31\n"
"v_fma_f32 v229, v229, s86, -v31\n"
"v_fma_f32 v230, v230, s86, -v31\n"
"v_fma_f32 v231, v231, s86, -v31\n"
"v_exp_f32 v216, v216\n"
"v_exp_f32 v217, v217\n"
"v_exp_f32 v218, v218\n"
"v_exp_f32 v219, v219\n"
"v_exp_f32 v220, v220\n"
"v_exp_f32 v221, v221\n"
"v_exp_f32 v222, v222\n"
"v_exp_f32 v223, v223\n"
"v_exp_f32 v224, v224\n"
"v_exp_f32 v225, v225\n"
"v_exp_f32 v226, v226\n"
"v_exp_f32 v227, v227\n"
"v_exp_f32 v228, v228\n"
"v_exp_f32 v229, v229\n"
"v_exp_f32 v230, v230\n"
"v_exp_f32 v231, v231\n"
"v_cvt_pk_bf16_f32 v82, v216, v217\n"
"v_cvt_pk_bf16_f32 v83, v218, v219\n"
"v_cvt_pk_bf16_f32 v86, v220, v221\n"
"v_cvt_pk_bf16_f32 v87, v222, v223\n"
"v_cvt_pk_bf16_f32 v90, v224, v225\n"
"v_cvt_pk_bf16_f32 v91, v226, v227\n"
"v_cvt_pk_bf16_f32 v94, v228, v229\n"
"v_cvt_pk_bf16_f32 v95, v230, v231\n"
"ds_read_b128 v[144:147], v244 offset:12288\n"
"ds_read_b128 v[148:151], v245 offset:12288\n"
"ds_read_b128 v[152:155], v244 offset:13312\n"
"ds_read_b128 v[156:159], v245 offset:13312\n"
"ds_read_b128 v[160:163], v244 offset:14336\n"
"ds_read_b128 v[164:167], v245 offset:14336\n"
"s_waitcnt lgkmcnt(0)\n"
"v_mfma_f32_16x16x32_bf16 v[216:219], v[144:147], v[32:35], 0\n"
"v_mfma_f32_16x16x32_bf16 v[216:219], v[148:151], v[36:39], v[216:219]\n"
"v_mfma_f32_16x16x32_bf16 v[216:219], v[152:155], v[40:43], v[216:219]\n"
"v_mfma_f32_16x16x32_bf16 v[216:219], v[156:159], v[44:47], v[216:219]\n"
"v_mfma_f32_16x16x32_bf16 v[216:219], v[160:163], v[48:51], v[216:219]\n"
"v_mfma_f32_16x16x32_bf16 v[216:219], v[164:167], v[52:55], v[216:219]\n"
"v_mfma_f32_16x16x32_bf16 v[220:223], v[144:147], v[56:59], 0\n"
"v_mfma_f32_16x16x32_bf16 v[220:223], v[148:151], v[60:63], v[220:223]\n"
"v_mfma_f32_16x16x32_bf16 v[220:223], v[152:155], v[64:67], v[220:223]\n"
"v_mfma_f32_16x16x32_bf16 v[220:223], v[156:159], v[68:71], v[220:223]\n"
"v_mfma_f32_16x16x32_bf16 v[220:223], v[160:163], v[72:75], v[220:223]\n"
"v_mfma_f32_16x16x32_bf16 v[220:223], v[164:167], v[76:79], v[220:223]\n"
"v_mfma_f32_16x16x32_bf16 v[224:227], v[144:147], a[208:211], 0\n"
"v_mfma_f32_16x16x32_bf16 v[224:227], v[148:151], a[212:215], v[224:227]\n"
"v_mfma_f32_16x16x32_bf16 v[224:227], v[152:155], a[216:219], v[224:227]\n"
"v_mfma_f32_16x16x32_bf16 v[224:227], v[156:159], a[220:223], v[224:227]\n"
"v_mfma_f32_16x16x32_bf16 v[224:227], v[160:163], a[224:227], v[224:227]\n"
"v_mfma_f32_16x16x32_bf16 v[224:227], v[164:167], a[228:231], v[224:227]\n"
"v_mfma_f32_16x16x32_bf16 v[228:231], v[144:147], a[232:235], 0\n"
"v_mfma_f32_16x16x32_bf16 v[228:231], v[148:151], a[236:239], v[228:231]\n"
"v_mfma_f32_16x16x32_bf16 v[228:231], v[152:155], a[240:243], v[228:231]\n"
"v_mfma_f32_16x16x32_bf16 v[228:231], v[156:159], a[244:247], v[228:231]\n"
"v_mfma_f32_16x16x32_bf16 v[228:231], v[160:163], a[248:251], v[228:231]\n"
"v_mfma_f32_16x16x32_bf16 v[228:231], v[164:167], a[252:255], v[228:231]\n"
"v_cvt_pk_bf16_f32 v216, 0, v216\n"
"v_cvt_pk_bf16_f32 v217, 0, v217\n"
"v_cvt_pk_bf16_f32 v218, 0, v218\n"
"v_cvt_pk_bf16_f32 v219, 0, v219\n"
"v_cvt_pk_bf16_f32 v220, 0, v220\n"
"v_cvt_pk_bf16_f32 v221, 0, v221\n"
"v_cvt_pk_bf16_f32 v222, 0, v222\n"
"v_cvt_pk_bf16_f32 v223, 0, v223\n"
"v_cvt_pk_bf16_f32 v224, 0, v224\n"
"v_cvt_pk_bf16_f32 v225, 0, v225\n"
"v_cvt_pk_bf16_f32 v226, 0, v226\n"
"v_cvt_pk_bf16_f32 v227, 0, v227\n"
"v_cvt_pk_bf16_f32 v228, 0, v228\n"
"v_cvt_pk_bf16_f32 v229, 0, v229\n"
"v_cvt_pk_bf16_f32 v230, 0, v230\n"
"v_cvt_pk_bf16_f32 v231, 0, v231\n"
"v_mul_f32 v216, s85, v216\n"
"v_mul_f32 v217, s85, v217\n"
"v_mul_f32 v218, s85, v218\n"
"v_mul_f32 v219, s85, v219\n"
"v_mul_f32 v220, s85, v220\n"
"v_mul_f32 v221, s85, v221\n"
"v_mul_f32 v222, s85, v222\n"
"v_mul_f32 v223, s85, v223\n"
"v_mul_f32 v224, s85, v224\n"
"v_mul_f32 v225, s85, v225\n"
"v_mul_f32 v226, s85, v226\n"
"v_mul_f32 v227, s85, v227\n"
"v_mul_f32 v228, s85, v228\n"
"v_mul_f32 v229, s85, v229\n"
"v_mul_f32 v230, s85, v230\n"
"v_mul_f32 v231, s85, v231\n"
"v_cvt_pk_bf16_f32 v216, 0, v216\n"
"v_cvt_pk_bf16_f32 v217, 0, v217\n"
"v_cvt_pk_bf16_f32 v218, 0, v218\n"
"v_cvt_pk_bf16_f32 v219, 0, v219\n"
"v_cvt_pk_bf16_f32 v220, 0, v220\n"
"v_cvt_pk_bf16_f32 v221, 0, v221\n"
"v_cvt_pk_bf16_f32 v222, 0, v222\n"
"v_cvt_pk_bf16_f32 v223, 0, v223\n"
"v_cvt_pk_bf16_f32 v224, 0, v224\n"
"v_cvt_pk_bf16_f32 v225, 0, v225\n"
"v_cvt_pk_bf16_f32 v226, 0, v226\n"
"v_cvt_pk_bf16_f32 v227, 0, v227\n"
"v_cvt_pk_bf16_f32 v228, 0, v228\n"
"v_cvt_pk_bf16_f32 v229, 0, v229\n"
"v_cvt_pk_bf16_f32 v230, 0, v230\n"
"v_cvt_pk_bf16_f32 v231, 0, v231\n"
"v_fma_f32 v216, v216, s86, -v28\n"
"v_fma_f32 v217, v217, s86, -v28\n"
"v_fma_f32 v218, v218, s86, -v28\n"
"v_fma_f32 v219, v219, s86, -v28\n"
"v_fma_f32 v220, v220, s86, -v29\n"
"v_fma_f32 v221, v221, s86, -v29\n"
"v_fma_f32 v222, v222, s86, -v29\n"
"v_fma_f32 v223, v223, s86, -v29\n"
"v_fma_f32 v224, v224, s86, -v30\n"
"v_fma_f32 v225, v225, s86, -v30\n"
"v_fma_f32 v226, v226, s86, -v30\n"
"v_fma_f32 v227, v227, s86, -v30\n"
"v_fma_f32 v228, v228, s86, -v31\n"
"v_fma_f32 v229, v229, s86, -v31\n"
"v_fma_f32 v230, v230, s86, -v31\n"
"v_fma_f32 v231, v231, s86, -v31\n"
"v_exp_f32 v216, v216\n"
"v_exp_f32 v217, v217\n"
"v_exp_f32 v218, v218\n"
"v_exp_f32 v219, v219\n"
"v_exp_f32 v220, v220\n"
"v_exp_f32 v221, v221\n"
"v_exp_f32 v222, v222\n"
"v_exp_f32 v223, v223\n"
"v_exp_f32 v224, v224\n"
"v_exp_f32 v225, v225\n"
"v_exp_f32 v226, v226\n"
"v_exp_f32 v227, v227\n"
"v_exp_f32 v228, v228\n"
"v_exp_f32 v229, v229\n"
"v_exp_f32 v230, v230\n"
"v_exp_f32 v231, v231\n"
"v_cvt_pk_bf16_f32 v96, v216, v217\n"
"v_cvt_pk_bf16_f32 v97, v218, v219\n"
"v_cvt_pk_bf16_f32 v100, v220, v221\n"
"v_cvt_pk_bf16_f32 v101, v222, v223\n"
"v_cvt_pk_bf16_f32 v104, v224, v225\n"
"v_cvt_pk_bf16_f32 v105, v226, v227\n"
"v_cvt_pk_bf16_f32 v108, v228, v229\n"
"v_cvt_pk_bf16_f32 v109, v230, v231\n"
"ds_read_b128 v[144:147], v244 offset:18432\n"
"ds_read_b128 v[148:151], v245 offset:18432\n"
"ds_read_b128 v[152:155], v244 offset:19456\n"
"ds_read_b128 v[156:159], v245 offset:19456\n"
"ds_read_b128 v[160:163], v244 offset:20480\n"
"ds_read_b128 v[164:167], v245 offset:20480\n"
"s_waitcnt lgkmcnt(0)\n"
"v_mfma_f32_16x16x32_bf16 v[216:219], v[144:147], v[32:35], 0\n"
"v_mfma_f32_16x16x32_bf16 v[216:219], v[148:151], v[36:39], v[216:219]\n"
"v_mfma_f32_16x16x32_bf16 v[216:219], v[152:155], v[40:43], v[216:219]\n"
"v_mfma_f32_16x16x32_bf16 v[216:219], v[156:159], v[44:47], v[216:219]\n"
"v_mfma_f32_16x16x32_bf16 v[216:219], v[160:163], v[48:51], v[216:219]\n"
"v_mfma_f32_16x16x32_bf16 v[216:219], v[164:167], v[52:55], v[216:219]\n"
"v_mfma_f32_16x16x32_bf16 v[220:223], v[144:147], v[56:59], 0\n"
"v_mfma_f32_16x16x32_bf16 v[220:223], v[148:151], v[60:63], v[220:223]\n"
"v_mfma_f32_16x16x32_bf16 v[220:223], v[152:155], v[64:67], v[220:223]\n"
"v_mfma_f32_16x16x32_bf16 v[220:223], v[156:159], v[68:71], v[220:223]\n"
"v_mfma_f32_16x16x32_bf16 v[220:223], v[160:163], v[72:75], v[220:223]\n"
"v_mfma_f32_16x16x32_bf16 v[220:223], v[164:167], v[76:79], v[220:223]\n"
"v_mfma_f32_16x16x32_bf16 v[224:227], v[144:147], a[208:211], 0\n"
"v_mfma_f32_16x16x32_bf16 v[224:227], v[148:151], a[212:215], v[224:227]\n"
"v_mfma_f32_16x16x32_bf16 v[224:227], v[152:155], a[216:219], v[224:227]\n"
"v_mfma_f32_16x16x32_bf16 v[224:227], v[156:159], a[220:223], v[224:227]\n"
"v_mfma_f32_16x16x32_bf16 v[224:227], v[160:163], a[224:227], v[224:227]\n"
"v_mfma_f32_16x16x32_bf16 v[224:227], v[164:167], a[228:231], v[224:227]\n"
"v_mfma_f32_16x16x32_bf16 v[228:231], v[144:147], a[232:235], 0\n"
"v_mfma_f32_16x16x32_bf16 v[228:231], v[148:151], a[236:239], v[228:231]\n"
"v_mfma_f32_16x16x32_bf16 v[228:231], v[152:155], a[240:243], v[228:231]\n"
"v_mfma_f32_16x16x32_bf16 v[228:231], v[156:159], a[244:247], v[228:231]\n"
"v_mfma_f32_16x16x32_bf16 v[228:231], v[160:163], a[248:251], v[228:231]\n"
"v_mfma_f32_16x16x32_bf16 v[228:231], v[164:167], a[252:255], v[228:231]\n"
"v_cvt_pk_bf16_f32 v216, 0, v216\n"
"v_cvt_pk_bf16_f32 v217, 0, v217\n"
"v_cvt_pk_bf16_f32 v218, 0, v218\n"
"v_cvt_pk_bf16_f32 v219, 0, v219\n"
"v_cvt_pk_bf16_f32 v220, 0, v220\n"
"v_cvt_pk_bf16_f32 v221, 0, v221\n"
"v_cvt_pk_bf16_f32 v222, 0, v222\n"
"v_cvt_pk_bf16_f32 v223, 0, v223\n"
"v_cvt_pk_bf16_f32 v224, 0, v224\n"
"v_cvt_pk_bf16_f32 v225, 0, v225\n"
"v_cvt_pk_bf16_f32 v226, 0, v226\n"
"v_cvt_pk_bf16_f32 v227, 0, v227\n"
"v_cvt_pk_bf16_f32 v228, 0, v228\n"
"v_cvt_pk_bf16_f32 v229, 0, v229\n"
"v_cvt_pk_bf16_f32 v230, 0, v230\n"
"v_cvt_pk_bf16_f32 v231, 0, v231\n"
"v_mul_f32 v216, s85, v216\n"
"v_mul_f32 v217, s85, v217\n"
"v_mul_f32 v218, s85, v218\n"
"v_mul_f32 v219, s85, v219\n"
"v_mul_f32 v220, s85, v220\n"
"v_mul_f32 v221, s85, v221\n"
"v_mul_f32 v222, s85, v222\n"
"v_mul_f32 v223, s85, v223\n"
"v_mul_f32 v224, s85, v224\n"
"v_mul_f32 v225, s85, v225\n"
"v_mul_f32 v226, s85, v226\n"
"v_mul_f32 v227, s85, v227\n"
"v_mul_f32 v228, s85, v228\n"
"v_mul_f32 v229, s85, v229\n"
"v_mul_f32 v230, s85, v230\n"
"v_mul_f32 v231, s85, v231\n"
"v_cvt_pk_bf16_f32 v216, 0, v216\n"
"v_cvt_pk_bf16_f32 v217, 0, v217\n"
"v_cvt_pk_bf16_f32 v218, 0, v218\n"
"v_cvt_pk_bf16_f32 v219, 0, v219\n"
"v_cvt_pk_bf16_f32 v220, 0, v220\n"
"v_cvt_pk_bf16_f32 v221, 0, v221\n"
"v_cvt_pk_bf16_f32 v222, 0, v222\n"
"v_cvt_pk_bf16_f32 v223, 0, v223\n"
"v_cvt_pk_bf16_f32 v224, 0, v224\n"
"v_cvt_pk_bf16_f32 v225, 0, v225\n"
"v_cvt_pk_bf16_f32 v226, 0, v226\n"
"v_cvt_pk_bf16_f32 v227, 0, v227\n"
"v_cvt_pk_bf16_f32 v228, 0, v228\n"
"v_cvt_pk_bf16_f32 v229, 0, v229\n"
"v_cvt_pk_bf16_f32 v230, 0, v230\n"
"v_cvt_pk_bf16_f32 v231, 0, v231\n"
"v_fma_f32 v216, v216, s86, -v28\n"
"v_fma_f32 v217, v217, s86, -v28\n"
"v_fma_f32 v218, v218, s86, -v28\n"
"v_fma_f32 v219, v219, s86, -v28\n"
"v_fma_f32 v220, v220, s86, -v29\n"
"v_fma_f32 v221, v221, s86, -v29\n"
"v_fma_f32 v222, v222, s86, -v29\n"
"v_fma_f32 v223, v223, s86, -v29\n"
"v_fma_f32 v224, v224, s86, -v30\n"
"v_fma_f32 v225, v225, s86, -v30\n"
"v_fma_f32 v226, v226, s86, -v30\n"
"v_fma_f32 v227, v227, s86, -v30\n"
"v_fma_f32 v228, v228, s86, -v31\n"
"v_fma_f32 v229, v229, s86, -v31\n"
"v_fma_f32 v230, v230, s86, -v31\n"
"v_fma_f32 v231, v231, s86, -v31\n"
"v_exp_f32 v216, v216\n"
"v_exp_f32 v217, v217\n"
"v_exp_f32 v218, v218\n"
"v_exp_f32 v219, v219\n"
"v_exp_f32 v220, v220\n"
"v_exp_f32 v221, v221\n"
"v_exp_f32 v222, v222\n"
"v_exp_f32 v223, v223\n"
"v_exp_f32 v224, v224\n"
"v_exp_f32 v225, v225\n"
"v_exp_f32 v226, v226\n"
"v_exp_f32 v227, v227\n"
"v_exp_f32 v228, v228\n"
"v_exp_f32 v229, v229\n"
"v_exp_f32 v230, v230\n"
"v_exp_f32 v231, v231\n"
"v_cvt_pk_bf16_f32 v98, v216, v217\n"
"v_cvt_pk_bf16_f32 v99, v218, v219\n"
"v_cvt_pk_bf16_f32 v102, v220, v221\n"
"v_cvt_pk_bf16_f32 v103, v222, v223\n"
"v_cvt_pk_bf16_f32 v106, v224, v225\n"
"v_cvt_pk_bf16_f32 v107, v226, v227\n"
"v_cvt_pk_bf16_f32 v110, v228, v229\n"
"v_cvt_pk_bf16_f32 v111, v230, v231\n"
"s_barrier\n"
"s_add_u32 m0, s83, 49152\n"
"s_nop 0\n"
"buffer_load_dwordx4 v7, s[48:51], s84 offen offset:0 lds\n"
"buffer_load_dwordx4 v7, s[48:51], s84 offen offset:3072 lds\n"
"s_add_u32 m0, s83, 50048\n"
"s_nop 0\n"
"buffer_load_dwordx4 v7, s[48:51], s84 offen offset:128 lds\n"
"buffer_load_dwordx4 v7, s[48:51], s84 offen offset:3200 lds\n"
"s_add_u32 m0, s83, 50944\n"
"s_nop 0\n"
"buffer_load_dwordx4 v7, s[48:51], s84 offen offset:256 lds\n"
"buffer_load_dwordx4 v7, s[48:51], s84 offen offset:3328 lds\n"
"s_waitcnt vmcnt(0)\n"
"s_barrier\n"
"ds_read_b64_tr_b16 v[192:193], v246 offset:0\n"
"ds_read_b64_tr_b16 v[194:195], v246 offset:6144\n"
"ds_read_b64_tr_b16 v[196:197], v246 offset:12288\n"
"ds_read_b64_tr_b16 v[198:199], v246 offset:18432\n"
"s_waitcnt lgkmcnt(0)\n"
"v_mfma_f32_16x16x32_bf16 a[0:3], v[192:195], v[80:83], a[0:3]\n"
"v_mfma_f32_16x16x32_bf16 a[0:3], v[196:199], v[96:99], a[0:3]\n"
"v_mfma_f32_16x16x32_bf16 a[4:7], v[192:195], v[84:87], a[4:7]\n"
"v_mfma_f32_16x16x32_bf16 a[4:7], v[196:199], v[100:103], a[4:7]\n"
"v_mfma_f32_16x16x32_bf16 a[8:11], v[192:195], v[88:91], a[8:11]\n"
"v_mfma_f32_16x16x32_bf16 a[8:11], v[196:199], v[104:107], a[8:11]\n"
"v_mfma_f32_16x16x32_bf16 a[12:15], v[192:195], v[92:95], a[12:15]\n"
"v_mfma_f32_16x16x32_bf16 a[12:15], v[196:199], v[108:111], a[12:15]\n"
"ds_read_b64_tr_b16 v[192:193], v247 offset:0\n"
"ds_read_b64_tr_b16 v[194:195], v247 offset:6144\n"
"ds_read_b64_tr_b16 v[196:197], v247 offset:12288\n"
"ds_read_b64_tr_b16 v[198:199], v247 offset:18432\n"
"s_waitcnt lgkmcnt(0)\n"
"v_mfma_f32_16x16x32_bf16 a[16:19], v[192:195], v[80:83], a[16:19]\n"
"v_mfma_f32_16x16x32_bf16 a[16:19], v[196:199], v[96:99], a[16:19]\n"
"v_mfma_f32_16x16x32_bf16 a[20:23], v[192:195], v[84:87], a[20:23]\n"
"v_mfma_f32_16x16x32_bf16 a[20:23], v[196:199], v[100:103], a[20:23]\n"
"v_mfma_f32_16x16x32_bf16 a[24:27], v[192:195], v[88:91], a[24:27]\n"
"v_mfma_f32_16x16x32_bf16 a[24:27], v[196:199], v[104:107], a[24:27]\n"
"v_mfma_f32_16x16x32_bf16 a[28:31], v[192:195], v[92:95], a[28:31]\n"
"v_mfma_f32_16x16x32_bf16 a[28:31], v[196:199], v[108:111], a[28:31]\n"
"ds_read_b64_tr_b16 v[192:193], v248 offset:0\n"
"ds_read_b64_tr_b16 v[194:195], v248 offset:6144\n"
"ds_read_b64_tr_b16 v[196:197], v248 offset:12288\n"
"ds_read_b64_tr_b16 v[198:199], v248 offset:18432\n"
"s_waitcnt lgkmcnt(0)\n"
"v_mfma_f32_16x16x32_bf16 a[32:35], v[192:195], v[80:83], a[32:35]\n"
"v_mfma_f32_16x16x32_bf16 a[32:35], v[196:199], v[96:99], a[32:35]\n"
"v_mfma_f32_16x16x32_bf16 a[36:39], v[192:195], v[84:87], a[36:39]\n"
"v_mfma_f32_16x16x32_bf16 a[36:39], v[196:199], v[100:103], a[36:39]\n"
"v_mfma_f32_16x16x32_bf16 a[40:43], v[192:195], v[88:91], a[40:43]\n"
"v_mfma_f32_16x16x32_bf16 a[40:43], v[196:199], v[104:107], a[40:43]\n"
"v_mfma_f32_16x16x32_bf16 a[44:47], v[192:195], v[92:95], a[44:47]\n"
"v_mfma_f32_16x16x32_bf16 a[44:47], v[196:199], v[108:111], a[44:47]\n"
"ds_read_b64_tr_b16 v[192:193], v249 offset:0\n"
"ds_read_b64_tr_b16 v[194:195], v249 offset:6144\n"
"ds_read_b64_tr_b16 v[196:197], v249 offset:12288\n"
"ds_read_b64_tr_b16 v[198:199], v249 offset:18432\n"
"s_waitcnt lgkmcnt(0)\n"
"v_mfma_f32_16x16x32_bf16 a[48:51], v[192:195], v[80:83], a[48:51]\n"
"v_mfma_f32_16x16x32_bf16 a[48:51], v[196:199], v[96:99], a[48:51]\n"
"v_mfma_f32_16x16x32_bf16 a[52:55], v[192:195], v[84:87], a[52:55]\n"
"v_mfma_f32_16x16x32_bf16 a[52:55], v[196:199], v[100:103], a[52:55]\n"
"v_mfma_f32_16x16x32_bf16 a[56:59], v[192:195], v[88:91], a[56:59]\n"
"v_mfma_f32_16x16x32_bf16 a[56:59], v[196:199], v[104:107], a[56:59]\n"
"v_mfma_f32_16x16x32_bf16 a[60:63], v[192:195], v[92:95], a[60:63]\n"
"v_mfma_f32_16x16x32_bf16 a[60:63], v[196:199], v[108:111], a[60:63]\n"
"ds_read_b64_tr_b16 v[192:193], v246 offset:1024\n"
"ds_read_b64_tr_b16 v[194:195], v246 offset:7168\n"
"ds_read_b64_tr_b16 v[196:197], v246 offset:13312\n"
"ds_read_b64_tr_b16 v[198:199], v246 offset:19456\n"
"s_waitcnt lgkmcnt(0)\n"
"v_mfma_f32_16x16x32_bf16 a[64:67], v[192:195], v[80:83], a[64:67]\n"
"v_mfma_f32_16x16x32_bf16 a[64:67], v[196:199], v[96:99], a[64:67]\n"
"v_mfma_f32_16x16x32_bf16 a[68:71], v[192:195], v[84:87], a[68:71]\n"
"v_mfma_f32_16x16x32_bf16 a[68:71], v[196:199], v[100:103], a[68:71]\n"
"v_mfma_f32_16x16x32_bf16 a[72:75], v[192:195], v[88:91], a[72:75]\n"
"v_mfma_f32_16x16x32_bf16 a[72:75], v[196:199], v[104:107], a[72:75]\n"
"v_mfma_f32_16x16x32_bf16 a[76:79], v[192:195], v[92:95], a[76:79]\n"
"v_mfma_f32_16x16x32_bf16 a[76:79], v[196:199], v[108:111], a[76:79]\n"
"ds_read_b64_tr_b16 v[192:193], v247 offset:1024\n"
"ds_read_b64_tr_b16 v[194:195], v247 offset:7168\n"
"ds_read_b64_tr_b16 v[196:197], v247 offset:13312\n"
"ds_read_b64_tr_b16 v[198:199], v247 offset:19456\n"
"s_waitcnt lgkmcnt(0)\n"
"v_mfma_f32_16x16x32_bf16 a[80:83], v[192:195], v[80:83], a[80:83]\n"
"v_mfma_f32_16x16x32_bf16 a[80:83], v[196:199], v[96:99], a[80:83]\n"
"v_mfma_f32_16x16x32_bf16 a[84:87], v[192:195], v[84:87], a[84:87]\n"
"v_mfma_f32_16x16x32_bf16 a[84:87], v[196:199], v[100:103], a[84:87]\n"
"v_mfma_f32_16x16x32_bf16 a[88:91], v[192:195], v[88:91], a[88:91]\n"
"v_mfma_f32_16x16x32_bf16 a[88:91], v[196:199], v[104:107], a[88:91]\n"
"v_mfma_f32_16x16x32_bf16 a[92:95], v[192:195], v[92:95], a[92:95]\n"
"v_mfma_f32_16x16x32_bf16 a[92:95], v[196:199], v[108:111], a[92:95]\n"
"ds_read_b64_tr_b16 v[192:193], v248 offset:1024\n"
"ds_read_b64_tr_b16 v[194:195], v248 offset:7168\n"
"ds_read_b64_tr_b16 v[196:197], v248 offset:13312\n"
"ds_read_b64_tr_b16 v[198:199], v248 offset:19456\n"
"s_waitcnt lgkmcnt(0)\n"
"v_mfma_f32_16x16x32_bf16 a[96:99], v[192:195], v[80:83], a[96:99]\n"
"v_mfma_f32_16x16x32_bf16 a[96:99], v[196:199], v[96:99], a[96:99]\n"
"v_mfma_f32_16x16x32_bf16 a[100:103], v[192:195], v[84:87], a[100:103]\n"
"v_mfma_f32_16x16x32_bf16 a[100:103], v[196:199], v[100:103], a[100:103]\n"
"v_mfma_f32_16x16x32_bf16 a[104:107], v[192:195], v[88:91], a[104:107]\n"
"v_mfma_f32_16x16x32_bf16 a[104:107], v[196:199], v[104:107], a[104:107]\n"
"v_mfma_f32_16x16x32_bf16 a[108:111], v[192:195], v[92:95], a[108:111]\n"
"v_mfma_f32_16x16x32_bf16 a[108:111], v[196:199], v[108:111], a[108:111]\n"
"ds_read_b64_tr_b16 v[192:193], v249 offset:1024\n"
"ds_read_b64_tr_b16 v[194:195], v249 offset:7168\n"
"ds_read_b64_tr_b16 v[196:197], v249 offset:13312\n"
"ds_read_b64_tr_b16 v[198:199], v249 offset:19456\n"
"s_waitcnt lgkmcnt(0)\n"
"v_mfma_f32_16x16x32_bf16 a[112:115], v[192:195], v[80:83], a[112:115]\n"
"v_mfma_f32_16x16x32_bf16 a[112:115], v[196:199], v[96:99], a[112:115]\n"
"v_mfma_f32_16x16x32_bf16 a[116:119], v[192:195], v[84:87], a[116:119]\n"
"v_mfma_f32_16x16x32_bf16 a[116:119], v[196:199], v[100:103], a[116:119]\n"
"v_mfma_f32_16x16x32_bf16 a[120:123], v[192:195], v[88:91], a[120:123]\n"
"v_mfma_f32_16x16x32_bf16 a[120:123], v[196:199], v[104:107], a[120:123]\n"
"v_mfma_f32_16x16x32_bf16 a[124:127], v[192:195], v[92:95], a[124:127]\n"
"v_mfma_f32_16x16x32_bf16 a[124:127], v[196:199], v[108:111], a[124:127]\n"
"ds_read_b64_tr_b16 v[192:193], v246 offset:2048\n"
"ds_read_b64_tr_b16 v[194:195], v246 offset:8192\n"
"ds_read_b64_tr_b16 v[196:197], v246 offset:14336\n"
"ds_read_b64_tr_b16 v[198:199], v246 offset:20480\n"
"s_waitcnt lgkmcnt(0)\n"
"v_mfma_f32_16x16x32_bf16 a[128:131], v[192:195], v[80:83], a[128:131]\n"
"v_mfma_f32_16x16x32_bf16 a[128:131], v[196:199], v[96:99], a[128:131]\n"
"v_mfma_f32_16x16x32_bf16 a[132:135], v[192:195], v[84:87], a[132:135]\n"
"v_mfma_f32_16x16x32_bf16 a[132:135], v[196:199], v[100:103], a[132:135]\n"
"v_mfma_f32_16x16x32_bf16 a[136:139], v[192:195], v[88:91], a[136:139]\n"
"v_mfma_f32_16x16x32_bf16 a[136:139], v[196:199], v[104:107], a[136:139]\n"
"v_mfma_f32_16x16x32_bf16 a[140:143], v[192:195], v[92:95], a[140:143]\n"
"v_mfma_f32_16x16x32_bf16 a[140:143], v[196:199], v[108:111], a[140:143]\n"
"ds_read_b64_tr_b16 v[192:193], v247 offset:2048\n"
"ds_read_b64_tr_b16 v[194:195], v247 offset:8192\n"
"ds_read_b64_tr_b16 v[196:197], v247 offset:14336\n"
"ds_read_b64_tr_b16 v[198:199], v247 offset:20480\n"
"s_waitcnt lgkmcnt(0)\n"
"v_mfma_f32_16x16x32_bf16 a[144:147], v[192:195], v[80:83], a[144:147]\n"
"v_mfma_f32_16x16x32_bf16 a[144:147], v[196:199], v[96:99], a[144:147]\n"
"v_mfma_f32_16x16x32_bf16 a[148:151], v[192:195], v[84:87], a[148:151]\n"
"v_mfma_f32_16x16x32_bf16 a[148:151], v[196:199], v[100:103], a[148:151]\n"
"v_mfma_f32_16x16x32_bf16 a[152:155], v[192:195], v[88:91], a[152:155]\n"
"v_mfma_f32_16x16x32_bf16 a[152:155], v[196:199], v[104:107], a[152:155]\n"
"v_mfma_f32_16x16x32_bf16 a[156:159], v[192:195], v[92:95], a[156:159]\n"
"v_mfma_f32_16x16x32_bf16 a[156:159], v[196:199], v[108:111], a[156:159]\n"
"ds_read_b64_tr_b16 v[192:193], v248 offset:2048\n"
"ds_read_b64_tr_b16 v[194:195], v248 offset:8192\n"
"ds_read_b64_tr_b16 v[196:197], v248 offset:14336\n"
"ds_read_b64_tr_b16 v[198:199], v248 offset:20480\n"
"s_waitcnt lgkmcnt(0)\n"
"v_mfma_f32_16x16x32_bf16 a[160:163], v[192:195], v[80:83], a[160:163]\n"
"v_mfma_f32_16x16x32_bf16 a[160:163], v[196:199], v[96:99], a[160:163]\n"
"v_mfma_f32_16x16x32_bf16 a[164:167], v[192:195], v[84:87], a[164:167]\n"
"v_mfma_f32_16x16x32_bf16 a[164:167], v[196:199], v[100:103], a[164:167]\n"
"v_mfma_f32_16x16x32_bf16 a[168:171], v[192:195], v[88:91], a[168:171]\n"
"v_mfma_f32_16x16x32_bf16 a[168:171], v[196:199], v[104:107], a[168:171]\n"
"v_mfma_f32_16x16x32_bf16 a[172:175], v[192:195], v[92:95], a[172:175]\n"
"v_mfma_f32_16x16x32_bf16 a[172:175], v[196:199], v[108:111], a[172:175]\n"
"ds_read_b64_tr_b16 v[192:193], v249 offset:2048\n"
"ds_read_b64_tr_b16 v[194:195], v249 offset:8192\n"
"ds_read_b64_tr_b16 v[196:197], v249 offset:14336\n"
"ds_read_b64_tr_b16 v[198:199], v249 offset:20480\n"
"s_waitcnt lgkmcnt(0)\n"
"v_mfma_f32_16x16x32_bf16 a[176:179], v[192:195], v[80:83], a[176:179]\n"
"v_mfma_f32_16x16x32_bf16 a[176:179], v[196:199], v[96:99], a[176:179]\n"
"v_mfma_f32_16x16x32_bf16 a[180:183], v[192:195], v[84:87], a[180:183]\n"
"v_mfma_f32_16x16x32_bf16 a[180:183], v[196:199], v[100:103], a[180:183]\n"
"v_mfma_f32_16x16x32_bf16 a[184:187], v[192:195], v[88:91], a[184:187]\n"
"v_mfma_f32_16x16x32_bf16 a[184:187], v[196:199], v[104:107], a[184:187]\n"
"v_mfma_f32_16x16x32_bf16 a[188:191], v[192:195], v[92:95], a[188:191]\n"
"v_mfma_f32_16x16x32_bf16 a[188:191], v[196:199], v[108:111], a[188:191]\n"
"v_mfma_f32_16x16x32_bf16 a[192:195], v[240:243], v[80:83], a[192:195]\n"
"v_mfma_f32_16x16x32_bf16 a[192:195], v[240:243], v[96:99], a[192:195]\n"
"v_mfma_f32_16x16x32_bf16 a[196:199], v[240:243], v[84:87], a[196:199]\n"
"v_mfma_f32_16x16x32_bf16 a[196:199], v[240:243], v[100:103], a[196:199]\n"
"v_mfma_f32_16x16x32_bf16 a[200:203], v[240:243], v[88:91], a[200:203]\n"
"v_mfma_f32_16x16x32_bf16 a[200:203], v[240:243], v[104:107], a[200:203]\n"
"v_mfma_f32_16x16x32_bf16 a[204:207], v[240:243], v[92:95], a[204:207]\n"
"v_mfma_f32_16x16x32_bf16 a[204:207], v[240:243], v[108:111], a[204:207]\n"
"s_add_u32 s84, s84, 24576\n"
"s_add_u32 s92, s92, 1\n"
"s_cmp_lt_u32 s92, 8\n"
"s_nop 7\n"
"s_cbranch_scc1 .Lslow2\n"
"v_mov_b32 v112, 0\n"
"v_mov_b32 v113, 0\n"
"v_mov_b32 v114, 0\n"
"v_mov_b32 v115, 0\n"
"v_mov_b32 v116, 0\n"
"v_mov_b32 v117, 0\n"
"v_mov_b32 v118, 0\n"
"v_mov_b32 v119, 0\n"
"v_mov_b32 v120, 0\n"
"v_mov_b32 v121, 0\n"
"v_mov_b32 v122, 0\n"
"v_mov_b32 v123, 0\n"
"v_mov_b32 v124, 0\n"
"v_mov_b32 v125, 0\n"
"v_mov_b32 v126, 0\n"
"v_mov_b32 v127, 0\n"
"v_mov_b32 v128, 0\n"
"v_mov_b32 v129, 0\n"
"v_mov_b32 v130, 0\n"
"v_mov_b32 v131, 0\n"
"v_mov_b32 v132, 0\n"
"v_mov_b32 v133, 0\n"
"v_mov_b32 v134, 0\n"
"v_mov_b32 v135, 0\n"
"v_mov_b32 v136, 0\n"
"v_mov_b32 v137, 0\n"
"v_mov_b32 v138, 0\n"
"v_mov_b32 v139, 0\n"
"v_mov_b32 v140, 0\n"
"v_mov_b32 v141, 0\n"
"v_mov_b32 v142, 0\n"
"v_mov_b32 v143, 0\n"
"s_mov_b32 m0, s93\n"
"s_nop 7\n"
"s_nop 7\n"
"s_branch .Lslow_back\n"
".fill 6144,4,0xBF800000\n"
  :: "s"(__builtin_amdgcn_kernarg_segment_ptr()), "s"(__builtin_amdgcn_readfirstlane(blockIdx.x)), "s"(w) : "v0","v1","v2","v3","v4","v5","v6","v7","v8","v9","v10","v11","v12","v13","v14","v15","v16","v17","v18","v19","v20","v21","v22","v23","v24","v25","v26","v27","v28","v29","v30","v31","v32","v33","v34","v35","v36","v37","v38","v39","v40","v41","v42","v43","v44","v45","v46","v47","v48","v49","v50","v51","v52","v53","v54","v55","v56","v57","v58","v59","v60","v61","v62","v63","v64","v65","v66","v67","v68","v69","v70","v71","v72","v73","v74","v75","v76","v77","v78","v79","v80","v81","v82","v83","v84","v85","v86","v87","v88","v89","v90","v91","v92","v93","v94","v95","v96","v97","v98","v99","v100","v101","v102","v103","v104","v105","v106","v107","v108","v109","v110","v111","v112","v113","v114","v115","v116","v117","v118","v119","v120","v121","v122","v123","v124","v125","v126","v127","v128","v129","v130","v131","v132","v133","v134","v135","v136","v137","v138","v139","v140","v141","v142","v143","v144","v145","v146","v147","v148","v149","v150","v151","v152","v153","v154","v155","v156","v157","v158","v159","v160","v161","v162","v163","v164","v165","v166","v167","v168","v169","v170","v171","v172","v173","v174","v175","v176","v177","v178","v179","v180","v181","v182","v183","v184","v185","v186","v187","v188","v189","v190","v191","v192","v193","v194","v195","v196","v197","v198","v199","v200","v201","v202","v203","v204","v205","v206","v207","v208","v209","v210","v211","v212","v213","v214","v215","v216","v217","v218","v219","v220","v221","v222","v223","v224","v225","v226","v227","v228","v229","v230","v231","v232","v233","v234","v235","v236","v237","v238","v239","v240","v241","v242","v243","v244","v245","v246","v247","v248","v249","v250","v251","v252","v253","v254","v255","a0","a1","a2","a3","a4","a5","a6","a7","a8","a9","a10","a11","a12","a13","a14","a15","a16","a17","a18","a19","a20","a21","a22","a23","a24","a25","a26","a27","a28","a29","a30","a31","a32","a33","a34","a35","a36","a37","a38","a39","a40","a41","a42","a43","a44","a45","a46","a47","a48","a49","a50","a51","a52","a53","a54","a55","a56","a57","a58","a59","a60","a61","a62","a63","a64","a65","a66","a67","a68","a69","a70","a71","a72","a73","a74","a75","a76","a77","a78","a79","a80","a81","a82","a83","a84","a85","a86","a87","a88","a89","a90","a91","a92","a93","a94","a95","a96","a97","a98","a99","a100","a101","a102","a103","a104","a105","a106","a107","a108","a109","a110","a111","a112","a113","a114","a115","a116","a117","a118","a119","a120","a121","a122","a123","a124","a125","a126","a127","a128","a129","a130","a131","a132","a133","a134","a135","a136","a137","a138","a139","a140","a141","a142","a143","a144","a145","a146","a147","a148","a149","a150","a151","a152","a153","a154","a155","a156","a157","a158","a159","a160","a161","a162","a163","a164","a165","a166","a167","a168","a169","a170","a171","a172","a173","a174","a175","a176","a177","a178","a179","a180","a181","a182","a183","a184","a185","a186","a187","a188","a189","a190","a191","a192","a193","a194","a195","a196","a197","a198","a199","a200","a201","a202","a203","a204","a205","a206","a207","a208","a209","a210","a211","a212","a213","a214","a215","a216","a217","a218","a219","a220","a221","a222","a223","a224","a225","a226","a227","a228","a229","a230","a231","a232","a233","a234","a235","a236","a237","a238","a239","a240","a241","a242","a243","a244","a245","a246","a247","a248","a249","a250","a251","a252","a253","a254","a255","s4","s5","s6","s7","s8","s9","s10","s11","s12","s13","s14","s15","s16","s17","s18","s19","s20","s21","s22","s23","s24","s25","s26","s27","s28","s29","s30","s31","s32","s33","s34","s35","s36","s37","s38","s39","s40","s41","s42","s43","s44","s45","s46","s47","s48","s49","s50","s51","s52","s53","s54","s55","s56","s57","s58","s59","s60","s61","s62","s63","s64","s65","s66","s67","s68","s69","s70","s71","s72","s73","s74","s75","s76","s77","s78","s79","s80","s81","s82","s83","s84","s85","s86","s87","s88","s89","s90","s91","s92","s93","s94","s95","s96","s97","s98","s99","s100","s101","vcc","memory");
}

extern "C" void kernel_launch(void* const* d_in, const int* in_sizes, int n_in, void* d_out, int out_size, void* d_ws, size_t ws_size, hipStream_t stream){
  (void)hipFuncSetAttribute((const void*)k_main, hipFuncAttributeMaxDynamicSharedMemorySize, 163840);
  k_main<<<256,256,163840,stream>>>(d_in[0],d_in[1],d_in[2],d_in[3],d_in[4],d_in[6],d_in[7],d_in[8],d_out,d_ws);
}
```
